```python
import torch

def _softmax(x, axis=-1):
    return torch.softmax(x, dim=axis)

def _gelu(x):
    return torch.nn.functional.gelu(x, approximate='tanh')

def _silu(x):
    return torch.nn.functional.silu(x)

def _sigmoid(x):
    return torch.sigmoid(x)

def _layernorm(x, axis=-1, eps=1e-05):
    m = x.mean(dim=axis, keepdim=True)
    v = x.var(dim=axis, keepdim=True, unbiased=False)
    return (x - m) / torch.sqrt(v + eps)

def _scatter_add(tgt, idx, src, axis=0):
    return tgt.clone().index_add_(axis, idx, src)

def _segment_sum(src, seg, n):
    out = torch.zeros((n,) + src.shape[1:], dtype=src.dtype)
    return out.index_add_(0, seg, src)

def _pad(x, pw):
    flat = tuple((v for pair in reversed(pw) for v in pair))
    return torch.nn.functional.pad(x, flat)
kernel_name = 'adaLN_cond__mha_block_cached__dense_activation_silu__prompt'

def setup_inputs():

    _w = lambda a: (lambda t: t.to(torch.bfloat16) if t.is_floating_point() else t)(torch.as_tensor(a))

    def _A_setup():
        B, Sq, D = (1, 2048, 448)
        H, Sk, Dh = (16, 2048, 256)
        return {'x': _w((lambda *s: torch.randn(*s, dtype=torch.bfloat16) * 0.1)(B, Sq, D)), 'k_cache': _w((lambda *s: torch.randn(*s, dtype=torch.bfloat16) * 0.8)(B, H, Sk, Dh)), 'v_cache': _w((lambda *s: torch.randn(*s, dtype=torch.bfloat16) * 3.2)(B, H, Sk, Dh)), 'wq': _w((lambda *s: torch.randn(*s, dtype=torch.bfloat16) * 0.8)(D, H * Dh) * D ** (-0.5)), 'wo': _w((lambda *s: torch.randn(*s, dtype=torch.bfloat16) * 3.2)(H * Dh, D) * (H * Dh) ** (-0.5))}
    _A_in = _A_setup()

    def _B_setup():
        B, S, D, F = (1, 2048, 448, 1792)
        return {'x': _w((lambda *s: torch.randn(*s, dtype=torch.bfloat16) * 0.1)(B, S, D)), 'w1': _w((lambda *s: torch.randn(*s, dtype=torch.bfloat16) * 3.2)(D, F) * D ** (-0.5)), 'w2': _w((lambda *s: torch.randn(*s, dtype=torch.bfloat16) * 3.2)(F, D) * F ** (-0.5))}
    _B_in = _B_setup()
    return {'A_x': _A_in['x'], 'A_k_cache': _A_in['k_cache'], 'A_v_cache': _A_in['v_cache'], 'A_wq': _A_in['wq'], 'A_wo': _A_in['wo'], 'B_x': _B_in['x'], 'B_w1': _B_in['w1'], 'B_w2': _B_in['w2'], 'c': _w((lambda *s: torch.randn(*s, dtype=torch.bfloat16) * 0.1)(1, 448) * 8.0), 'w_mod': _w((lambda *s: torch.randn(*s, dtype=torch.bfloat16) * 0.1)(448, 6 * 448) * 448 ** (-0.5) * 8.0)}

def reference(A_x, A_k_cache, A_v_cache, A_wq, A_wo, B_x, B_w1, B_w2, c, w_mod):

    def _A_ref(x, k_cache, v_cache, wq, wo):
        B, Sq, D = x.shape
        H, Dh = (16, 256)
        q = (lambda x, *d: x.permute(*d))((x @ wq).reshape(B, Sq, H, Dh), 0, 2, 1, 3)
        scores = torch.einsum('bhqd,bhkd->bhqk', q, k_cache) * Dh ** (-0.5)
        p = _softmax(scores, axis=-1)
        o = torch.einsum('bhqk,bhkd->bhqd', p, v_cache)
        return (lambda x, *d: x.permute(*d))(o, 0, 2, 1, 3).reshape(B, Sq, H * Dh) @ wo

    def _B_ref(x, w1, w2):
        h = torch.einsum('bsd,df->bsf', x, w1)
        h = _silu(h)
        out = torch.einsum('bsf,fd->bsd', h, w2)
        return out
    x = A_x
    mod = (_silu(c) @ w_mod).reshape(c.shape[0], 6, 1, -1)
    sa, ca, ga, sb, cb, gb = (mod[:, 0].clone(), mod[:, 1].clone(), mod[:, 2].clone(), mod[:, 3].clone(), mod[:, 4].clone(), mod[:, 5].clone())
    h = _layernorm(x, axis=-1) * (1 + ca) + sa
    x = x + ga * _A_ref(h, A_k_cache, A_v_cache, A_wq, A_wo)
    h = _layernorm(x, axis=-1) * (1 + cb) + sb
    x = x + gb * _B_ref(h, B_w1, B_w2)
    return x
```

```cpp
#include <hip/hip_runtime.h>
#include <hip/hip_bf16.h>
typedef unsigned short u16;
__device__ __forceinline__ float bf2f(u16 x){ return __uint_as_float(((unsigned)x)<<16); }
__device__ __forceinline__ u16 f2bf(float f){ __hip_bfloat16 b = __float2bfloat16(f); return *(u16*)&b; }
constexpr int S=2048,D=448,H=16,DH=256,F=1792;
__global__ void k_mod(const u16* c,const u16* w,float* mod){
  int n=blockIdx.x*64+threadIdx.x; if(n>=6*D) return; float a=0;
  for(int k=0;k<D;k++){ float x=bf2f(c[k]); float s=x/(1.f+__expf(-x)); s=bf2f(f2bf(s)); a+=s*bf2f(w[k*6*D+n]); }
  mod[n]=bf2f(f2bf(a));
}
__global__ void k_ln(const float* x,const float* sh,const float* sc,u16* h){
  int r=blockIdx.x; int t=threadIdx.x; __shared__ float red[64];
  float s=0; for(int k=t;k<D;k+=64) s+=x[r*D+k];
  for(int o=32;o;o>>=1) s+=__shfl_xor(s,o); float m=s/D;
  float v=0; for(int k=t;k<D;k+=64){ float d=x[r*D+k]-m; v+=d*d; }
  for(int o=32;o;o>>=1) v+=__shfl_xor(v,o); float rs=rsqrtf(v/D+1e-5f);
  for(int k=t;k<D;k+=64) h[r*D+k]=f2bf((x[r*D+k]-m)*rs*(1.f+sc[k])+sh[k]);
}
__global__ void k_cvt(const u16* x,float* y,int n){ int i=blockIdx.x*256+threadIdx.x; if(i<n) y[i]=bf2f(x[i]); }
template<int MODE> __global__ void k_gemm(const u16* A,const u16* B,int M,int N,int K,u16* C,const float* xin,const float* g,float* xout){
  __shared__ float As[64][33], Bs[32][65];
  int tx=threadIdx.x%16, ty=threadIdx.x/16; int m0=blockIdx.y*64,n0=blockIdx.x*64; float acc[4][4]={};
  for(int k0=0;k0<K;k0+=32){
    for(int i=threadIdx.x;i<64*32;i+=256){ As[i/32][i%32]=bf2f(A[(size_t)(m0+i/32)*K+k0+i%32]); Bs[i/64][i%64]=bf2f(B[(size_t)(k0+i/64)*N+n0+i%64]); }
    __syncthreads();
    for(int k=0;k<32;k++){ float a[4],b[4]; for(int i=0;i<4;i++){a[i]=As[ty*4+i][k]; b[i]=Bs[k][tx*4+i];}
      for(int i=0;i<4;i++)for(int j=0;j<4;j++)acc[i][j]+=a[i]*b[j]; }
    __syncthreads();
  }
  for(int i=0;i<4;i++)for(int j=0;j<4;j++){ int m=m0+ty*4+i,n=n0+tx*4+j; float v=acc[i][j];
    if(MODE==0) C[(size_t)m*N+n]=f2bf(v);
    if(MODE==1) C[(size_t)m*N+n]=f2bf(v/(1.f+__expf(-v)));
    if(MODE==2) xout[(size_t)m*N+n]=xin[(size_t)m*N+n]+g[n]*v;
    if(MODE==3) C[(size_t)m*N+n]=f2bf(xin[(size_t)m*N+n]+g[n]*v); }
}
__global__ void k_attn(const u16* q,const u16* K,const u16* V,u16* o){
  __shared__ float qs[16][DH]; __shared__ float p[16][S];
  int h=blockIdx.y,r0=blockIdx.x*16,t=threadIdx.x;
  for(int i=t;i<16*DH;i+=256) qs[i/DH][i%DH]=bf2f(q[(size_t)(r0+i/DH)*(H*DH)+h*DH+i%DH]);
  __syncthreads();
  for(int key=t;key<S;key+=256){ float a[16]={}; const u16* kr=K+((size_t)h*S+key)*DH;
    for(int d=0;d<DH;d++){ float kv=bf2f(kr[d]); for(int i=0;i<16;i++)a[i]+=qs[i][d]*kv; }
    for(int i=0;i<16;i++)p[i][key]=a[i]*0.0625f; }
  __syncthreads();
  int w=t/64,l=t%64;
  for(int i=w;i<16;i+=4){ float m=-1e30f; for(int k=l;k<S;k+=64)m=fmaxf(m,p[i][k]); for(int o=32;o;o>>=1)m=fmaxf(m,__shfl_xor(m,o));
    float s=0; for(int k=l;k<S;k+=64){float e=__expf(p[i][k]-m);p[i][k]=e;s+=e;} for(int o=32;o;o>>=1)s+=__shfl_xor(s,o);
    for(int k=l;k<S;k+=64)p[i][k]/=s; }
  __syncthreads();
  float a[16]={}; for(int key=0;key<S;key++){ float vv=bf2f(V[((size_t)h*S+key)*DH+t]); for(int i=0;i<16;i++)a[i]+=p[i][key]*vv; }
  for(int i=0;i<16;i++) o[(size_t)(r0+i)*(H*DH)+h*DH+t]=f2bf(a[i]);
}
extern "C" void kernel_launch(void* const* d_in, const int* in_sizes, int n_in, void* d_out, int out_size, void* d_ws, size_t ws_size, hipStream_t stream){
  const u16 *x=(const u16*)d_in[0],*K=(const u16*)d_in[1],*V=(const u16*)d_in[2],*wq=(const u16*)d_in[3],*wo=(const u16*)d_in[4],*w1=(const u16*)d_in[6],*w2=(const u16*)d_in[7],*c=(const u16*)d_in[8],*wm=(const u16*)d_in[9];
  char* ws=(char*)d_ws; float* mod=(float*)ws; float* xf=(float*)(ws+(1<<20)); float* x1=(float*)(ws+(8<<20)); u16* h=(u16*)(ws+(16<<20)); u16* q=(u16*)(ws+(32<<20)); u16* o=(u16*)(ws+(64<<20)); u16* h1=(u16*)(ws+(96<<20));
  k_mod<<<42,64,0,stream>>>(c,wm,mod);
  k_cvt<<<S*D/256,256,0,stream>>>(x,xf,S*D);
  k_ln<<<S,64,0,stream>>>(xf,mod,mod+D,h);
  k_gemm<0><<<dim3(H*DH/64,S/64),256,0,stream>>>(h,wq,S,H*DH,D,q,nullptr,nullptr,nullptr);
  k_attn<<<dim3(S/16,H),256,0,stream>>>(q,K,V,o);
  k_gemm<2><<<dim3(D/64,S/64),256,0,stream>>>(o,wo,S,D,H*DH,nullptr,xf,mod+2*D,x1);
  k_ln<<<S,64,0,stream>>>(x1,mod+3*D,mod+4*D,h);
  k_gemm<1><<<dim3(F/64,S/64),256,0,stream>>>(h,w1,S,F,D,h1,nullptr,nullptr,nullptr);
  k_gemm<3><<<dim3(D/64,S/64),256,0,stream>>>(h1,w2,S,D,F,(u16*)d_out,x1,mod+5*D,nullptr);
}
```

```cpp
#include <hip/hip_runtime.h>

extern "C" __global__ __launch_bounds__(256, 1) void k_mega(const void* x, const void* wq, const void* kc, const void* vc, const void* wo, const void* w1, const void* w2, const void* out, const void* c, const void* wmod, const void* ws) {
  extern __shared__ __attribute__((aligned(16))) char lds[];
  const unsigned wv = __builtin_amdgcn_readfirstlane(threadIdx.x >> 6);
  asm volatile(
  "s_load_dwordx8 s[40:47], %0, 0x0\n\t"
  "s_load_dwordx4 s[92:95], %0, 0x40\n\t"
  "s_load_dwordx2 s[98:99], %0, 0x50\n\t"
  "s_load_dwordx8 s[48:55], %0, 0x20\n\t"
  "s_mov_b32 s63, %1\n\t"
  "s_mov_b32 s62, %2\n\t"
  "v_mbcnt_lo_u32_b32 v254, -1, 0\n\t"
  "v_mbcnt_hi_u32_b32 v254, -1, v254\n\t"
  "s_lshr_b32 s64, s63, 3\n\t"
  "s_lshl_b32 s64, s64, 2\n\t"
  "s_add_u32 s64, s64, s62\n\t"
  "s_lshl_b32 s64, s64, 9\n\t"
  "v_and_b32_e32 v252, 3, v254\n\t"
  "v_lshlrev_b32_e32 v252, 7, v252\n\t"
  "v_add_u32_e32 v252, s64, v252\n\t"
  "s_getpc_b64 s[96:97]\n\t"
  "global_load_dword v253, v252, s[96:97]\n\t"
  "s_bfe_u32 s60, s63, 131073\n\t"
  "s_lshl_b32 s60, s60, 2\n\t"
  "s_bfe_u32 s64, s63, 131075\n\t"
  "s_or_b32 s60, s60, s64\n\t"
  "s_and_b32 s61, s63, 1\n\t"
  "s_lshl_b32 s61, s61, 3\n\t"
  "s_lshr_b32 s64, s63, 5\n\t"
  "s_or_b32 s61, s61, s64\n\t"
  "s_lshl_b32 s30, s62, 13\n\t"
  "s_and_b32 s56, s60, 2\n\t"
  "s_lshl_b32 s56, s56, 2\n\t"
  "s_lshr_b32 s64, s60, 2\n\t"
  "s_lshl_b32 s64, s64, 1\n\t"
  "s_or_b32 s56, s56, s64\n\t"
  "s_and_b32 s64, s60, 1\n\t"
  "s_or_b32 s56, s56, s64\n\t"
  "s_and_b32 s59, s56, 2\n\t"
  "s_mul_i32 s59, s59, 7168\n\t"
  "s_and_b32 s64, s63, 3\n\t"
  "s_lshl_b32 s64, s64, 2\n\t"
  "s_add_u32 s64, s64, s62\n\t"
  "s_mul_i32 s64, s64, 28\n\t"
  "s_cmp_ge_u32 s63, 224\n\t"
  "s_cselect_b32 s64, 1024, s64\n\t"
  "v_lshrrev_b32_e32 v36, 1, v254\n\t"
  "v_add_u32_e32 v37, 8, v254\n\t"
  "v_and_b32_e32 v37, 64, v37\n\t"
  "v_lshl_add_u32 v36, v37, 4, v36\n\t"
  "v_add_u32_e32 v36, s64, v36\n\t"
  "v_and_b32_e32 v72, 1, v254\n\t"
  "v_mul_u32_u24_e32 v4, 5376, v36\n\t"
  "v_lshl_add_u32 v4, v72, 4, v4\n\t"
  "v_lshlrev_b32_e32 v12, 1, v36\n\t"
  "v_mov_b32_e32 v13, 262144\n\t"
  "v_add_u32_e32 v36, 0, v254\n\t"
  "v_mul_u32_u24_e32 v36, 1171, v36\n\t"
  "v_bfe_u32 v36, v36, 17, 2\n\t"
  "v_lshlrev_b32_e32 v36, 1, v36\n\t"
  "v_xor_b32_e32 v36, v36, v254\n\t"
  "v_lshlrev_b32_e32 v104, 4, v36\n\t"
  "v_add_u32_e32 v36, 64, v254\n\t"
  "v_mul_u32_u24_e32 v36, 1171, v36\n\t"
  "v_bfe_u32 v36, v36, 17, 2\n\t"
  "v_lshlrev_b32_e32 v36, 1, v36\n\t"
  "v_xor_b32_e32 v36, v36, v254\n\t"
  "v_lshlrev_b32_e32 v105, 4, v36\n\t"
  "v_add_u32_e32 v36, 128, v254\n\t"
  "v_mul_u32_u24_e32 v36, 1171, v36\n\t"
  "v_bfe_u32 v36, v36, 17, 2\n\t"
  "v_lshlrev_b32_e32 v36, 1, v36\n\t"
  "v_xor_b32_e32 v36, v36, v254\n\t"
  "v_lshlrev_b32_e32 v106, 4, v36\n\t"
  "v_add_u32_e32 v36, 192, v254\n\t"
  "v_mul_u32_u24_e32 v36, 1171, v36\n\t"
  "v_bfe_u32 v36, v36, 17, 2\n\t"
  "v_lshlrev_b32_e32 v36, 1, v36\n\t"
  "v_xor_b32_e32 v36, v36, v254\n\t"
  "v_lshlrev_b32_e32 v107, 4, v36\n\t"
  "v_add_u32_e32 v36, 256, v254\n\t"
  "v_mul_u32_u24_e32 v36, 1171, v36\n\t"
  "v_bfe_u32 v36, v36, 17, 2\n\t"
  "v_lshlrev_b32_e32 v36, 1, v36\n\t"
  "v_xor_b32_e32 v36, v36, v254\n\t"
  "v_lshlrev_b32_e32 v120, 4, v36\n\t"
  "v_add_u32_e32 v120, 4096, v120\n\t"
  "v_add_u32_e32 v36, 320, v254\n\t"
  "v_mul_u32_u24_e32 v36, 1171, v36\n\t"
  "v_bfe_u32 v36, v36, 17, 2\n\t"
  "v_lshlrev_b32_e32 v36, 1, v36\n\t"
  "v_xor_b32_e32 v36, v36, v254\n\t"
  "v_lshlrev_b32_e32 v121, 4, v36\n\t"
  "v_add_u32_e32 v121, 4096, v121\n\t"
  "v_add_u32_e32 v36, 384, v254\n\t"
  "v_mul_u32_u24_e32 v36, 1171, v36\n\t"
  "v_bfe_u32 v36, v36, 17, 2\n\t"
  "v_lshlrev_b32_e32 v36, 1, v36\n\t"
  "v_xor_b32_e32 v36, v36, v254\n\t"
  "v_lshlrev_b32_e32 v122, 4, v36\n\t"
  "v_add_u32_e32 v122, 4096, v122\n\t"
  "s_lshl_b32 s64, s61, 7\n\t"
  "s_lshl_b32 s65, s62, 5\n\t"
  "s_add_u32 s64, s64, s65\n\t"
  "s_mul_i32 s66, s64, 896\n\t"
  "s_add_u32 s66, s66, s59\n\t"
  "s_lshl_b32 s59, s59, 1\n\t"
  "s_sub_u32 s59, 7168, s59\n\t"
  "s_cmp_eq_u32 s62, 3\n\t"
  "s_cselect_b32 s64, 4, s62\n\t"
  "s_lshl_b32 s31, s64, 15\n\t"
  "s_waitcnt lgkmcnt(0)\n\t"
  "s_mov_b64 s[90:91], s[40:41]\n\t"
  "s_lshr_b32 s64, s63, 2\n\t"
  "s_lshl_b32 s64, s64, 5\n\t"
  "s_add_u32 s8, s94, s64\n\t"
  "s_addc_u32 s9, s95, 0\n\t"
  "s_and_b32 s9, s9, 0xffff\n\t"
  "s_mov_b32 s10, 0\n\t"
  "s_mov_b32 s11, 0x20000\n\t"
  "s_sub_u32 s10, 2408448, s64\n\t"
  "s_add_u32 s12, s92, 0\n\t"
  "s_addc_u32 s13, s93, 0\n\t"
  "s_and_b32 s13, s13, 0xffff\n\t"
  "s_mov_b32 s14, 896\n\t"
  "s_mov_b32 s15, 0x20000\n\t"
  "s_add_u32 s20, s40, 0\n\t"
  "s_addc_u32 s21, s41, 0\n\t"
  "s_and_b32 s21, s21, 0xffff\n\t"
  "s_mov_b32 s22, 1835008\n\t"
  "s_mov_b32 s23, 0x20000\n\t"
  "buffer_load_dwordx4 v[0:3], v4, s[8:11], 0 offen\n\t"
  "buffer_load_ushort v32, v12, s[12:15], 0 offen\n\t"
  "global_load_dword v255, v13, s[98:99] sc1\n\t"
  "s_add_u32 m0, s31, 0\n\t"
  "s_nop 0\n\t"
  "buffer_load_dwordx4 v104, s[20:23], s66 offen lds\n\t"
  "buffer_load_dwordx4 v105, s[20:23], s66 offen offset:1024 lds\n\t"
  "buffer_load_dwordx4 v106, s[20:23], s66 offen offset:2048 lds\n\t"
  "buffer_load_dwordx4 v107, s[20:23], s66 offen offset:3072 lds\n\t"
  "s_add_u32 m0, s31, 4096\n\t"
  "s_nop 0\n\t"
  "buffer_load_dwordx4 v120, s[20:23], s66 offen lds\n\t"
  "buffer_load_dwordx4 v121, s[20:23], s66 offen offset:1024 lds\n\t"
  "buffer_load_dwordx4 v122, s[20:23], s66 offen offset:2048 lds\n\t"
  "s_add_u32 s66, s66, 7168\n\t"
  "s_add_u32 m0, s31, 7168\n\t"
  "s_nop 0\n\t"
  "buffer_load_dwordx4 v104, s[20:23], s66 offen lds\n\t"
  "buffer_load_dwordx4 v105, s[20:23], s66 offen offset:1024 lds\n\t"
  "buffer_load_dwordx4 v106, s[20:23], s66 offen offset:2048 lds\n\t"
  "buffer_load_dwordx4 v107, s[20:23], s66 offen offset:3072 lds\n\t"
  "s_add_u32 m0, s31, 11264\n\t"
  "s_nop 0\n\t"
  "buffer_load_dwordx4 v120, s[20:23], s66 offen lds\n\t"
  "buffer_load_dwordx4 v121, s[20:23], s66 offen offset:1024 lds\n\t"
  "buffer_load_dwordx4 v122, s[20:23], s66 offen offset:2048 lds\n\t"
  "s_add_u32 s66, s66, s59\n\t"
  "v_bfe_u32 v36, v254, 2, 3\n\t"
  "v_and_b32_e32 v37, 3, v254\n\t"
  "v_lshrrev_b32_e32 v73, 5, v254\n\t"
  "v_xor_b32_e32 v38, 0, v37\n\t"
  "v_lshl_add_u32 v38, v73, 2, v38\n\t"
  "v_lshlrev_b32_e32 v38, 4, v38\n\t"
  "v_lshl_add_u32 v38, v36, 13, v38\n\t"
  "v_add_u32_e32 v224, 3072, v38\n\t"
  "v_add_u32_e32 v225, 2176, v38\n\t"
  "v_add_u32_e32 v226, 1280, v38\n\t"
  "v_add_u32_e32 v227, 384, v38\n\t"
  "v_xor_b32_e32 v38, 2, v37\n\t"
  "v_lshl_add_u32 v38, v73, 2, v38\n\t"
  "v_lshlrev_b32_e32 v38, 4, v38\n\t"
  "v_lshl_add_u32 v38, v36, 13, v38\n\t"
  "v_add_u32_e32 v228, 3072, v38\n\t"
  "v_add_u32_e32 v229, 2176, v38\n\t"
  "v_add_u32_e32 v230, 1280, v38\n\t"
  "v_add_u32_e32 v231, 384, v38\n\t"
  "s_lshl_b32 s64, s60, 9\n\t"
  "s_add_u32 s16, s42, s64\n\t"
  "s_addc_u32 s17, s43, 0\n\t"
  "s_sub_u32 s16, s16, 3072\n\t"
  "s_subb_u32 s17, s17, 0\n\t"
  "s_and_b32 s17, s17, 0xffff\n\t"
  "s_mov_b32 s18, 3665408\n\t"
  "s_mov_b32 s19, 0x20000\n\t"
  "s_lshl_b32 s36, s62, 17\n\t"
  "s_and_b32 s64, s61, 7\n\t"
  "s_lshl_b32 s64, s64, 2\n\t"
  "s_add_u32 s64, s64, s62\n\t"
  "s_mul_i32 s64, s64, 56\n\t"
  "v_min_u32_e32 v80, 55, v254\n\t"
  "v_add_u32_e32 v80, s64, v80\n\t"
  "v_lshrrev_b32_e32 v81, 2, v80\n\t"
  "v_and_b32_e32 v80, 3, v80\n\t"
  "v_lshlrev_b32_e32 v80, 7, v80\n\t"
  "v_lshl_add_u32 v137, v81, 13, v80\n\t"
  "v_lshlrev_b32_e32 v74, 5, v72\n\t"
  "v_lshl_add_u32 v74, v73, 4, v74\n\t"
  "v_bfe_u32 v75, v254, 4, 1\n\t"
  "v_lshl_add_u32 v74, v75, 3, v74\n\t"
  "s_lshl_b32 s64, s62, 6\n\t"
  "s_add_u32 s64, s64, 163328\n\t"
  "v_add_u32_e32 v74, s64, v74\n\t"
  "s_lshl_b32 s64, s60, 20\n\t"
  "s_add_u32 s8, s44, s64\n\t"
  "s_addc_u32 s9, s45, 0\n\t"
  "s_sub_u32 s8, s8, 3072\n\t"
  "s_subb_u32 s9, s9, 0\n\t"
  "s_and_b32 s9, s9, 0xffff\n\t"
  "s_mov_b32 s10, 1051648\n\t"
  "s_mov_b32 s11, 0x20000\n\t"
  "s_add_u32 s12, s46, s64\n\t"
  "s_addc_u32 s13, s47, 0\n\t"
  "s_sub_u32 s12, s12, 3072\n\t"
  "s_subb_u32 s13, s13, 0\n\t"
  "s_and_b32 s13, s13, 0xffff\n\t"
  "s_mov_b32 s14, 1051648\n\t"
  "s_mov_b32 s15, 0x20000\n\t"
  "s_and_b32 s65, s61, 7\n\t"
  "s_cmp_lt_u32 s65, 4\n\t"
  "s_cselect_b32 s24, s44, s46\n\t"
  "s_cselect_b32 s25, s45, s47\n\t"
  "s_add_u32 s24, s24, s64\n\t"
  "s_addc_u32 s25, s25, 0\n\t"
  "s_and_b32 s25, s25, 0xffff\n\t"
  "s_mov_b32 s26, 1048576\n\t"
  "s_mov_b32 s27, 0x20000\n\t"
  "s_mov_b32 s35, 0\n\t"
  "s_mov_b32 s28, s30\n\t"
  "s_mov_b32 s29, s30\n\t"
  "s_mov_b32 s34, 0\n\t"
  "s_mov_b32 s68, 0\n\t"
  "s_mov_b32 s96, 0xffff0000\n\t"
  "s_and_b32 s39, s62, 1\n\t"
  "s_lshl_b32 s39, s39, 14\n\t"
  "s_mov_b32 s57, 98304\n\t"
  "s_cmp_lt_u32 s62, 2\n\t"
  "s_cselect_b32 s57, 131072, s57\n\t"
  "s_and_b32 s64, s61, 3\n\t"
  "s_lshl_b32 s64, s64, 2\n\t"
  "s_add_u32 s64, s64, s62\n\t"
  "s_lshl_b32 s67, s64, 11\n\t"
  "s_add_u32 s58, s39, s57\n\t"
  "v_and_b32_e32 v252, 15, v254\n\t"
  "v_lshrrev_b32_e32 v233, 4, v254\n\t"
  "v_bfe_u32 v234, v254, 1, 2\n\t"
  "v_lshlrev_b32_e32 v234, 1, v234\n\t"
  "v_xor_b32_e32 v234, v234, v233\n\t"
  "v_lshlrev_b32_e32 v234, 4, v234\n\t"
  "v_mul_u32_u24_e32 v235, 896, v252\n\t"
  "v_add_u32_e32 v234, v234, v235\n\t"
  "v_add_u32_e32 v240, s31, v234\n\t"
  "v_xor_b32_e32 v234, 64, v234\n\t"
  "v_add_u32_e32 v88, s31, v234\n\t"
  "v_mov_b32_e32 v89, v240\n\t"
  "v_mov_b32_e32 v232, 0x3f803f80\n\t"
  "v_mov_b32_e32 v233, 0x3f803f80\n\t"
  "v_mov_b32_e32 v234, 0x3f803f80\n\t"
  "v_mov_b32_e32 v235, 0x3f803f80\n\t"
  "v_and_b32_e32 v82, 3, v254\n\t"
  "v_lshrrev_b32_e32 v83, 4, v254\n\t"
  "v_lshrrev_b32_e32 v90, 2, v252\n\t"
  "v_cmp_eq_u32_e64 s[40:41], 1, v82\n\t"
  "v_cmp_eq_u32_e64 s[42:43], 2, v82\n\t"
  "v_cmp_eq_u32_e64 s[44:45], 3, v82\n\t"
  "v_cmp_eq_u32_e64 s[46:47], v83, v90\n\t"
  "s_waitcnt vmcnt(14)\n\t"
  "v_readfirstlane_b32 s69, v255\n\t"
  "v_lshlrev_b32_e32 v32, 16, v32\n\t"
  "v_mul_f32_e32 v40, 0xbfb8aa3b, v32\n\t"
  "v_exp_f32_e32 v40, v40\n\t"
  "s_add_u32 m0, s31, 14336\n\t"
  "s_nop 0\n\t"
  "buffer_load_dwordx4 v104, s[20:23], s66 offen lds\n\t"
  "s_add_u32 s69, s69, 1\n\t"
  "v_lshlrev_b32_e32 v48, 16, v0\n\t"
  "v_and_b32_e32 v49, s96, v0\n\t"
  "buffer_load_dwordx4 v105, s[20:23], s66 offen offset:1024 lds\n\t"
  "v_add_f32_e32 v40, 1.0, v40\n\t"
  "v_rcp_f32_e32 v40, v40\n\t"
  "v_lshlrev_b32_e32 v50, 16, v1\n\t"
  "buffer_load_dwordx4 v106, s[20:23], s66 offen offset:2048 lds\n\t"
  "v_and_b32_e32 v51, s96, v1\n\t"
  "v_lshlrev_b32_e32 v56, 16, v2\n\t"
  "v_and_b32_e32 v57, s96, v2\n\t"
  "buffer_load_dwordx4 v107, s[20:23], s66 offen offset:3072 lds\n\t"
  "v_lshlrev_b32_e32 v58, 16, v3\n\t"
  "v_and_b32_e32 v59, s96, v3\n\t"
  "v_mul_f32_e32 v40, v40, v32\n\t"
  "s_add_u32 m0, s31, 18432\n\t"
  "s_nop 0\n\t"
  "buffer_load_dwordx4 v120, s[20:23], s66 offen lds\n\t"
  "v_cvt_pk_bf16_f32 v40, v40, v40\n\t"
  "v_and_b32_e32 v40, s96, v40\n\t"
  "v_mul_f32_e32 v48, v40, v48\n\t"
  "buffer_load_dwordx4 v121, s[20:23], s66 offen offset:1024 lds\n\t"
  "v_mul_f32_e32 v49, v40, v49\n\t"
  "v_mul_f32_e32 v50, v40, v50\n\t"
  "v_mul_f32_e32 v51, v40, v51\n\t"
  "buffer_load_dwordx4 v122, s[20:23], s66 offen offset:2048 lds\n\t"
  "s_add_u32 s66, s66, 7168\n\t"
  "v_mul_f32_e32 v56, v40, v56\n\t"
  "v_mul_f32_e32 v57, v40, v57\n\t"
  "v_mul_f32_e32 v58, v40, v58\n\t"
  "s_add_u32 m0, s31, 21504\n\t"
  "s_nop 0\n\t"
  "buffer_load_dwordx4 v104, s[20:23], s66 offen lds\n\t"
  "v_mul_f32_e32 v59, v40, v59\n\t"
  "v_permlane32_swap_b32 v48, v56\n\t"
  "v_permlane32_swap_b32 v49, v57\n\t"
  "buffer_load_dwordx4 v105, s[20:23], s66 offen offset:1024 lds\n\t"
  "v_permlane32_swap_b32 v50, v58\n\t"
  "v_permlane32_swap_b32 v51, v59\n\t"
  "v_add_f32_e32 v48, v48, v56\n\t"
  "buffer_load_dwordx4 v106, s[20:23], s66 offen offset:2048 lds\n\t"
  "v_add_f32_e32 v49, v49, v57\n\t"
  "v_add_f32_e32 v50, v50, v58\n\t"
  "v_add_f32_e32 v51, v51, v59\n\t"
  "buffer_load_dwordx4 v107, s[20:23], s66 offen offset:3072 lds\n\t"
  "v_permlane16_swap_b32 v48, v50\n\t"
  "v_permlane16_swap_b32 v49, v51\n\t"
  "v_add_f32_e32 v48, v48, v50\n\t"
  "s_add_u32 m0, s31, 25600\n\t"
  "s_nop 0\n\t"
  "buffer_load_dwordx4 v120, s[20:23], s66 offen lds\n\t"
  "v_add_f32_e32 v49, v49, v51\n\t"
  "s_nop 1\n\t"
  "v_add_f32_dpp v48, v48, v48 row_ror:8 row_mask:0xf bank_mask:0xf\n\t"
  "v_add_f32_dpp v49, v49, v49 row_ror:8 row_mask:0xf bank_mask:0xf\n\t"
  "s_nop 1\n\t"
  "v_add_f32_dpp v48, v48, v48 row_ror:4 row_mask:0xf bank_mask:0xf\n\t"
  "v_add_f32_dpp v49, v49, v49 row_ror:4 row_mask:0xf bank_mask:0xf\n\t"
  "buffer_load_dwordx4 v121, s[20:23], s66 offen offset:1024 lds\n\t"
  "s_nop 1\n\t"
  "v_add_f32_dpp v48, v48, v48 quad_perm:[2,3,0,1] row_mask:0xf bank_mask:0xf\n\t"
  "v_add_f32_dpp v49, v49, v49 quad_perm:[2,3,0,1] row_mask:0xf bank_mask:0xf\n\t"
  "ds_write_b64 v74, v[48:49]\n\t"
  "s_waitcnt lgkmcnt(0)\n\t"
  "s_barrier\n\t"
  "s_cmp_ge_u32 s63, 224\n\t"
  "s_cselect_b32 s64, 1, s62\n\t"
  "s_cmp_lg_u32 s64, 0\n\t"
  "s_cbranch_scc1 .Lnp1\n\t"
  "v_lshlrev_b32_e32 v64, 2, v252\n\t"
  "v_add_u32_e32 v65, 163328, v64\n\t"
  "ds_read_b32 v32, v65\n\t"
  "ds_read_b32 v33, v65 offset:64\n\t"
  "ds_read_b32 v34, v65 offset:128\n\t"
  "ds_read_b32 v35, v65 offset:192\n\t"
  "s_and_b32 s64, s63, 3\n\t"
  "s_mul_i32 s64, s64, 7168\n\t"
  "s_lshr_b32 s65, s63, 2\n\t"
  "s_lshl_b32 s65, s65, 7\n\t"
  "s_add_u32 s64, s64, s65\n\t"
  "s_add_u32 s64, s64, 458752\n\t"
  "v_lshl_add_u32 v65, v64, 1, s64\n\t"
  "v_mov_b32_e32 v41, s69\n\t"
  "s_waitcnt lgkmcnt(2)\n\t"
  "v_add_f32_e32 v32, v32, v33\n\t"
  "s_waitcnt lgkmcnt(0)\n\t"
  "v_add_f32_e32 v34, v34, v35\n\t"
  "v_add_f32_e32 v40, v32, v34\n\t"
  "global_store_dwordx2 v65, v[40:41], s[98:99] sc1\n\t"
  "s_nop 1\n\t"
  ".Lnp1:\n\t"
  "s_waitcnt vmcnt(13)\n\t"
  "ds_read_b128 v[4:7], v89\n\t"
  "ds_read_b128 v[20:23], v88\n\t"
  "ds_read_b128 v[36:39], v89 offset:128\n\t"
  "ds_read_b128 v[52:55], v88 offset:128\n\t"
  "ds_read_b128 v[68:71], v89 offset:256\n\t"
  "ds_read_b128 v[84:87], v88 offset:256\n\t"
  "ds_read_b128 v[100:103], v89 offset:384\n\t"
  "ds_read_b128 v[116:119], v88 offset:384\n\t"
  "ds_read_b128 v[132:135], v89 offset:512\n\t"
  "ds_read_b128 v[148:151], v88 offset:512\n\t"
  "ds_read_b128 v[164:167], v89 offset:640\n\t"
  "ds_read_b128 v[180:183], v88 offset:640\n\t"
  "ds_read_b128 v[196:199], v89 offset:768\n\t"
  "ds_read_b128 v[212:215], v88 offset:768\n\t"
  "s_waitcnt lgkmcnt(13)\n\t"
  "v_mfma_f32_16x16x32_bf16 v[236:239], v[232:235], v[4:7], 0\n\t"
  "v_mfma_f32_16x16x32_bf16 v[244:247], v[4:7], v[4:7], 0\n\t"
  "s_waitcnt lgkmcnt(12)\n\t"
  "v_mfma_f32_16x16x32_bf16 v[236:239], v[232:235], v[20:23], v[236:239]\n\t"
  "v_mfma_f32_16x16x32_bf16 v[244:247], v[20:23], v[20:23], v[244:247]\n\t"
  "buffer_load_dwordx4 v122, s[20:23], s66 offen offset:2048 lds\n\t"
  "s_add_u32 s66, s66, 7168\n\t"
  "s_waitcnt lgkmcnt(11)\n\t"
  "v_mfma_f32_16x16x32_bf16 v[236:239], v[232:235], v[36:39], v[236:239]\n\t"
  "v_mfma_f32_16x16x32_bf16 v[244:247], v[36:39], v[36:39], v[244:247]\n\t"
  "s_waitcnt lgkmcnt(10)\n\t"
  "v_mfma_f32_16x16x32_bf16 v[236:239], v[232:235], v[52:55], v[236:239]\n\t"
  "v_mfma_f32_16x16x32_bf16 v[244:247], v[52:55], v[52:55], v[244:247]\n\t"
  "s_mov_b32 s64, 3072\n\t"
  "buffer_load_dword v255, v137, s[16:19], s64 offen\n\t"
  "s_waitcnt lgkmcnt(9)\n\t"
  "v_mfma_f32_16x16x32_bf16 v[236:239], v[232:235], v[68:71], v[236:239]\n\t"
  "v_mfma_f32_16x16x32_bf16 v[244:247], v[68:71], v[68:71], v[244:247]\n\t"
  "s_waitcnt lgkmcnt(8)\n\t"
  "v_mfma_f32_16x16x32_bf16 v[236:239], v[232:235], v[84:87], v[236:239]\n\t"
  "v_mfma_f32_16x16x32_bf16 v[244:247], v[84:87], v[84:87], v[244:247]\n\t"
  "s_waitcnt lgkmcnt(7)\n\t"
  "v_mfma_f32_16x16x32_bf16 v[236:239], v[232:235], v[100:103], v[236:239]\n\t"
  "v_mfma_f32_16x16x32_bf16 v[244:247], v[100:103], v[100:103], v[244:247]\n\t"
  "s_waitcnt lgkmcnt(6)\n\t"
  "v_mfma_f32_16x16x32_bf16 v[236:239], v[232:235], v[116:119], v[236:239]\n\t"
  "v_mfma_f32_16x16x32_bf16 v[244:247], v[116:119], v[116:119], v[244:247]\n\t"
  "s_waitcnt lgkmcnt(5)\n\t"
  "v_mfma_f32_16x16x32_bf16 v[236:239], v[232:235], v[132:135], v[236:239]\n\t"
  "v_mfma_f32_16x16x32_bf16 v[244:247], v[132:135], v[132:135], v[244:247]\n\t"
  "s_waitcnt lgkmcnt(4)\n\t"
  "v_mfma_f32_16x16x32_bf16 v[236:239], v[232:235], v[148:151], v[236:239]\n\t"
  "v_mfma_f32_16x16x32_bf16 v[244:247], v[148:151], v[148:151], v[244:247]\n\t"
  "s_waitcnt lgkmcnt(3)\n\t"
  "v_mfma_f32_16x16x32_bf16 v[236:239], v[232:235], v[164:167], v[236:239]\n\t"
  "v_mfma_f32_16x16x32_bf16 v[244:247], v[164:167], v[164:167], v[244:247]\n\t"
  "s_waitcnt lgkmcnt(2)\n\t"
  "v_mfma_f32_16x16x32_bf16 v[236:239], v[232:235], v[180:183], v[236:239]\n\t"
  "v_mfma_f32_16x16x32_bf16 v[244:247], v[180:183], v[180:183], v[244:247]\n\t"
  "s_waitcnt lgkmcnt(1)\n\t"
  "v_mfma_f32_16x16x32_bf16 v[236:239], v[232:235], v[196:199], v[236:239]\n\t"
  "v_mfma_f32_16x16x32_bf16 v[244:247], v[196:199], v[196:199], v[244:247]\n\t"
  "s_waitcnt lgkmcnt(0)\n\t"
  "v_mfma_f32_16x16x32_bf16 v[236:239], v[232:235], v[212:215], v[236:239]\n\t"
  "v_mfma_f32_16x16x32_bf16 v[244:247], v[212:215], v[212:215], v[244:247]\n\t"
  "v_lshlrev_b32_e32 v0, 16, v4\n\t"
  "v_and_b32_e32 v1, s96, v4\n\t"
  "v_lshlrev_b32_e32 v2, 16, v5\n\t"
  "v_and_b32_e32 v3, s96, v5\n\t"
  "v_lshlrev_b32_e32 v4, 16, v6\n\t"
  "v_and_b32_e32 v5, s96, v6\n\t"
  "s_nop 0\n\t"
  "v_mul_f32_e32 v237, 0x3b124925, v236\n\t"
  "v_lshlrev_b32_e32 v6, 16, v7\n\t"
  "v_and_b32_e32 v7, s96, v7\n\t"
  "v_cndmask_b32_e64 v244, v244, v245, s[40:41]\n\t"
  "v_lshlrev_b32_e32 v16, 16, v20\n\t"
  "v_and_b32_e32 v17, s96, v20\n\t"
  "v_cndmask_b32_e64 v244, v244, v246, s[42:43]\n\t"
  "v_lshlrev_b32_e32 v18, 16, v21\n\t"
  "v_and_b32_e32 v19, s96, v21\n\t"
  "v_cndmask_b32_e64 v244, v244, v247, s[44:45]\n\t"
  "v_lshlrev_b32_e32 v20, 16, v22\n\t"
  "v_and_b32_e32 v21, s96, v22\n\t"
  "v_cndmask_b32_e64 v244, 0, v244, s[46:47]\n\t"
  "v_lshlrev_b32_e32 v22, 16, v23\n\t"
  "v_and_b32_e32 v23, s96, v23\n\t"
  "v_mov_b32_e32 v245, v244\n\t"
  "v_lshlrev_b32_e32 v32, 16, v36\n\t"
  "v_and_b32_e32 v33, s96, v36\n\t"
  "v_permlane16_swap_b32 v245, v244\n\t"
  "v_lshlrev_b32_e32 v34, 16, v37\n\t"
  "v_and_b32_e32 v35, s96, v37\n\t"
  "v_add_f32_e32 v244, v244, v245\n\t"
  "v_lshlrev_b32_e32 v36, 16, v38\n\t"
  "v_and_b32_e32 v37, s96, v38\n\t"
  "v_mov_b32_e32 v245, v244\n\t"
  "v_lshlrev_b32_e32 v38, 16, v39\n\t"
  "v_and_b32_e32 v39, s96, v39\n\t"
  "v_permlane32_swap_b32 v245, v244\n\t"
  "v_lshlrev_b32_e32 v48, 16, v52\n\t"
  "v_and_b32_e32 v49, s96, v52\n\t"
  "v_add_f32_e32 v244, v244, v245\n\t"
  "v_lshlrev_b32_e32 v50, 16, v53\n\t"
  "v_and_b32_e32 v51, s96, v53\n\t"
  "v_mul_f32_e32 v244, 0x3b124925, v244\n\t"
  "v_lshlrev_b32_e32 v52, 16, v54\n\t"
  "v_and_b32_e32 v53, s96, v54\n\t"
  "v_fma_f32 v244, -v237, v237, v244\n\t"
  "v_lshlrev_b32_e32 v54, 16, v55\n\t"
  "v_and_b32_e32 v55, s96, v55\n\t"
  "v_max_f32_e32 v244, 0, v244\n\t"
  "v_lshlrev_b32_e32 v64, 16, v68\n\t"
  "v_and_b32_e32 v65, s96, v68\n\t"
  "v_add_f32_e32 v244, 0x3727c5ac, v244\n\t"
  "v_lshlrev_b32_e32 v66, 16, v69\n\t"
  "v_and_b32_e32 v67, s96, v69\n\t"
  "v_rsq_f32_e32 v236, v244\n\t"
  "v_lshlrev_b32_e32 v68, 16, v70\n\t"
  "v_and_b32_e32 v69, s96, v70\n\t"
  "v_mul_f32_e32 v237, v237, v236\n\t"
  "v_lshlrev_b32_e32 v70, 16, v71\n\t"
  "v_and_b32_e32 v71, s96, v71\n\t"
  "v_lshlrev_b32_e32 v80, 16, v84\n\t"
  "v_and_b32_e32 v81, s96, v84\n\t"
  "v_lshlrev_b32_e32 v82, 16, v85\n\t"
  "v_and_b32_e32 v83, s96, v85\n\t"
  "v_lshlrev_b32_e32 v84, 16, v86\n\t"
  "v_and_b32_e32 v85, s96, v86\n\t"
  "v_lshlrev_b32_e32 v86, 16, v87\n\t"
  "v_and_b32_e32 v87, s96, v87\n\t"
  "v_lshlrev_b32_e32 v96, 16, v100\n\t"
  "v_and_b32_e32 v97, s96, v100\n\t"
  "v_lshlrev_b32_e32 v98, 16, v101\n\t"
  "v_and_b32_e32 v99, s96, v101\n\t"
  "v_lshlrev_b32_e32 v100, 16, v102\n\t"
  "v_and_b32_e32 v101, s96, v102\n\t"
  "v_lshlrev_b32_e32 v102, 16, v103\n\t"
  "v_and_b32_e32 v103, s96, v103\n\t"
  "v_lshlrev_b32_e32 v112, 16, v116\n\t"
  "v_and_b32_e32 v113, s96, v116\n\t"
  "v_lshlrev_b32_e32 v114, 16, v117\n\t"
  "v_and_b32_e32 v115, s96, v117\n\t"
  "v_lshlrev_b32_e32 v116, 16, v118\n\t"
  "v_and_b32_e32 v117, s96, v118\n\t"
  "v_lshlrev_b32_e32 v118, 16, v119\n\t"
  "v_and_b32_e32 v119, s96, v119\n\t"
  "v_lshlrev_b32_e32 v128, 16, v132\n\t"
  "v_and_b32_e32 v129, s96, v132\n\t"
  "v_lshlrev_b32_e32 v130, 16, v133\n\t"
  "v_and_b32_e32 v131, s96, v133\n\t"
  "v_lshlrev_b32_e32 v132, 16, v134\n\t"
  "v_and_b32_e32 v133, s96, v134\n\t"
  "v_lshlrev_b32_e32 v134, 16, v135\n\t"
  "v_and_b32_e32 v135, s96, v135\n\t"
  "v_lshlrev_b32_e32 v144, 16, v148\n\t"
  "v_and_b32_e32 v145, s96, v148\n\t"
  "v_lshlrev_b32_e32 v146, 16, v149\n\t"
  "v_and_b32_e32 v147, s96, v149\n\t"
  "v_lshlrev_b32_e32 v148, 16, v150\n\t"
  "v_and_b32_e32 v149, s96, v150\n\t"
  "v_lshlrev_b32_e32 v150, 16, v151\n\t"
  "v_and_b32_e32 v151, s96, v151\n\t"
  "v_lshlrev_b32_e32 v160, 16, v164\n\t"
  "v_and_b32_e32 v161, s96, v164\n\t"
  "v_lshlrev_b32_e32 v162, 16, v165\n\t"
  "v_and_b32_e32 v163, s96, v165\n\t"
  "v_lshlrev_b32_e32 v164, 16, v166\n\t"
  "v_and_b32_e32 v165, s96, v166\n\t"
  "v_lshlrev_b32_e32 v166, 16, v167\n\t"
  "v_and_b32_e32 v167, s96, v167\n\t"
  "v_lshlrev_b32_e32 v176, 16, v180\n\t"
  "v_and_b32_e32 v177, s96, v180\n\t"
  "v_lshlrev_b32_e32 v178, 16, v181\n\t"
  "v_and_b32_e32 v179, s96, v181\n\t"
  "v_lshlrev_b32_e32 v180, 16, v182\n\t"
  "v_and_b32_e32 v181, s96, v182\n\t"
  "v_lshlrev_b32_e32 v182, 16, v183\n\t"
  "v_and_b32_e32 v183, s96, v183\n\t"
  "v_lshlrev_b32_e32 v192, 16, v196\n\t"
  "v_and_b32_e32 v193, s96, v196\n\t"
  "v_lshlrev_b32_e32 v194, 16, v197\n\t"
  "v_and_b32_e32 v195, s96, v197\n\t"
  "v_lshlrev_b32_e32 v196, 16, v198\n\t"
  "v_and_b32_e32 v197, s96, v198\n\t"
  "v_lshlrev_b32_e32 v198, 16, v199\n\t"
  "v_and_b32_e32 v199, s96, v199\n\t"
  "v_lshlrev_b32_e32 v208, 16, v212\n\t"
  "v_and_b32_e32 v209, s96, v212\n\t"
  "v_lshlrev_b32_e32 v210, 16, v213\n\t"
  "v_and_b32_e32 v211, s96, v213\n\t"
  "v_lshlrev_b32_e32 v212, 16, v214\n\t"
  "v_and_b32_e32 v213, s96, v214\n\t"
  "v_lshlrev_b32_e32 v214, 16, v215\n\t"
  "v_and_b32_e32 v215, s96, v215\n\t"
  "v_pk_fma_f32 v[0:1], v[0:1], v[236:237], v[236:237] op_sel:[0,0,1] op_sel_hi:[1,0,1] neg_lo:[0,0,1] neg_hi:[0,0,1]\n\t"
  "v_pk_fma_f32 v[2:3], v[2:3], v[236:237], v[236:237] op_sel:[0,0,1] op_sel_hi:[1,0,1] neg_lo:[0,0,1] neg_hi:[0,0,1]\n\t"
  "v_pk_fma_f32 v[4:5], v[4:5], v[236:237], v[236:237] op_sel:[0,0,1] op_sel_hi:[1,0,1] neg_lo:[0,0,1] neg_hi:[0,0,1]\n\t"
  "v_pk_fma_f32 v[6:7], v[6:7], v[236:237], v[236:237] op_sel:[0,0,1] op_sel_hi:[1,0,1] neg_lo:[0,0,1] neg_hi:[0,0,1]\n\t"
  "v_pk_fma_f32 v[16:17], v[16:17], v[236:237], v[236:237] op_sel:[0,0,1] op_sel_hi:[1,0,1] neg_lo:[0,0,1] neg_hi:[0,0,1]\n\t"
  "v_pk_fma_f32 v[18:19], v[18:19], v[236:237], v[236:237] op_sel:[0,0,1] op_sel_hi:[1,0,1] neg_lo:[0,0,1] neg_hi:[0,0,1]\n\t"
  "v_pk_fma_f32 v[20:21], v[20:21], v[236:237], v[236:237] op_sel:[0,0,1] op_sel_hi:[1,0,1] neg_lo:[0,0,1] neg_hi:[0,0,1]\n\t"
  "v_pk_fma_f32 v[22:23], v[22:23], v[236:237], v[236:237] op_sel:[0,0,1] op_sel_hi:[1,0,1] neg_lo:[0,0,1] neg_hi:[0,0,1]\n\t"
  "v_pk_fma_f32 v[32:33], v[32:33], v[236:237], v[236:237] op_sel:[0,0,1] op_sel_hi:[1,0,1] neg_lo:[0,0,1] neg_hi:[0,0,1]\n\t"
  "v_pk_fma_f32 v[34:35], v[34:35], v[236:237], v[236:237] op_sel:[0,0,1] op_sel_hi:[1,0,1] neg_lo:[0,0,1] neg_hi:[0,0,1]\n\t"
  "v_pk_fma_f32 v[36:37], v[36:37], v[236:237], v[236:237] op_sel:[0,0,1] op_sel_hi:[1,0,1] neg_lo:[0,0,1] neg_hi:[0,0,1]\n\t"
  "v_pk_fma_f32 v[38:39], v[38:39], v[236:237], v[236:237] op_sel:[0,0,1] op_sel_hi:[1,0,1] neg_lo:[0,0,1] neg_hi:[0,0,1]\n\t"
  "v_pk_fma_f32 v[48:49], v[48:49], v[236:237], v[236:237] op_sel:[0,0,1] op_sel_hi:[1,0,1] neg_lo:[0,0,1] neg_hi:[0,0,1]\n\t"
  "v_pk_fma_f32 v[50:51], v[50:51], v[236:237], v[236:237] op_sel:[0,0,1] op_sel_hi:[1,0,1] neg_lo:[0,0,1] neg_hi:[0,0,1]\n\t"
  "v_pk_fma_f32 v[52:53], v[52:53], v[236:237], v[236:237] op_sel:[0,0,1] op_sel_hi:[1,0,1] neg_lo:[0,0,1] neg_hi:[0,0,1]\n\t"
  "v_pk_fma_f32 v[54:55], v[54:55], v[236:237], v[236:237] op_sel:[0,0,1] op_sel_hi:[1,0,1] neg_lo:[0,0,1] neg_hi:[0,0,1]\n\t"
  "v_pk_fma_f32 v[64:65], v[64:65], v[236:237], v[236:237] op_sel:[0,0,1] op_sel_hi:[1,0,1] neg_lo:[0,0,1] neg_hi:[0,0,1]\n\t"
  "v_pk_fma_f32 v[66:67], v[66:67], v[236:237], v[236:237] op_sel:[0,0,1] op_sel_hi:[1,0,1] neg_lo:[0,0,1] neg_hi:[0,0,1]\n\t"
  "v_pk_fma_f32 v[68:69], v[68:69], v[236:237], v[236:237] op_sel:[0,0,1] op_sel_hi:[1,0,1] neg_lo:[0,0,1] neg_hi:[0,0,1]\n\t"
  "v_pk_fma_f32 v[70:71], v[70:71], v[236:237], v[236:237] op_sel:[0,0,1] op_sel_hi:[1,0,1] neg_lo:[0,0,1] neg_hi:[0,0,1]\n\t"
  "v_pk_fma_f32 v[80:81], v[80:81], v[236:237], v[236:237] op_sel:[0,0,1] op_sel_hi:[1,0,1] neg_lo:[0,0,1] neg_hi:[0,0,1]\n\t"
  "v_pk_fma_f32 v[82:83], v[82:83], v[236:237], v[236:237] op_sel:[0,0,1] op_sel_hi:[1,0,1] neg_lo:[0,0,1] neg_hi:[0,0,1]\n\t"
  "v_pk_fma_f32 v[84:85], v[84:85], v[236:237], v[236:237] op_sel:[0,0,1] op_sel_hi:[1,0,1] neg_lo:[0,0,1] neg_hi:[0,0,1]\n\t"
  "v_pk_fma_f32 v[86:87], v[86:87], v[236:237], v[236:237] op_sel:[0,0,1] op_sel_hi:[1,0,1] neg_lo:[0,0,1] neg_hi:[0,0,1]\n\t"
  "v_pk_fma_f32 v[96:97], v[96:97], v[236:237], v[236:237] op_sel:[0,0,1] op_sel_hi:[1,0,1] neg_lo:[0,0,1] neg_hi:[0,0,1]\n\t"
  "v_pk_fma_f32 v[98:99], v[98:99], v[236:237], v[236:237] op_sel:[0,0,1] op_sel_hi:[1,0,1] neg_lo:[0,0,1] neg_hi:[0,0,1]\n\t"
  "v_pk_fma_f32 v[100:101], v[100:101], v[236:237], v[236:237] op_sel:[0,0,1] op_sel_hi:[1,0,1] neg_lo:[0,0,1] neg_hi:[0,0,1]\n\t"
  "v_pk_fma_f32 v[102:103], v[102:103], v[236:237], v[236:237] op_sel:[0,0,1] op_sel_hi:[1,0,1] neg_lo:[0,0,1] neg_hi:[0,0,1]\n\t"
  "v_pk_fma_f32 v[112:113], v[112:113], v[236:237], v[236:237] op_sel:[0,0,1] op_sel_hi:[1,0,1] neg_lo:[0,0,1] neg_hi:[0,0,1]\n\t"
  "v_pk_fma_f32 v[114:115], v[114:115], v[236:237], v[236:237] op_sel:[0,0,1] op_sel_hi:[1,0,1] neg_lo:[0,0,1] neg_hi:[0,0,1]\n\t"
  "v_pk_fma_f32 v[116:117], v[116:117], v[236:237], v[236:237] op_sel:[0,0,1] op_sel_hi:[1,0,1] neg_lo:[0,0,1] neg_hi:[0,0,1]\n\t"
  "v_pk_fma_f32 v[118:119], v[118:119], v[236:237], v[236:237] op_sel:[0,0,1] op_sel_hi:[1,0,1] neg_lo:[0,0,1] neg_hi:[0,0,1]\n\t"
  "v_pk_fma_f32 v[128:129], v[128:129], v[236:237], v[236:237] op_sel:[0,0,1] op_sel_hi:[1,0,1] neg_lo:[0,0,1] neg_hi:[0,0,1]\n\t"
  "v_pk_fma_f32 v[130:131], v[130:131], v[236:237], v[236:237] op_sel:[0,0,1] op_sel_hi:[1,0,1] neg_lo:[0,0,1] neg_hi:[0,0,1]\n\t"
  "v_pk_fma_f32 v[132:133], v[132:133], v[236:237], v[236:237] op_sel:[0,0,1] op_sel_hi:[1,0,1] neg_lo:[0,0,1] neg_hi:[0,0,1]\n\t"
  "v_pk_fma_f32 v[134:135], v[134:135], v[236:237], v[236:237] op_sel:[0,0,1] op_sel_hi:[1,0,1] neg_lo:[0,0,1] neg_hi:[0,0,1]\n\t"
  "v_pk_fma_f32 v[144:145], v[144:145], v[236:237], v[236:237] op_sel:[0,0,1] op_sel_hi:[1,0,1] neg_lo:[0,0,1] neg_hi:[0,0,1]\n\t"
  "v_pk_fma_f32 v[146:147], v[146:147], v[236:237], v[236:237] op_sel:[0,0,1] op_sel_hi:[1,0,1] neg_lo:[0,0,1] neg_hi:[0,0,1]\n\t"
  "v_pk_fma_f32 v[148:149], v[148:149], v[236:237], v[236:237] op_sel:[0,0,1] op_sel_hi:[1,0,1] neg_lo:[0,0,1] neg_hi:[0,0,1]\n\t"
  "v_pk_fma_f32 v[150:151], v[150:151], v[236:237], v[236:237] op_sel:[0,0,1] op_sel_hi:[1,0,1] neg_lo:[0,0,1] neg_hi:[0,0,1]\n\t"
  "v_pk_fma_f32 v[160:161], v[160:161], v[236:237], v[236:237] op_sel:[0,0,1] op_sel_hi:[1,0,1] neg_lo:[0,0,1] neg_hi:[0,0,1]\n\t"
  "v_pk_fma_f32 v[162:163], v[162:163], v[236:237], v[236:237] op_sel:[0,0,1] op_sel_hi:[1,0,1] neg_lo:[0,0,1] neg_hi:[0,0,1]\n\t"
  "v_pk_fma_f32 v[164:165], v[164:165], v[236:237], v[236:237] op_sel:[0,0,1] op_sel_hi:[1,0,1] neg_lo:[0,0,1] neg_hi:[0,0,1]\n\t"
  "v_pk_fma_f32 v[166:167], v[166:167], v[236:237], v[236:237] op_sel:[0,0,1] op_sel_hi:[1,0,1] neg_lo:[0,0,1] neg_hi:[0,0,1]\n\t"
  "v_pk_fma_f32 v[176:177], v[176:177], v[236:237], v[236:237] op_sel:[0,0,1] op_sel_hi:[1,0,1] neg_lo:[0,0,1] neg_hi:[0,0,1]\n\t"
  "v_pk_fma_f32 v[178:179], v[178:179], v[236:237], v[236:237] op_sel:[0,0,1] op_sel_hi:[1,0,1] neg_lo:[0,0,1] neg_hi:[0,0,1]\n\t"
  "v_pk_fma_f32 v[180:181], v[180:181], v[236:237], v[236:237] op_sel:[0,0,1] op_sel_hi:[1,0,1] neg_lo:[0,0,1] neg_hi:[0,0,1]\n\t"
  "v_pk_fma_f32 v[182:183], v[182:183], v[236:237], v[236:237] op_sel:[0,0,1] op_sel_hi:[1,0,1] neg_lo:[0,0,1] neg_hi:[0,0,1]\n\t"
  "v_pk_fma_f32 v[192:193], v[192:193], v[236:237], v[236:237] op_sel:[0,0,1] op_sel_hi:[1,0,1] neg_lo:[0,0,1] neg_hi:[0,0,1]\n\t"
  "v_pk_fma_f32 v[194:195], v[194:195], v[236:237], v[236:237] op_sel:[0,0,1] op_sel_hi:[1,0,1] neg_lo:[0,0,1] neg_hi:[0,0,1]\n\t"
  "v_pk_fma_f32 v[196:197], v[196:197], v[236:237], v[236:237] op_sel:[0,0,1] op_sel_hi:[1,0,1] neg_lo:[0,0,1] neg_hi:[0,0,1]\n\t"
  "v_pk_fma_f32 v[198:199], v[198:199], v[236:237], v[236:237] op_sel:[0,0,1] op_sel_hi:[1,0,1] neg_lo:[0,0,1] neg_hi:[0,0,1]\n\t"
  "v_pk_fma_f32 v[208:209], v[208:209], v[236:237], v[236:237] op_sel:[0,0,1] op_sel_hi:[1,0,1] neg_lo:[0,0,1] neg_hi:[0,0,1]\n\t"
  "v_pk_fma_f32 v[210:211], v[210:211], v[236:237], v[236:237] op_sel:[0,0,1] op_sel_hi:[1,0,1] neg_lo:[0,0,1] neg_hi:[0,0,1]\n\t"
  "v_pk_fma_f32 v[212:213], v[212:213], v[236:237], v[236:237] op_sel:[0,0,1] op_sel_hi:[1,0,1] neg_lo:[0,0,1] neg_hi:[0,0,1]\n\t"
  "v_pk_fma_f32 v[214:215], v[214:215], v[236:237], v[236:237] op_sel:[0,0,1] op_sel_hi:[1,0,1] neg_lo:[0,0,1] neg_hi:[0,0,1]\n\t"
  "s_waitcnt vmcnt(1)\n\t"
  "ds_read_b128 v[12:15], v89 offset:14336\n\t"
  "ds_read_b128 v[28:31], v88 offset:14336\n\t"
  "ds_read_b128 v[44:47], v89 offset:14464\n\t"
  "ds_read_b128 v[60:63], v88 offset:14464\n\t"
  "ds_read_b128 v[76:79], v89 offset:14592\n\t"
  "ds_read_b128 v[92:95], v88 offset:14592\n\t"
  "ds_read_b128 v[108:111], v89 offset:14720\n\t"
  "ds_read_b128 v[124:127], v88 offset:14720\n\t"
  "ds_read_b128 v[140:143], v89 offset:14848\n\t"
  "ds_read_b128 v[156:159], v88 offset:14848\n\t"
  "ds_read_b128 v[172:175], v89 offset:14976\n\t"
  "ds_read_b128 v[188:191], v88 offset:14976\n\t"
  "ds_read_b128 v[204:207], v89 offset:15104\n\t"
  "ds_read_b128 v[220:223], v88 offset:15104\n\t"
  "s_waitcnt lgkmcnt(13)\n\t"
  "v_mfma_f32_16x16x32_bf16 v[240:243], v[232:235], v[12:15], 0\n\t"
  "v_mfma_f32_16x16x32_bf16 v[248:251], v[12:15], v[12:15], 0\n\t"
  "s_add_u32 m0, s30, 98304\n\t"
  "s_nop 0\n\t"
  "buffer_load_dwordx4 v224, s[16:19], s36 offen lds\n\t"
  "s_waitcnt lgkmcnt(12)\n\t"
  "v_mfma_f32_16x16x32_bf16 v[240:243], v[232:235], v[28:31], v[240:243]\n\t"
  "v_mfma_f32_16x16x32_bf16 v[248:251], v[28:31], v[28:31], v[248:251]\n\t"
  "buffer_load_dwordx4 v225, s[16:19], s36 offen offset:1024 lds\n\t"
  "s_waitcnt lgkmcnt(11)\n\t"
  "v_mfma_f32_16x16x32_bf16 v[240:243], v[232:235], v[44:47], v[240:243]\n\t"
  "v_mfma_f32_16x16x32_bf16 v[248:251], v[44:47], v[44:47], v[248:251]\n\t"
  "s_waitcnt lgkmcnt(10)\n\t"
  "v_mfma_f32_16x16x32_bf16 v[240:243], v[232:235], v[60:63], v[240:243]\n\t"
  "v_mfma_f32_16x16x32_bf16 v[248:251], v[60:63], v[60:63], v[248:251]\n\t"
  "buffer_load_dwordx4 v226, s[16:19], s36 offen offset:2048 lds\n\t"
  "s_waitcnt lgkmcnt(9)\n\t"
  "v_mfma_f32_16x16x32_bf16 v[240:243], v[232:235], v[76:79], v[240:243]\n\t"
  "v_mfma_f32_16x16x32_bf16 v[248:251], v[76:79], v[76:79], v[248:251]\n\t"
  "s_waitcnt lgkmcnt(8)\n\t"
  "v_mfma_f32_16x16x32_bf16 v[240:243], v[232:235], v[92:95], v[240:243]\n\t"
  "v_mfma_f32_16x16x32_bf16 v[248:251], v[92:95], v[92:95], v[248:251]\n\t"
  "buffer_load_dwordx4 v227, s[16:19], s36 offen offset:3072 lds\n\t"
  "s_add_u32 s36, s36, 65536\n\t"
  "s_waitcnt lgkmcnt(7)\n\t"
  "v_mfma_f32_16x16x32_bf16 v[240:243], v[232:235], v[108:111], v[240:243]\n\t"
  "v_mfma_f32_16x16x32_bf16 v[248:251], v[108:111], v[108:111], v[248:251]\n\t"
  "s_waitcnt lgkmcnt(6)\n\t"
  "v_mfma_f32_16x16x32_bf16 v[240:243], v[232:235], v[124:127], v[240:243]\n\t"
  "v_mfma_f32_16x16x32_bf16 v[248:251], v[124:127], v[124:127], v[248:251]\n\t"
  "s_add_u32 m0, s30, 102400\n\t"
  "s_nop 0\n\t"
  "buffer_load_dwordx4 v228, s[16:19], s36 offen lds\n\t"
  "s_waitcnt lgkmcnt(5)\n\t"
  "v_mfma_f32_16x16x32_bf16 v[240:243], v[232:235], v[140:143], v[240:243]\n\t"
  "v_mfma_f32_16x16x32_bf16 v[248:251], v[140:143], v[140:143], v[248:251]\n\t"
  "s_waitcnt lgkmcnt(4)\n\t"
  "v_mfma_f32_16x16x32_bf16 v[240:243], v[232:235], v[156:159], v[240:243]\n\t"
  "v_mfma_f32_16x16x32_bf16 v[248:251], v[156:159], v[156:159], v[248:251]\n\t"
  "buffer_load_dwordx4 v229, s[16:19], s36 offen offset:1024 lds\n\t"
  "s_waitcnt lgkmcnt(3)\n\t"
  "v_mfma_f32_16x16x32_bf16 v[240:243], v[232:235], v[172:175], v[240:243]\n\t"
  "v_mfma_f32_16x16x32_bf16 v[248:251], v[172:175], v[172:175], v[248:251]\n\t"
  "s_waitcnt lgkmcnt(2)\n\t"
  "v_mfma_f32_16x16x32_bf16 v[240:243], v[232:235], v[188:191], v[240:243]\n\t"
  "v_mfma_f32_16x16x32_bf16 v[248:251], v[188:191], v[188:191], v[248:251]\n\t"
  "buffer_load_dwordx4 v230, s[16:19], s36 offen offset:2048 lds\n\t"
  "s_waitcnt lgkmcnt(1)\n\t"
  "v_mfma_f32_16x16x32_bf16 v[240:243], v[232:235], v[204:207], v[240:243]\n\t"
  "v_mfma_f32_16x16x32_bf16 v[248:251], v[204:207], v[204:207], v[248:251]\n\t"
  "s_waitcnt lgkmcnt(0)\n\t"
  "v_mfma_f32_16x16x32_bf16 v[240:243], v[232:235], v[220:223], v[240:243]\n\t"
  "v_mfma_f32_16x16x32_bf16 v[248:251], v[220:223], v[220:223], v[248:251]\n\t"
  "buffer_load_dwordx4 v231, s[16:19], s36 offen offset:3072 lds\n\t"
  "s_add_u32 s36, s36, 458752\n\t"
  "v_mov_b32_e32 v239, v137\n\t"
  "s_barrier\n\t"
  "v_lshlrev_b32_e32 v8, 16, v12\n\t"
  "v_and_b32_e32 v9, s96, v12\n\t"
  "v_lshlrev_b32_e32 v10, 16, v13\n\t"
  "v_and_b32_e32 v11, s96, v13\n\t"
  "v_lshlrev_b32_e32 v12, 16, v14\n\t"
  "v_and_b32_e32 v13, s96, v14\n\t"
  "v_mul_f32_e32 v241, 0x3b124925, v240\n\t"
  "v_lshlrev_b32_e32 v14, 16, v15\n\t"
  "v_and_b32_e32 v15, s96, v15\n\t"
  "v_cndmask_b32_e64 v248, v248, v249, s[40:41]\n\t"
  "v_lshlrev_b32_e32 v24, 16, v28\n\t"
  "v_and_b32_e32 v25, s96, v28\n\t"
  "v_cndmask_b32_e64 v248, v248, v250, s[42:43]\n\t"
  "v_lshlrev_b32_e32 v26, 16, v29\n\t"
  "v_and_b32_e32 v27, s96, v29\n\t"
  "v_cndmask_b32_e64 v248, v248, v251, s[44:45]\n\t"
  "v_lshlrev_b32_e32 v28, 16, v30\n\t"
  "v_and_b32_e32 v29, s96, v30\n\t"
  "v_cndmask_b32_e64 v248, 0, v248, s[46:47]\n\t"
  "v_lshlrev_b32_e32 v30, 16, v31\n\t"
  "v_and_b32_e32 v31, s96, v31\n\t"
  "v_mov_b32_e32 v249, v248\n\t"
  "v_lshlrev_b32_e32 v40, 16, v44\n\t"
  "v_and_b32_e32 v41, s96, v44\n\t"
  "v_permlane16_swap_b32 v249, v248\n\t"
  "v_lshlrev_b32_e32 v42, 16, v45\n\t"
  "v_and_b32_e32 v43, s96, v45\n\t"
  "v_add_f32_e32 v248, v248, v249\n\t"
  "v_lshlrev_b32_e32 v44, 16, v46\n\t"
  "v_and_b32_e32 v45, s96, v46\n\t"
  "v_mov_b32_e32 v249, v248\n\t"
  "v_lshlrev_b32_e32 v46, 16, v47\n\t"
  "v_and_b32_e32 v47, s96, v47\n\t"
  "v_permlane32_swap_b32 v249, v248\n\t"
  "v_lshlrev_b32_e32 v56, 16, v60\n\t"
  "v_and_b32_e32 v57, s96, v60\n\t"
  "v_add_f32_e32 v248, v248, v249\n\t"
  "v_lshlrev_b32_e32 v58, 16, v61\n\t"
  "v_and_b32_e32 v59, s96, v61\n\t"
  "v_mul_f32_e32 v248, 0x3b124925, v248\n\t"
  "v_lshlrev_b32_e32 v60, 16, v62\n\t"
  "v_and_b32_e32 v61, s96, v62\n\t"
  "v_fma_f32 v248, -v241, v241, v248\n\t"
  "v_lshlrev_b32_e32 v62, 16, v63\n\t"
  "v_and_b32_e32 v63, s96, v63\n\t"
  "v_max_f32_e32 v248, 0, v248\n\t"
  "v_lshlrev_b32_e32 v72, 16, v76\n\t"
  "v_and_b32_e32 v73, s96, v76\n\t"
  "v_add_f32_e32 v248, 0x3727c5ac, v248\n\t"
  "v_lshlrev_b32_e32 v74, 16, v77\n\t"
  "v_and_b32_e32 v75, s96, v77\n\t"
  "v_rsq_f32_e32 v240, v248\n\t"
  "v_lshlrev_b32_e32 v76, 16, v78\n\t"
  "v_and_b32_e32 v77, s96, v78\n\t"
  "v_mul_f32_e32 v241, v241, v240\n\t"
  "v_lshlrev_b32_e32 v78, 16, v79\n\t"
  "v_and_b32_e32 v79, s96, v79\n\t"
  "s_mul_i32 s65, s62, 112\n\t"
  "v_add_u32_e32 v233, 64, v254\n\t"
  "v_min_u32_e32 v233, 111, v233\n\t"
  "v_add_u32_e32 v233, s65, v233\n\t"
  "v_add_u32_e32 v232, s65, v254\n\t"
  "v_lshlrev_b32_e32 v234, 4, v232\n\t"
  "v_lshlrev_b32_e32 v235, 4, v233\n\t"
  "s_mov_b32 s65, 458752\n\t"
  "s_add_u32 s40, s98, s65\n\t"
  "s_addc_u32 s41, s99, 0\n\t"
  "s_and_b32 s41, s41, 0xffff\n\t"
  "s_mov_b32 s42, 28672\n\t"
  "s_mov_b32 s43, 0x20000\n\t"
  "s_mov_b32 s44, 7168\n\t"
  "s_mov_b32 s45, 14336\n\t"
  "s_mov_b32 s46, 21504\n\t"
  "buffer_load_dwordx4 a[112:115], v234, s[40:43], 0 offen sc1\n\t"
  "buffer_load_dwordx4 a[116:119], v235, s[40:43], 0 offen sc1\n\t"
  "buffer_load_dwordx4 a[120:123], v234, s[40:43], s44 offen sc1\n\t"
  "buffer_load_dwordx4 a[124:127], v235, s[40:43], s44 offen sc1\n\t"
  "buffer_load_dwordx4 a[128:131], v234, s[40:43], s45 offen sc1\n\t"
  "buffer_load_dwordx4 a[132:135], v235, s[40:43], s45 offen sc1\n\t"
  "buffer_load_dwordx4 a[136:139], v234, s[40:43], s46 offen sc1\n\t"
  "buffer_load_dwordx4 a[140:143], v235, s[40:43], s46 offen sc1\n\t"
  "v_lshlrev_b32_e32 v88, 16, v92\n\t"
  "v_and_b32_e32 v89, s96, v92\n\t"
  "v_lshlrev_b32_e32 v90, 16, v93\n\t"
  "v_and_b32_e32 v91, s96, v93\n\t"
  "v_lshlrev_b32_e32 v92, 16, v94\n\t"
  "v_and_b32_e32 v93, s96, v94\n\t"
  "v_lshlrev_b32_e32 v94, 16, v95\n\t"
  "v_and_b32_e32 v95, s96, v95\n\t"
  "v_lshlrev_b32_e32 v104, 16, v108\n\t"
  "v_and_b32_e32 v105, s96, v108\n\t"
  "s_add_u32 m0, s30, 65536\n\t"
  "s_nop 0\n\t"
  "buffer_load_dwordx4 v224, s[16:19], s36 offen lds\n\t"
  "v_lshlrev_b32_e32 v106, 16, v109\n\t"
  "v_and_b32_e32 v107, s96, v109\n\t"
  "v_lshlrev_b32_e32 v108, 16, v110\n\t"
  "v_and_b32_e32 v109, s96, v110\n\t"
  "v_lshlrev_b32_e32 v110, 16, v111\n\t"
  "v_and_b32_e32 v111, s96, v111\n\t"
  "v_lshlrev_b32_e32 v120, 16, v124\n\t"
  "v_and_b32_e32 v121, s96, v124\n\t"
  "v_lshlrev_b32_e32 v122, 16, v125\n\t"
  "v_and_b32_e32 v123, s96, v125\n\t"
  "buffer_load_dwordx4 v225, s[16:19], s36 offen offset:1024 lds\n\t"
  "v_lshlrev_b32_e32 v124, 16, v126\n\t"
  "v_and_b32_e32 v125, s96, v126\n\t"
  "v_lshlrev_b32_e32 v126, 16, v127\n\t"
  "v_and_b32_e32 v127, s96, v127\n\t"
  "v_lshlrev_b32_e32 v136, 16, v140\n\t"
  "v_and_b32_e32 v137, s96, v140\n\t"
  "v_lshlrev_b32_e32 v138, 16, v141\n\t"
  "v_and_b32_e32 v139, s96, v141\n\t"
  "v_lshlrev_b32_e32 v140, 16, v142\n\t"
  "v_and_b32_e32 v141, s96, v142\n\t"
  "buffer_load_dwordx4 v226, s[16:19], s36 offen offset:2048 lds\n\t"
  "v_lshlrev_b32_e32 v142, 16, v143\n\t"
  "v_and_b32_e32 v143, s96, v143\n\t"
  "v_lshlrev_b32_e32 v152, 16, v156\n\t"
  "v_and_b32_e32 v153, s96, v156\n\t"
  "v_lshlrev_b32_e32 v154, 16, v157\n\t"
  "v_and_b32_e32 v155, s96, v157\n\t"
  "v_lshlrev_b32_e32 v156, 16, v158\n\t"
  "v_and_b32_e32 v157, s96, v158\n\t"
  "v_lshlrev_b32_e32 v158, 16, v159\n\t"
  "v_and_b32_e32 v159, s96, v159\n\t"
  "buffer_load_dwordx4 v227, s[16:19], s36 offen offset:3072 lds\n\t"
  "s_add_u32 s36, s36, 65536\n\t"
  "v_lshlrev_b32_e32 v168, 16, v172\n\t"
  "v_and_b32_e32 v169, s96, v172\n\t"
  "v_lshlrev_b32_e32 v170, 16, v173\n\t"
  "v_and_b32_e32 v171, s96, v173\n\t"
  "v_lshlrev_b32_e32 v172, 16, v174\n\t"
  "v_and_b32_e32 v173, s96, v174\n\t"
  "v_lshlrev_b32_e32 v174, 16, v175\n\t"
  "v_and_b32_e32 v175, s96, v175\n\t"
  "v_lshlrev_b32_e32 v184, 16, v188\n\t"
  "v_and_b32_e32 v185, s96, v188\n\t"
  "s_add_u32 m0, s30, 69632\n\t"
  "s_nop 0\n\t"
  "buffer_load_dwordx4 v228, s[16:19], s36 offen lds\n\t"
  "v_lshlrev_b32_e32 v186, 16, v189\n\t"
  "v_and_b32_e32 v187, s96, v189\n\t"
  "v_lshlrev_b32_e32 v188, 16, v190\n\t"
  "v_and_b32_e32 v189, s96, v190\n\t"
  "v_lshlrev_b32_e32 v190, 16, v191\n\t"
  "v_and_b32_e32 v191, s96, v191\n\t"
  "v_lshlrev_b32_e32 v200, 16, v204\n\t"
  "v_and_b32_e32 v201, s96, v204\n\t"
  "v_lshlrev_b32_e32 v202, 16, v205\n\t"
  "v_and_b32_e32 v203, s96, v205\n\t"
  "buffer_load_dwordx4 v229, s[16:19], s36 offen offset:1024 lds\n\t"
  "v_lshlrev_b32_e32 v204, 16, v206\n\t"
  "v_and_b32_e32 v205, s96, v206\n\t"
  "v_lshlrev_b32_e32 v206, 16, v207\n\t"
  "v_and_b32_e32 v207, s96, v207\n\t"
  "v_lshlrev_b32_e32 v216, 16, v220\n\t"
  "v_and_b32_e32 v217, s96, v220\n\t"
  "v_lshlrev_b32_e32 v218, 16, v221\n\t"
  "v_and_b32_e32 v219, s96, v221\n\t"
  "v_lshlrev_b32_e32 v220, 16, v222\n\t"
  "v_and_b32_e32 v221, s96, v222\n\t"
  "buffer_load_dwordx4 v230, s[16:19], s36 offen offset:2048 lds\n\t"
  "v_lshlrev_b32_e32 v222, 16, v223\n\t"
  "v_and_b32_e32 v223, s96, v223\n\t"
  "v_pk_fma_f32 v[8:9], v[8:9], v[240:241], v[240:241] op_sel:[0,0,1] op_sel_hi:[1,0,1] neg_lo:[0,0,1] neg_hi:[0,0,1]\n\t"
  "v_pk_fma_f32 v[10:11], v[10:11], v[240:241], v[240:241] op_sel:[0,0,1] op_sel_hi:[1,0,1] neg_lo:[0,0,1] neg_hi:[0,0,1]\n\t"
  "v_pk_fma_f32 v[12:13], v[12:13], v[240:241], v[240:241] op_sel:[0,0,1] op_sel_hi:[1,0,1] neg_lo:[0,0,1] neg_hi:[0,0,1]\n\t"
  "v_pk_fma_f32 v[14:15], v[14:15], v[240:241], v[240:241] op_sel:[0,0,1] op_sel_hi:[1,0,1] neg_lo:[0,0,1] neg_hi:[0,0,1]\n\t"
  "v_pk_fma_f32 v[24:25], v[24:25], v[240:241], v[240:241] op_sel:[0,0,1] op_sel_hi:[1,0,1] neg_lo:[0,0,1] neg_hi:[0,0,1]\n\t"
  "v_pk_fma_f32 v[26:27], v[26:27], v[240:241], v[240:241] op_sel:[0,0,1] op_sel_hi:[1,0,1] neg_lo:[0,0,1] neg_hi:[0,0,1]\n\t"
  "v_pk_fma_f32 v[28:29], v[28:29], v[240:241], v[240:241] op_sel:[0,0,1] op_sel_hi:[1,0,1] neg_lo:[0,0,1] neg_hi:[0,0,1]\n\t"
  "v_pk_fma_f32 v[30:31], v[30:31], v[240:241], v[240:241] op_sel:[0,0,1] op_sel_hi:[1,0,1] neg_lo:[0,0,1] neg_hi:[0,0,1]\n\t"
  "buffer_load_dwordx4 v231, s[16:19], s36 offen offset:3072 lds\n\t"
  "s_add_u32 s36, s36, 458752\n\t"
  "v_pk_fma_f32 v[40:41], v[40:41], v[240:241], v[240:241] op_sel:[0,0,1] op_sel_hi:[1,0,1] neg_lo:[0,0,1] neg_hi:[0,0,1]\n\t"
  "v_pk_fma_f32 v[42:43], v[42:43], v[240:241], v[240:241] op_sel:[0,0,1] op_sel_hi:[1,0,1] neg_lo:[0,0,1] neg_hi:[0,0,1]\n\t"
  "v_pk_fma_f32 v[44:45], v[44:45], v[240:241], v[240:241] op_sel:[0,0,1] op_sel_hi:[1,0,1] neg_lo:[0,0,1] neg_hi:[0,0,1]\n\t"
  "v_pk_fma_f32 v[46:47], v[46:47], v[240:241], v[240:241] op_sel:[0,0,1] op_sel_hi:[1,0,1] neg_lo:[0,0,1] neg_hi:[0,0,1]\n\t"
  "v_pk_fma_f32 v[56:57], v[56:57], v[240:241], v[240:241] op_sel:[0,0,1] op_sel_hi:[1,0,1] neg_lo:[0,0,1] neg_hi:[0,0,1]\n\t"
  "v_pk_fma_f32 v[58:59], v[58:59], v[240:241], v[240:241] op_sel:[0,0,1] op_sel_hi:[1,0,1] neg_lo:[0,0,1] neg_hi:[0,0,1]\n\t"
  "v_pk_fma_f32 v[60:61], v[60:61], v[240:241], v[240:241] op_sel:[0,0,1] op_sel_hi:[1,0,1] neg_lo:[0,0,1] neg_hi:[0,0,1]\n\t"
  "v_pk_fma_f32 v[62:63], v[62:63], v[240:241], v[240:241] op_sel:[0,0,1] op_sel_hi:[1,0,1] neg_lo:[0,0,1] neg_hi:[0,0,1]\n\t"
  "v_pk_fma_f32 v[72:73], v[72:73], v[240:241], v[240:241] op_sel:[0,0,1] op_sel_hi:[1,0,1] neg_lo:[0,0,1] neg_hi:[0,0,1]\n\t"
  "v_pk_fma_f32 v[74:75], v[74:75], v[240:241], v[240:241] op_sel:[0,0,1] op_sel_hi:[1,0,1] neg_lo:[0,0,1] neg_hi:[0,0,1]\n\t"
  "v_lshrrev_b32_e32 v253, 2, v254\n\t"
  "v_lshlrev_b32_e32 v253, 7, v253\n\t"
  "v_add_u32_e32 v253, s67, v253\n\t"
  "v_pk_fma_f32 v[76:77], v[76:77], v[240:241], v[240:241] op_sel:[0,0,1] op_sel_hi:[1,0,1] neg_lo:[0,0,1] neg_hi:[0,0,1]\n\t"
  "v_pk_fma_f32 v[78:79], v[78:79], v[240:241], v[240:241] op_sel:[0,0,1] op_sel_hi:[1,0,1] neg_lo:[0,0,1] neg_hi:[0,0,1]\n\t"
  "v_pk_fma_f32 v[88:89], v[88:89], v[240:241], v[240:241] op_sel:[0,0,1] op_sel_hi:[1,0,1] neg_lo:[0,0,1] neg_hi:[0,0,1]\n\t"
  "v_pk_fma_f32 v[90:91], v[90:91], v[240:241], v[240:241] op_sel:[0,0,1] op_sel_hi:[1,0,1] neg_lo:[0,0,1] neg_hi:[0,0,1]\n\t"
  "v_pk_fma_f32 v[92:93], v[92:93], v[240:241], v[240:241] op_sel:[0,0,1] op_sel_hi:[1,0,1] neg_lo:[0,0,1] neg_hi:[0,0,1]\n\t"
  "v_pk_fma_f32 v[94:95], v[94:95], v[240:241], v[240:241] op_sel:[0,0,1] op_sel_hi:[1,0,1] neg_lo:[0,0,1] neg_hi:[0,0,1]\n\t"
  "v_pk_fma_f32 v[104:105], v[104:105], v[240:241], v[240:241] op_sel:[0,0,1] op_sel_hi:[1,0,1] neg_lo:[0,0,1] neg_hi:[0,0,1]\n\t"
  "v_pk_fma_f32 v[106:107], v[106:107], v[240:241], v[240:241] op_sel:[0,0,1] op_sel_hi:[1,0,1] neg_lo:[0,0,1] neg_hi:[0,0,1]\n\t"
  "v_pk_fma_f32 v[108:109], v[108:109], v[240:241], v[240:241] op_sel:[0,0,1] op_sel_hi:[1,0,1] neg_lo:[0,0,1] neg_hi:[0,0,1]\n\t"
  "v_pk_fma_f32 v[110:111], v[110:111], v[240:241], v[240:241] op_sel:[0,0,1] op_sel_hi:[1,0,1] neg_lo:[0,0,1] neg_hi:[0,0,1]\n\t"
  "buffer_load_dword v255, v253, s[24:27], s35 offen\n\t"
  "s_add_u32 s35, s35, 32768\n\t"
  "v_pk_fma_f32 v[120:121], v[120:121], v[240:241], v[240:241] op_sel:[0,0,1] op_sel_hi:[1,0,1] neg_lo:[0,0,1] neg_hi:[0,0,1]\n\t"
  "v_pk_fma_f32 v[122:123], v[122:123], v[240:241], v[240:241] op_sel:[0,0,1] op_sel_hi:[1,0,1] neg_lo:[0,0,1] neg_hi:[0,0,1]\n\t"
  "v_pk_fma_f32 v[124:125], v[124:125], v[240:241], v[240:241] op_sel:[0,0,1] op_sel_hi:[1,0,1] neg_lo:[0,0,1] neg_hi:[0,0,1]\n\t"
  "v_pk_fma_f32 v[126:127], v[126:127], v[240:241], v[240:241] op_sel:[0,0,1] op_sel_hi:[1,0,1] neg_lo:[0,0,1] neg_hi:[0,0,1]\n\t"
  "v_pk_fma_f32 v[136:137], v[136:137], v[240:241], v[240:241] op_sel:[0,0,1] op_sel_hi:[1,0,1] neg_lo:[0,0,1] neg_hi:[0,0,1]\n\t"
  "v_pk_fma_f32 v[138:139], v[138:139], v[240:241], v[240:241] op_sel:[0,0,1] op_sel_hi:[1,0,1] neg_lo:[0,0,1] neg_hi:[0,0,1]\n\t"
  "v_pk_fma_f32 v[140:141], v[140:141], v[240:241], v[240:241] op_sel:[0,0,1] op_sel_hi:[1,0,1] neg_lo:[0,0,1] neg_hi:[0,0,1]\n\t"
  "v_pk_fma_f32 v[142:143], v[142:143], v[240:241], v[240:241] op_sel:[0,0,1] op_sel_hi:[1,0,1] neg_lo:[0,0,1] neg_hi:[0,0,1]\n\t"
  "v_pk_fma_f32 v[152:153], v[152:153], v[240:241], v[240:241] op_sel:[0,0,1] op_sel_hi:[1,0,1] neg_lo:[0,0,1] neg_hi:[0,0,1]\n\t"
  "v_pk_fma_f32 v[154:155], v[154:155], v[240:241], v[240:241] op_sel:[0,0,1] op_sel_hi:[1,0,1] neg_lo:[0,0,1] neg_hi:[0,0,1]\n\t"
  "buffer_load_dword v255, v253, s[24:27], s35 offen\n\t"
  "s_add_u32 s35, s35, 32768\n\t"
  "v_pk_fma_f32 v[156:157], v[156:157], v[240:241], v[240:241] op_sel:[0,0,1] op_sel_hi:[1,0,1] neg_lo:[0,0,1] neg_hi:[0,0,1]\n\t"
  "v_pk_fma_f32 v[158:159], v[158:159], v[240:241], v[240:241] op_sel:[0,0,1] op_sel_hi:[1,0,1] neg_lo:[0,0,1] neg_hi:[0,0,1]\n\t"
  "v_pk_fma_f32 v[168:169], v[168:169], v[240:241], v[240:241] op_sel:[0,0,1] op_sel_hi:[1,0,1] neg_lo:[0,0,1] neg_hi:[0,0,1]\n\t"
  "v_pk_fma_f32 v[170:171], v[170:171], v[240:241], v[240:241] op_sel:[0,0,1] op_sel_hi:[1,0,1] neg_lo:[0,0,1] neg_hi:[0,0,1]\n\t"
  "v_pk_fma_f32 v[172:173], v[172:173], v[240:241], v[240:241] op_sel:[0,0,1] op_sel_hi:[1,0,1] neg_lo:[0,0,1] neg_hi:[0,0,1]\n\t"
  "v_pk_fma_f32 v[174:175], v[174:175], v[240:241], v[240:241] op_sel:[0,0,1] op_sel_hi:[1,0,1] neg_lo:[0,0,1] neg_hi:[0,0,1]\n\t"
  "v_pk_fma_f32 v[184:185], v[184:185], v[240:241], v[240:241] op_sel:[0,0,1] op_sel_hi:[1,0,1] neg_lo:[0,0,1] neg_hi:[0,0,1]\n\t"
  "v_pk_fma_f32 v[186:187], v[186:187], v[240:241], v[240:241] op_sel:[0,0,1] op_sel_hi:[1,0,1] neg_lo:[0,0,1] neg_hi:[0,0,1]\n\t"
  "v_pk_fma_f32 v[188:189], v[188:189], v[240:241], v[240:241] op_sel:[0,0,1] op_sel_hi:[1,0,1] neg_lo:[0,0,1] neg_hi:[0,0,1]\n\t"
  "v_pk_fma_f32 v[190:191], v[190:191], v[240:241], v[240:241] op_sel:[0,0,1] op_sel_hi:[1,0,1] neg_lo:[0,0,1] neg_hi:[0,0,1]\n\t"
  "buffer_load_dword v255, v253, s[24:27], s35 offen\n\t"
  "s_add_u32 s35, s35, 32768\n\t"
  "v_pk_fma_f32 v[200:201], v[200:201], v[240:241], v[240:241] op_sel:[0,0,1] op_sel_hi:[1,0,1] neg_lo:[0,0,1] neg_hi:[0,0,1]\n\t"
  "v_pk_fma_f32 v[202:203], v[202:203], v[240:241], v[240:241] op_sel:[0,0,1] op_sel_hi:[1,0,1] neg_lo:[0,0,1] neg_hi:[0,0,1]\n\t"
  "v_pk_fma_f32 v[204:205], v[204:205], v[240:241], v[240:241] op_sel:[0,0,1] op_sel_hi:[1,0,1] neg_lo:[0,0,1] neg_hi:[0,0,1]\n\t"
  "v_pk_fma_f32 v[206:207], v[206:207], v[240:241], v[240:241] op_sel:[0,0,1] op_sel_hi:[1,0,1] neg_lo:[0,0,1] neg_hi:[0,0,1]\n\t"
  "v_pk_fma_f32 v[216:217], v[216:217], v[240:241], v[240:241] op_sel:[0,0,1] op_sel_hi:[1,0,1] neg_lo:[0,0,1] neg_hi:[0,0,1]\n\t"
  "v_pk_fma_f32 v[218:219], v[218:219], v[240:241], v[240:241] op_sel:[0,0,1] op_sel_hi:[1,0,1] neg_lo:[0,0,1] neg_hi:[0,0,1]\n\t"
  "v_pk_fma_f32 v[220:221], v[220:221], v[240:241], v[240:241] op_sel:[0,0,1] op_sel_hi:[1,0,1] neg_lo:[0,0,1] neg_hi:[0,0,1]\n\t"
  "v_pk_fma_f32 v[222:223], v[222:223], v[240:241], v[240:241] op_sel:[0,0,1] op_sel_hi:[1,0,1] neg_lo:[0,0,1] neg_hi:[0,0,1]\n\t"
  "s_mov_b32 s97, 1048576\n\t"
  "s_waitcnt vmcnt(11)\n\t"
  "s_branch .Lsweepc\n\t"
  ".Lsweep:\n\t"
  "buffer_load_dwordx4 a[112:115], v234, s[40:43], 0 offen sc1\n\t"
  "buffer_load_dwordx4 a[116:119], v235, s[40:43], 0 offen sc1\n\t"
  "buffer_load_dwordx4 a[120:123], v234, s[40:43], s44 offen sc1\n\t"
  "buffer_load_dwordx4 a[124:127], v235, s[40:43], s44 offen sc1\n\t"
  "buffer_load_dwordx4 a[128:131], v234, s[40:43], s45 offen sc1\n\t"
  "buffer_load_dwordx4 a[132:135], v235, s[40:43], s45 offen sc1\n\t"
  "buffer_load_dwordx4 a[136:139], v234, s[40:43], s46 offen sc1\n\t"
  "buffer_load_dwordx4 a[140:143], v235, s[40:43], s46 offen sc1\n\t"
  "s_waitcnt vmcnt(0)\n\t"
  ".Lsweepc:\n\t"
  "v_accvgpr_read_b32 v244, a112\n\t"
  "v_accvgpr_read_b32 v245, a113\n\t"
  "v_accvgpr_read_b32 v246, a114\n\t"
  "v_accvgpr_read_b32 v247, a115\n\t"
  "v_accvgpr_read_b32 v248, a116\n\t"
  "v_accvgpr_read_b32 v249, a117\n\t"
  "v_accvgpr_read_b32 v250, a118\n\t"
  "v_accvgpr_read_b32 v251, a119\n\t"
  "v_xor_b32_e32 v238, s69, v245\n\t"
  "v_mov_b32_e32 v240, v244\n\t"
  "v_mov_b32_e32 v241, v246\n\t"
  "v_mov_b32_e32 v242, v248\n\t"
  "v_mov_b32_e32 v243, v250\n\t"
  "v_xor_b32_e32 v247, s69, v247\n\t"
  "v_xor_b32_e32 v249, s69, v249\n\t"
  "v_xor_b32_e32 v251, s69, v251\n\t"
  "v_or3_b32 v238, v238, v247, v249\n\t"
  "v_or3_b32 v238, v238, v251, v251\n\t"
  "v_accvgpr_read_b32 v244, a120\n\t"
  "v_accvgpr_read_b32 v245, a121\n\t"
  "v_accvgpr_read_b32 v246, a122\n\t"
  "v_accvgpr_read_b32 v247, a123\n\t"
  "v_accvgpr_read_b32 v248, a124\n\t"
  "v_accvgpr_read_b32 v249, a125\n\t"
  "v_accvgpr_read_b32 v250, a126\n\t"
  "v_accvgpr_read_b32 v251, a127\n\t"
  "v_add_f32_e32 v240, v240, v244\n\t"
  "v_add_f32_e32 v241, v241, v246\n\t"
  "v_add_f32_e32 v242, v242, v248\n\t"
  "v_add_f32_e32 v243, v243, v250\n\t"
  "v_xor_b32_e32 v245, s69, v245\n\t"
  "v_xor_b32_e32 v247, s69, v247\n\t"
  "v_xor_b32_e32 v249, s69, v249\n\t"
  "v_xor_b32_e32 v251, s69, v251\n\t"
  "v_or3_b32 v238, v238, v247, v249\n\t"
  "v_or3_b32 v238, v238, v251, v245\n\t"
  "v_accvgpr_read_b32 v244, a128\n\t"
  "v_accvgpr_read_b32 v245, a129\n\t"
  "v_accvgpr_read_b32 v246, a130\n\t"
  "v_accvgpr_read_b32 v247, a131\n\t"
  "v_accvgpr_read_b32 v248, a132\n\t"
  "v_accvgpr_read_b32 v249, a133\n\t"
  "v_accvgpr_read_b32 v250, a134\n\t"
  "v_accvgpr_read_b32 v251, a135\n\t"
  "v_add_f32_e32 v240, v240, v244\n\t"
  "v_add_f32_e32 v241, v241, v246\n\t"
  "v_add_f32_e32 v242, v242, v248\n\t"
  "v_add_f32_e32 v243, v243, v250\n\t"
  "v_xor_b32_e32 v245, s69, v245\n\t"
  "v_xor_b32_e32 v247, s69, v247\n\t"
  "v_xor_b32_e32 v249, s69, v249\n\t"
  "v_xor_b32_e32 v251, s69, v251\n\t"
  "v_or3_b32 v238, v238, v247, v249\n\t"
  "v_or3_b32 v238, v238, v251, v245\n\t"
  "v_accvgpr_read_b32 v244, a136\n\t"
  "v_accvgpr_read_b32 v245, a137\n\t"
  "v_accvgpr_read_b32 v246, a138\n\t"
  "v_accvgpr_read_b32 v247, a139\n\t"
  "v_accvgpr_read_b32 v248, a140\n\t"
  "v_accvgpr_read_b32 v249, a141\n\t"
  "v_accvgpr_read_b32 v250, a142\n\t"
  "v_accvgpr_read_b32 v251, a143\n\t"
  "v_add_f32_e32 v240, v240, v244\n\t"
  "v_add_f32_e32 v241, v241, v246\n\t"
  "v_add_f32_e32 v242, v242, v248\n\t"
  "v_add_f32_e32 v243, v243, v250\n\t"
  "v_xor_b32_e32 v245, s69, v245\n\t"
  "v_xor_b32_e32 v247, s69, v247\n\t"
  "v_xor_b32_e32 v249, s69, v249\n\t"
  "v_xor_b32_e32 v251, s69, v251\n\t"
  "v_or3_b32 v238, v238, v247, v249\n\t"
  "v_or3_b32 v238, v238, v251, v245\n\t"
  "v_cmp_ne_u32_e32 vcc, 0, v238\n\t"
  "s_sub_u32 s97, s97, 1\n\t"
  "s_nop 2\n\t"
  "s_cbranch_vccz .Lswept\n\t"
  "s_sleep 4\n\t"
  "s_cmp_lg_u32 s97, 0\n\t"
  "s_cbranch_scc1 .Lsweep\n\t"
  ".Lswept:\n\t"
  "s_mov_b32 s64, 0x3db8aa3b\n\t"
  "s_cmp_ge_u32 s62, 2\n\t"
  "s_cselect_b32 s65, s64, 0\n\t"
  "v_mov_b32_e32 v238, s65\n\t"
  "v_cvt_pk_bf16_f32 v244, v240, v241\n\t"
  "v_lshlrev_b32_e32 v240, 16, v244\n\t"
  "v_and_b32_e32 v241, s96, v244\n\t"
  "v_fma_f32 v240, v240, s64, v238\n\t"
  "v_fma_f32 v241, v241, s64, v238\n\t"
  "v_lshlrev_b32_e32 v232, 3, v232\n\t"
  "v_add_u32_e32 v232, 159744, v232\n\t"
  "ds_write_b64 v232, v[240:241]\n\t"
  "v_cvt_pk_bf16_f32 v245, v242, v243\n\t"
  "v_lshlrev_b32_e32 v242, 16, v245\n\t"
  "v_and_b32_e32 v243, s96, v245\n\t"
  "v_fma_f32 v242, v242, s64, v238\n\t"
  "v_fma_f32 v243, v243, s64, v238\n\t"
  "v_lshlrev_b32_e32 v233, 3, v233\n\t"
  "v_add_u32_e32 v233, 159744, v233\n\t"
  "ds_write_b64 v233, v[242:243]\n\t"
  "v_bfe_u32 v238, v254, 1, 1\n\t"
  "v_bfe_u32 v239, v254, 4, 1\n\t"
  "v_lshlrev_b32_e32 v239, 1, v239\n\t"
  "v_xor_b32_e32 v238, v238, v239\n\t"
  "v_and_b32_e32 v239, 1, v254\n\t"
  "v_lshlrev_b32_e32 v239, 3, v239\n\t"
  "v_lshl_add_u32 v238, v238, 4, v239\n\t"
  "v_bfe_u32 v239, v254, 2, 2\n\t"
  "v_lshl_add_u32 v238, v239, 6, v238\n\t"
  "v_lshrrev_b32_e32 v239, 4, v254\n\t"
  "v_lshlrev_b32_e32 v252, 5, v239\n\t"
  "v_add_u32_e32 v252, 159744, v252\n\t"
  "s_waitcnt lgkmcnt(0)\n\t"
  "s_barrier\n\t"
  "v_lshl_add_u32 v248, v239, 12, v238\n\t"
  "v_xor_b32_e32 v249, 32, v248\n\t"
  "v_add_u32_e32 v250, 65536, v248\n\t"
  "v_add_u32_e32 v251, 65536, v249\n\t"
  "s_mov_b32 s97, 32768\n\t"
  "ds_read_b128 v[232:235], v252\n\t"
  "ds_read_b128 v[236:239], v252 offset:16\n\t"
  "ds_read_b128 v[240:243], v252 offset:1792\n\t"
  "ds_read_b128 v[244:247], v252 offset:1808\n\t"
  "s_waitcnt lgkmcnt(1)\n\t"
  "v_pk_fma_f32 v[0:1], v[0:1], v[240:241], v[232:233]\n\t"
  "v_pk_fma_f32 v[2:3], v[2:3], v[242:243], v[234:235]\n\t"
  "s_waitcnt lgkmcnt(0)\n\t"
  "v_pk_fma_f32 v[4:5], v[4:5], v[244:245], v[236:237]\n\t"
  "v_pk_fma_f32 v[6:7], v[6:7], v[246:247], v[238:239]\n\t"
  "v_pk_fma_f32 v[8:9], v[8:9], v[240:241], v[232:233]\n\t"
  "v_pk_fma_f32 v[10:11], v[10:11], v[242:243], v[234:235]\n\t"
  "v_pk_fma_f32 v[12:13], v[12:13], v[244:245], v[236:237]\n\t"
  "v_pk_fma_f32 v[14:15], v[14:15], v[246:247], v[238:239]\n\t"
  "ds_read_b128 v[232:235], v252 offset:128\n\t"
  "ds_read_b128 v[236:239], v252 offset:144\n\t"
  "ds_read_b128 v[240:243], v252 offset:1920\n\t"
  "ds_read_b128 v[244:247], v252 offset:1936\n\t"
  "v_cvt_pk_bf16_f32 v0, v0, v1\n\t"
  "v_cvt_pk_bf16_f32 v1, v2, v3\n\t"
  "v_cvt_pk_bf16_f32 v2, v4, v5\n\t"
  "v_cvt_pk_bf16_f32 v3, v6, v7\n\t"
  "v_accvgpr_write_b32 a0, v0\n\t"
  "v_accvgpr_write_b32 a1, v1\n\t"
  "v_accvgpr_write_b32 a2, v2\n\t"
  "v_accvgpr_write_b32 a3, v3\n\t"
  "v_cvt_pk_bf16_f32 v8, v8, v9\n\t"
  "v_cvt_pk_bf16_f32 v9, v10, v11\n\t"
  "v_cvt_pk_bf16_f32 v10, v12, v13\n\t"
  "v_cvt_pk_bf16_f32 v11, v14, v15\n\t"
  "v_accvgpr_write_b32 a4, v8\n\t"
  "v_accvgpr_write_b32 a5, v9\n\t"
  "v_accvgpr_write_b32 a6, v10\n\t"
  "v_accvgpr_write_b32 a7, v11\n\t"
  "ds_read_b128 v[0:3], v252 offset:256\n\t"
  "ds_read_b128 v[4:7], v252 offset:272\n\t"
  "ds_read_b128 v[8:11], v252 offset:2048\n\t"
  "ds_read_b128 v[12:15], v252 offset:2064\n\t"
  "s_waitcnt lgkmcnt(5)\n\t"
  "v_pk_fma_f32 v[16:17], v[16:17], v[240:241], v[232:233]\n\t"
  "v_pk_fma_f32 v[18:19], v[18:19], v[242:243], v[234:235]\n\t"
  "s_waitcnt lgkmcnt(4)\n\t"
  "v_pk_fma_f32 v[20:21], v[20:21], v[244:245], v[236:237]\n\t"
  "v_pk_fma_f32 v[22:23], v[22:23], v[246:247], v[238:239]\n\t"
  "v_pk_fma_f32 v[24:25], v[24:25], v[240:241], v[232:233]\n\t"
  "v_pk_fma_f32 v[26:27], v[26:27], v[242:243], v[234:235]\n\t"
  "v_pk_fma_f32 v[28:29], v[28:29], v[244:245], v[236:237]\n\t"
  "v_pk_fma_f32 v[30:31], v[30:31], v[246:247], v[238:239]\n\t"
  "v_cvt_pk_bf16_f32 v16, v16, v17\n\t"
  "v_cvt_pk_bf16_f32 v17, v18, v19\n\t"
  "v_cvt_pk_bf16_f32 v18, v20, v21\n\t"
  "v_cvt_pk_bf16_f32 v19, v22, v23\n\t"
  "v_accvgpr_write_b32 a8, v16\n\t"
  "v_accvgpr_write_b32 a9, v17\n\t"
  "v_accvgpr_write_b32 a10, v18\n\t"
  "v_accvgpr_write_b32 a11, v19\n\t"
  "v_cvt_pk_bf16_f32 v24, v24, v25\n\t"
  "v_cvt_pk_bf16_f32 v25, v26, v27\n\t"
  "v_cvt_pk_bf16_f32 v26, v28, v29\n\t"
  "v_cvt_pk_bf16_f32 v27, v30, v31\n\t"
  "v_accvgpr_write_b32 a12, v24\n\t"
  "v_accvgpr_write_b32 a13, v25\n\t"
  "v_accvgpr_write_b32 a14, v26\n\t"
  "v_accvgpr_write_b32 a15, v27\n\t"
  "ds_read_b128 v[16:19], v252 offset:384\n\t"
  "ds_read_b128 v[20:23], v252 offset:400\n\t"
  "ds_read_b128 v[24:27], v252 offset:2176\n\t"
  "ds_read_b128 v[28:31], v252 offset:2192\n\t"
  "s_waitcnt lgkmcnt(5)\n\t"
  "v_pk_fma_f32 v[32:33], v[32:33], v[8:9], v[0:1]\n\t"
  "v_pk_fma_f32 v[34:35], v[34:35], v[10:11], v[2:3]\n\t"
  "s_waitcnt lgkmcnt(4)\n\t"
  "v_pk_fma_f32 v[36:37], v[36:37], v[12:13], v[4:5]\n\t"
  "v_pk_fma_f32 v[38:39], v[38:39], v[14:15], v[6:7]\n\t"
  "v_pk_fma_f32 v[40:41], v[40:41], v[8:9], v[0:1]\n\t"
  "v_pk_fma_f32 v[42:43], v[42:43], v[10:11], v[2:3]\n\t"
  "v_pk_fma_f32 v[44:45], v[44:45], v[12:13], v[4:5]\n\t"
  "v_pk_fma_f32 v[46:47], v[46:47], v[14:15], v[6:7]\n\t"
  "v_cvt_pk_bf16_f32 v32, v32, v33\n\t"
  "v_cvt_pk_bf16_f32 v33, v34, v35\n\t"
  "v_cvt_pk_bf16_f32 v34, v36, v37\n\t"
  "v_cvt_pk_bf16_f32 v35, v38, v39\n\t"
  "v_accvgpr_write_b32 a16, v32\n\t"
  "v_accvgpr_write_b32 a17, v33\n\t"
  "v_accvgpr_write_b32 a18, v34\n\t"
  "v_accvgpr_write_b32 a19, v35\n\t"
  "v_cvt_pk_bf16_f32 v40, v40, v41\n\t"
  "v_cvt_pk_bf16_f32 v41, v42, v43\n\t"
  "v_cvt_pk_bf16_f32 v42, v44, v45\n\t"
  "v_cvt_pk_bf16_f32 v43, v46, v47\n\t"
  "v_accvgpr_write_b32 a20, v40\n\t"
  "v_accvgpr_write_b32 a21, v41\n\t"
  "v_accvgpr_write_b32 a22, v42\n\t"
  "v_accvgpr_write_b32 a23, v43\n\t"
  "ds_read_b128 v[32:35], v252 offset:512\n\t"
  "ds_read_b128 v[36:39], v252 offset:528\n\t"
  "ds_read_b128 v[40:43], v252 offset:2304\n\t"
  "ds_read_b128 v[44:47], v252 offset:2320\n\t"
  "s_lshl_b32 s66, s62, 5\n\t"
  "s_cmp_ge_u32 s63, 112\n\t"
  "s_cselect_b32 s66, 1024, s66\n\t"
  "v_lshrrev_b32_e32 v243, 1, v254\n\t"
  "v_add_u32_e32 v243, s66, v243\n\t"
  "v_and_b32_e32 v246, 1, v254\n\t"
  "v_mul_u32_u24_e32 v244, 5376, v243\n\t"
  "v_lshl_add_u32 v244, v246, 4, v244\n\t"
  "v_lshlrev_b32_e32 v243, 1, v243\n\t"
  "s_lshl_b32 s66, s63, 5\n\t"
  "s_add_u32 s66, s66, 1792\n\t"
  "s_min_u32 s66, s66, 5344\n\t"
  "s_add_u32 s44, s94, s66\n\t"
  "s_addc_u32 s45, s95, 0\n\t"
  "s_and_b32 s45, s45, 0xffff\n\t"
  "s_mov_b32 s46, 0\n\t"
  "s_mov_b32 s47, 0x20000\n\t"
  "s_sub_u32 s46, 2408448, s66\n\t"
  "s_add_u32 s40, s92, 0\n\t"
  "s_addc_u32 s41, s93, 0\n\t"
  "s_and_b32 s41, s41, 0xffff\n\t"
  "s_mov_b32 s42, 896\n\t"
  "s_mov_b32 s43, 0x20000\n\t"
  "s_waitcnt lgkmcnt(5)\n\t"
  "v_pk_fma_f32 v[48:49], v[48:49], v[24:25], v[16:17]\n\t"
  "v_pk_fma_f32 v[50:51], v[50:51], v[26:27], v[18:19]\n\t"
  "s_waitcnt lgkmcnt(4)\n\t"
  "v_pk_fma_f32 v[52:53], v[52:53], v[28:29], v[20:21]\n\t"
  "v_pk_fma_f32 v[54:55], v[54:55], v[30:31], v[22:23]\n\t"
  "v_pk_fma_f32 v[56:57], v[56:57], v[24:25], v[16:17]\n\t"
  "v_pk_fma_f32 v[58:59], v[58:59], v[26:27], v[18:19]\n\t"
  "v_pk_fma_f32 v[60:61], v[60:61], v[28:29], v[20:21]\n\t"
  "v_pk_fma_f32 v[62:63], v[62:63], v[30:31], v[22:23]\n\t"
  "v_cvt_pk_bf16_f32 v48, v48, v49\n\t"
  "v_cvt_pk_bf16_f32 v49, v50, v51\n\t"
  "v_cvt_pk_bf16_f32 v50, v52, v53\n\t"
  "v_cvt_pk_bf16_f32 v51, v54, v55\n\t"
  "v_accvgpr_write_b32 a24, v48\n\t"
  "v_accvgpr_write_b32 a25, v49\n\t"
  "v_accvgpr_write_b32 a26, v50\n\t"
  "v_accvgpr_write_b32 a27, v51\n\t"
  "v_cvt_pk_bf16_f32 v56, v56, v57\n\t"
  "v_cvt_pk_bf16_f32 v57, v58, v59\n\t"
  "v_cvt_pk_bf16_f32 v58, v60, v61\n\t"
  "v_cvt_pk_bf16_f32 v59, v62, v63\n\t"
  "v_accvgpr_write_b32 a28, v56\n\t"
  "v_accvgpr_write_b32 a29, v57\n\t"
  "v_accvgpr_write_b32 a30, v58\n\t"
  "v_accvgpr_write_b32 a31, v59\n\t"
  "ds_read_b128 v[48:51], v252 offset:640\n\t"
  "ds_read_b128 v[52:55], v252 offset:656\n\t"
  "ds_read_b128 v[56:59], v252 offset:2432\n\t"
  "ds_read_b128 v[60:63], v252 offset:2448\n\t"
  "buffer_load_dword v255, v253, s[24:27], s35 offen\n\t"
  "s_add_u32 s35, s35, 32768\n\t"
  "s_waitcnt lgkmcnt(5)\n\t"
  "v_pk_fma_f32 v[64:65], v[64:65], v[40:41], v[32:33]\n\t"
  "v_pk_fma_f32 v[66:67], v[66:67], v[42:43], v[34:35]\n\t"
  "s_waitcnt lgkmcnt(4)\n\t"
  "v_pk_fma_f32 v[68:69], v[68:69], v[44:45], v[36:37]\n\t"
  "v_pk_fma_f32 v[70:71], v[70:71], v[46:47], v[38:39]\n\t"
  "v_pk_fma_f32 v[72:73], v[72:73], v[40:41], v[32:33]\n\t"
  "v_pk_fma_f32 v[74:75], v[74:75], v[42:43], v[34:35]\n\t"
  "v_pk_fma_f32 v[76:77], v[76:77], v[44:45], v[36:37]\n\t"
  "v_pk_fma_f32 v[78:79], v[78:79], v[46:47], v[38:39]\n\t"
  "v_cvt_pk_bf16_f32 v64, v64, v65\n\t"
  "v_cvt_pk_bf16_f32 v65, v66, v67\n\t"
  "v_cvt_pk_bf16_f32 v66, v68, v69\n\t"
  "v_cvt_pk_bf16_f32 v67, v70, v71\n\t"
  "v_accvgpr_write_b32 a32, v64\n\t"
  "v_accvgpr_write_b32 a33, v65\n\t"
  "v_accvgpr_write_b32 a34, v66\n\t"
  "v_accvgpr_write_b32 a35, v67\n\t"
  "v_cvt_pk_bf16_f32 v72, v72, v73\n\t"
  "v_cvt_pk_bf16_f32 v73, v74, v75\n\t"
  "v_cvt_pk_bf16_f32 v74, v76, v77\n\t"
  "v_cvt_pk_bf16_f32 v75, v78, v79\n\t"
  "v_accvgpr_write_b32 a36, v72\n\t"
  "v_accvgpr_write_b32 a37, v73\n\t"
  "v_accvgpr_write_b32 a38, v74\n\t"
  "v_accvgpr_write_b32 a39, v75\n\t"
  "ds_read_b128 v[64:67], v252 offset:768\n\t"
  "ds_read_b128 v[68:71], v252 offset:784\n\t"
  "ds_read_b128 v[72:75], v252 offset:2560\n\t"
  "ds_read_b128 v[76:79], v252 offset:2576\n\t"
  "buffer_load_dword v255, v253, s[24:27], s35 offen\n\t"
  "s_add_u32 s35, s35, 32768\n\t"
  "s_waitcnt lgkmcnt(5)\n\t"
  "v_pk_fma_f32 v[80:81], v[80:81], v[56:57], v[48:49]\n\t"
  "v_pk_fma_f32 v[82:83], v[82:83], v[58:59], v[50:51]\n\t"
  "s_waitcnt lgkmcnt(4)\n\t"
  "v_pk_fma_f32 v[84:85], v[84:85], v[60:61], v[52:53]\n\t"
  "v_pk_fma_f32 v[86:87], v[86:87], v[62:63], v[54:55]\n\t"
  "v_pk_fma_f32 v[88:89], v[88:89], v[56:57], v[48:49]\n\t"
  "v_pk_fma_f32 v[90:91], v[90:91], v[58:59], v[50:51]\n\t"
  "v_pk_fma_f32 v[92:93], v[92:93], v[60:61], v[52:53]\n\t"
  "v_pk_fma_f32 v[94:95], v[94:95], v[62:63], v[54:55]\n\t"
  "v_cvt_pk_bf16_f32 v80, v80, v81\n\t"
  "v_cvt_pk_bf16_f32 v81, v82, v83\n\t"
  "v_cvt_pk_bf16_f32 v82, v84, v85\n\t"
  "v_cvt_pk_bf16_f32 v83, v86, v87\n\t"
  "v_accvgpr_write_b32 a40, v80\n\t"
  "v_accvgpr_write_b32 a41, v81\n\t"
  "v_accvgpr_write_b32 a42, v82\n\t"
  "v_accvgpr_write_b32 a43, v83\n\t"
  "v_cvt_pk_bf16_f32 v88, v88, v89\n\t"
  "v_cvt_pk_bf16_f32 v89, v90, v91\n\t"
  "v_cvt_pk_bf16_f32 v90, v92, v93\n\t"
  "v_cvt_pk_bf16_f32 v91, v94, v95\n\t"
  "v_accvgpr_write_b32 a44, v88\n\t"
  "v_accvgpr_write_b32 a45, v89\n\t"
  "v_accvgpr_write_b32 a46, v90\n\t"
  "v_accvgpr_write_b32 a47, v91\n\t"
  "ds_read_b128 v[80:83], v252 offset:896\n\t"
  "ds_read_b128 v[84:87], v252 offset:912\n\t"
  "ds_read_b128 v[88:91], v252 offset:2688\n\t"
  "ds_read_b128 v[92:95], v252 offset:2704\n\t"
  "buffer_load_dword v255, v253, s[24:27], s35 offen\n\t"
  "s_add_u32 s35, s35, 32768\n\t"
  "s_waitcnt lgkmcnt(5)\n\t"
  "v_pk_fma_f32 v[96:97], v[96:97], v[72:73], v[64:65]\n\t"
  "v_pk_fma_f32 v[98:99], v[98:99], v[74:75], v[66:67]\n\t"
  "s_waitcnt lgkmcnt(4)\n\t"
  "v_pk_fma_f32 v[100:101], v[100:101], v[76:77], v[68:69]\n\t"
  "v_pk_fma_f32 v[102:103], v[102:103], v[78:79], v[70:71]\n\t"
  "v_pk_fma_f32 v[104:105], v[104:105], v[72:73], v[64:65]\n\t"
  "v_pk_fma_f32 v[106:107], v[106:107], v[74:75], v[66:67]\n\t"
  "v_pk_fma_f32 v[108:109], v[108:109], v[76:77], v[68:69]\n\t"
  "v_pk_fma_f32 v[110:111], v[110:111], v[78:79], v[70:71]\n\t"
  "v_cvt_pk_bf16_f32 v96, v96, v97\n\t"
  "v_cvt_pk_bf16_f32 v97, v98, v99\n\t"
  "v_cvt_pk_bf16_f32 v98, v100, v101\n\t"
  "v_cvt_pk_bf16_f32 v99, v102, v103\n\t"
  "v_accvgpr_write_b32 a48, v96\n\t"
  "v_accvgpr_write_b32 a49, v97\n\t"
  "v_accvgpr_write_b32 a50, v98\n\t"
  "v_accvgpr_write_b32 a51, v99\n\t"
  "v_cvt_pk_bf16_f32 v104, v104, v105\n\t"
  "v_cvt_pk_bf16_f32 v105, v106, v107\n\t"
  "v_cvt_pk_bf16_f32 v106, v108, v109\n\t"
  "v_cvt_pk_bf16_f32 v107, v110, v111\n\t"
  "v_accvgpr_write_b32 a52, v104\n\t"
  "v_accvgpr_write_b32 a53, v105\n\t"
  "v_accvgpr_write_b32 a54, v106\n\t"
  "v_accvgpr_write_b32 a55, v107\n\t"
  "ds_read_b128 v[96:99], v252 offset:1024\n\t"
  "ds_read_b128 v[100:103], v252 offset:1040\n\t"
  "ds_read_b128 v[104:107], v252 offset:2816\n\t"
  "ds_read_b128 v[108:111], v252 offset:2832\n\t"
  "s_waitcnt lgkmcnt(5)\n\t"
  "v_pk_fma_f32 v[112:113], v[112:113], v[88:89], v[80:81]\n\t"
  "v_pk_fma_f32 v[114:115], v[114:115], v[90:91], v[82:83]\n\t"
  "s_waitcnt lgkmcnt(4)\n\t"
  "v_pk_fma_f32 v[116:117], v[116:117], v[92:93], v[84:85]\n\t"
  "v_pk_fma_f32 v[118:119], v[118:119], v[94:95], v[86:87]\n\t"
  "v_pk_fma_f32 v[120:121], v[120:121], v[88:89], v[80:81]\n\t"
  "v_pk_fma_f32 v[122:123], v[122:123], v[90:91], v[82:83]\n\t"
  "v_pk_fma_f32 v[124:125], v[124:125], v[92:93], v[84:85]\n\t"
  "v_pk_fma_f32 v[126:127], v[126:127], v[94:95], v[86:87]\n\t"
  "v_cvt_pk_bf16_f32 v112, v112, v113\n\t"
  "v_cvt_pk_bf16_f32 v113, v114, v115\n\t"
  "v_cvt_pk_bf16_f32 v114, v116, v117\n\t"
  "v_cvt_pk_bf16_f32 v115, v118, v119\n\t"
  "v_accvgpr_write_b32 a56, v112\n\t"
  "v_accvgpr_write_b32 a57, v113\n\t"
  "v_accvgpr_write_b32 a58, v114\n\t"
  "v_accvgpr_write_b32 a59, v115\n\t"
  "v_cvt_pk_bf16_f32 v120, v120, v121\n\t"
  "v_cvt_pk_bf16_f32 v121, v122, v123\n\t"
  "v_cvt_pk_bf16_f32 v122, v124, v125\n\t"
  "v_cvt_pk_bf16_f32 v123, v126, v127\n\t"
  "v_accvgpr_write_b32 a60, v120\n\t"
  "v_accvgpr_write_b32 a61, v121\n\t"
  "v_accvgpr_write_b32 a62, v122\n\t"
  "v_accvgpr_write_b32 a63, v123\n\t"
  "ds_read_b128 v[112:115], v252 offset:1152\n\t"
  "ds_read_b128 v[116:119], v252 offset:1168\n\t"
  "ds_read_b128 v[120:123], v252 offset:2944\n\t"
  "ds_read_b128 v[124:127], v252 offset:2960\n\t"
  "s_waitcnt lgkmcnt(5)\n\t"
  "v_pk_fma_f32 v[128:129], v[128:129], v[104:105], v[96:97]\n\t"
  "v_pk_fma_f32 v[130:131], v[130:131], v[106:107], v[98:99]\n\t"
  "s_waitcnt lgkmcnt(4)\n\t"
  "v_pk_fma_f32 v[132:133], v[132:133], v[108:109], v[100:101]\n\t"
  "v_pk_fma_f32 v[134:135], v[134:135], v[110:111], v[102:103]\n\t"
  "v_pk_fma_f32 v[136:137], v[136:137], v[104:105], v[96:97]\n\t"
  "v_pk_fma_f32 v[138:139], v[138:139], v[106:107], v[98:99]\n\t"
  "v_pk_fma_f32 v[140:141], v[140:141], v[108:109], v[100:101]\n\t"
  "v_pk_fma_f32 v[142:143], v[142:143], v[110:111], v[102:103]\n\t"
  "v_cvt_pk_bf16_f32 v128, v128, v129\n\t"
  "v_cvt_pk_bf16_f32 v129, v130, v131\n\t"
  "v_cvt_pk_bf16_f32 v130, v132, v133\n\t"
  "v_cvt_pk_bf16_f32 v131, v134, v135\n\t"
  "v_accvgpr_write_b32 a64, v128\n\t"
  "v_accvgpr_write_b32 a65, v129\n\t"
  "v_accvgpr_write_b32 a66, v130\n\t"
  "v_accvgpr_write_b32 a67, v131\n\t"
  "v_cvt_pk_bf16_f32 v136, v136, v137\n\t"
  "v_cvt_pk_bf16_f32 v137, v138, v139\n\t"
  "v_cvt_pk_bf16_f32 v138, v140, v141\n\t"
  "v_cvt_pk_bf16_f32 v139, v142, v143\n\t"
  "v_accvgpr_write_b32 a68, v136\n\t"
  "v_accvgpr_write_b32 a69, v137\n\t"
  "v_accvgpr_write_b32 a70, v138\n\t"
  "v_accvgpr_write_b32 a71, v139\n\t"
  "ds_read_b128 v[128:131], v252 offset:1280\n\t"
  "ds_read_b128 v[132:135], v252 offset:1296\n\t"
  "ds_read_b128 v[136:139], v252 offset:3072\n\t"
  "ds_read_b128 v[140:143], v252 offset:3088\n\t"
  "s_waitcnt lgkmcnt(5)\n\t"
  "v_pk_fma_f32 v[144:145], v[144:145], v[120:121], v[112:113]\n\t"
  "v_pk_fma_f32 v[146:147], v[146:147], v[122:123], v[114:115]\n\t"
  "s_waitcnt lgkmcnt(4)\n\t"
  "v_pk_fma_f32 v[148:149], v[148:149], v[124:125], v[116:117]\n\t"
  "v_pk_fma_f32 v[150:151], v[150:151], v[126:127], v[118:119]\n\t"
  "v_pk_fma_f32 v[152:153], v[152:153], v[120:121], v[112:113]\n\t"
  "v_pk_fma_f32 v[154:155], v[154:155], v[122:123], v[114:115]\n\t"
  "v_pk_fma_f32 v[156:157], v[156:157], v[124:125], v[116:117]\n\t"
  "v_pk_fma_f32 v[158:159], v[158:159], v[126:127], v[118:119]\n\t"
  "v_cvt_pk_bf16_f32 v144, v144, v145\n\t"
  "v_cvt_pk_bf16_f32 v145, v146, v147\n\t"
  "v_cvt_pk_bf16_f32 v146, v148, v149\n\t"
  "v_cvt_pk_bf16_f32 v147, v150, v151\n\t"
  "v_accvgpr_write_b32 a72, v144\n\t"
  "v_accvgpr_write_b32 a73, v145\n\t"
  "v_accvgpr_write_b32 a74, v146\n\t"
  "v_accvgpr_write_b32 a75, v147\n\t"
  "v_cvt_pk_bf16_f32 v152, v152, v153\n\t"
  "v_cvt_pk_bf16_f32 v153, v154, v155\n\t"
  "v_cvt_pk_bf16_f32 v154, v156, v157\n\t"
  "v_cvt_pk_bf16_f32 v155, v158, v159\n\t"
  "v_accvgpr_write_b32 a76, v152\n\t"
  "v_accvgpr_write_b32 a77, v153\n\t"
  "v_accvgpr_write_b32 a78, v154\n\t"
  "v_accvgpr_write_b32 a79, v155\n\t"
  "ds_read_b128 v[144:147], v252 offset:1408\n\t"
  "ds_read_b128 v[148:151], v252 offset:1424\n\t"
  "ds_read_b128 v[152:155], v252 offset:3200\n\t"
  "ds_read_b128 v[156:159], v252 offset:3216\n\t"
  "s_waitcnt lgkmcnt(5)\n\t"
  "v_pk_fma_f32 v[160:161], v[160:161], v[136:137], v[128:129]\n\t"
  "v_pk_fma_f32 v[162:163], v[162:163], v[138:139], v[130:131]\n\t"
  "s_waitcnt lgkmcnt(4)\n\t"
  "v_pk_fma_f32 v[164:165], v[164:165], v[140:141], v[132:133]\n\t"
  "v_pk_fma_f32 v[166:167], v[166:167], v[142:143], v[134:135]\n\t"
  "v_pk_fma_f32 v[168:169], v[168:169], v[136:137], v[128:129]\n\t"
  "v_pk_fma_f32 v[170:171], v[170:171], v[138:139], v[130:131]\n\t"
  "v_pk_fma_f32 v[172:173], v[172:173], v[140:141], v[132:133]\n\t"
  "v_pk_fma_f32 v[174:175], v[174:175], v[142:143], v[134:135]\n\t"
  "v_cvt_pk_bf16_f32 v160, v160, v161\n\t"
  "v_cvt_pk_bf16_f32 v161, v162, v163\n\t"
  "v_cvt_pk_bf16_f32 v162, v164, v165\n\t"
  "v_cvt_pk_bf16_f32 v163, v166, v167\n\t"
  "v_accvgpr_write_b32 a80, v160\n\t"
  "v_accvgpr_write_b32 a81, v161\n\t"
  "v_accvgpr_write_b32 a82, v162\n\t"
  "v_accvgpr_write_b32 a83, v163\n\t"
  "v_cvt_pk_bf16_f32 v168, v168, v169\n\t"
  "v_cvt_pk_bf16_f32 v169, v170, v171\n\t"
  "v_cvt_pk_bf16_f32 v170, v172, v173\n\t"
  "v_cvt_pk_bf16_f32 v171, v174, v175\n\t"
  "v_accvgpr_write_b32 a84, v168\n\t"
  "v_accvgpr_write_b32 a85, v169\n\t"
  "v_accvgpr_write_b32 a86, v170\n\t"
  "v_accvgpr_write_b32 a87, v171\n\t"
  "ds_read_b128 v[160:163], v252 offset:1536\n\t"
  "ds_read_b128 v[164:167], v252 offset:1552\n\t"
  "ds_read_b128 v[168:171], v252 offset:3328\n\t"
  "ds_read_b128 v[172:175], v252 offset:3344\n\t"
  "s_waitcnt lgkmcnt(5)\n\t"
  "v_pk_fma_f32 v[176:177], v[176:177], v[152:153], v[144:145]\n\t"
  "v_pk_fma_f32 v[178:179], v[178:179], v[154:155], v[146:147]\n\t"
  "s_waitcnt lgkmcnt(4)\n\t"
  "v_pk_fma_f32 v[180:181], v[180:181], v[156:157], v[148:149]\n\t"
  "v_pk_fma_f32 v[182:183], v[182:183], v[158:159], v[150:151]\n\t"
  "v_pk_fma_f32 v[184:185], v[184:185], v[152:153], v[144:145]\n\t"
  "v_pk_fma_f32 v[186:187], v[186:187], v[154:155], v[146:147]\n\t"
  "v_pk_fma_f32 v[188:189], v[188:189], v[156:157], v[148:149]\n\t"
  "v_pk_fma_f32 v[190:191], v[190:191], v[158:159], v[150:151]\n\t"
  "v_cvt_pk_bf16_f32 v176, v176, v177\n\t"
  "v_cvt_pk_bf16_f32 v177, v178, v179\n\t"
  "v_cvt_pk_bf16_f32 v178, v180, v181\n\t"
  "v_cvt_pk_bf16_f32 v179, v182, v183\n\t"
  "v_accvgpr_write_b32 a88, v176\n\t"
  "v_accvgpr_write_b32 a89, v177\n\t"
  "v_accvgpr_write_b32 a90, v178\n\t"
  "v_accvgpr_write_b32 a91, v179\n\t"
  "v_cvt_pk_bf16_f32 v184, v184, v185\n\t"
  "v_cvt_pk_bf16_f32 v185, v186, v187\n\t"
  "v_cvt_pk_bf16_f32 v186, v188, v189\n\t"
  "v_cvt_pk_bf16_f32 v187, v190, v191\n\t"
  "v_accvgpr_write_b32 a92, v184\n\t"
  "v_accvgpr_write_b32 a93, v185\n\t"
  "v_accvgpr_write_b32 a94, v186\n\t"
  "v_accvgpr_write_b32 a95, v187\n\t"
  "ds_read_b128 v[176:179], v252 offset:1664\n\t"
  "ds_read_b128 v[180:183], v252 offset:1680\n\t"
  "ds_read_b128 v[184:187], v252 offset:3456\n\t"
  "ds_read_b128 v[188:191], v252 offset:3472\n\t"
  "s_waitcnt lgkmcnt(5)\n\t"
  "v_pk_fma_f32 v[192:193], v[192:193], v[168:169], v[160:161]\n\t"
  "v_pk_fma_f32 v[194:195], v[194:195], v[170:171], v[162:163]\n\t"
  "s_waitcnt lgkmcnt(4)\n\t"
  "v_pk_fma_f32 v[196:197], v[196:197], v[172:173], v[164:165]\n\t"
  "v_pk_fma_f32 v[198:199], v[198:199], v[174:175], v[166:167]\n\t"
  "v_pk_fma_f32 v[200:201], v[200:201], v[168:169], v[160:161]\n\t"
  "v_pk_fma_f32 v[202:203], v[202:203], v[170:171], v[162:163]\n\t"
  "v_pk_fma_f32 v[204:205], v[204:205], v[172:173], v[164:165]\n\t"
  "v_pk_fma_f32 v[206:207], v[206:207], v[174:175], v[166:167]\n\t"
  "v_cvt_pk_bf16_f32 v192, v192, v193\n\t"
  "v_cvt_pk_bf16_f32 v193, v194, v195\n\t"
  "v_cvt_pk_bf16_f32 v194, v196, v197\n\t"
  "v_cvt_pk_bf16_f32 v195, v198, v199\n\t"
  "v_accvgpr_write_b32 a96, v192\n\t"
  "v_accvgpr_write_b32 a97, v193\n\t"
  "v_accvgpr_write_b32 a98, v194\n\t"
  "v_accvgpr_write_b32 a99, v195\n\t"
  "v_cvt_pk_bf16_f32 v200, v200, v201\n\t"
  "v_cvt_pk_bf16_f32 v201, v202, v203\n\t"
  "v_cvt_pk_bf16_f32 v202, v204, v205\n\t"
  "v_cvt_pk_bf16_f32 v203, v206, v207\n\t"
  "v_accvgpr_write_b32 a100, v200\n\t"
  "v_accvgpr_write_b32 a101, v201\n\t"
  "v_accvgpr_write_b32 a102, v202\n\t"
  "v_accvgpr_write_b32 a103, v203\n\t"
  "s_waitcnt lgkmcnt(1)\n\t"
  "v_pk_fma_f32 v[208:209], v[208:209], v[184:185], v[176:177]\n\t"
  "v_pk_fma_f32 v[210:211], v[210:211], v[186:187], v[178:179]\n\t"
  "s_waitcnt lgkmcnt(0)\n\t"
  "v_pk_fma_f32 v[212:213], v[212:213], v[188:189], v[180:181]\n\t"
  "v_pk_fma_f32 v[214:215], v[214:215], v[190:191], v[182:183]\n\t"
  "v_pk_fma_f32 v[216:217], v[216:217], v[184:185], v[176:177]\n\t"
  "v_pk_fma_f32 v[218:219], v[218:219], v[186:187], v[178:179]\n\t"
  "v_pk_fma_f32 v[220:221], v[220:221], v[188:189], v[180:181]\n\t"
  "v_pk_fma_f32 v[222:223], v[222:223], v[190:191], v[182:183]\n\t"
  "v_cvt_pk_bf16_f32 v208, v208, v209\n\t"
  "v_cvt_pk_bf16_f32 v209, v210, v211\n\t"
  "v_cvt_pk_bf16_f32 v210, v212, v213\n\t"
  "v_cvt_pk_bf16_f32 v211, v214, v215\n\t"
  "v_accvgpr_write_b32 a104, v208\n\t"
  "v_accvgpr_write_b32 a105, v209\n\t"
  "v_accvgpr_write_b32 a106, v210\n\t"
  "v_accvgpr_write_b32 a107, v211\n\t"
  "v_cvt_pk_bf16_f32 v216, v216, v217\n\t"
  "v_cvt_pk_bf16_f32 v217, v218, v219\n\t"
  "v_cvt_pk_bf16_f32 v218, v220, v221\n\t"
  "v_cvt_pk_bf16_f32 v219, v222, v223\n\t"
  "v_accvgpr_write_b32 a108, v216\n\t"
  "v_accvgpr_write_b32 a109, v217\n\t"
  "v_accvgpr_write_b32 a110, v218\n\t"
  "v_accvgpr_write_b32 a111, v219\n\t"
  "v_add_u32_e32 v252, 131072, v248\n\t"
  "v_add_u32_e32 v253, 131072, v249\n\t"
  "ds_read_b64_tr_b16 v[128:129], v250 offset:32768\n\t"
  "ds_read_b64_tr_b16 v[130:131], v250 offset:33024\n\t"
  "ds_read_b64_tr_b16 v[132:133], v250 offset:49152\n\t"
  "ds_read_b64_tr_b16 v[134:135], v250 offset:49408\n\t"
  "ds_read_b64_tr_b16 v[136:137], v251 offset:32768\n\t"
  "ds_read_b64_tr_b16 v[138:139], v251 offset:33024\n\t"
  "ds_read_b64_tr_b16 v[140:141], v251 offset:49152\n\t"
  "ds_read_b64_tr_b16 v[142:143], v251 offset:49408\n\t"
  "ds_read_b64_tr_b16 v[144:145], v250 offset:33280\n\t"
  "ds_read_b64_tr_b16 v[146:147], v250 offset:33536\n\t"
  "ds_read_b64_tr_b16 v[148:149], v250 offset:49664\n\t"
  "ds_read_b64_tr_b16 v[150:151], v250 offset:49920\n\t"
  "ds_read_b64_tr_b16 v[152:153], v251 offset:33280\n\t"
  "ds_read_b64_tr_b16 v[154:155], v251 offset:33536\n\t"
  "s_waitcnt lgkmcnt(6)\n\t"
  "v_mfma_f32_16x16x32_bf16 v[0:3], v[128:131], a[0:3], 0\n\t"
  "ds_read_b64_tr_b16 v[156:157], v251 offset:49664\n\t"
  "s_add_u32 m0, s30, 0\n\t"
  "v_mfma_f32_16x16x32_bf16 v[4:7], v[128:131], a[4:7], 0\n\t"
  "ds_read_b64_tr_b16 v[158:159], v251 offset:49920\n\t"
  "buffer_load_dwordx4 v224, s[16:19], s36 offen lds\n\t"
  "v_mfma_f32_16x16x32_bf16 v[4:7], v[132:135], a[12:15], v[4:7]\n\t"
  "ds_read_b64_tr_b16 v[160:161], v250 offset:33792\n\t"
  "v_bfe_u32 v192, v254, 2, 3\n\t"
  "v_mfma_f32_16x16x32_bf16 v[0:3], v[132:135], a[8:11], v[0:3]\n\t"
  "ds_read_b64_tr_b16 v[162:163], v250 offset:34048\n\t"
  "v_mfma_f32_16x16x32_bf16 v[8:11], v[136:139], a[0:3], 0\n\t"
  "ds_read_b64_tr_b16 v[164:165], v250 offset:50176\n\t"
  "v_and_b32_e32 v193, 3, v254\n\t"
  "v_mfma_f32_16x16x32_bf16 v[12:15], v[136:139], a[4:7], 0\n\t"
  "ds_read_b64_tr_b16 v[166:167], v250 offset:50432\n\t"
  "buffer_load_dwordx4 v225, s[16:19], s36 offen offset:1024 lds\n\t"
  "v_mfma_f32_16x16x32_bf16 v[12:15], v[140:143], a[12:15], v[12:15]\n\t"
  "ds_read_b64_tr_b16 v[168:169], v251 offset:33792\n\t"
  "v_lshrrev_b32_e32 v194, 5, v254\n\t"
  "v_mfma_f32_16x16x32_bf16 v[8:11], v[140:143], a[8:11], v[8:11]\n\t"
  "ds_read_b64_tr_b16 v[170:171], v251 offset:34048\n\t"
  "s_waitcnt lgkmcnt(6)\n\t"
  "v_mfma_f32_16x16x32_bf16 v[16:19], v[144:147], a[0:3], 0\n\t"
  "ds_read_b64_tr_b16 v[172:173], v251 offset:50176\n\t"
  "v_and_b32_e32 v196, 15, v254\n\t"
  "v_mfma_f32_16x16x32_bf16 v[20:23], v[144:147], a[4:7], 0\n\t"
  "ds_read_b64_tr_b16 v[174:175], v251 offset:50432\n\t"
  "buffer_load_dwordx4 v226, s[16:19], s36 offen offset:2048 lds\n\t"
  "v_mfma_f32_16x16x32_bf16 v[20:23], v[148:151], a[12:15], v[20:23]\n\t"
  "ds_read_b64_tr_b16 v[176:177], v250 offset:34304\n\t"
  "v_lshrrev_b32_e32 v197, 4, v254\n\t"
  "v_mfma_f32_16x16x32_bf16 v[16:19], v[148:151], a[8:11], v[16:19]\n\t"
  "ds_read_b64_tr_b16 v[178:179], v250 offset:34560\n\t"
  "v_mfma_f32_16x16x32_bf16 v[24:27], v[152:155], a[0:3], 0\n\t"
  "ds_read_b64_tr_b16 v[180:181], v250 offset:50688\n\t"
  "v_lshrrev_b32_e32 v199, 2, v254\n\t"
  "v_mfma_f32_16x16x32_bf16 v[28:31], v[152:155], a[4:7], 0\n\t"
  "ds_read_b64_tr_b16 v[182:183], v250 offset:50944\n\t"
  "buffer_load_dwordx4 v227, s[16:19], s36 offen offset:3072 lds\n\t"
  "v_mfma_f32_16x16x32_bf16 v[28:31], v[156:159], a[12:15], v[28:31]\n\t"
  "ds_read_b64_tr_b16 v[184:185], v251 offset:34304\n\t"
  "s_add_u32 s36, s36, 65536\n\t"
  "v_mfma_f32_16x16x32_bf16 v[24:27], v[156:159], a[8:11], v[24:27]\n\t"
  "ds_read_b64_tr_b16 v[186:187], v251 offset:34560\n\t"
  "s_waitcnt lgkmcnt(6)\n\t"
  "v_mfma_f32_16x16x32_bf16 v[32:35], v[160:163], a[0:3], 0\n\t"
  "ds_read_b64_tr_b16 v[188:189], v251 offset:50688\n\t"
  "s_add_u32 m0, s30, 4096\n\t"
  "v_mfma_f32_16x16x32_bf16 v[36:39], v[160:163], a[4:7], 0\n\t"
  "ds_read_b64_tr_b16 v[190:191], v251 offset:50944\n\t"
  "buffer_load_dwordx4 v228, s[16:19], s36 offen lds\n\t"
  "v_mfma_f32_16x16x32_bf16 v[36:39], v[164:167], a[12:15], v[36:39]\n\t"
  "ds_read_b64_tr_b16 v[128:129], v250 offset:34816\n\t"
  "v_lshlrev_b32_e32 v199, 7, v199\n\t"
  "v_mfma_f32_16x16x32_bf16 v[32:35], v[164:167], a[8:11], v[32:35]\n\t"
  "ds_read_b64_tr_b16 v[130:131], v250 offset:35072\n\t"
  "v_mfma_f32_16x16x32_bf16 v[40:43], v[168:171], a[0:3], 0\n\t"
  "ds_read_b64_tr_b16 v[132:133], v250 offset:51200\n\t"
  "v_add_u32_e32 v245, s67, v199\n\t"
  "v_mfma_f32_16x16x32_bf16 v[44:47], v[168:171], a[4:7], 0\n\t"
  "ds_read_b64_tr_b16 v[134:135], v250 offset:51456\n\t"
  "buffer_load_dwordx4 v229, s[16:19], s36 offen offset:1024 lds\n\t"
  "v_mfma_f32_16x16x32_bf16 v[44:47], v[172:175], a[12:15], v[44:47]\n\t"
  "ds_read_b64_tr_b16 v[136:137], v251 offset:34816\n\t"
  "v_bfe_u32 v195, v254, 4, 1\n\t"
  "v_mfma_f32_16x16x32_bf16 v[40:43], v[172:175], a[8:11], v[40:43]\n\t"
  "ds_read_b64_tr_b16 v[138:139], v251 offset:35072\n\t"
  "s_waitcnt lgkmcnt(6)\n\t"
  "v_mfma_f32_16x16x32_bf16 v[48:51], v[176:179], a[0:3], 0\n\t"
  "ds_read_b64_tr_b16 v[140:141], v251 offset:51200\n\t"
  "v_lshlrev_b32_e32 v202, 1, v195\n\t"
  "v_mfma_f32_16x16x32_bf16 v[52:55], v[176:179], a[4:7], 0\n\t"
  "ds_read_b64_tr_b16 v[142:143], v251 offset:51456\n\t"
  "buffer_load_dwordx4 v230, s[16:19], s36 offen offset:2048 lds\n\t"
  "v_mfma_f32_16x16x32_bf16 v[52:55], v[180:183], a[12:15], v[52:55]\n\t"
  "ds_read_b64_tr_b16 v[144:145], v250 offset:35328\n\t"
  "v_xor_b32_e32 v195, v202, v193\n\t"
  "v_mfma_f32_16x16x32_bf16 v[48:51], v[180:183], a[8:11], v[48:51]\n\t"
  "ds_read_b64_tr_b16 v[146:147], v250 offset:35584\n\t"
  "v_mfma_f32_16x16x32_bf16 v[56:59], v[184:187], a[0:3], 0\n\t"
  "ds_read_b64_tr_b16 v[148:149], v250 offset:51712\n\t"
  "v_lshl_add_u32 v195, v194, 2, v195\n\t"
  "v_mfma_f32_16x16x32_bf16 v[60:63], v[184:187], a[4:7], 0\n\t"
  "ds_read_b64_tr_b16 v[150:151], v250 offset:51968\n\t"
  "buffer_load_dwordx4 v231, s[16:19], s36 offen offset:3072 lds\n\t"
  "v_mfma_f32_16x16x32_bf16 v[60:63], v[188:191], a[12:15], v[60:63]\n\t"
  "ds_read_b64_tr_b16 v[152:153], v251 offset:35328\n\t"
  "s_add_u32 s36, s36, 458752\n\t"
  "v_mfma_f32_16x16x32_bf16 v[56:59], v[188:191], a[8:11], v[56:59]\n\t"
  "ds_read_b64_tr_b16 v[154:155], v251 offset:35584\n\t"
  "s_waitcnt lgkmcnt(6)\n\t"
  "v_mfma_f32_16x16x32_bf16 v[64:67], v[128:131], a[0:3], 0\n\t"
  "ds_read_b64_tr_b16 v[156:157], v251 offset:51712\n\t"
  "v_lshlrev_b32_e32 v200, 4, v195\n\t"
  "v_mfma_f32_16x16x32_bf16 v[68:71], v[128:131], a[4:7], 0\n\t"
  "ds_read_b64_tr_b16 v[158:159], v251 offset:51968\n\t"
  "v_lshl_add_u32 v195, v192, 9, v200\n\t"
  "v_mfma_f32_16x16x32_bf16 v[68:71], v[132:135], a[12:15], v[68:71]\n\t"
  "ds_read_b64_tr_b16 v[160:161], v250 offset:35840\n\t"
  "v_add_u32_e32 v235, 3072, v195\n\t"
  "buffer_load_dwordx4 a[112:115], v244, s[44:47], 0 offen\n\t"
  "v_mfma_f32_16x16x32_bf16 v[64:67], v[132:135], a[8:11], v[64:67]\n\t"
  "ds_read_b64_tr_b16 v[162:163], v250 offset:36096\n\t"
  "v_mfma_f32_16x16x32_bf16 v[72:75], v[136:139], a[0:3], 0\n\t"
  "ds_read_b64_tr_b16 v[164:165], v250 offset:52224\n\t"
  "v_add_u32_e32 v236, 2176, v195\n\t"
  "v_mfma_f32_16x16x32_bf16 v[76:79], v[136:139], a[4:7], 0\n\t"
  "ds_read_b64_tr_b16 v[166:167], v250 offset:52480\n\t"
  "v_add_u32_e32 v237, 1280, v195\n\t"
  "v_mfma_f32_16x16x32_bf16 v[76:79], v[140:143], a[12:15], v[76:79]\n\t"
  "ds_read_b64_tr_b16 v[168:169], v251 offset:35840\n\t"
  "v_add_u32_e32 v238, 384, v195\n\t"
  "v_mfma_f32_16x16x32_bf16 v[72:75], v[140:143], a[8:11], v[72:75]\n\t"
  "ds_read_b64_tr_b16 v[170:171], v251 offset:36096\n\t"
  "s_waitcnt lgkmcnt(6)\n\t"
  "v_mfma_f32_16x16x32_bf16 v[80:83], v[144:147], a[0:3], 0\n\t"
  "ds_read_b64_tr_b16 v[172:173], v251 offset:52224\n\t"
  "v_bfe_u32 v199, v254, 3, 1\n\t"
  "buffer_load_ushort v239, v243, s[40:43], 0 offen\n\t"
  "v_add_u32_e32 v244, 688128, v244\n\t"
  "v_mfma_f32_16x16x32_bf16 v[84:87], v[144:147], a[4:7], 0\n\t"
  "ds_read_b64_tr_b16 v[174:175], v251 offset:52480\n\t"
  "v_and_b32_e32 v200, 7, v254\n\t"
  "v_mfma_f32_16x16x32_bf16 v[84:87], v[148:151], a[12:15], v[84:87]\n\t"
  "ds_read_b64_tr_b16 v[176:177], v250 offset:36352\n\t"
  "v_bfe_u32 v201, v254, 2, 1\n\t"
  "v_mfma_f32_16x16x32_bf16 v[80:83], v[148:151], a[8:11], v[80:83]\n\t"
  "ds_read_b64_tr_b16 v[178:179], v250 offset:36608\n\t"
  "v_mfma_f32_16x16x32_bf16 v[88:91], v[152:155], a[0:3], 0\n\t"
  "ds_read_b64_tr_b16 v[180:181], v250 offset:52736\n\t"
  "v_lshlrev_b32_e32 v201, 1, v201\n\t"
  "v_mfma_f32_16x16x32_bf16 v[92:95], v[152:155], a[4:7], 0\n\t"
  "ds_read_b64_tr_b16 v[182:183], v250 offset:52992\n\t"
  "v_xor_b32_e32 v201, v201, v197\n\t"
  "v_mfma_f32_16x16x32_bf16 v[92:95], v[156:159], a[12:15], v[92:95]\n\t"
  "ds_read_b64_tr_b16 v[184:185], v251 offset:36352\n\t"
  "v_lshlrev_b32_e32 v201, 4, v201\n\t"
  "v_mfma_f32_16x16x32_bf16 v[88:91], v[156:159], a[8:11], v[88:91]\n\t"
  "ds_read_b64_tr_b16 v[186:187], v251 offset:36608\n\t"
  "s_waitcnt lgkmcnt(6)\n\t"
  "v_mfma_f32_16x16x32_bf16 v[96:99], v[160:163], a[0:3], 0\n\t"
  "ds_read_b64_tr_b16 v[188:189], v251 offset:52736\n\t"
  "s_waitcnt vmcnt(16)\n\t"
  "s_barrier\n\t"
  "buffer_load_dwordx4 a[116:119], v244, s[44:47], 0 offen\n\t"
  "v_mfma_f32_16x16x32_bf16 v[100:103], v[160:163], a[4:7], 0\n\t"
  "ds_read_b64_tr_b16 v[190:191], v251 offset:52992\n\t"
  "v_lshl_add_u32 v201, v200, 6, v201\n\t"
  "v_mfma_f32_16x16x32_bf16 v[100:103], v[164:167], a[12:15], v[100:103]\n\t"
  "ds_read_b64_tr_b16 v[128:129], v250\n\t"
  "v_lshl_add_u32 v201, v199, 12, v201\n\t"
  "v_mfma_f32_16x16x32_bf16 v[96:99], v[164:167], a[8:11], v[96:99]\n\t"
  "ds_read_b64_tr_b16 v[130:131], v250 offset:256\n\t"
  "v_mfma_f32_16x16x32_bf16 v[104:107], v[168:171], a[0:3], 0\n\t"
  "ds_read_b64_tr_b16 v[132:133], v250 offset:16384\n\t"
  "v_add_u32_e32 v232, s39, v201\n\t"
  "v_mfma_f32_16x16x32_bf16 v[108:111], v[168:171], a[4:7], 0\n\t"
  "ds_read_b64_tr_b16 v[134:135], v250 offset:16640\n\t"
  "v_bfe_u32 v201, v254, 1, 1\n\t"
  "buffer_load_ushort v240, v243, s[40:43], 0 offen offset:256\n\t"
  "v_add_u32_e32 v244, 688128, v244\n\t"
  "v_mfma_f32_16x16x32_bf16 v[108:111], v[172:175], a[12:15], v[108:111]\n\t"
  "ds_read_b64_tr_b16 v[136:137], v251\n\t"
  "v_and_b32_e32 v203, 1, v254\n\t"
  "v_mfma_f32_16x16x32_bf16 v[104:107], v[172:175], a[8:11], v[104:107]\n\t"
  "ds_read_b64_tr_b16 v[138:139], v251 offset:256\n\t"
  "s_waitcnt lgkmcnt(6)\n\t"
  "v_mfma_f32_16x16x32_bf16 v[112:115], v[176:179], a[0:3], 0\n\t"
  "ds_read_b64_tr_b16 v[140:141], v251 offset:16384\n\t"
  "v_lshlrev_b32_e32 v203, 3, v203\n\t"
  "v_mfma_f32_16x16x32_bf16 v[116:119], v[176:179], a[4:7], 0\n\t"
  "ds_read_b64_tr_b16 v[142:143], v251 offset:16640\n\t"
  "v_lshl_add_u32 v199, v192, 6, v203\n\t"
  "v_mfma_f32_16x16x32_bf16 v[116:119], v[180:183], a[12:15], v[116:119]\n\t"
  "ds_read_b64_tr_b16 v[144:145], v250 offset:512\n\t"
  "v_lshl_add_u32 v199, v194, 12, v199\n\t"
  "v_mfma_f32_16x16x32_bf16 v[112:115], v[180:183], a[8:11], v[112:115]\n\t"
  "ds_read_b64_tr_b16 v[146:147], v250 offset:768\n\t"
  "v_mfma_f32_16x16x32_bf16 v[120:123], v[184:187], a[0:3], 0\n\t"
  "ds_read_b64_tr_b16 v[148:149], v250 offset:16896\n\t"
  "v_add_u32_e32 v199, s39, v199\n\t"
  "v_mfma_f32_16x16x32_bf16 v[124:127], v[184:187], a[4:7], 0\n\t"
  "ds_read_b64_tr_b16 v[150:151], v250 offset:17152\n\t"
  "v_add_u32_e32 v199, 65536, v199\n\t"
  "v_mfma_f32_16x16x32_bf16 v[124:127], v[188:191], a[12:15], v[124:127]\n\t"
  "ds_read_b64_tr_b16 v[152:153], v251 offset:512\n\t"
  "v_xor_b32_e32 v205, v201, v202\n\t"
  "v_mfma_f32_16x16x32_bf16 v[120:123], v[188:191], a[8:11], v[120:123]\n\t"
  "ds_read_b64_tr_b16 v[154:155], v251 offset:768\n\t"
  "s_waitcnt lgkmcnt(6)\n\t"
  "v_mfma_f32_16x16x32_bf16 v[0:3], v[128:131], a[16:19], v[0:3]\n\t"
  "ds_read_b64_tr_b16 v[156:157], v251 offset:16896\n\t"
  "s_add_u32 m0, s30, 32768\n\t"
  "v_mfma_f32_16x16x32_bf16 v[4:7], v[128:131], a[20:23], v[4:7]\n\t"
  "ds_read_b64_tr_b16 v[158:159], v251 offset:17152\n\t"
  "buffer_load_dwordx4 v224, s[16:19], s36 offen lds\n\t"
  "v_mfma_f32_16x16x32_bf16 v[4:7], v[132:135], a[28:31], v[4:7]\n\t"
  "ds_read_b64_tr_b16 v[160:161], v250 offset:1024\n\t"
  "v_lshl_add_u32 v233, v205, 4, v199\n\t"
  "v_mfma_f32_16x16x32_bf16 v[0:3], v[132:135], a[24:27], v[0:3]\n\t"
  "ds_read_b64_tr_b16 v[162:163], v250 offset:1280\n\t"
  "v_mfma_f32_16x16x32_bf16 v[8:11], v[136:139], a[16:19], v[8:11]\n\t"
  "ds_read_b64_tr_b16 v[164:165], v250 offset:17408\n\t"
  "v_xor_b32_e32 v234, 32, v233\n\t"
  "v_mfma_f32_16x16x32_bf16 v[12:15], v[136:139], a[20:23], v[12:15]\n\t"
  "ds_read_b64_tr_b16 v[166:167], v250 offset:17664\n\t"
  "buffer_load_dwordx4 v225, s[16:19], s36 offen offset:1024 lds\n\t"
  "v_mfma_f32_16x16x32_bf16 v[12:15], v[140:143], a[28:31], v[12:15]\n\t"
  "ds_read_b64_tr_b16 v[168:169], v251 offset:1024\n\t"
  "v_xor_b32_e32 v199, v194, v196\n\t"
  "v_mfma_f32_16x16x32_bf16 v[8:11], v[140:143], a[24:27], v[8:11]\n\t"
  "ds_read_b64_tr_b16 v[170:171], v251 offset:1280\n\t"
  "s_waitcnt lgkmcnt(6)\n\t"
  "v_mfma_f32_16x16x32_bf16 v[16:19], v[144:147], a[16:19], v[16:19]\n\t"
  "ds_read_b64_tr_b16 v[172:173], v251 offset:17408\n\t"
  "v_lshlrev_b32_e32 v199, 4, v199\n\t"
  "v_mfma_f32_16x16x32_bf16 v[20:23], v[144:147], a[20:23], v[20:23]\n\t"
  "ds_read_b64_tr_b16 v[174:175], v251 offset:17664\n\t"
  "buffer_load_dwordx4 v226, s[16:19], s36 offen offset:2048 lds\n\t"
  "v_mfma_f32_16x16x32_bf16 v[20:23], v[148:151], a[28:31], v[20:23]\n\t"
  "ds_read_b64_tr_b16 v[176:177], v250 offset:1536\n\t"
  "v_bfe_u32 v200, v254, 4, 1\n\t"
  "v_mfma_f32_16x16x32_bf16 v[16:19], v[148:151], a[24:27], v[16:19]\n\t"
  "ds_read_b64_tr_b16 v[178:179], v250 offset:1792\n\t"
  "v_mfma_f32_16x16x32_bf16 v[24:27], v[152:155], a[16:19], v[24:27]\n\t"
  "ds_read_b64_tr_b16 v[180:181], v250 offset:17920\n\t"
  "v_lshl_add_u32 v199, v200, 3, v199\n\t"
  "v_mfma_f32_16x16x32_bf16 v[28:31], v[152:155], a[20:23], v[28:31]\n\t"
  "ds_read_b64_tr_b16 v[182:183], v250 offset:18176\n\t"
  "buffer_load_dwordx4 v227, s[16:19], s36 offen offset:3072 lds\n\t"
  "v_mfma_f32_16x16x32_bf16 v[28:31], v[156:159], a[28:31], v[28:31]\n\t"
  "ds_read_b64_tr_b16 v[184:185], v251 offset:1536\n\t"
  "s_add_u32 s36, s36, 65536\n\t"
  "v_mfma_f32_16x16x32_bf16 v[24:27], v[156:159], a[24:27], v[24:27]\n\t"
  "ds_read_b64_tr_b16 v[186:187], v251 offset:1792\n\t"
  "s_waitcnt lgkmcnt(6)\n\t"
  "v_mfma_f32_16x16x32_bf16 v[32:35], v[160:163], a[16:19], v[32:35]\n\t"
  "ds_read_b64_tr_b16 v[188:189], v251 offset:17920\n\t"
  "s_add_u32 m0, s30, 36864\n\t"
  "v_mfma_f32_16x16x32_bf16 v[36:39], v[160:163], a[20:23], v[36:39]\n\t"
  "ds_read_b64_tr_b16 v[190:191], v251 offset:18176\n\t"
  "buffer_load_dwordx4 v228, s[16:19], s36 offen lds\n\t"
  "v_mfma_f32_16x16x32_bf16 v[36:39], v[164:167], a[28:31], v[36:39]\n\t"
  "ds_read_b64_tr_b16 v[128:129], v250 offset:2048\n\t"
  "v_lshl_add_u32 v199, v196, 9, v199\n\t"
  "v_mfma_f32_16x16x32_bf16 v[32:35], v[164:167], a[24:27], v[32:35]\n\t"
  "ds_read_b64_tr_b16 v[130:131], v250 offset:2304\n\t"
  "v_mfma_f32_16x16x32_bf16 v[40:43], v[168:171], a[16:19], v[40:43]\n\t"
  "ds_read_b64_tr_b16 v[132:133], v250 offset:18432\n\t"
  "v_add_u32_e32 v199, s58, v199\n\t"
  "v_mfma_f32_16x16x32_bf16 v[44:47], v[168:171], a[20:23], v[44:47]\n\t"
  "ds_read_b64_tr_b16 v[134:135], v250 offset:18688\n\t"
  "buffer_load_dwordx4 v229, s[16:19], s36 offen offset:1024 lds\n\t"
  "v_mfma_f32_16x16x32_bf16 v[44:47], v[172:175], a[28:31], v[44:47]\n\t"
  "ds_read_b64_tr_b16 v[136:137], v251 offset:2048\n\t"
  "v_xor_b32_e32 v206, 0, v199\n\t"
  "v_mfma_f32_16x16x32_bf16 v[40:43], v[172:175], a[24:27], v[40:43]\n\t"
  "ds_read_b64_tr_b16 v[138:139], v251 offset:2304\n\t"
  "s_waitcnt lgkmcnt(6)\n\t"
  "v_mfma_f32_16x16x32_bf16 v[48:51], v[176:179], a[16:19], v[48:51]\n\t"
  "ds_read_b64_tr_b16 v[140:141], v251 offset:18432\n\t"
  "v_xor_b32_e32 v207, 32, v199\n\t"
  "v_mfma_f32_16x16x32_bf16 v[52:55], v[176:179], a[20:23], v[52:55]\n\t"
  "ds_read_b64_tr_b16 v[142:143], v251 offset:18688\n\t"
  "buffer_load_dwordx4 v230, s[16:19], s36 offen offset:2048 lds\n\t"
  "v_mfma_f32_16x16x32_bf16 v[52:55], v[180:183], a[28:31], v[52:55]\n\t"
  "ds_read_b64_tr_b16 v[144:145], v250 offset:2560\n\t"
  "v_xor_b32_e32 v208, 64, v199\n\t"
  "v_mfma_f32_16x16x32_bf16 v[48:51], v[180:183], a[24:27], v[48:51]\n\t"
  "ds_read_b64_tr_b16 v[146:147], v250 offset:2816\n\t"
  "v_mfma_f32_16x16x32_bf16 v[56:59], v[184:187], a[16:19], v[56:59]\n\t"
  "ds_read_b64_tr_b16 v[148:149], v250 offset:18944\n\t"
  "v_xor_b32_e32 v209, 96, v199\n\t"
  "v_mfma_f32_16x16x32_bf16 v[60:63], v[184:187], a[20:23], v[60:63]\n\t"
  "ds_read_b64_tr_b16 v[150:151], v250 offset:19200\n\t"
  "buffer_load_dwordx4 v231, s[16:19], s36 offen offset:3072 lds\n\t"
  "v_mfma_f32_16x16x32_bf16 v[60:63], v[188:191], a[28:31], v[60:63]\n\t"
  "ds_read_b64_tr_b16 v[152:153], v251 offset:2560\n\t"
  "s_add_u32 s36, s36, 458752\n\t"
  "v_mfma_f32_16x16x32_bf16 v[56:59], v[188:191], a[24:27], v[56:59]\n\t"
  "ds_read_b64_tr_b16 v[154:155], v251 offset:2816\n\t"
  "s_waitcnt lgkmcnt(6)\n\t"
  "v_mfma_f32_16x16x32_bf16 v[64:67], v[128:131], a[16:19], v[64:67]\n\t"
  "ds_read_b64_tr_b16 v[156:157], v251 offset:18944\n\t"
  "v_xor_b32_e32 v210, 128, v199\n\t"
  "v_mfma_f32_16x16x32_bf16 v[68:71], v[128:131], a[20:23], v[68:71]\n\t"
  "ds_read_b64_tr_b16 v[158:159], v251 offset:19200\n\t"
  "v_xor_b32_e32 v211, 160, v199\n\t"
  "v_mfma_f32_16x16x32_bf16 v[68:71], v[132:135], a[28:31], v[68:71]\n\t"
  "ds_read_b64_tr_b16 v[160:161], v250 offset:3072\n\t"
  "v_xor_b32_e32 v212, 192, v199\n\t"
  "buffer_load_dwordx4 a[120:123], v244, s[44:47], 0 offen\n\t"
  "v_mfma_f32_16x16x32_bf16 v[64:67], v[132:135], a[24:27], v[64:67]\n\t"
  "ds_read_b64_tr_b16 v[162:163], v250 offset:3328\n\t"
  "v_mfma_f32_16x16x32_bf16 v[72:75], v[136:139], a[16:19], v[72:75]\n\t"
  "ds_read_b64_tr_b16 v[164:165], v250 offset:19456\n\t"
  "v_xor_b32_e32 v213, 224, v199\n\t"
  "v_mfma_f32_16x16x32_bf16 v[76:79], v[136:139], a[20:23], v[76:79]\n\t"
  "ds_read_b64_tr_b16 v[166:167], v250 offset:19712\n\t"
  "v_xor_b32_e32 v199, v197, v196\n\t"
  "v_mfma_f32_16x16x32_bf16 v[76:79], v[140:143], a[28:31], v[76:79]\n\t"
  "ds_read_b64_tr_b16 v[168:169], v251 offset:3072\n\t"
  "v_lshlrev_b32_e32 v199, 4, v199\n\t"
  "v_mfma_f32_16x16x32_bf16 v[72:75], v[140:143], a[24:27], v[72:75]\n\t"
  "ds_read_b64_tr_b16 v[170:171], v251 offset:3328\n\t"
  "s_waitcnt lgkmcnt(6)\n\t"
  "v_mfma_f32_16x16x32_bf16 v[80:83], v[144:147], a[16:19], v[80:83]\n\t"
  "ds_read_b64_tr_b16 v[172:173], v251 offset:19456\n\t"
  "v_lshl_add_u32 v199, v196, 9, v199\n\t"
  "buffer_load_ushort v241, v243, s[40:43], 0 offen offset:512\n\t"
  "v_add_u32_e32 v244, 688128, v244\n\t"
  "v_mfma_f32_16x16x32_bf16 v[84:87], v[144:147], a[20:23], v[84:87]\n\t"
  "ds_read_b64_tr_b16 v[174:175], v251 offset:19712\n\t"
  "v_add_u32_e32 v214, s58, v199\n\t"
  "v_mfma_f32_16x16x32_bf16 v[84:87], v[148:151], a[28:31], v[84:87]\n\t"
  "ds_read_b64_tr_b16 v[176:177], v250 offset:3584\n\t"
  "v_mfma_f32_16x16x32_bf16 v[80:83], v[148:151], a[24:27], v[80:83]\n\t"
  "ds_read_b64_tr_b16 v[178:179], v250 offset:3840\n\t"
  "v_mfma_f32_16x16x32_bf16 v[88:91], v[152:155], a[16:19], v[88:91]\n\t"
  "ds_read_b64_tr_b16 v[180:181], v250 offset:19968\n\t"
  "v_mfma_f32_16x16x32_bf16 v[92:95], v[152:155], a[20:23], v[92:95]\n\t"
  "ds_read_b64_tr_b16 v[182:183], v250 offset:20224\n\t"
  "v_mfma_f32_16x16x32_bf16 v[92:95], v[156:159], a[28:31], v[92:95]\n\t"
  "ds_read_b64_tr_b16 v[184:185], v251 offset:3584\n\t"
  "v_mfma_f32_16x16x32_bf16 v[88:91], v[156:159], a[24:27], v[88:91]\n\t"
  "ds_read_b64_tr_b16 v[186:187], v251 offset:3840\n\t"
  "s_waitcnt lgkmcnt(6)\n\t"
  "v_mfma_f32_16x16x32_bf16 v[96:99], v[160:163], a[16:19], v[96:99]\n\t"
  "ds_read_b64_tr_b16 v[188:189], v251 offset:19968\n\t"
  "s_waitcnt vmcnt(14)\n\t"
  "s_barrier\n\t"
  "buffer_load_dwordx4 a[124:127], v244, s[44:47], 0 offen\n\t"
  "v_mfma_f32_16x16x32_bf16 v[100:103], v[160:163], a[20:23], v[100:103]\n\t"
  "ds_read_b64_tr_b16 v[190:191], v251 offset:20224\n\t"
  "v_mfma_f32_16x16x32_bf16 v[100:103], v[164:167], a[28:31], v[100:103]\n\t"
  "ds_read_b64_tr_b16 v[128:129], v248\n\t"
  "v_mfma_f32_16x16x32_bf16 v[96:99], v[164:167], a[24:27], v[96:99]\n\t"
  "ds_read_b64_tr_b16 v[130:131], v248 offset:256\n\t"
  "v_mfma_f32_16x16x32_bf16 v[104:107], v[168:171], a[16:19], v[104:107]\n\t"
  "ds_read_b64_tr_b16 v[132:133], v248 offset:16384\n\t"
  "v_mfma_f32_16x16x32_bf16 v[108:111], v[168:171], a[20:23], v[108:111]\n\t"
  "ds_read_b64_tr_b16 v[134:135], v248 offset:16640\n\t"
  "buffer_load_ushort v242, v243, s[40:43], 0 offen offset:768\n\t"
  "v_add_u32_e32 v244, 688128, v244\n\t"
  "v_mfma_f32_16x16x32_bf16 v[108:111], v[172:175], a[28:31], v[108:111]\n\t"
  "ds_read_b64_tr_b16 v[136:137], v249\n\t"
  "v_mfma_f32_16x16x32_bf16 v[104:107], v[172:175], a[24:27], v[104:107]\n\t"
  "ds_read_b64_tr_b16 v[138:139], v249 offset:256\n\t"
  "s_waitcnt lgkmcnt(6)\n\t"
  "v_mfma_f32_16x16x32_bf16 v[112:115], v[176:179], a[16:19], v[112:115]\n\t"
  "ds_read_b64_tr_b16 v[140:141], v249 offset:16384\n\t"
  "v_mfma_f32_16x16x32_bf16 v[116:119], v[176:179], a[20:23], v[116:119]\n\t"
  "ds_read_b64_tr_b16 v[142:143], v249 offset:16640\n\t"
  "v_mfma_f32_16x16x32_bf16 v[116:119], v[180:183], a[28:31], v[116:119]\n\t"
  "ds_read_b64_tr_b16 v[144:145], v248 offset:512\n\t"
  "v_mfma_f32_16x16x32_bf16 v[112:115], v[180:183], a[24:27], v[112:115]\n\t"
  "ds_read_b64_tr_b16 v[146:147], v248 offset:768\n\t"
  "v_mfma_f32_16x16x32_bf16 v[120:123], v[184:187], a[16:19], v[120:123]\n\t"
  "ds_read_b64_tr_b16 v[148:149], v248 offset:16896\n\t"
  "v_mfma_f32_16x16x32_bf16 v[124:127], v[184:187], a[20:23], v[124:127]\n\t"
  "ds_read_b64_tr_b16 v[150:151], v248 offset:17152\n\t"
  "v_mfma_f32_16x16x32_bf16 v[124:127], v[188:191], a[28:31], v[124:127]\n\t"
  "ds_read_b64_tr_b16 v[152:153], v249 offset:512\n\t"
  "v_mfma_f32_16x16x32_bf16 v[120:123], v[188:191], a[24:27], v[120:123]\n\t"
  "ds_read_b64_tr_b16 v[154:155], v249 offset:768\n\t"
  "s_waitcnt lgkmcnt(6)\n\t"
  "v_mfma_f32_16x16x32_bf16 v[0:3], v[128:131], a[32:35], v[0:3]\n\t"
  "ds_read_b64_tr_b16 v[156:157], v249 offset:16896\n\t"
  "s_add_u32 m0, s30, 131072\n\t"
  "v_mfma_f32_16x16x32_bf16 v[4:7], v[128:131], a[36:39], v[4:7]\n\t"
  "ds_read_b64_tr_b16 v[158:159], v249 offset:17152\n\t"
  "buffer_load_dwordx4 v224, s[16:19], s36 offen lds\n\t"
  "v_mfma_f32_16x16x32_bf16 v[4:7], v[132:135], a[44:47], v[4:7]\n\t"
  "ds_read_b64_tr_b16 v[160:161], v248 offset:1024\n\t"
  "v_mfma_f32_16x16x32_bf16 v[0:3], v[132:135], a[40:43], v[0:3]\n\t"
  "ds_read_b64_tr_b16 v[162:163], v248 offset:1280\n\t"
  "v_mfma_f32_16x16x32_bf16 v[8:11], v[136:139], a[32:35], v[8:11]\n\t"
  "ds_read_b64_tr_b16 v[164:165], v248 offset:17408\n\t"
  "v_mfma_f32_16x16x32_bf16 v[12:15], v[136:139], a[36:39], v[12:15]\n\t"
  "ds_read_b64_tr_b16 v[166:167], v248 offset:17664\n\t"
  "buffer_load_dwordx4 v225, s[16:19], s36 offen offset:1024 lds\n\t"
  "v_mfma_f32_16x16x32_bf16 v[12:15], v[140:143], a[44:47], v[12:15]\n\t"
  "ds_read_b64_tr_b16 v[168:169], v249 offset:1024\n\t"
  "v_mfma_f32_16x16x32_bf16 v[8:11], v[140:143], a[40:43], v[8:11]\n\t"
  "ds_read_b64_tr_b16 v[170:171], v249 offset:1280\n\t"
  "s_waitcnt lgkmcnt(6)\n\t"
  "v_mfma_f32_16x16x32_bf16 v[16:19], v[144:147], a[32:35], v[16:19]\n\t"
  "ds_read_b64_tr_b16 v[172:173], v249 offset:17408\n\t"
  "v_mfma_f32_16x16x32_bf16 v[20:23], v[144:147], a[36:39], v[20:23]\n\t"
  "ds_read_b64_tr_b16 v[174:175], v249 offset:17664\n\t"
  "buffer_load_dwordx4 v226, s[16:19], s36 offen offset:2048 lds\n\t"
  "v_mfma_f32_16x16x32_bf16 v[20:23], v[148:151], a[44:47], v[20:23]\n\t"
  "ds_read_b64_tr_b16 v[176:177], v248 offset:1536\n\t"
  "v_mfma_f32_16x16x32_bf16 v[16:19], v[148:151], a[40:43], v[16:19]\n\t"
  "ds_read_b64_tr_b16 v[178:179], v248 offset:1792\n\t"
  "v_mfma_f32_16x16x32_bf16 v[24:27], v[152:155], a[32:35], v[24:27]\n\t"
  "ds_read_b64_tr_b16 v[180:181], v248 offset:17920\n\t"
  "v_mfma_f32_16x16x32_bf16 v[28:31], v[152:155], a[36:39], v[28:31]\n\t"
  "ds_read_b64_tr_b16 v[182:183], v248 offset:18176\n\t"
  "buffer_load_dwordx4 v227, s[16:19], s36 offen offset:3072 lds\n\t"
  "v_mfma_f32_16x16x32_bf16 v[28:31], v[156:159], a[44:47], v[28:31]\n\t"
  "ds_read_b64_tr_b16 v[184:185], v249 offset:1536\n\t"
  "s_add_u32 s36, s36, 65536\n\t"
  "s_waitcnt vmcnt(4)\n\t"
  "v_mfma_f32_16x16x32_bf16 v[24:27], v[156:159], a[40:43], v[24:27]\n\t"
  "ds_read_b64_tr_b16 v[186:187], v249 offset:1792\n\t"
  "s_waitcnt lgkmcnt(6)\n\t"
  "v_mfma_f32_16x16x32_bf16 v[32:35], v[160:163], a[32:35], v[32:35]\n\t"
  "ds_read_b64_tr_b16 v[188:189], v249 offset:17920\n\t"
  "s_add_u32 m0, s30, 135168\n\t"
  "v_lshlrev_b32_e32 v239, 16, v239\n\t"
  "v_mfma_f32_16x16x32_bf16 v[36:39], v[160:163], a[36:39], v[36:39]\n\t"
  "ds_read_b64_tr_b16 v[190:191], v249 offset:18176\n\t"
  "buffer_load_dwordx4 v228, s[16:19], s36 offen lds\n\t"
  "v_mfma_f32_16x16x32_bf16 v[36:39], v[164:167], a[44:47], v[36:39]\n\t"
  "ds_read_b64_tr_b16 v[128:129], v248 offset:2048\n\t"
  "v_mul_f32_e32 v192, 0xbfb8aa3b, v239\n\t"
  "v_mfma_f32_16x16x32_bf16 v[32:35], v[164:167], a[40:43], v[32:35]\n\t"
  "ds_read_b64_tr_b16 v[130:131], v248 offset:2304\n\t"
  "v_mfma_f32_16x16x32_bf16 v[40:43], v[168:171], a[32:35], v[40:43]\n\t"
  "ds_read_b64_tr_b16 v[132:133], v248 offset:18432\n\t"
  "v_exp_f32_e32 v192, v192\n\t"
  "v_mfma_f32_16x16x32_bf16 v[44:47], v[168:171], a[36:39], v[44:47]\n\t"
  "ds_read_b64_tr_b16 v[134:135], v248 offset:18688\n\t"
  "buffer_load_dwordx4 v229, s[16:19], s36 offen offset:1024 lds\n\t"
  "v_mfma_f32_16x16x32_bf16 v[44:47], v[172:175], a[44:47], v[44:47]\n\t"
  "ds_read_b64_tr_b16 v[136:137], v249 offset:2048\n\t"
  "v_lshlrev_b32_e32 v240, 16, v240\n\t"
  "v_mfma_f32_16x16x32_bf16 v[40:43], v[172:175], a[40:43], v[40:43]\n\t"
  "ds_read_b64_tr_b16 v[138:139], v249 offset:2304\n\t"
  "s_waitcnt lgkmcnt(6)\n\t"
  "v_mfma_f32_16x16x32_bf16 v[48:51], v[176:179], a[32:35], v[48:51]\n\t"
  "ds_read_b64_tr_b16 v[140:141], v249 offset:18432\n\t"
  "v_mul_f32_e32 v193, 0xbfb8aa3b, v240\n\t"
  "v_mfma_f32_16x16x32_bf16 v[52:55], v[176:179], a[36:39], v[52:55]\n\t"
  "ds_read_b64_tr_b16 v[142:143], v249 offset:18688\n\t"
  "buffer_load_dwordx4 v230, s[16:19], s36 offen offset:2048 lds\n\t"
  "v_mfma_f32_16x16x32_bf16 v[52:55], v[180:183], a[44:47], v[52:55]\n\t"
  "ds_read_b64_tr_b16 v[144:145], v248 offset:2560\n\t"
  "v_exp_f32_e32 v193, v193\n\t"
  "v_mfma_f32_16x16x32_bf16 v[48:51], v[180:183], a[40:43], v[48:51]\n\t"
  "ds_read_b64_tr_b16 v[146:147], v248 offset:2816\n\t"
  "v_mfma_f32_16x16x32_bf16 v[56:59], v[184:187], a[32:35], v[56:59]\n\t"
  "ds_read_b64_tr_b16 v[148:149], v248 offset:18944\n\t"
  "v_lshlrev_b32_e32 v241, 16, v241\n\t"
  "v_mfma_f32_16x16x32_bf16 v[60:63], v[184:187], a[36:39], v[60:63]\n\t"
  "ds_read_b64_tr_b16 v[150:151], v248 offset:19200\n\t"
  "buffer_load_dwordx4 v231, s[16:19], s36 offen offset:3072 lds\n\t"
  "v_mfma_f32_16x16x32_bf16 v[60:63], v[188:191], a[44:47], v[60:63]\n\t"
  "ds_read_b64_tr_b16 v[152:153], v249 offset:2560\n\t"
  "s_add_u32 s36, s36, 458752\n\t"
  "v_mul_f32_e32 v194, 0xbfb8aa3b, v241\n\t"
  "v_mfma_f32_16x16x32_bf16 v[56:59], v[188:191], a[40:43], v[56:59]\n\t"
  "ds_read_b64_tr_b16 v[154:155], v249 offset:2816\n\t"
  "s_waitcnt lgkmcnt(6)\n\t"
  "v_mfma_f32_16x16x32_bf16 v[64:67], v[128:131], a[32:35], v[64:67]\n\t"
  "ds_read_b64_tr_b16 v[156:157], v249 offset:18944\n\t"
  "s_add_u32 m0, s30, 98304\n\t"
  "v_exp_f32_e32 v194, v194\n\t"
  "v_mfma_f32_16x16x32_bf16 v[68:71], v[128:131], a[36:39], v[68:71]\n\t"
  "ds_read_b64_tr_b16 v[158:159], v249 offset:19200\n\t"
  "buffer_load_dwordx4 v224, s[16:19], s36 offen lds\n\t"
  "v_mfma_f32_16x16x32_bf16 v[68:71], v[132:135], a[44:47], v[68:71]\n\t"
  "ds_read_b64_tr_b16 v[160:161], v248 offset:3072\n\t"
  "v_lshlrev_b32_e32 v242, 16, v242\n\t"
  "v_mfma_f32_16x16x32_bf16 v[64:67], v[132:135], a[40:43], v[64:67]\n\t"
  "ds_read_b64_tr_b16 v[162:163], v248 offset:3328\n\t"
  "v_mfma_f32_16x16x32_bf16 v[72:75], v[136:139], a[32:35], v[72:75]\n\t"
  "ds_read_b64_tr_b16 v[164:165], v248 offset:19456\n\t"
  "v_mul_f32_e32 v195, 0xbfb8aa3b, v242\n\t"
  "v_mfma_f32_16x16x32_bf16 v[76:79], v[136:139], a[36:39], v[76:79]\n\t"
  "ds_read_b64_tr_b16 v[166:167], v248 offset:19712\n\t"
  "buffer_load_dwordx4 v225, s[16:19], s36 offen offset:1024 lds\n\t"
  "v_mfma_f32_16x16x32_bf16 v[76:79], v[140:143], a[44:47], v[76:79]\n\t"
  "ds_read_b64_tr_b16 v[168:169], v249 offset:3072\n\t"
  "v_exp_f32_e32 v195, v195\n\t"
  "v_mfma_f32_16x16x32_bf16 v[72:75], v[140:143], a[40:43], v[72:75]\n\t"
  "ds_read_b64_tr_b16 v[170:171], v249 offset:3328\n\t"
  "s_waitcnt lgkmcnt(6)\n\t"
  "v_mfma_f32_16x16x32_bf16 v[80:83], v[144:147], a[32:35], v[80:83]\n\t"
  "ds_read_b64_tr_b16 v[172:173], v249 offset:19456\n\t"
  "v_add_f32_e32 v192, 1.0, v192\n\t"
  "v_mfma_f32_16x16x32_bf16 v[84:87], v[144:147], a[36:39], v[84:87]\n\t"
  "ds_read_b64_tr_b16 v[174:175], v249 offset:19712\n\t"
  "buffer_load_dwordx4 v226, s[16:19], s36 offen offset:2048 lds\n\t"
  "v_mfma_f32_16x16x32_bf16 v[84:87], v[148:151], a[44:47], v[84:87]\n\t"
  "ds_read_b64_tr_b16 v[176:177], v248 offset:3584\n\t"
  "v_add_f32_e32 v193, 1.0, v193\n\t"
  "v_mfma_f32_16x16x32_bf16 v[80:83], v[148:151], a[40:43], v[80:83]\n\t"
  "ds_read_b64_tr_b16 v[178:179], v248 offset:3840\n\t"
  "v_mfma_f32_16x16x32_bf16 v[88:91], v[152:155], a[32:35], v[88:91]\n\t"
  "ds_read_b64_tr_b16 v[180:181], v248 offset:19968\n\t"
  "v_add_f32_e32 v194, 1.0, v194\n\t"
  "v_mfma_f32_16x16x32_bf16 v[92:95], v[152:155], a[36:39], v[92:95]\n\t"
  "ds_read_b64_tr_b16 v[182:183], v248 offset:20224\n\t"
  "buffer_load_dwordx4 v227, s[16:19], s36 offen offset:3072 lds\n\t"
  "v_mfma_f32_16x16x32_bf16 v[92:95], v[156:159], a[44:47], v[92:95]\n\t"
  "ds_read_b64_tr_b16 v[184:185], v249 offset:3584\n\t"
  "s_add_u32 s36, s36, 65536\n\t"
  "v_add_f32_e32 v195, 1.0, v195\n\t"
  "v_mfma_f32_16x16x32_bf16 v[88:91], v[156:159], a[40:43], v[88:91]\n\t"
  "ds_read_b64_tr_b16 v[186:187], v249 offset:3840\n\t"
  "s_waitcnt lgkmcnt(6)\n\t"
  "v_mfma_f32_16x16x32_bf16 v[96:99], v[160:163], a[32:35], v[96:99]\n\t"
  "ds_read_b64_tr_b16 v[188:189], v249 offset:19968\n\t"
  "s_add_u32 m0, s30, 102400\n\t"
  "s_barrier\n\t"
  "v_mfma_f32_16x16x32_bf16 v[100:103], v[160:163], a[36:39], v[100:103]\n\t"
  "ds_read_b64_tr_b16 v[190:191], v249 offset:20224\n\t"
  "buffer_load_dwordx4 v228, s[16:19], s36 offen lds\n\t"
  "v_mfma_f32_16x16x32_bf16 v[100:103], v[164:167], a[44:47], v[100:103]\n\t"
  "ds_read_b64_tr_b16 v[128:129], v248 offset:32768\n\t"
  "v_rcp_f32_e32 v192, v192\n\t"
  "v_mfma_f32_16x16x32_bf16 v[96:99], v[164:167], a[40:43], v[96:99]\n\t"
  "ds_read_b64_tr_b16 v[130:131], v248 offset:33024\n\t"
  "v_mfma_f32_16x16x32_bf16 v[104:107], v[168:171], a[32:35], v[104:107]\n\t"
  "ds_read_b64_tr_b16 v[132:133], v248 offset:49152\n\t"
  "v_rcp_f32_e32 v193, v193\n\t"
  "v_mfma_f32_16x16x32_bf16 v[108:111], v[168:171], a[36:39], v[108:111]\n\t"
  "ds_read_b64_tr_b16 v[134:135], v248 offset:49408\n\t"
  "buffer_load_dwordx4 v229, s[16:19], s36 offen offset:1024 lds\n\t"
  "v_mfma_f32_16x16x32_bf16 v[108:111], v[172:175], a[44:47], v[108:111]\n\t"
  "ds_read_b64_tr_b16 v[136:137], v249 offset:32768\n\t"
  "v_rcp_f32_e32 v194, v194\n\t"
  "v_mfma_f32_16x16x32_bf16 v[104:107], v[172:175], a[40:43], v[104:107]\n\t"
  "ds_read_b64_tr_b16 v[138:139], v249 offset:33024\n\t"
  "s_waitcnt lgkmcnt(6)\n\t"
  "v_mfma_f32_16x16x32_bf16 v[112:115], v[176:179], a[32:35], v[112:115]\n\t"
  "ds_read_b64_tr_b16 v[140:141], v249 offset:49152\n\t"
  "v_rcp_f32_e32 v195, v195\n\t"
  "v_mfma_f32_16x16x32_bf16 v[116:119], v[176:179], a[36:39], v[116:119]\n\t"
  "ds_read_b64_tr_b16 v[142:143], v249 offset:49408\n\t"
  "buffer_load_dwordx4 v230, s[16:19], s36 offen offset:2048 lds\n\t"
  "v_mfma_f32_16x16x32_bf16 v[116:119], v[180:183], a[44:47], v[116:119]\n\t"
  "ds_read_b64_tr_b16 v[144:145], v248 offset:33280\n\t"
  "v_mul_f32_e32 v192, v192, v239\n\t"
  "v_mfma_f32_16x16x32_bf16 v[112:115], v[180:183], a[40:43], v[112:115]\n\t"
  "ds_read_b64_tr_b16 v[146:147], v248 offset:33536\n\t"
  "v_mfma_f32_16x16x32_bf16 v[120:123], v[184:187], a[32:35], v[120:123]\n\t"
  "ds_read_b64_tr_b16 v[148:149], v248 offset:49664\n\t"
  "v_mul_f32_e32 v193, v193, v240\n\t"
  "v_mfma_f32_16x16x32_bf16 v[124:127], v[184:187], a[36:39], v[124:127]\n\t"
  "ds_read_b64_tr_b16 v[150:151], v248 offset:49920\n\t"
  "buffer_load_dwordx4 v231, s[16:19], s36 offen offset:3072 lds\n\t"
  "v_mfma_f32_16x16x32_bf16 v[124:127], v[188:191], a[44:47], v[124:127]\n\t"
  "ds_read_b64_tr_b16 v[152:153], v249 offset:33280\n\t"
  "s_add_u32 s36, s36, 458752\n\t"
  "v_mul_f32_e32 v194, v194, v241\n\t"
  "v_mfma_f32_16x16x32_bf16 v[120:123], v[188:191], a[40:43], v[120:123]\n\t"
  "ds_read_b64_tr_b16 v[154:155], v249 offset:33536\n\t"
  "s_waitcnt lgkmcnt(6)\n\t"
  "v_mfma_f32_16x16x32_bf16 v[0:3], v[128:131], a[48:51], v[0:3]\n\t"
  "ds_read_b64_tr_b16 v[156:157], v249 offset:49664\n\t"
  "s_add_u32 m0, s30, 65536\n\t"
  "v_mul_f32_e32 v195, v195, v242\n\t"
  "v_mfma_f32_16x16x32_bf16 v[4:7], v[128:131], a[52:55], v[4:7]\n\t"
  "ds_read_b64_tr_b16 v[158:159], v249 offset:49920\n\t"
  "buffer_load_dwordx4 v224, s[16:19], s36 offen lds\n\t"
  "v_mfma_f32_16x16x32_bf16 v[4:7], v[132:135], a[60:63], v[4:7]\n\t"
  "ds_read_b64_tr_b16 v[160:161], v248 offset:33792\n\t"
  "v_cvt_pk_bf16_f32 v192, v192, v192\n\t"
  "v_mfma_f32_16x16x32_bf16 v[0:3], v[132:135], a[56:59], v[0:3]\n\t"
  "ds_read_b64_tr_b16 v[162:163], v248 offset:34048\n\t"
  "v_mfma_f32_16x16x32_bf16 v[8:11], v[136:139], a[48:51], v[8:11]\n\t"
  "ds_read_b64_tr_b16 v[164:165], v248 offset:50176\n\t"
  "v_cvt_pk_bf16_f32 v193, v193, v193\n\t"
  "v_mfma_f32_16x16x32_bf16 v[12:15], v[136:139], a[52:55], v[12:15]\n\t"
  "ds_read_b64_tr_b16 v[166:167], v248 offset:50432\n\t"
  "buffer_load_dwordx4 v225, s[16:19], s36 offen offset:1024 lds\n\t"
  "v_mfma_f32_16x16x32_bf16 v[12:15], v[140:143], a[60:63], v[12:15]\n\t"
  "ds_read_b64_tr_b16 v[168:169], v249 offset:33792\n\t"
  "v_cvt_pk_bf16_f32 v194, v194, v194\n\t"
  "v_mfma_f32_16x16x32_bf16 v[8:11], v[140:143], a[56:59], v[8:11]\n\t"
  "ds_read_b64_tr_b16 v[170:171], v249 offset:34048\n\t"
  "s_waitcnt lgkmcnt(6)\n\t"
  "v_mfma_f32_16x16x32_bf16 v[16:19], v[144:147], a[48:51], v[16:19]\n\t"
  "ds_read_b64_tr_b16 v[172:173], v249 offset:50176\n\t"
  "v_cvt_pk_bf16_f32 v195, v195, v195\n\t"
  "v_mfma_f32_16x16x32_bf16 v[20:23], v[144:147], a[52:55], v[20:23]\n\t"
  "ds_read_b64_tr_b16 v[174:175], v249 offset:50432\n\t"
  "buffer_load_dwordx4 v226, s[16:19], s36 offen offset:2048 lds\n\t"
  "v_mfma_f32_16x16x32_bf16 v[20:23], v[148:151], a[60:63], v[20:23]\n\t"
  "ds_read_b64_tr_b16 v[176:177], v248 offset:34304\n\t"
  "v_and_b32_e32 v192, s96, v192\n\t"
  "v_mfma_f32_16x16x32_bf16 v[16:19], v[148:151], a[56:59], v[16:19]\n\t"
  "ds_read_b64_tr_b16 v[178:179], v248 offset:34560\n\t"
  "v_mfma_f32_16x16x32_bf16 v[24:27], v[152:155], a[48:51], v[24:27]\n\t"
  "ds_read_b64_tr_b16 v[180:181], v248 offset:50688\n\t"
  "v_and_b32_e32 v193, s96, v193\n\t"
  "v_mfma_f32_16x16x32_bf16 v[28:31], v[152:155], a[52:55], v[28:31]\n\t"
  "ds_read_b64_tr_b16 v[182:183], v248 offset:50944\n\t"
  "buffer_load_dwordx4 v227, s[16:19], s36 offen offset:3072 lds\n\t"
  "v_mfma_f32_16x16x32_bf16 v[28:31], v[156:159], a[60:63], v[28:31]\n\t"
  "ds_read_b64_tr_b16 v[184:185], v249 offset:34304\n\t"
  "s_add_u32 s36, s36, 65536\n\t"
  "v_and_b32_e32 v194, s96, v194\n\t"
  "v_mfma_f32_16x16x32_bf16 v[24:27], v[156:159], a[56:59], v[24:27]\n\t"
  "ds_read_b64_tr_b16 v[186:187], v249 offset:34560\n\t"
  "s_waitcnt lgkmcnt(6)\n\t"
  "v_mfma_f32_16x16x32_bf16 v[32:35], v[160:163], a[48:51], v[32:35]\n\t"
  "ds_read_b64_tr_b16 v[188:189], v249 offset:50688\n\t"
  "s_add_u32 m0, s30, 69632\n\t"
  "v_and_b32_e32 v195, s96, v195\n\t"
  "v_mfma_f32_16x16x32_bf16 v[36:39], v[160:163], a[52:55], v[36:39]\n\t"
  "ds_read_b64_tr_b16 v[190:191], v249 offset:50944\n\t"
  "buffer_load_dwordx4 v228, s[16:19], s36 offen lds\n\t"
  "v_mfma_f32_16x16x32_bf16 v[36:39], v[164:167], a[60:63], v[36:39]\n\t"
  "ds_read_b64_tr_b16 v[128:129], v248 offset:34816\n\t"
  "v_accvgpr_read_b32 v197, a112\n\t"
  "v_mfma_f32_16x16x32_bf16 v[32:35], v[164:167], a[56:59], v[32:35]\n\t"
  "ds_read_b64_tr_b16 v[130:131], v248 offset:35072\n\t"
  "v_mfma_f32_16x16x32_bf16 v[40:43], v[168:171], a[48:51], v[40:43]\n\t"
  "ds_read_b64_tr_b16 v[132:133], v248 offset:51200\n\t"
  "v_lshlrev_b32_e32 v196, 16, v197\n\t"
  "v_mfma_f32_16x16x32_bf16 v[44:47], v[168:171], a[52:55], v[44:47]\n\t"
  "ds_read_b64_tr_b16 v[134:135], v248 offset:51456\n\t"
  "buffer_load_dwordx4 v229, s[16:19], s36 offen offset:1024 lds\n\t"
  "v_mfma_f32_16x16x32_bf16 v[44:47], v[172:175], a[60:63], v[44:47]\n\t"
  "ds_read_b64_tr_b16 v[136:137], v249 offset:34816\n\t"
  "v_and_b32_e32 v197, s96, v197\n\t"
  "v_mfma_f32_16x16x32_bf16 v[40:43], v[172:175], a[56:59], v[40:43]\n\t"
  "ds_read_b64_tr_b16 v[138:139], v249 offset:35072\n\t"
  "s_waitcnt lgkmcnt(6)\n\t"
  "v_mfma_f32_16x16x32_bf16 v[48:51], v[176:179], a[48:51], v[48:51]\n\t"
  "ds_read_b64_tr_b16 v[140:141], v249 offset:51200\n\t"
  "v_mul_f32_e32 v216, v192, v196\n\t"
  "v_mfma_f32_16x16x32_bf16 v[52:55], v[176:179], a[52:55], v[52:55]\n\t"
  "ds_read_b64_tr_b16 v[142:143], v249 offset:51456\n\t"
  "buffer_load_dwordx4 v230, s[16:19], s36 offen offset:2048 lds\n\t"
  "v_mfma_f32_16x16x32_bf16 v[52:55], v[180:183], a[60:63], v[52:55]\n\t"
  "ds_read_b64_tr_b16 v[144:145], v248 offset:35328\n\t"
  "v_mul_f32_e32 v217, v192, v197\n\t"
  "v_mfma_f32_16x16x32_bf16 v[48:51], v[180:183], a[56:59], v[48:51]\n\t"
  "ds_read_b64_tr_b16 v[146:147], v248 offset:35584\n\t"
  "v_mfma_f32_16x16x32_bf16 v[56:59], v[184:187], a[48:51], v[56:59]\n\t"
  "ds_read_b64_tr_b16 v[148:149], v248 offset:51712\n\t"
  "v_accvgpr_read_b32 v197, a113\n\t"
  "v_mfma_f32_16x16x32_bf16 v[60:63], v[184:187], a[52:55], v[60:63]\n\t"
  "ds_read_b64_tr_b16 v[150:151], v248 offset:51968\n\t"
  "buffer_load_dwordx4 v231, s[16:19], s36 offen offset:3072 lds\n\t"
  "v_mfma_f32_16x16x32_bf16 v[60:63], v[188:191], a[60:63], v[60:63]\n\t"
  "ds_read_b64_tr_b16 v[152:153], v249 offset:35328\n\t"
  "s_add_u32 s36, s36, 458752\n\t"
  "v_lshlrev_b32_e32 v196, 16, v197\n\t"
  "v_mfma_f32_16x16x32_bf16 v[56:59], v[188:191], a[56:59], v[56:59]\n\t"
  "ds_read_b64_tr_b16 v[154:155], v249 offset:35584\n\t"
  "s_waitcnt lgkmcnt(6)\n\t"
  "v_mfma_f32_16x16x32_bf16 v[64:67], v[128:131], a[48:51], v[64:67]\n\t"
  "ds_read_b64_tr_b16 v[156:157], v249 offset:51712\n\t"
  "v_and_b32_e32 v197, s96, v197\n\t"
  "v_mfma_f32_16x16x32_bf16 v[68:71], v[128:131], a[52:55], v[68:71]\n\t"
  "ds_read_b64_tr_b16 v[158:159], v249 offset:51968\n\t"
  "v_mul_f32_e32 v218, v192, v196\n\t"
  "v_mfma_f32_16x16x32_bf16 v[68:71], v[132:135], a[60:63], v[68:71]\n\t"
  "ds_read_b64_tr_b16 v[160:161], v248 offset:35840\n\t"
  "v_mul_f32_e32 v219, v192, v197\n\t"
  "v_mfma_f32_16x16x32_bf16 v[64:67], v[132:135], a[56:59], v[64:67]\n\t"
  "ds_read_b64_tr_b16 v[162:163], v248 offset:36096\n\t"
  "v_mfma_f32_16x16x32_bf16 v[72:75], v[136:139], a[48:51], v[72:75]\n\t"
  "ds_read_b64_tr_b16 v[164:165], v248 offset:52224\n\t"
  "v_accvgpr_read_b32 v197, a114\n\t"
  "v_mfma_f32_16x16x32_bf16 v[76:79], v[136:139], a[52:55], v[76:79]\n\t"
  "ds_read_b64_tr_b16 v[166:167], v248 offset:52480\n\t"
  "v_lshlrev_b32_e32 v196, 16, v197\n\t"
  "v_mfma_f32_16x16x32_bf16 v[76:79], v[140:143], a[60:63], v[76:79]\n\t"
  "ds_read_b64_tr_b16 v[168:169], v249 offset:35840\n\t"
  "v_and_b32_e32 v197, s96, v197\n\t"
  "v_mfma_f32_16x16x32_bf16 v[72:75], v[140:143], a[56:59], v[72:75]\n\t"
  "ds_read_b64_tr_b16 v[170:171], v249 offset:36096\n\t"
  "s_waitcnt lgkmcnt(6)\n\t"
  "v_mfma_f32_16x16x32_bf16 v[80:83], v[144:147], a[48:51], v[80:83]\n\t"
  "ds_read_b64_tr_b16 v[172:173], v249 offset:52224\n\t"
  "v_mul_f32_e32 v220, v192, v196\n\t"
  "v_mfma_f32_16x16x32_bf16 v[84:87], v[144:147], a[52:55], v[84:87]\n\t"
  "ds_read_b64_tr_b16 v[174:175], v249 offset:52480\n\t"
  "v_mul_f32_e32 v221, v192, v197\n\t"
  "v_mfma_f32_16x16x32_bf16 v[84:87], v[148:151], a[60:63], v[84:87]\n\t"
  "ds_read_b64_tr_b16 v[176:177], v248 offset:36352\n\t"
  "v_accvgpr_read_b32 v197, a115\n\t"
  "v_mfma_f32_16x16x32_bf16 v[80:83], v[148:151], a[56:59], v[80:83]\n\t"
  "ds_read_b64_tr_b16 v[178:179], v248 offset:36608\n\t"
  "v_mfma_f32_16x16x32_bf16 v[88:91], v[152:155], a[48:51], v[88:91]\n\t"
  "ds_read_b64_tr_b16 v[180:181], v248 offset:52736\n\t"
  "v_lshlrev_b32_e32 v196, 16, v197\n\t"
  "v_mfma_f32_16x16x32_bf16 v[92:95], v[152:155], a[52:55], v[92:95]\n\t"
  "ds_read_b64_tr_b16 v[182:183], v248 offset:52992\n\t"
  "v_and_b32_e32 v197, s96, v197\n\t"
  "v_mfma_f32_16x16x32_bf16 v[92:95], v[156:159], a[60:63], v[92:95]\n\t"
  "ds_read_b64_tr_b16 v[184:185], v249 offset:36352\n\t"
  "v_mul_f32_e32 v222, v192, v196\n\t"
  "v_mfma_f32_16x16x32_bf16 v[88:91], v[156:159], a[56:59], v[88:91]\n\t"
  "ds_read_b64_tr_b16 v[186:187], v249 offset:36608\n\t"
  "s_waitcnt lgkmcnt(6)\n\t"
  "v_mfma_f32_16x16x32_bf16 v[96:99], v[160:163], a[48:51], v[96:99]\n\t"
  "ds_read_b64_tr_b16 v[188:189], v249 offset:52736\n\t"
  "s_waitcnt vmcnt(16)\n\t"
  "s_barrier\n\t"
  "v_mfma_f32_16x16x32_bf16 v[100:103], v[160:163], a[52:55], v[100:103]\n\t"
  "ds_read_b64_tr_b16 v[190:191], v249 offset:52992\n\t"
  "s_add_u32 m0, s30, 0\n\t"
  "v_mul_f32_e32 v223, v192, v197\n\t"
  "v_mfma_f32_16x16x32_bf16 v[100:103], v[164:167], a[60:63], v[100:103]\n\t"
  "ds_read_b64_tr_b16 v[128:129], v252\n\t"
  "buffer_load_dwordx4 v235, s[8:11], s28 offen lds\n\t"
  "v_mfma_f32_16x16x32_bf16 v[96:99], v[164:167], a[56:59], v[96:99]\n\t"
  "ds_read_b64_tr_b16 v[130:131], v252 offset:256\n\t"
  "v_mfma_f32_16x16x32_bf16 v[104:107], v[168:171], a[48:51], v[104:107]\n\t"
  "ds_read_b64_tr_b16 v[132:133], v252 offset:16384\n\t"
  "v_accvgpr_read_b32 v197, a116\n\t"
  "v_mfma_f32_16x16x32_bf16 v[108:111], v[168:171], a[52:55], v[108:111]\n\t"
  "ds_read_b64_tr_b16 v[134:135], v252 offset:16640\n\t"
  "v_lshlrev_b32_e32 v196, 16, v197\n\t"
  "v_mfma_f32_16x16x32_bf16 v[108:111], v[172:175], a[60:63], v[108:111]\n\t"
  "ds_read_b64_tr_b16 v[136:137], v253\n\t"
  "buffer_load_dwordx4 v236, s[8:11], s28 offen offset:1024 lds\n\t"
  "v_mfma_f32_16x16x32_bf16 v[104:107], v[172:175], a[56:59], v[104:107]\n\t"
  "ds_read_b64_tr_b16 v[138:139], v253 offset:256\n\t"
  "s_waitcnt lgkmcnt(6)\n\t"
  "v_mfma_f32_16x16x32_bf16 v[112:115], v[176:179], a[48:51], v[112:115]\n\t"
  "ds_read_b64_tr_b16 v[140:141], v253 offset:16384\n\t"
  "v_and_b32_e32 v197, s96, v197\n\t"
  "v_mfma_f32_16x16x32_bf16 v[116:119], v[176:179], a[52:55], v[116:119]\n\t"
  "ds_read_b64_tr_b16 v[142:143], v253 offset:16640\n\t"
  "v_fmac_f32_e32 v216, v193, v196\n\t"
  "v_mfma_f32_16x16x32_bf16 v[116:119], v[180:183], a[60:63], v[116:119]\n\t"
  "ds_read_b64_tr_b16 v[144:145], v252 offset:512\n\t"
  "buffer_load_dwordx4 v237, s[8:11], s28 offen offset:2048 lds\n\t"
  "v_mfma_f32_16x16x32_bf16 v[112:115], v[180:183], a[56:59], v[112:115]\n\t"
  "ds_read_b64_tr_b16 v[146:147], v252 offset:768\n\t"
  "v_mfma_f32_16x16x32_bf16 v[120:123], v[184:187], a[48:51], v[120:123]\n\t"
  "ds_read_b64_tr_b16 v[148:149], v252 offset:16896\n\t"
  "v_fmac_f32_e32 v217, v193, v197\n\t"
  "v_mfma_f32_16x16x32_bf16 v[124:127], v[184:187], a[52:55], v[124:127]\n\t"
  "ds_read_b64_tr_b16 v[150:151], v252 offset:17152\n\t"
  "v_accvgpr_read_b32 v197, a117\n\t"
  "v_mfma_f32_16x16x32_bf16 v[124:127], v[188:191], a[60:63], v[124:127]\n\t"
  "ds_read_b64_tr_b16 v[152:153], v253 offset:512\n\t"
  "buffer_load_dwordx4 v238, s[8:11], s28 offen offset:3072 lds\n\t"
  "v_mfma_f32_16x16x32_bf16 v[120:123], v[188:191], a[56:59], v[120:123]\n\t"
  "ds_read_b64_tr_b16 v[154:155], v253 offset:768\n\t"
  "s_add_u32 s28, s28, 4096\n\t"
  "s_waitcnt lgkmcnt(6)\n\t"
  "v_mfma_f32_16x16x32_bf16 v[0:3], v[128:131], a[64:67], v[0:3]\n\t"
  "ds_read_b64_tr_b16 v[156:157], v253 offset:16896\n\t"
  "v_lshlrev_b32_e32 v196, 16, v197\n\t"
  "v_mfma_f32_16x16x32_bf16 v[4:7], v[128:131], a[68:71], v[4:7]\n\t"
  "ds_read_b64_tr_b16 v[158:159], v253 offset:17152\n\t"
  "s_add_u32 m0, s30, 4096\n\t"
  "v_and_b32_e32 v197, s96, v197\n\t"
  "v_mfma_f32_16x16x32_bf16 v[4:7], v[132:135], a[76:79], v[4:7]\n\t"
  "ds_read_b64_tr_b16 v[160:161], v252 offset:1024\n\t"
  "buffer_load_dwordx4 v235, s[8:11], s28 offen lds\n\t"
  "v_mfma_f32_16x16x32_bf16 v[0:3], v[132:135], a[72:75], v[0:3]\n\t"
  "ds_read_b64_tr_b16 v[162:163], v252 offset:1280\n\t"
  "v_mfma_f32_16x16x32_bf16 v[8:11], v[136:139], a[64:67], v[8:11]\n\t"
  "ds_read_b64_tr_b16 v[164:165], v252 offset:17408\n\t"
  "v_fmac_f32_e32 v218, v193, v196\n\t"
  "v_mfma_f32_16x16x32_bf16 v[12:15], v[136:139], a[68:71], v[12:15]\n\t"
  "ds_read_b64_tr_b16 v[166:167], v252 offset:17664\n\t"
  "v_fmac_f32_e32 v219, v193, v197\n\t"
  "v_mfma_f32_16x16x32_bf16 v[12:15], v[140:143], a[76:79], v[12:15]\n\t"
  "ds_read_b64_tr_b16 v[168:169], v253 offset:1024\n\t"
  "buffer_load_dwordx4 v236, s[8:11], s28 offen offset:1024 lds\n\t"
  "v_mfma_f32_16x16x32_bf16 v[8:11], v[140:143], a[72:75], v[8:11]\n\t"
  "ds_read_b64_tr_b16 v[170:171], v253 offset:1280\n\t"
  "s_waitcnt lgkmcnt(6)\n\t"
  "v_mfma_f32_16x16x32_bf16 v[16:19], v[144:147], a[64:67], v[16:19]\n\t"
  "ds_read_b64_tr_b16 v[172:173], v253 offset:17408\n\t"
  "v_accvgpr_read_b32 v197, a118\n\t"
  "v_mfma_f32_16x16x32_bf16 v[20:23], v[144:147], a[68:71], v[20:23]\n\t"
  "ds_read_b64_tr_b16 v[174:175], v253 offset:17664\n\t"
  "v_lshlrev_b32_e32 v196, 16, v197\n\t"
  "v_mfma_f32_16x16x32_bf16 v[20:23], v[148:151], a[76:79], v[20:23]\n\t"
  "ds_read_b64_tr_b16 v[176:177], v252 offset:1536\n\t"
  "buffer_load_dwordx4 v237, s[8:11], s28 offen offset:2048 lds\n\t"
  "v_mfma_f32_16x16x32_bf16 v[16:19], v[148:151], a[72:75], v[16:19]\n\t"
  "ds_read_b64_tr_b16 v[178:179], v252 offset:1792\n\t"
  "v_mfma_f32_16x16x32_bf16 v[24:27], v[152:155], a[64:67], v[24:27]\n\t"
  "ds_read_b64_tr_b16 v[180:181], v252 offset:17920\n\t"
  "v_and_b32_e32 v197, s96, v197\n\t"
  "v_mfma_f32_16x16x32_bf16 v[28:31], v[152:155], a[68:71], v[28:31]\n\t"
  "ds_read_b64_tr_b16 v[182:183], v252 offset:18176\n\t"
  "v_fmac_f32_e32 v220, v193, v196\n\t"
  "v_mfma_f32_16x16x32_bf16 v[28:31], v[156:159], a[76:79], v[28:31]\n\t"
  "ds_read_b64_tr_b16 v[184:185], v253 offset:1536\n\t"
  "buffer_load_dwordx4 v238, s[8:11], s28 offen offset:3072 lds\n\t"
  "v_mfma_f32_16x16x32_bf16 v[24:27], v[156:159], a[72:75], v[24:27]\n\t"
  "ds_read_b64_tr_b16 v[186:187], v253 offset:1792\n\t"
  "s_add_u32 s28, s28, 28672\n\t"
  "s_waitcnt lgkmcnt(6)\n\t"
  "v_mfma_f32_16x16x32_bf16 v[32:35], v[160:163], a[64:67], v[32:35]\n\t"
  "ds_read_b64_tr_b16 v[188:189], v253 offset:17920\n\t"
  "v_fmac_f32_e32 v221, v193, v197\n\t"
  "v_mfma_f32_16x16x32_bf16 v[36:39], v[160:163], a[68:71], v[36:39]\n\t"
  "ds_read_b64_tr_b16 v[190:191], v253 offset:18176\n\t"
  "v_accvgpr_read_b32 v197, a119\n\t"
  "v_mfma_f32_16x16x32_bf16 v[36:39], v[164:167], a[76:79], v[36:39]\n\t"
  "ds_read_b64_tr_b16 v[128:129], v252 offset:2048\n\t"
  "v_lshlrev_b32_e32 v196, 16, v197\n\t"
  "v_mfma_f32_16x16x32_bf16 v[32:35], v[164:167], a[72:75], v[32:35]\n\t"
  "ds_read_b64_tr_b16 v[130:131], v252 offset:2304\n\t"
  "v_mfma_f32_16x16x32_bf16 v[40:43], v[168:171], a[64:67], v[40:43]\n\t"
  "ds_read_b64_tr_b16 v[132:133], v252 offset:18432\n\t"
  "v_and_b32_e32 v197, s96, v197\n\t"
  "v_mfma_f32_16x16x32_bf16 v[44:47], v[168:171], a[68:71], v[44:47]\n\t"
  "ds_read_b64_tr_b16 v[134:135], v252 offset:18688\n\t"
  "v_fmac_f32_e32 v222, v193, v196\n\t"
  "v_mfma_f32_16x16x32_bf16 v[44:47], v[172:175], a[76:79], v[44:47]\n\t"
  "ds_read_b64_tr_b16 v[136:137], v253 offset:2048\n\t"
  "v_fmac_f32_e32 v223, v193, v197\n\t"
  "v_mfma_f32_16x16x32_bf16 v[40:43], v[172:175], a[72:75], v[40:43]\n\t"
  "ds_read_b64_tr_b16 v[138:139], v253 offset:2304\n\t"
  "s_waitcnt lgkmcnt(6)\n\t"
  "v_mfma_f32_16x16x32_bf16 v[48:51], v[176:179], a[64:67], v[48:51]\n\t"
  "ds_read_b64_tr_b16 v[140:141], v253 offset:18432\n\t"
  "v_accvgpr_read_b32 v197, a120\n\t"
  "v_mfma_f32_16x16x32_bf16 v[52:55], v[176:179], a[68:71], v[52:55]\n\t"
  "ds_read_b64_tr_b16 v[142:143], v253 offset:18688\n\t"
  "v_lshlrev_b32_e32 v196, 16, v197\n\t"
  "v_mfma_f32_16x16x32_bf16 v[52:55], v[180:183], a[76:79], v[52:55]\n\t"
  "ds_read_b64_tr_b16 v[144:145], v252 offset:2560\n\t"
  "v_and_b32_e32 v197, s96, v197\n\t"
  "v_mfma_f32_16x16x32_bf16 v[48:51], v[180:183], a[72:75], v[48:51]\n\t"
  "ds_read_b64_tr_b16 v[146:147], v252 offset:2816\n\t"
  "v_mfma_f32_16x16x32_bf16 v[56:59], v[184:187], a[64:67], v[56:59]\n\t"
  "ds_read_b64_tr_b16 v[148:149], v252 offset:18944\n\t"
  "v_fmac_f32_e32 v216, v194, v196\n\t"
  "v_mfma_f32_16x16x32_bf16 v[60:63], v[184:187], a[68:71], v[60:63]\n\t"
  "ds_read_b64_tr_b16 v[150:151], v252 offset:19200\n\t"
  "v_fmac_f32_e32 v217, v194, v197\n\t"
  "v_mfma_f32_16x16x32_bf16 v[60:63], v[188:191], a[76:79], v[60:63]\n\t"
  "ds_read_b64_tr_b16 v[152:153], v253 offset:2560\n\t"
  "v_accvgpr_read_b32 v197, a121\n\t"
  "v_mfma_f32_16x16x32_bf16 v[56:59], v[188:191], a[72:75], v[56:59]\n\t"
  "ds_read_b64_tr_b16 v[154:155], v253 offset:2816\n\t"
  "s_waitcnt lgkmcnt(6)\n\t"
  "v_mfma_f32_16x16x32_bf16 v[64:67], v[128:131], a[64:67], v[64:67]\n\t"
  "ds_read_b64_tr_b16 v[156:157], v253 offset:18944\n\t"
  "v_lshlrev_b32_e32 v196, 16, v197\n\t"
  "v_mfma_f32_16x16x32_bf16 v[68:71], v[128:131], a[68:71], v[68:71]\n\t"
  "ds_read_b64_tr_b16 v[158:159], v253 offset:19200\n\t"
  "v_and_b32_e32 v197, s96, v197\n\t"
  "v_mfma_f32_16x16x32_bf16 v[68:71], v[132:135], a[76:79], v[68:71]\n\t"
  "ds_read_b64_tr_b16 v[160:161], v252 offset:3072\n\t"
  "v_fmac_f32_e32 v218, v194, v196\n\t"
  "v_mfma_f32_16x16x32_bf16 v[64:67], v[132:135], a[72:75], v[64:67]\n\t"
  "ds_read_b64_tr_b16 v[162:163], v252 offset:3328\n\t"
  "v_mfma_f32_16x16x32_bf16 v[72:75], v[136:139], a[64:67], v[72:75]\n\t"
  "ds_read_b64_tr_b16 v[164:165], v252 offset:19456\n\t"
  "v_fmac_f32_e32 v219, v194, v197\n\t"
  "v_mfma_f32_16x16x32_bf16 v[76:79], v[136:139], a[68:71], v[76:79]\n\t"
  "ds_read_b64_tr_b16 v[166:167], v252 offset:19712\n\t"
  "v_accvgpr_read_b32 v197, a122\n\t"
  "v_mfma_f32_16x16x32_bf16 v[76:79], v[140:143], a[76:79], v[76:79]\n\t"
  "ds_read_b64_tr_b16 v[168:169], v253 offset:3072\n\t"
  "v_lshlrev_b32_e32 v196, 16, v197\n\t"
  "v_mfma_f32_16x16x32_bf16 v[72:75], v[140:143], a[72:75], v[72:75]\n\t"
  "ds_read_b64_tr_b16 v[170:171], v253 offset:3328\n\t"
  "s_waitcnt lgkmcnt(6)\n\t"
  "v_mfma_f32_16x16x32_bf16 v[80:83], v[144:147], a[64:67], v[80:83]\n\t"
  "ds_read_b64_tr_b16 v[172:173], v253 offset:19456\n\t"
  "v_and_b32_e32 v197, s96, v197\n\t"
  "v_mfma_f32_16x16x32_bf16 v[84:87], v[144:147], a[68:71], v[84:87]\n\t"
  "ds_read_b64_tr_b16 v[174:175], v253 offset:19712\n\t"
  "v_fmac_f32_e32 v220, v194, v196\n\t"
  "v_mfma_f32_16x16x32_bf16 v[84:87], v[148:151], a[76:79], v[84:87]\n\t"
  "ds_read_b64_tr_b16 v[176:177], v252 offset:3584\n\t"
  "v_fmac_f32_e32 v221, v194, v197\n\t"
  "v_mfma_f32_16x16x32_bf16 v[80:83], v[148:151], a[72:75], v[80:83]\n\t"
  "ds_read_b64_tr_b16 v[178:179], v252 offset:3840\n\t"
  "v_mfma_f32_16x16x32_bf16 v[88:91], v[152:155], a[64:67], v[88:91]\n\t"
  "ds_read_b64_tr_b16 v[180:181], v252 offset:19968\n\t"
  "v_accvgpr_read_b32 v197, a123\n\t"
  "v_mfma_f32_16x16x32_bf16 v[92:95], v[152:155], a[68:71], v[92:95]\n\t"
  "ds_read_b64_tr_b16 v[182:183], v252 offset:20224\n\t"
  "v_lshlrev_b32_e32 v196, 16, v197\n\t"
  "v_mfma_f32_16x16x32_bf16 v[92:95], v[156:159], a[76:79], v[92:95]\n\t"
  "ds_read_b64_tr_b16 v[184:185], v253 offset:3584\n\t"
  "v_and_b32_e32 v197, s96, v197\n\t"
  "v_mfma_f32_16x16x32_bf16 v[88:91], v[156:159], a[72:75], v[88:91]\n\t"
  "ds_read_b64_tr_b16 v[186:187], v253 offset:3840\n\t"
  "s_waitcnt lgkmcnt(6)\n\t"
  "v_mfma_f32_16x16x32_bf16 v[96:99], v[160:163], a[64:67], v[96:99]\n\t"
  "ds_read_b64_tr_b16 v[188:189], v253 offset:19968\n\t"
  "s_waitcnt vmcnt(16)\n\t"
  "s_barrier\n\t"
  "v_mfma_f32_16x16x32_bf16 v[100:103], v[160:163], a[68:71], v[100:103]\n\t"
  "ds_read_b64_tr_b16 v[190:191], v253 offset:20224\n\t"
  "s_add_u32 m0, s30, 32768\n\t"
  "v_fmac_f32_e32 v222, v194, v196\n\t"
  "v_mfma_f32_16x16x32_bf16 v[100:103], v[164:167], a[76:79], v[100:103]\n\t"
  "ds_read_b64_tr_b16 v[128:129], v250 offset:32768\n\t"
  "buffer_load_dwordx4 v235, s[8:11], s28 offen lds\n\t"
  "v_mfma_f32_16x16x32_bf16 v[96:99], v[164:167], a[72:75], v[96:99]\n\t"
  "ds_read_b64_tr_b16 v[130:131], v250 offset:33024\n\t"
  "v_mfma_f32_16x16x32_bf16 v[104:107], v[168:171], a[64:67], v[104:107]\n\t"
  "ds_read_b64_tr_b16 v[132:133], v250 offset:49152\n\t"
  "v_fmac_f32_e32 v223, v194, v197\n\t"
  "v_mfma_f32_16x16x32_bf16 v[108:111], v[168:171], a[68:71], v[108:111]\n\t"
  "ds_read_b64_tr_b16 v[134:135], v250 offset:49408\n\t"
  "v_accvgpr_read_b32 v197, a124\n\t"
  "v_mfma_f32_16x16x32_bf16 v[108:111], v[172:175], a[76:79], v[108:111]\n\t"
  "ds_read_b64_tr_b16 v[136:137], v251 offset:32768\n\t"
  "buffer_load_dwordx4 v236, s[8:11], s28 offen offset:1024 lds\n\t"
  "v_mfma_f32_16x16x32_bf16 v[104:107], v[172:175], a[72:75], v[104:107]\n\t"
  "ds_read_b64_tr_b16 v[138:139], v251 offset:33024\n\t"
  "s_waitcnt lgkmcnt(6)\n\t"
  "v_mfma_f32_16x16x32_bf16 v[112:115], v[176:179], a[64:67], v[112:115]\n\t"
  "ds_read_b64_tr_b16 v[140:141], v251 offset:49152\n\t"
  "v_lshlrev_b32_e32 v196, 16, v197\n\t"
  "v_mfma_f32_16x16x32_bf16 v[116:119], v[176:179], a[68:71], v[116:119]\n\t"
  "ds_read_b64_tr_b16 v[142:143], v251 offset:49408\n\t"
  "v_and_b32_e32 v197, s96, v197\n\t"
  "v_mfma_f32_16x16x32_bf16 v[116:119], v[180:183], a[76:79], v[116:119]\n\t"
  "ds_read_b64_tr_b16 v[144:145], v250 offset:33280\n\t"
  "buffer_load_dwordx4 v237, s[8:11], s28 offen offset:2048 lds\n\t"
  "v_mfma_f32_16x16x32_bf16 v[112:115], v[180:183], a[72:75], v[112:115]\n\t"
  "ds_read_b64_tr_b16 v[146:147], v250 offset:33536\n\t"
  "v_mfma_f32_16x16x32_bf16 v[120:123], v[184:187], a[64:67], v[120:123]\n\t"
  "ds_read_b64_tr_b16 v[148:149], v250 offset:49664\n\t"
  "v_fmac_f32_e32 v216, v195, v196\n\t"
  "v_mfma_f32_16x16x32_bf16 v[124:127], v[184:187], a[68:71], v[124:127]\n\t"
  "ds_read_b64_tr_b16 v[150:151], v250 offset:49920\n\t"
  "v_fmac_f32_e32 v217, v195, v197\n\t"
  "v_mfma_f32_16x16x32_bf16 v[124:127], v[188:191], a[76:79], v[124:127]\n\t"
  "ds_read_b64_tr_b16 v[152:153], v251 offset:33280\n\t"
  "buffer_load_dwordx4 v238, s[8:11], s28 offen offset:3072 lds\n\t"
  "v_mfma_f32_16x16x32_bf16 v[120:123], v[188:191], a[72:75], v[120:123]\n\t"
  "ds_read_b64_tr_b16 v[154:155], v251 offset:33536\n\t"
  "s_add_u32 s28, s28, 4096\n\t"
  "s_waitcnt lgkmcnt(6)\n\t"
  "v_mfma_f32_16x16x32_bf16 v[0:3], v[128:131], a[80:83], v[0:3]\n\t"
  "ds_read_b64_tr_b16 v[156:157], v251 offset:49664\n\t"
  "v_accvgpr_read_b32 v197, a125\n\t"
  "v_mfma_f32_16x16x32_bf16 v[4:7], v[128:131], a[84:87], v[4:7]\n\t"
  "ds_read_b64_tr_b16 v[158:159], v251 offset:49920\n\t"
  "s_add_u32 m0, s30, 36864\n\t"
  "v_lshlrev_b32_e32 v196, 16, v197\n\t"
  "v_mfma_f32_16x16x32_bf16 v[4:7], v[132:135], a[92:95], v[4:7]\n\t"
  "ds_read_b64_tr_b16 v[160:161], v250 offset:33792\n\t"
  "buffer_load_dwordx4 v235, s[8:11], s28 offen lds\n\t"
  "v_mfma_f32_16x16x32_bf16 v[0:3], v[132:135], a[88:91], v[0:3]\n\t"
  "ds_read_b64_tr_b16 v[162:163], v250 offset:34048\n\t"
  "v_mfma_f32_16x16x32_bf16 v[8:11], v[136:139], a[80:83], v[8:11]\n\t"
  "ds_read_b64_tr_b16 v[164:165], v250 offset:50176\n\t"
  "v_and_b32_e32 v197, s96, v197\n\t"
  "v_mfma_f32_16x16x32_bf16 v[12:15], v[136:139], a[84:87], v[12:15]\n\t"
  "ds_read_b64_tr_b16 v[166:167], v250 offset:50432\n\t"
  "v_fmac_f32_e32 v218, v195, v196\n\t"
  "v_mfma_f32_16x16x32_bf16 v[12:15], v[140:143], a[92:95], v[12:15]\n\t"
  "ds_read_b64_tr_b16 v[168:169], v251 offset:33792\n\t"
  "buffer_load_dwordx4 v236, s[8:11], s28 offen offset:1024 lds\n\t"
  "v_mfma_f32_16x16x32_bf16 v[8:11], v[140:143], a[88:91], v[8:11]\n\t"
  "ds_read_b64_tr_b16 v[170:171], v251 offset:34048\n\t"
  "s_waitcnt lgkmcnt(6)\n\t"
  "v_mfma_f32_16x16x32_bf16 v[16:19], v[144:147], a[80:83], v[16:19]\n\t"
  "ds_read_b64_tr_b16 v[172:173], v251 offset:50176\n\t"
  "v_fmac_f32_e32 v219, v195, v197\n\t"
  "v_mfma_f32_16x16x32_bf16 v[20:23], v[144:147], a[84:87], v[20:23]\n\t"
  "ds_read_b64_tr_b16 v[174:175], v251 offset:50432\n\t"
  "v_accvgpr_read_b32 v197, a126\n\t"
  "v_mfma_f32_16x16x32_bf16 v[20:23], v[148:151], a[92:95], v[20:23]\n\t"
  "ds_read_b64_tr_b16 v[176:177], v250 offset:34304\n\t"
  "buffer_load_dwordx4 v237, s[8:11], s28 offen offset:2048 lds\n\t"
  "v_mfma_f32_16x16x32_bf16 v[16:19], v[148:151], a[88:91], v[16:19]\n\t"
  "ds_read_b64_tr_b16 v[178:179], v250 offset:34560\n\t"
  "v_mfma_f32_16x16x32_bf16 v[24:27], v[152:155], a[80:83], v[24:27]\n\t"
  "ds_read_b64_tr_b16 v[180:181], v250 offset:50688\n\t"
  "v_lshlrev_b32_e32 v196, 16, v197\n\t"
  "v_mfma_f32_16x16x32_bf16 v[28:31], v[152:155], a[84:87], v[28:31]\n\t"
  "ds_read_b64_tr_b16 v[182:183], v250 offset:50944\n\t"
  "v_and_b32_e32 v197, s96, v197\n\t"
  "v_mfma_f32_16x16x32_bf16 v[28:31], v[156:159], a[92:95], v[28:31]\n\t"
  "ds_read_b64_tr_b16 v[184:185], v251 offset:34304\n\t"
  "buffer_load_dwordx4 v238, s[8:11], s28 offen offset:3072 lds\n\t"
  "v_mfma_f32_16x16x32_bf16 v[24:27], v[156:159], a[88:91], v[24:27]\n\t"
  "ds_read_b64_tr_b16 v[186:187], v251 offset:34560\n\t"
  "s_add_u32 s28, s28, 28672\n\t"
  "s_waitcnt lgkmcnt(6)\n\t"
  "v_mfma_f32_16x16x32_bf16 v[32:35], v[160:163], a[80:83], v[32:35]\n\t"
  "ds_read_b64_tr_b16 v[188:189], v251 offset:50688\n\t"
  "v_fmac_f32_e32 v220, v195, v196\n\t"
  "v_mfma_f32_16x16x32_bf16 v[36:39], v[160:163], a[84:87], v[36:39]\n\t"
  "ds_read_b64_tr_b16 v[190:191], v251 offset:50944\n\t"
  "v_fmac_f32_e32 v221, v195, v197\n\t"
  "v_mfma_f32_16x16x32_bf16 v[36:39], v[164:167], a[92:95], v[36:39]\n\t"
  "ds_read_b64_tr_b16 v[128:129], v250 offset:34816\n\t"
  "v_accvgpr_read_b32 v197, a127\n\t"
  "v_mfma_f32_16x16x32_bf16 v[32:35], v[164:167], a[88:91], v[32:35]\n\t"
  "ds_read_b64_tr_b16 v[130:131], v250 offset:35072\n\t"
  "v_mfma_f32_16x16x32_bf16 v[40:43], v[168:171], a[80:83], v[40:43]\n\t"
  "ds_read_b64_tr_b16 v[132:133], v250 offset:51200\n\t"
  "s_lshl_b32 s64, s63, 2\n\t"
  "s_add_u32 s64, s64, s62\n\t"
  "v_lshlrev_b32_e32 v215, 7, v254\n\t"
  "s_mov_b32 s65, 644\n\t"
  "s_cmp_lt_u32 s64, 644\n\t"
  "s_cselect_b32 s92, s50, s52\n\t"
  "s_cselect_b32 s93, s51, s53\n\t"
  "s_cselect_b32 s65, 448, s65\n\t"
  "s_cmp_lt_u32 s64, 448\n\t"
  "s_cselect_b32 s92, s48, s92\n\t"
  "s_cselect_b32 s93, s49, s93\n\t"
  "s_cselect_b32 s65, 0, s65\n\t"
  "s_min_u32 s64, s64, 839\n\t"
  "s_sub_u32 s64, s64, s65\n\t"
  "s_lshl_b32 s64, s64, 13\n\t"
  "v_add_u32_e32 v215, s64, v215\n\t"
  "global_load_dword v255, v215, s[92:93]\n\t"
  "v_mfma_f32_16x16x32_bf16 v[44:47], v[168:171], a[84:87], v[44:47]\n\t"
  "ds_read_b64_tr_b16 v[134:135], v250 offset:51456\n\t"
  "v_lshlrev_b32_e32 v196, 16, v197\n\t"
  "v_mfma_f32_16x16x32_bf16 v[44:47], v[172:175], a[92:95], v[44:47]\n\t"
  "ds_read_b64_tr_b16 v[136:137], v251 offset:34816\n\t"
  "v_and_b32_e32 v197, s96, v197\n\t"
  "v_mfma_f32_16x16x32_bf16 v[40:43], v[172:175], a[88:91], v[40:43]\n\t"
  "ds_read_b64_tr_b16 v[138:139], v251 offset:35072\n\t"
  "s_waitcnt lgkmcnt(6)\n\t"
  "v_mfma_f32_16x16x32_bf16 v[48:51], v[176:179], a[80:83], v[48:51]\n\t"
  "ds_read_b64_tr_b16 v[140:141], v251 offset:51200\n\t"
  "v_fmac_f32_e32 v222, v195, v196\n\t"
  "v_mfma_f32_16x16x32_bf16 v[52:55], v[176:179], a[84:87], v[52:55]\n\t"
  "ds_read_b64_tr_b16 v[142:143], v251 offset:51456\n\t"
  "v_fmac_f32_e32 v223, v195, v197\n\t"
  "v_mfma_f32_16x16x32_bf16 v[52:55], v[180:183], a[92:95], v[52:55]\n\t"
  "ds_read_b64_tr_b16 v[144:145], v250 offset:35328\n\t"
  "v_permlane32_swap_b32 v216, v220\n\t"
  "v_mfma_f32_16x16x32_bf16 v[48:51], v[180:183], a[88:91], v[48:51]\n\t"
  "ds_read_b64_tr_b16 v[146:147], v250 offset:35584\n\t"
  "v_mfma_f32_16x16x32_bf16 v[56:59], v[184:187], a[80:83], v[56:59]\n\t"
  "ds_read_b64_tr_b16 v[148:149], v250 offset:51712\n\t"
  "v_permlane32_swap_b32 v217, v221\n\t"
  "v_mfma_f32_16x16x32_bf16 v[60:63], v[184:187], a[84:87], v[60:63]\n\t"
  "ds_read_b64_tr_b16 v[150:151], v250 offset:51968\n\t"
  "v_permlane32_swap_b32 v218, v222\n\t"
  "v_mfma_f32_16x16x32_bf16 v[60:63], v[188:191], a[92:95], v[60:63]\n\t"
  "ds_read_b64_tr_b16 v[152:153], v251 offset:35328\n\t"
  "v_permlane32_swap_b32 v219, v223\n\t"
  "v_mfma_f32_16x16x32_bf16 v[56:59], v[188:191], a[88:91], v[56:59]\n\t"
  "ds_read_b64_tr_b16 v[154:155], v251 offset:35584\n\t"
  "s_waitcnt lgkmcnt(6)\n\t"
  "v_mfma_f32_16x16x32_bf16 v[64:67], v[128:131], a[80:83], v[64:67]\n\t"
  "ds_read_b64_tr_b16 v[156:157], v251 offset:51712\n\t"
  "v_add_f32_e32 v216, v216, v220\n\t"
  "v_mfma_f32_16x16x32_bf16 v[68:71], v[128:131], a[84:87], v[68:71]\n\t"
  "ds_read_b64_tr_b16 v[158:159], v251 offset:51968\n\t"
  "v_add_f32_e32 v217, v217, v221\n\t"
  "v_mfma_f32_16x16x32_bf16 v[68:71], v[132:135], a[92:95], v[68:71]\n\t"
  "ds_read_b64_tr_b16 v[160:161], v250 offset:35840\n\t"
  "v_add_f32_e32 v218, v218, v222\n\t"
  "v_mfma_f32_16x16x32_bf16 v[64:67], v[132:135], a[88:91], v[64:67]\n\t"
  "ds_read_b64_tr_b16 v[162:163], v250 offset:36096\n\t"
  "v_mfma_f32_16x16x32_bf16 v[72:75], v[136:139], a[80:83], v[72:75]\n\t"
  "ds_read_b64_tr_b16 v[164:165], v250 offset:52224\n\t"
  "v_add_f32_e32 v219, v219, v223\n\t"
  "v_mfma_f32_16x16x32_bf16 v[76:79], v[136:139], a[84:87], v[76:79]\n\t"
  "ds_read_b64_tr_b16 v[166:167], v250 offset:52480\n\t"
  "v_permlane16_swap_b32 v216, v218\n\t"
  "v_mfma_f32_16x16x32_bf16 v[76:79], v[140:143], a[92:95], v[76:79]\n\t"
  "ds_read_b64_tr_b16 v[168:169], v251 offset:35840\n\t"
  "v_permlane16_swap_b32 v217, v219\n\t"
  "v_mfma_f32_16x16x32_bf16 v[72:75], v[140:143], a[88:91], v[72:75]\n\t"
  "ds_read_b64_tr_b16 v[170:171], v251 offset:36096\n\t"
  "s_waitcnt lgkmcnt(6)\n\t"
  "v_mfma_f32_16x16x32_bf16 v[80:83], v[144:147], a[80:83], v[80:83]\n\t"
  "ds_read_b64_tr_b16 v[172:173], v251 offset:52224\n\t"
  "v_add_f32_e32 v216, v216, v218\n\t"
  "v_mfma_f32_16x16x32_bf16 v[84:87], v[144:147], a[84:87], v[84:87]\n\t"
  "ds_read_b64_tr_b16 v[174:175], v251 offset:52480\n\t"
  "v_add_f32_e32 v217, v217, v219\n\t"
  "v_mfma_f32_16x16x32_bf16 v[84:87], v[148:151], a[92:95], v[84:87]\n\t"
  "ds_read_b64_tr_b16 v[176:177], v250 offset:36352\n\t"
  "s_nop 1\n\t"
  "v_add_f32_dpp v216, v216, v216 row_ror:8 row_mask:0xf bank_mask:0xf\n\t"
  "v_add_f32_dpp v217, v217, v217 row_ror:8 row_mask:0xf bank_mask:0xf\n\t"
  "v_mfma_f32_16x16x32_bf16 v[80:83], v[148:151], a[88:91], v[80:83]\n\t"
  "ds_read_b64_tr_b16 v[178:179], v250 offset:36608\n\t"
  "v_mfma_f32_16x16x32_bf16 v[88:91], v[152:155], a[80:83], v[88:91]\n\t"
  "ds_read_b64_tr_b16 v[180:181], v250 offset:52736\n\t"
  "s_nop 1\n\t"
  "v_add_f32_dpp v216, v216, v216 row_ror:4 row_mask:0xf bank_mask:0xf\n\t"
  "v_add_f32_dpp v217, v217, v217 row_ror:4 row_mask:0xf bank_mask:0xf\n\t"
  "v_mfma_f32_16x16x32_bf16 v[92:95], v[152:155], a[84:87], v[92:95]\n\t"
  "ds_read_b64_tr_b16 v[182:183], v250 offset:52992\n\t"
  "s_nop 1\n\t"
  "v_add_f32_dpp v216, v216, v216 quad_perm:[2,3,0,1] row_mask:0xf bank_mask:0xf\n\t"
  "v_add_f32_dpp v217, v217, v217 quad_perm:[2,3,0,1] row_mask:0xf bank_mask:0xf\n\t"
  "v_mfma_f32_16x16x32_bf16 v[92:95], v[156:159], a[92:95], v[92:95]\n\t"
  "ds_read_b64_tr_b16 v[184:185], v251 offset:36352\n\t"
  "v_and_b32_e32 v218, 1, v254\n\t"
  "v_mfma_f32_16x16x32_bf16 v[88:91], v[156:159], a[88:91], v[88:91]\n\t"
  "ds_read_b64_tr_b16 v[186:187], v251 offset:36608\n\t"
  "s_waitcnt lgkmcnt(6)\n\t"
  "v_mfma_f32_16x16x32_bf16 v[96:99], v[160:163], a[80:83], v[96:99]\n\t"
  "ds_read_b64_tr_b16 v[188:189], v251 offset:52736\n\t"
  "s_waitcnt vmcnt(17)\n\t"
  "s_barrier\n\t"
  "v_mfma_f32_16x16x32_bf16 v[100:103], v[160:163], a[84:87], v[100:103]\n\t"
  "ds_read_b64_tr_b16 v[190:191], v251 offset:52992\n\t"
  "v_lshlrev_b32_e32 v218, 5, v218\n\t"
  "v_mfma_f32_16x16x32_bf16 v[100:103], v[164:167], a[92:95], v[100:103]\n\t"
  "ds_read_b64_tr_b16 v[128:129], v250\n\t"
  "v_lshrrev_b32_e32 v219, 4, v254\n\t"
  "v_mfma_f32_16x16x32_bf16 v[96:99], v[164:167], a[88:91], v[96:99]\n\t"
  "ds_read_b64_tr_b16 v[130:131], v250 offset:256\n\t"
  "v_mfma_f32_16x16x32_bf16 v[104:107], v[168:171], a[80:83], v[104:107]\n\t"
  "ds_read_b64_tr_b16 v[132:133], v250 offset:16384\n\t"
  "v_lshl_add_u32 v218, v219, 3, v218\n\t"
  "v_mfma_f32_16x16x32_bf16 v[108:111], v[168:171], a[84:87], v[108:111]\n\t"
  "ds_read_b64_tr_b16 v[134:135], v250 offset:16640\n\t"
  "s_lshl_b32 s66, s62, 6\n\t"
  "v_mfma_f32_16x16x32_bf16 v[108:111], v[172:175], a[92:95], v[108:111]\n\t"
  "ds_read_b64_tr_b16 v[136:137], v251\n\t"
  "s_add_u32 s66, s66, 131072\n\t"
  "v_mfma_f32_16x16x32_bf16 v[104:107], v[172:175], a[88:91], v[104:107]\n\t"
  "ds_read_b64_tr_b16 v[138:139], v251 offset:256\n\t"
  "s_waitcnt lgkmcnt(6)\n\t"
  "v_mfma_f32_16x16x32_bf16 v[112:115], v[176:179], a[80:83], v[112:115]\n\t"
  "ds_read_b64_tr_b16 v[140:141], v251 offset:16384\n\t"
  "v_add_u32_e32 v218, s66, v218\n\t"
  "v_mfma_f32_16x16x32_bf16 v[116:119], v[176:179], a[84:87], v[116:119]\n\t"
  "ds_read_b64_tr_b16 v[142:143], v251 offset:16640\n\t"
  "v_mfma_f32_16x16x32_bf16 v[116:119], v[180:183], a[92:95], v[116:119]\n\t"
  "ds_read_b64_tr_b16 v[144:145], v250 offset:512\n\t"
  "v_mfma_f32_16x16x32_bf16 v[112:115], v[180:183], a[88:91], v[112:115]\n\t"
  "ds_read_b64_tr_b16 v[146:147], v250 offset:768\n\t"
  "v_mfma_f32_16x16x32_bf16 v[120:123], v[184:187], a[80:83], v[120:123]\n\t"
  "ds_read_b64_tr_b16 v[148:149], v250 offset:16896\n\t"
  "v_mfma_f32_16x16x32_bf16 v[124:127], v[184:187], a[84:87], v[124:127]\n\t"
  "ds_read_b64_tr_b16 v[150:151], v250 offset:17152\n\t"
  "v_mfma_f32_16x16x32_bf16 v[124:127], v[188:191], a[92:95], v[124:127]\n\t"
  "ds_read_b64_tr_b16 v[152:153], v251 offset:512\n\t"
  "v_mfma_f32_16x16x32_bf16 v[120:123], v[188:191], a[88:91], v[120:123]\n\t"
  "ds_read_b64_tr_b16 v[154:155], v251 offset:768\n\t"
  "s_waitcnt lgkmcnt(6)\n\t"
  "v_mfma_f32_16x16x32_bf16 v[0:3], v[128:131], a[96:99], v[0:3]\n\t"
  "ds_read_b64_tr_b16 v[156:157], v251 offset:16896\n\t"
  "v_mfma_f32_16x16x32_bf16 v[4:7], v[128:131], a[100:103], v[4:7]\n\t"
  "ds_read_b64_tr_b16 v[158:159], v251 offset:17152\n\t"
  "v_mfma_f32_16x16x32_bf16 v[4:7], v[132:135], a[108:111], v[4:7]\n\t"
  "ds_read_b64_tr_b16 v[160:161], v250 offset:1024\n\t"
  "v_mfma_f32_16x16x32_bf16 v[0:3], v[132:135], a[104:107], v[0:3]\n\t"
  "ds_read_b64_tr_b16 v[162:163], v250 offset:1280\n\t"
  "v_mfma_f32_16x16x32_bf16 v[8:11], v[136:139], a[96:99], v[8:11]\n\t"
  "ds_read_b64_tr_b16 v[164:165], v250 offset:17408\n\t"
  "v_mfma_f32_16x16x32_bf16 v[12:15], v[136:139], a[100:103], v[12:15]\n\t"
  "ds_read_b64_tr_b16 v[166:167], v250 offset:17664\n\t"
  "v_mfma_f32_16x16x32_bf16 v[12:15], v[140:143], a[108:111], v[12:15]\n\t"
  "ds_read_b64_tr_b16 v[168:169], v251 offset:1024\n\t"
  "v_mfma_f32_16x16x32_bf16 v[8:11], v[140:143], a[104:107], v[8:11]\n\t"
  "ds_read_b64_tr_b16 v[170:171], v251 offset:1280\n\t"
  "s_waitcnt lgkmcnt(6)\n\t"
  "v_mfma_f32_16x16x32_bf16 v[16:19], v[144:147], a[96:99], v[16:19]\n\t"
  "ds_read_b64_tr_b16 v[172:173], v251 offset:17408\n\t"
  "v_mfma_f32_16x16x32_bf16 v[20:23], v[144:147], a[100:103], v[20:23]\n\t"
  "ds_read_b64_tr_b16 v[174:175], v251 offset:17664\n\t"
  "v_mfma_f32_16x16x32_bf16 v[20:23], v[148:151], a[108:111], v[20:23]\n\t"
  "ds_read_b64_tr_b16 v[176:177], v250 offset:1536\n\t"
  "s_barrier\n\t"
  "v_mfma_f32_16x16x32_bf16 v[16:19], v[148:151], a[104:107], v[16:19]\n\t"
  "ds_read_b64_tr_b16 v[178:179], v250 offset:1792\n\t"
  "v_mfma_f32_16x16x32_bf16 v[24:27], v[152:155], a[96:99], v[24:27]\n\t"
  "ds_read_b64_tr_b16 v[180:181], v250 offset:17920\n\t"
  "v_cvt_pk_bf16_f32 v0, v0, v1\n\t"
  "v_mfma_f32_16x16x32_bf16 v[28:31], v[152:155], a[100:103], v[28:31]\n\t"
  "ds_read_b64_tr_b16 v[182:183], v250 offset:18176\n\t"
  "v_cvt_pk_bf16_f32 v1, v2, v3\n\t"
  "v_mfma_f32_16x16x32_bf16 v[28:31], v[156:159], a[108:111], v[28:31]\n\t"
  "ds_read_b64_tr_b16 v[184:185], v251 offset:1536\n\t"
  "ds_write_b64 v206, v[0:1]\n\t"
  "v_mfma_f32_16x16x32_bf16 v[24:27], v[156:159], a[104:107], v[24:27]\n\t"
  "ds_read_b64_tr_b16 v[186:187], v251 offset:1792\n\t"
  "s_waitcnt lgkmcnt(7)\n\t"
  "v_mfma_f32_16x16x32_bf16 v[32:35], v[160:163], a[96:99], v[32:35]\n\t"
  "ds_read_b64_tr_b16 v[188:189], v251 offset:17920\n\t"
  "v_cvt_pk_bf16_f32 v4, v4, v5\n\t"
  "v_mfma_f32_16x16x32_bf16 v[36:39], v[160:163], a[100:103], v[36:39]\n\t"
  "ds_read_b64_tr_b16 v[190:191], v251 offset:18176\n\t"
  "v_cvt_pk_bf16_f32 v5, v6, v7\n\t"
  "v_mfma_f32_16x16x32_bf16 v[36:39], v[164:167], a[108:111], v[36:39]\n\t"
  "ds_read_b64_tr_b16 v[128:129], v250 offset:2048\n\t"
  "ds_write_b64 v206, v[4:5] offset:8192\n\t"
  "v_mfma_f32_16x16x32_bf16 v[32:35], v[164:167], a[104:107], v[32:35]\n\t"
  "ds_read_b64_tr_b16 v[130:131], v250 offset:2304\n\t"
  "v_mfma_f32_16x16x32_bf16 v[40:43], v[168:171], a[96:99], v[40:43]\n\t"
  "ds_read_b64_tr_b16 v[132:133], v250 offset:18432\n\t"
  "v_cvt_pk_bf16_f32 v8, v8, v9\n\t"
  "v_mfma_f32_16x16x32_bf16 v[44:47], v[168:171], a[100:103], v[44:47]\n\t"
  "ds_read_b64_tr_b16 v[134:135], v250 offset:18688\n\t"
  "v_cvt_pk_bf16_f32 v9, v10, v11\n\t"
  "v_mfma_f32_16x16x32_bf16 v[44:47], v[172:175], a[108:111], v[44:47]\n\t"
  "ds_read_b64_tr_b16 v[136:137], v251 offset:2048\n\t"
  "s_waitcnt lgkmcnt(13)\n\t"
  "ds_write_b64 v207, v[8:9]\n\t"
  "v_mfma_f32_16x16x32_bf16 v[40:43], v[172:175], a[104:107], v[40:43]\n\t"
  "ds_read_b64_tr_b16 v[138:139], v251 offset:2304\n\t"
  "s_waitcnt lgkmcnt(8)\n\t"
  "v_mfma_f32_16x16x32_bf16 v[48:51], v[176:179], a[96:99], v[48:51]\n\t"
  "ds_read_b64_tr_b16 v[140:141], v251 offset:18432\n\t"
  "v_cvt_pk_bf16_f32 v12, v12, v13\n\t"
  "v_mfma_f32_16x16x32_bf16 v[52:55], v[176:179], a[100:103], v[52:55]\n\t"
  "ds_read_b64_tr_b16 v[142:143], v251 offset:18688\n\t"
  "v_cvt_pk_bf16_f32 v13, v14, v15\n\t"
  "v_mfma_f32_16x16x32_bf16 v[52:55], v[180:183], a[108:111], v[52:55]\n\t"
  "ds_read_b64_tr_b16 v[144:145], v250 offset:2560\n\t"
  "ds_write_b64 v207, v[12:13] offset:8192\n\t"
  "v_mfma_f32_16x16x32_bf16 v[48:51], v[180:183], a[104:107], v[48:51]\n\t"
  "ds_read_b64_tr_b16 v[146:147], v250 offset:2816\n\t"
  "v_mfma_f32_16x16x32_bf16 v[56:59], v[184:187], a[96:99], v[56:59]\n\t"
  "ds_read_b64_tr_b16 v[148:149], v250 offset:18944\n\t"
  "v_cvt_pk_bf16_f32 v16, v16, v17\n\t"
  "v_mfma_f32_16x16x32_bf16 v[60:63], v[184:187], a[100:103], v[60:63]\n\t"
  "ds_read_b64_tr_b16 v[150:151], v250 offset:19200\n\t"
  "v_cvt_pk_bf16_f32 v17, v18, v19\n\t"
  "v_mfma_f32_16x16x32_bf16 v[60:63], v[188:191], a[108:111], v[60:63]\n\t"
  "s_waitcnt lgkmcnt(13)\n\t"
  "ds_read_b64_tr_b16 v[152:153], v251 offset:2560\n\t"
  "ds_write_b64 v208, v[16:17]\n\t"
  "v_mfma_f32_16x16x32_bf16 v[56:59], v[188:191], a[104:107], v[56:59]\n\t"
  "s_waitcnt lgkmcnt(13)\n\t"
  "ds_read_b64_tr_b16 v[154:155], v251 offset:2816\n\t"
  "s_waitcnt lgkmcnt(8)\n\t"
  "v_mfma_f32_16x16x32_bf16 v[64:67], v[128:131], a[96:99], v[64:67]\n\t"
  "ds_read_b64_tr_b16 v[156:157], v251 offset:18944\n\t"
  "v_cvt_pk_bf16_f32 v20, v20, v21\n\t"
  "v_mfma_f32_16x16x32_bf16 v[68:71], v[128:131], a[100:103], v[68:71]\n\t"
  "ds_read_b64_tr_b16 v[158:159], v251 offset:19200\n\t"
  "v_cvt_pk_bf16_f32 v21, v22, v23\n\t"
  "v_mfma_f32_16x16x32_bf16 v[68:71], v[132:135], a[108:111], v[68:71]\n\t"
  "ds_read_b64_tr_b16 v[160:161], v250 offset:3072\n\t"
  "ds_write_b64 v208, v[20:21] offset:8192\n\t"
  "v_mfma_f32_16x16x32_bf16 v[64:67], v[132:135], a[104:107], v[64:67]\n\t"
  "ds_read_b64_tr_b16 v[162:163], v250 offset:3328\n\t"
  "v_mfma_f32_16x16x32_bf16 v[72:75], v[136:139], a[96:99], v[72:75]\n\t"
  "ds_read_b64_tr_b16 v[164:165], v250 offset:19456\n\t"
  "v_cvt_pk_bf16_f32 v24, v24, v25\n\t"
  "v_mfma_f32_16x16x32_bf16 v[76:79], v[136:139], a[100:103], v[76:79]\n\t"
  "ds_read_b64_tr_b16 v[166:167], v250 offset:19712\n\t"
  "v_cvt_pk_bf16_f32 v25, v26, v27\n\t"
  "v_mfma_f32_16x16x32_bf16 v[76:79], v[140:143], a[108:111], v[76:79]\n\t"
  "s_waitcnt lgkmcnt(13)\n\t"
  "ds_read_b64_tr_b16 v[168:169], v251 offset:3072\n\t"
  "ds_write_b64 v209, v[24:25]\n\t"
  "v_mfma_f32_16x16x32_bf16 v[72:75], v[140:143], a[104:107], v[72:75]\n\t"
  "s_waitcnt lgkmcnt(13)\n\t"
  "ds_read_b64_tr_b16 v[170:171], v251 offset:3328\n\t"
  "s_waitcnt lgkmcnt(8)\n\t"
  "v_mfma_f32_16x16x32_bf16 v[80:83], v[144:147], a[96:99], v[80:83]\n\t"
  "ds_read_b64_tr_b16 v[172:173], v251 offset:19456\n\t"
  "v_cvt_pk_bf16_f32 v28, v28, v29\n\t"
  "v_mfma_f32_16x16x32_bf16 v[84:87], v[144:147], a[100:103], v[84:87]\n\t"
  "ds_read_b64_tr_b16 v[174:175], v251 offset:19712\n\t"
  "v_cvt_pk_bf16_f32 v29, v30, v31\n\t"
  "v_mfma_f32_16x16x32_bf16 v[84:87], v[148:151], a[108:111], v[84:87]\n\t"
  "ds_read_b64_tr_b16 v[176:177], v250 offset:3584\n\t"
  "ds_write_b64 v209, v[28:29] offset:8192\n\t"
  "v_mfma_f32_16x16x32_bf16 v[80:83], v[148:151], a[104:107], v[80:83]\n\t"
  "ds_read_b64_tr_b16 v[178:179], v250 offset:3840\n\t"
  "v_mfma_f32_16x16x32_bf16 v[88:91], v[152:155], a[96:99], v[88:91]\n\t"
  "ds_read_b64_tr_b16 v[180:181], v250 offset:19968\n\t"
  "v_cvt_pk_bf16_f32 v32, v32, v33\n\t"
  "v_mfma_f32_16x16x32_bf16 v[92:95], v[152:155], a[100:103], v[92:95]\n\t"
  "ds_read_b64_tr_b16 v[182:183], v250 offset:20224\n\t"
  "v_cvt_pk_bf16_f32 v33, v34, v35\n\t"
  "v_mfma_f32_16x16x32_bf16 v[92:95], v[156:159], a[108:111], v[92:95]\n\t"
  "s_waitcnt lgkmcnt(13)\n\t"
  "ds_read_b64_tr_b16 v[184:185], v251 offset:3584\n\t"
  "ds_write_b64 v210, v[32:33]\n\t"
  "v_mfma_f32_16x16x32_bf16 v[88:91], v[156:159], a[104:107], v[88:91]\n\t"
  "s_waitcnt lgkmcnt(13)\n\t"
  "ds_read_b64_tr_b16 v[186:187], v251 offset:3840\n\t"
  "s_waitcnt lgkmcnt(8)\n\t"
  "v_mfma_f32_16x16x32_bf16 v[96:99], v[160:163], a[96:99], v[96:99]\n\t"
  "ds_read_b64_tr_b16 v[188:189], v251 offset:19968\n\t"
  "v_cvt_pk_bf16_f32 v36, v36, v37\n\t"
  "v_mfma_f32_16x16x32_bf16 v[100:103], v[160:163], a[100:103], v[100:103]\n\t"
  "ds_read_b64_tr_b16 v[190:191], v251 offset:20224\n\t"
  "v_cvt_pk_bf16_f32 v37, v38, v39\n\t"
  "v_mfma_f32_16x16x32_bf16 v[100:103], v[164:167], a[108:111], v[100:103]\n\t"
  "ds_write_b64 v210, v[36:37] offset:8192\n\t"
  "v_mfma_f32_16x16x32_bf16 v[96:99], v[164:167], a[104:107], v[96:99]\n\t"
  "v_mfma_f32_16x16x32_bf16 v[104:107], v[168:171], a[96:99], v[104:107]\n\t"
  "v_cvt_pk_bf16_f32 v40, v40, v41\n\t"
  "v_mfma_f32_16x16x32_bf16 v[108:111], v[168:171], a[100:103], v[108:111]\n\t"
  "v_cvt_pk_bf16_f32 v41, v42, v43\n\t"
  "v_mfma_f32_16x16x32_bf16 v[108:111], v[172:175], a[108:111], v[108:111]\n\t"
  "ds_write_b64 v211, v[40:41]\n\t"
  "v_mfma_f32_16x16x32_bf16 v[104:107], v[172:175], a[104:107], v[104:107]\n\t"
  "s_waitcnt lgkmcnt(2)\n\t"
  "v_mfma_f32_16x16x32_bf16 v[112:115], v[176:179], a[96:99], v[112:115]\n\t"
  "v_cvt_pk_bf16_f32 v44, v44, v45\n\t"
  "v_mfma_f32_16x16x32_bf16 v[116:119], v[176:179], a[100:103], v[116:119]\n\t"
  "v_cvt_pk_bf16_f32 v45, v46, v47\n\t"
  "v_mfma_f32_16x16x32_bf16 v[116:119], v[180:183], a[108:111], v[116:119]\n\t"
  "ds_write_b64 v211, v[44:45] offset:8192\n\t"
  "v_mfma_f32_16x16x32_bf16 v[112:115], v[180:183], a[104:107], v[112:115]\n\t"
  "v_mfma_f32_16x16x32_bf16 v[120:123], v[184:187], a[96:99], v[120:123]\n\t"
  "v_cvt_pk_bf16_f32 v48, v48, v49\n\t"
  "v_mfma_f32_16x16x32_bf16 v[124:127], v[184:187], a[100:103], v[124:127]\n\t"
  "v_cvt_pk_bf16_f32 v49, v50, v51\n\t"
  "v_mfma_f32_16x16x32_bf16 v[124:127], v[188:191], a[108:111], v[124:127]\n\t"
  "ds_write_b64 v212, v[48:49]\n\t"
  "v_mfma_f32_16x16x32_bf16 v[120:123], v[188:191], a[104:107], v[120:123]\n\t"
  "v_cvt_pk_bf16_f32 v52, v52, v53\n\t"
  "v_cvt_pk_bf16_f32 v53, v54, v55\n\t"
  "ds_write_b64 v212, v[52:53] offset:8192\n\t"
  "v_cvt_pk_bf16_f32 v56, v56, v57\n\t"
  "v_cvt_pk_bf16_f32 v57, v58, v59\n\t"
  "ds_write_b64 v213, v[56:57]\n\t"
  "v_cvt_pk_bf16_f32 v60, v60, v61\n\t"
  "v_cvt_pk_bf16_f32 v61, v62, v63\n\t"
  "ds_write_b64 v213, v[60:61] offset:8192\n\t"
  "v_cvt_pk_bf16_f32 v64, v64, v65\n\t"
  "v_cvt_pk_bf16_f32 v65, v66, v67\n\t"
  "ds_write_b64 v206, v[64:65] offset:256\n\t"
  "v_cvt_pk_bf16_f32 v68, v68, v69\n\t"
  "v_cvt_pk_bf16_f32 v69, v70, v71\n\t"
  "ds_write_b64 v206, v[68:69] offset:8448\n\t"
  "v_cvt_pk_bf16_f32 v72, v72, v73\n\t"
  "v_cvt_pk_bf16_f32 v73, v74, v75\n\t"
  "ds_write_b64 v207, v[72:73] offset:256\n\t"
  "v_cvt_pk_bf16_f32 v76, v76, v77\n\t"
  "v_cvt_pk_bf16_f32 v77, v78, v79\n\t"
  "ds_write_b64 v207, v[76:77] offset:8448\n\t"
  "v_cvt_pk_bf16_f32 v80, v80, v81\n\t"
  "v_cvt_pk_bf16_f32 v81, v82, v83\n\t"
  "ds_write_b64 v208, v[80:81] offset:256\n\t"
  "v_cvt_pk_bf16_f32 v84, v84, v85\n\t"
  "v_cvt_pk_bf16_f32 v85, v86, v87\n\t"
  "ds_write_b64 v208, v[84:85] offset:8448\n\t"
  "v_cvt_pk_bf16_f32 v88, v88, v89\n\t"
  "v_cvt_pk_bf16_f32 v89, v90, v91\n\t"
  "ds_write_b64 v209, v[88:89] offset:256\n\t"
  "v_cvt_pk_bf16_f32 v92, v92, v93\n\t"
  "v_cvt_pk_bf16_f32 v93, v94, v95\n\t"
  "ds_write_b64 v209, v[92:93] offset:8448\n\t"
  "v_cvt_pk_bf16_f32 v96, v96, v97\n\t"
  "v_cvt_pk_bf16_f32 v97, v98, v99\n\t"
  "s_waitcnt lgkmcnt(13)\n\t"
  "ds_write_b64 v210, v[96:97] offset:256\n\t"
  "v_cvt_pk_bf16_f32 v100, v100, v101\n\t"
  "v_cvt_pk_bf16_f32 v101, v102, v103\n\t"
  "ds_write_b64 v210, v[100:101] offset:8448\n\t"
  "v_cvt_pk_bf16_f32 v104, v104, v105\n\t"
  "v_cvt_pk_bf16_f32 v105, v106, v107\n\t"
  "s_waitcnt lgkmcnt(13)\n\t"
  "ds_write_b64 v211, v[104:105] offset:256\n\t"
  "v_cvt_pk_bf16_f32 v108, v108, v109\n\t"
  "v_cvt_pk_bf16_f32 v109, v110, v111\n\t"
  "ds_write_b64 v211, v[108:109] offset:8448\n\t"
  "v_cvt_pk_bf16_f32 v112, v112, v113\n\t"
  "v_cvt_pk_bf16_f32 v113, v114, v115\n\t"
  "s_waitcnt lgkmcnt(13)\n\t"
  "ds_write_b64 v212, v[112:113] offset:256\n\t"
  "v_cvt_pk_bf16_f32 v116, v116, v117\n\t"
  "v_cvt_pk_bf16_f32 v117, v118, v119\n\t"
  "ds_write_b64 v212, v[116:117] offset:8448\n\t"
  "v_cvt_pk_bf16_f32 v120, v120, v121\n\t"
  "v_cvt_pk_bf16_f32 v121, v122, v123\n\t"
  "s_waitcnt lgkmcnt(13)\n\t"
  "ds_write_b64 v213, v[120:121] offset:256\n\t"
  "v_cvt_pk_bf16_f32 v124, v124, v125\n\t"
  "v_cvt_pk_bf16_f32 v125, v126, v127\n\t"
  "ds_write_b64 v213, v[124:125] offset:8448\n\t"
  "s_mov_b32 s96, 1015808\n\t"
  "s_waitcnt vmcnt(9) lgkmcnt(0)\n\t"
  "s_barrier\n\t"
  "v_mov_b32_e32 v184, v216\n\t"
  "v_mov_b32_e32 v185, v217\n\t"
  "v_mov_b32_e32 v186, v218\n\t"
  "v_xor_b32_e32 v160, 0, v214\n\t"
  "v_xor_b32_e32 v161, 64, v214\n\t"
  "v_xor_b32_e32 v162, 128, v214\n\t"
  "v_xor_b32_e32 v163, 192, v214\n\t"
  "v_xor_b32_e32 v164, 16384, v214\n\t"
  "v_xor_b32_e32 v165, 16448, v214\n\t"
  "v_xor_b32_e32 v166, 16512, v214\n\t"
  "v_xor_b32_e32 v167, 16576, v214\n\t"
  "ds_read_b128 a[128:131], v160\n\t"
  "ds_read_b128 a[144:147], v161\n\t"
  "ds_read_b128 a[160:163], v162\n\t"
  "ds_read_b128 a[176:179], v163\n\t"
  "ds_read_b128 a[180:183], v163 offset:8192\n\t"
  "ds_read_b128 a[164:167], v162 offset:8192\n\t"
  "v_mov_b32_e32 v239, 0\n\t"
  "v_mov_b32_e32 v240, 0\n\t"
  "v_mov_b32_e32 v241, 0\n\t"
  "v_mov_b32_e32 v242, 0\n\t"
  "ds_read_b128 v[192:195], v232\n\t"
  "ds_read_b128 v[196:199], v232 offset:512\n\t"
  "ds_read_b128 v[200:203], v232 offset:1024\n\t"
  "ds_read_b128 v[204:207], v232 offset:1536\n\t"
  "s_waitcnt lgkmcnt(0)\n\t"
  "v_mfma_f32_16x16x32_bf16 v[128:131], v[192:195], a[128:131], -4.0\n\t"
  "ds_read_b128 a[148:151], v161 offset:8192\n\t"
  "ds_read_b128 v[208:211], v232 offset:2048\n\t"
  "v_mfma_f32_16x16x32_bf16 v[128:131], v[196:199], a[144:147], v[128:131]\n\t"
  "ds_read_b128 a[132:135], v160 offset:8192\n\t"
  "ds_read_b128 v[212:215], v232 offset:2560\n\t"
  "v_mfma_f32_16x16x32_bf16 v[128:131], v[200:203], a[160:163], v[128:131]\n\t"
  "ds_read_b128 a[136:139], v164\n\t"
  "s_lshl_b32 s66, s62, 10\n\t"
  "v_mfma_f32_16x16x32_bf16 v[128:131], v[204:207], a[176:179], v[128:131]\n\t"
  "ds_read_b128 a[152:155], v165\n\t"
  "ds_read_b128 v[216:219], v232 offset:3072\n\t"
  "v_mfma_f32_16x16x32_bf16 v[136:139], v[204:207], a[180:183], -4.0\n\t"
  "ds_read_b128 a[168:171], v166\n\t"
  "ds_read_b128 v[220:223], v232 offset:3584\n\t"
  "v_mfma_f32_16x16x32_bf16 v[136:139], v[200:203], a[164:167], v[136:139]\n\t"
  "ds_read_b128 a[184:187], v167\n\t"
  "s_add_u32 m0, s30, 65536\n\t"
  "s_waitcnt lgkmcnt(9)\n\t"
  "v_mfma_f32_16x16x32_bf16 v[136:139], v[196:199], a[148:151], v[136:139]\n\t"
  "ds_read_b128 a[188:191], v167 offset:8192\n\t"
  "buffer_load_dwordx4 v235, s[12:15], s29 offen lds\n\t"
  "s_waitcnt lgkmcnt(8)\n\t"
  "v_mfma_f32_16x16x32_bf16 v[136:139], v[192:195], a[132:135], v[136:139]\n\t"
  "ds_read_b128 a[172:175], v166 offset:8192\n\t"
  "s_add_u32 s66, s66, 110592\n\t"
  "s_waitcnt lgkmcnt(7)\n\t"
  "v_mfma_f32_16x16x32_bf16 v[144:147], v[192:195], a[136:139], -4.0\n\t"
  "ds_read_b128 a[156:159], v165 offset:8192\n\t"
  "v_lshlrev_b32_e32 v246, 4, v254\n\t"
  "s_waitcnt lgkmcnt(7)\n\t"
  "v_mfma_f32_16x16x32_bf16 v[144:147], v[196:199], a[152:155], v[144:147]\n\t"
  "ds_read_b128 a[140:143], v164 offset:8192\n\t"
  "v_add_u32_e32 v250, s66, v246\n\t"
  "s_waitcnt lgkmcnt(6)\n\t"
  "v_mfma_f32_16x16x32_bf16 v[144:147], v[200:203], a[168:171], v[144:147]\n\t"
  "ds_read_b128 a[204:207], v164 offset:8448\n\t"
  "buffer_load_dwordx4 v236, s[12:15], s29 offen offset:1024 lds\n\t"
  "s_waitcnt lgkmcnt(5)\n\t"
  "v_mfma_f32_16x16x32_bf16 v[144:147], v[204:207], a[184:187], v[144:147]\n\t"
  "ds_read_b128 a[220:223], v165 offset:8448\n\t"
  "v_xor_b32_e32 v251, 1024, v250\n\t"
  "s_waitcnt lgkmcnt(5)\n\t"
  "v_mfma_f32_16x16x32_bf16 v[152:155], v[204:207], a[188:191], -4.0\n\t"
  "ds_read_b128 a[236:239], v166 offset:8448\n\t"
  "s_lshl_b32 s66, s62, 14\n\t"
  "s_waitcnt lgkmcnt(5)\n\t"
  "v_mfma_f32_16x16x32_bf16 v[152:155], v[200:203], a[172:175], v[152:155]\n\t"
  "ds_read_b128 a[252:255], v167 offset:8448\n\t"
  "s_add_u32 s66, s66, 65536\n\t"
  "s_waitcnt lgkmcnt(5)\n\t"
  "v_mfma_f32_16x16x32_bf16 v[152:155], v[196:199], a[156:159], v[152:155]\n\t"
  "ds_read_b128 a[248:251], v167 offset:256\n\t"
  "buffer_load_dwordx4 v237, s[12:15], s29 offen offset:2048 lds\n\t"
  "s_waitcnt lgkmcnt(5)\n\t"
  "v_mfma_f32_16x16x32_bf16 v[152:155], v[192:195], a[140:143], v[152:155]\n\t"
  "ds_read_b128 a[232:235], v166 offset:256\n\t"
  "s_waitcnt lgkmcnt(5)\n\t"
  "v_mfma_f32_16x16x32_bf16 v[152:155], v[208:211], a[204:207], v[152:155]\n\t"
  "ds_read_b128 a[216:219], v165 offset:256\n\t"
  "ds_read_b128 v[192:195], v232 offset:8192\n\t"
  "s_waitcnt lgkmcnt(6)\n\t"
  "v_mfma_f32_16x16x32_bf16 v[152:155], v[212:215], a[220:223], v[152:155]\n\t"
  "ds_read_b128 a[200:203], v164 offset:256\n\t"
  "ds_read_b128 v[196:199], v232 offset:8704\n\t"
  "s_waitcnt lgkmcnt(7)\n\t"
  "v_mfma_f32_16x16x32_bf16 v[152:155], v[216:219], a[236:239], v[152:155]\n\t"
  "ds_read_b128 a[196:199], v160 offset:8448\n\t"
  "buffer_load_dwordx4 v238, s[12:15], s29 offen offset:3072 lds\n\t"
  "s_waitcnt lgkmcnt(7)\n\t"
  "v_mfma_f32_16x16x32_bf16 v[152:155], v[220:223], a[252:255], v[152:155]\n\t"
  "ds_read_b128 a[212:215], v161 offset:8448\n\t"
  "ds_read_b128 v[200:203], v232 offset:9216\n\t"
  "s_add_u32 s29, s29, 4096\n\t"
  "s_waitcnt lgkmcnt(8)\n\t"
  "v_mfma_f32_16x16x32_bf16 v[144:147], v[220:223], a[248:251], v[144:147]\n\t"
  "ds_read_b128 a[228:231], v162 offset:8448\n\t"
  "ds_read_b128 v[204:207], v232 offset:9728\n\t"
  "s_waitcnt lgkmcnt(9)\n\t"
  "v_mfma_f32_16x16x32_bf16 v[144:147], v[216:219], a[232:235], v[144:147]\n\t"
  "ds_read_b128 a[244:247], v163 offset:8448\n\t"
  "s_add_u32 m0, s30, 69632\n\t"
  "s_waitcnt lgkmcnt(9)\n\t"
  "v_mfma_f32_16x16x32_bf16 v[144:147], v[212:215], a[216:219], v[144:147]\n\t"
  "ds_read_b128 a[240:243], v163 offset:256\n\t"
  "buffer_load_dwordx4 v235, s[12:15], s29 offen lds\n\t"
  "s_waitcnt lgkmcnt(8)\n\t"
  "v_mfma_f32_16x16x32_bf16 v[144:147], v[208:211], a[200:203], v[144:147]\n\t"
  "ds_read_b128 a[224:227], v162 offset:256\n\t"
  "v_add_u32_e32 v230, s66, v246\n\t"
  "s_waitcnt lgkmcnt(7)\n\t"
  "v_mfma_f32_16x16x32_bf16 v[136:139], v[208:211], a[196:199], v[136:139]\n\t"
  "ds_read_b128 a[208:211], v161 offset:256\n\t"
  "v_xor_b32_e32 v231, 16384, v230\n\t"
  "s_waitcnt lgkmcnt(7)\n\t"
  "v_mfma_f32_16x16x32_bf16 v[136:139], v[212:215], a[212:215], v[136:139]\n\t"
  "ds_read_b128 a[192:195], v160 offset:256\n\t"
  "v_mov_b32_e32 v253, s66\n\t"
  "s_waitcnt lgkmcnt(6)\n\t"
  "v_mfma_f32_16x16x32_bf16 v[136:139], v[216:219], a[228:231], v[136:139]\n\t"
  "buffer_load_dwordx4 v236, s[12:15], s29 offen offset:1024 lds\n\t"
  "s_waitcnt lgkmcnt(4)\n\t"
  "v_mfma_f32_16x16x32_bf16 v[136:139], v[220:223], a[244:247], v[136:139]\n\t"
  "v_and_b32_e32 v246, 3, v254\n\t"
  "v_lshlrev_b32_e32 v246, 14, v246\n\t"
  "s_waitcnt lgkmcnt(3)\n\t"
  "v_mfma_f32_16x16x32_bf16 v[128:131], v[220:223], a[240:243], v[128:131]\n\t"
  "s_lshl_b32 s66, s62, 2\n\t"
  "s_add_u32 s66, s66, 65536\n\t"
  "s_waitcnt lgkmcnt(2)\n\t"
  "v_mfma_f32_16x16x32_bf16 v[128:131], v[216:219], a[224:227], v[128:131]\n\t"
  "v_add_u32_e32 v252, s66, v246\n\t"
  "v_and_b32_e32 v246, 15, v254\n\t"
  "s_waitcnt lgkmcnt(1)\n\t"
  "v_mfma_f32_16x16x32_bf16 v[128:131], v[212:215], a[208:211], v[128:131]\n\t"
  "buffer_load_dwordx4 v237, s[12:15], s29 offen offset:2048 lds\n\t"
  "s_waitcnt lgkmcnt(0)\n\t"
  "v_mfma_f32_16x16x32_bf16 v[128:131], v[208:211], a[192:195], v[128:131]\n\t"
  "v_lshrrev_b32_e32 v248, 4, v254\n\t"
  "v_mfma_f32_16x16x32_bf16 v[132:135], v[192:195], a[128:131], -4.0\n\t"
  "ds_read_b128 v[208:211], v232 offset:10240\n\t"
  "v_lshlrev_b32_e32 v249, 2, v248\n\t"
  "v_mfma_f32_16x16x32_bf16 v[132:135], v[196:199], a[144:147], v[132:135]\n\t"
  "ds_read_b128 v[212:215], v232 offset:10752\n\t"
  "v_sub_u32_e32 v246, v246, v249\n\t"
  "v_mfma_f32_16x16x32_bf16 v[132:135], v[200:203], a[160:163], v[132:135]\n\t"
  "buffer_load_dwordx4 v238, s[12:15], s29 offen offset:3072 lds\n\t"
  "v_mfma_f32_16x16x32_bf16 v[132:135], v[204:207], a[176:179], v[132:135]\n\t"
  "ds_read_b128 v[216:219], v232 offset:11264\n\t"
  "s_add_u32 s29, s29, 28672\n\t"
  "v_mfma_f32_16x16x32_bf16 v[140:143], v[204:207], a[180:183], -4.0\n\t"
  "ds_read_b128 v[220:223], v232 offset:11776\n\t"
  "v_mov_b32_e32 v224, 0\n\t"
  "v_mfma_f32_16x16x32_bf16 v[140:143], v[200:203], a[164:167], v[140:143]\n\t"
  "v_exp_f32_e32 v128, v128\n\t"
  "v_mfma_f32_16x16x32_bf16 v[140:143], v[196:199], a[148:151], v[140:143]\n\t"
  "v_exp_f32_e32 v129, v129\n\t"
  "v_mfma_f32_16x16x32_bf16 v[140:143], v[192:195], a[132:135], v[140:143]\n\t"
  "v_add_f32_e32 v239, v239, v128\n\t"
  "v_mov_b32_e32 v225, 0\n\t"
  "v_mfma_f32_16x16x32_bf16 v[148:151], v[192:195], a[136:139], -4.0\n\t"
  "v_exp_f32_e32 v130, v130\n\t"
  "v_mfma_f32_16x16x32_bf16 v[148:151], v[196:199], a[152:155], v[148:151]\n\t"
  "v_add_f32_e32 v239, v239, v129\n\t"
  "v_cvt_pk_bf16_f32 v128, v128, v129\n\t"
  "v_mfma_f32_16x16x32_bf16 v[148:151], v[200:203], a[168:171], v[148:151]\n\t"
  "v_exp_f32_e32 v131, v131\n\t"
  "v_mfma_f32_16x16x32_bf16 v[148:151], v[204:207], a[184:187], v[148:151]\n\t"
  "v_add_f32_e32 v239, v239, v130\n\t"
  "v_mov_b32_e32 v228, 0\n\t"
  "v_mfma_f32_16x16x32_bf16 v[156:159], v[204:207], a[188:191], -4.0\n\t"
  "v_add_f32_e32 v239, v239, v131\n\t"
  "v_cvt_pk_bf16_f32 v129, v130, v131\n\t"
  "v_mfma_f32_16x16x32_bf16 v[156:159], v[200:203], a[172:175], v[156:159]\n\t"
  "v_exp_f32_e32 v136, v136\n\t"
  "v_mfma_f32_16x16x32_bf16 v[156:159], v[196:199], a[156:159], v[156:159]\n\t"
  "s_waitcnt vmcnt(9) lgkmcnt(0)\n\t"
  "s_barrier\n\t"
  "v_mfma_f32_16x16x32_bf16 v[156:159], v[192:195], a[140:143], v[156:159]\n\t"
  "v_add_f32_e32 v240, v240, v136\n\t"
  "v_mfma_f32_16x16x32_bf16 v[156:159], v[208:211], a[204:207], v[156:159]\n\t"
  "v_exp_f32_e32 v137, v137\n\t"
  "v_mfma_f32_16x16x32_bf16 v[156:159], v[212:215], a[220:223], v[156:159]\n\t"
  "v_mov_b32_e32 v229, 0\n\t"
  "s_add_u32 m0, s30, 0\n\t"
  "v_mfma_f32_16x16x32_bf16 v[156:159], v[216:219], a[236:239], v[156:159]\n\t"
  "buffer_load_dwordx4 v235, s[8:11], s28 offen lds\n\t"
  "v_mfma_f32_16x16x32_bf16 v[156:159], v[220:223], a[252:255], v[156:159]\n\t"
  "v_exp_f32_e32 v138, v138\n\t"
  "v_mfma_f32_16x16x32_bf16 v[148:151], v[220:223], a[248:251], v[148:151]\n\t"
  "v_add_f32_e32 v240, v240, v137\n\t"
  "v_cvt_pk_bf16_f32 v136, v136, v137\n\t"
  "v_mfma_f32_16x16x32_bf16 v[148:151], v[216:219], a[232:235], v[148:151]\n\t"
  "v_exp_f32_e32 v139, v139\n\t"
  "v_mfma_f32_16x16x32_bf16 v[148:151], v[212:215], a[216:219], v[148:151]\n\t"
  "buffer_load_dwordx4 v236, s[8:11], s28 offen offset:1024 lds\n\t"
  "v_mfma_f32_16x16x32_bf16 v[148:151], v[208:211], a[200:203], v[148:151]\n\t"
  "v_add_f32_e32 v240, v240, v138\n\t"
  "v_add_f32_e32 v240, v240, v139\n\t"
  "v_mfma_f32_16x16x32_bf16 v[140:143], v[208:211], a[196:199], v[140:143]\n\t"
  "v_cvt_pk_bf16_f32 v137, v138, v139\n\t"
  "v_mov_b32_e32 v248, 0x3f80\n\t"
  "v_mfma_f32_16x16x32_bf16 v[140:143], v[212:215], a[212:215], v[140:143]\n\t"
  "v_exp_f32_e32 v144, v144\n\t"
  "v_mfma_f32_16x16x32_bf16 v[140:143], v[216:219], a[228:231], v[140:143]\n\t"
  "buffer_load_dwordx4 v237, s[8:11], s28 offen offset:2048 lds\n\t"
  "v_mfma_f32_16x16x32_bf16 v[140:143], v[220:223], a[244:247], v[140:143]\n\t"
  "v_exp_f32_e32 v145, v145\n\t"
  "v_mfma_f32_16x16x32_bf16 v[132:135], v[220:223], a[240:243], v[132:135]\n\t"
  "v_add_f32_e32 v241, v241, v144\n\t"
  "v_mov_b32_e32 v249, 0x3f800000\n\t"
  "v_mfma_f32_16x16x32_bf16 v[132:135], v[216:219], a[224:227], v[132:135]\n\t"
  "v_exp_f32_e32 v146, v146\n\t"
  "v_mfma_f32_16x16x32_bf16 v[132:135], v[212:215], a[208:211], v[132:135]\n\t"
  "buffer_load_dwordx4 v238, s[8:11], s28 offen offset:3072 lds\n\t"
  "v_mfma_f32_16x16x32_bf16 v[132:135], v[208:211], a[192:195], v[132:135]\n\t"
  "s_add_u32 s28, s28, 4096\n\t"
  "v_add_f32_e32 v241, v241, v145\n\t"
  "v_cvt_pk_bf16_f32 v144, v144, v145\n\t"
  "v_exp_f32_e32 v147, v147\n\t"
  "v_add_f32_e32 v241, v241, v146\n\t"
  "v_exp_f32_e32 v152, v152\n\t"
  "v_exp_f32_e32 v153, v153\n\t"
  "v_exp_f32_e32 v154, v154\n\t"
  "v_exp_f32_e32 v155, v155\n\t"
  "ds_write_b64 v186, v[184:185]\n\t"
  "s_waitcnt lgkmcnt(0)\n\t"
  "s_barrier\n\t"
  "s_cmp_ge_u32 s63, 112\n\t"
  "s_cselect_b32 s64, 1, s62\n\t"
  "s_cmp_lg_u32 s64, 0\n\t"
  "s_cbranch_scc1 .Llp\n\t"
  "v_and_b32_e32 v176, 15, v254\n\t"
  "v_lshlrev_b32_e32 v176, 2, v176\n\t"
  "v_add_u32_e32 v177, 131072, v176\n\t"
  "ds_read_b32 v178, v177\n\t"
  "ds_read_b32 v179, v177 offset:64\n\t"
  "ds_read_b32 v180, v177 offset:128\n\t"
  "ds_read_b32 v181, v177 offset:192\n\t"
  "s_lshl_b32 s64, s63, 6\n\t"
  "s_add_u32 s64, s64, 3584\n\t"
  "v_add_u32_e32 v176, s64, v176\n\t"
  "s_waitcnt lgkmcnt(0)\n\t"
  "v_add_f32_e32 v178, v178, v179\n\t"
  "v_add_f32_e32 v180, v180, v181\n\t"
  "v_add_f32_e32 v178, v178, v180\n\t"
  "v_cvt_pk_bf16_f32 v178, v178, v178\n\t"
  "v_and_b32_e32 v178, 0xffff0000, v178\n\t"
  "global_store_dword v176, v178, s[98:99] sc1\n\t"
  ".Llp:\n\t"
  "s_add_u32 m0, s30, 4096\n\t"
  "v_add_f32_e32 v241, v241, v147\n\t"
  "v_cvt_pk_bf16_f32 v145, v146, v147\n\t"
  "v_add_f32_e32 v242, v242, v152\n\t"
  "v_add_f32_e32 v242, v242, v153\n\t"
  "v_cvt_pk_bf16_f32 v152, v152, v153\n\t"
  "v_add_f32_e32 v242, v242, v154\n\t"
  "v_add_f32_e32 v242, v242, v155\n\t"
  "v_cvt_pk_bf16_f32 v153, v154, v155\n\t"
  "buffer_load_dwordx4 v235, s[8:11], s28 offen lds\n\t"
  "v_exp_f32_e32 v132, v132\n\t"
  "v_exp_f32_e32 v133, v133\n\t"
  "v_add_f32_e32 v239, v239, v132\n\t"
  "buffer_load_dwordx4 v236, s[8:11], s28 offen offset:1024 lds\n\t"
  "buffer_load_dwordx4 v237, s[8:11], s28 offen offset:2048 lds\n\t"
  "buffer_load_dwordx4 v238, s[8:11], s28 offen offset:3072 lds\n\t"
  "s_add_u32 s28, s28, 28672\n\t"
  "buffer_load_dword v244, v245, s[24:27], s35 offen\n\t"
  "s_add_u32 s35, s35, s97\n\t"
  "s_min_u32 s35, s35, s96\n\t"
  "ds_read_b128 v[192:195], v232 offset:32768\n\t"
  "ds_read_b128 v[196:199], v232 offset:33280\n\t"
  "ds_read_b128 v[200:203], v232 offset:33792\n\t"
  "ds_read_b128 v[204:207], v232 offset:34304\n\t"
  "s_waitcnt lgkmcnt(0)\n\t"
  "v_mfma_f32_16x16x32_bf16 v[160:163], v[192:195], a[128:131], -4.0\n\t"
  "ds_read_b128 v[208:211], v232 offset:34816\n\t"
  "v_add_f32_e32 v239, v239, v133\n\t"
  "v_mfma_f32_16x16x32_bf16 v[160:163], v[196:199], a[144:147], v[160:163]\n\t"
  "ds_read_b128 v[212:215], v232 offset:35328\n\t"
  "v_cvt_pk_bf16_f32 v130, v132, v133\n\t"
  "v_mfma_f32_16x16x32_bf16 v[160:163], v[200:203], a[160:163], v[160:163]\n\t"
  "v_exp_f32_e32 v134, v134\n\t"
  "v_mfma_f32_16x16x32_bf16 v[160:163], v[204:207], a[176:179], v[160:163]\n\t"
  "ds_read_b128 v[216:219], v232 offset:35840\n\t"
  "v_cmp_eq_u32_e32 vcc, 0, v246\n\t"
  "v_mfma_f32_16x16x32_bf16 v[168:171], v[204:207], a[180:183], -4.0\n\t"
  "ds_read_b128 v[220:223], v232 offset:36352\n\t"
  "v_add_f32_e32 v239, v239, v134\n\t"
  "v_mfma_f32_16x16x32_bf16 v[168:171], v[200:203], a[164:167], v[168:171]\n\t"
  "v_exp_f32_e32 v135, v135\n\t"
  "v_mfma_f32_16x16x32_bf16 v[168:171], v[196:199], a[148:151], v[168:171]\n\t"
  "v_exp_f32_e32 v140, v140\n\t"
  "v_mfma_f32_16x16x32_bf16 v[168:171], v[192:195], a[132:135], v[168:171]\n\t"
  "v_add_f32_e32 v239, v239, v135\n\t"
  "v_cvt_pk_bf16_f32 v131, v134, v135\n\t"
  "v_mfma_f32_16x16x32_bf16 v[176:179], v[192:195], a[136:139], -4.0\n\t"
  "v_exp_f32_e32 v141, v141\n\t"
  "v_mfma_f32_16x16x32_bf16 v[176:179], v[196:199], a[152:155], v[176:179]\n\t"
  "v_add_f32_e32 v240, v240, v140\n\t"
  "s_nop 1\n\t"
  "v_mfma_f32_16x16x32_bf16 v[176:179], v[200:203], a[168:171], v[176:179]\n\t"
  "v_exp_f32_e32 v142, v142\n\t"
  "v_mfma_f32_16x16x32_bf16 v[176:179], v[204:207], a[184:187], v[176:179]\n\t"
  "v_add_f32_e32 v240, v240, v141\n\t"
  "v_cvt_pk_bf16_f32 v138, v140, v141\n\t"
  "v_mfma_f32_16x16x32_bf16 v[184:187], v[204:207], a[188:191], -4.0\n\t"
  "v_exp_f32_e32 v143, v143\n\t"
  "v_mfma_f32_16x16x32_bf16 v[184:187], v[200:203], a[172:175], v[184:187]\n\t"
  "v_add_f32_e32 v240, v240, v142\n\t"
  "v_cndmask_b32_e32 v226, 0, v248, vcc\n\t"
  "v_mfma_f32_16x16x32_bf16 v[184:187], v[196:199], a[156:159], v[184:187]\n\t"
  "v_add_f32_e32 v240, v240, v143\n\t"
  "v_cvt_pk_bf16_f32 v139, v142, v143\n\t"
  "v_mfma_f32_16x16x32_bf16 v[184:187], v[192:195], a[140:143], v[184:187]\n\t"
  "v_cmp_eq_u32_e32 vcc, 1, v246\n\t"
  "s_waitcnt lgkmcnt(0)\n\t"
  "v_mfma_f32_16x16x32_bf16 v[184:187], v[208:211], a[204:207], v[184:187]\n\t"
  "ds_read_b128 v[192:195], v232 offset:40960\n\t"
  "s_nop 1\n\t"
  "v_mfma_f32_16x16x32_bf16 v[184:187], v[212:215], a[220:223], v[184:187]\n\t"
  "ds_read_b128 v[196:199], v232 offset:41472\n\t"
  "v_cndmask_b32_e32 v226, v226, v249, vcc\n\t"
  "v_mfma_f32_16x16x32_bf16 v[184:187], v[216:219], a[236:239], v[184:187]\n\t"
  "v_exp_f32_e32 v148, v148\n\t"
  "v_mfma_f32_16x16x32_bf16 v[184:187], v[220:223], a[252:255], v[184:187]\n\t"
  "ds_read_b128 v[200:203], v232 offset:41984\n\t"
  "v_cmp_eq_u32_e32 vcc, 2, v246\n\t"
  "v_mfma_f32_16x16x32_bf16 v[176:179], v[220:223], a[248:251], v[176:179]\n\t"
  "ds_read_b128 v[204:207], v232 offset:42496\n\t"
  "v_add_f32_e32 v241, v241, v148\n\t"
  "v_mfma_f32_16x16x32_bf16 v[176:179], v[216:219], a[232:235], v[176:179]\n\t"
  "v_exp_f32_e32 v149, v149\n\t"
  "v_mfma_f32_16x16x32_bf16 v[176:179], v[212:215], a[216:219], v[176:179]\n\t"
  "v_exp_f32_e32 v150, v150\n\t"
  "v_mfma_f32_16x16x32_bf16 v[176:179], v[208:211], a[200:203], v[176:179]\n\t"
  "v_add_f32_e32 v241, v241, v149\n\t"
  "v_cvt_pk_bf16_f32 v146, v148, v149\n\t"
  "v_mfma_f32_16x16x32_bf16 v[168:171], v[208:211], a[196:199], v[168:171]\n\t"
  "v_exp_f32_e32 v151, v151\n\t"
  "v_mfma_f32_16x16x32_bf16 v[168:171], v[212:215], a[212:215], v[168:171]\n\t"
  "v_add_f32_e32 v241, v241, v150\n\t"
  "s_nop 1\n\t"
  "v_mfma_f32_16x16x32_bf16 v[168:171], v[216:219], a[228:231], v[168:171]\n\t"
  "v_add_f32_e32 v241, v241, v151\n\t"
  "v_cvt_pk_bf16_f32 v147, v150, v151\n\t"
  "v_mfma_f32_16x16x32_bf16 v[168:171], v[220:223], a[244:247], v[168:171]\n\t"
  "v_exp_f32_e32 v156, v156\n\t"
  "v_mfma_f32_16x16x32_bf16 v[160:163], v[220:223], a[240:243], v[160:163]\n\t"
  "v_exp_f32_e32 v157, v157\n\t"
  "v_mfma_f32_16x16x32_bf16 v[160:163], v[216:219], a[224:227], v[160:163]\n\t"
  "v_add_f32_e32 v242, v242, v156\n\t"
  "v_cndmask_b32_e32 v227, 0, v248, vcc\n\t"
  "v_mfma_f32_16x16x32_bf16 v[160:163], v[212:215], a[208:211], v[160:163]\n\t"
  "v_exp_f32_e32 v158, v158\n\t"
  "v_mfma_f32_16x16x32_bf16 v[160:163], v[208:211], a[192:195], v[160:163]\n\t"
  "v_add_f32_e32 v242, v242, v157\n\t"
  "s_waitcnt lgkmcnt(0)\n\t"
  "v_mfma_f32_16x16x32_bf16 v[164:167], v[192:195], a[128:131], -4.0\n\t"
  "ds_read_b128 v[208:211], v232 offset:43008\n\t"
  "v_cvt_pk_bf16_f32 v154, v156, v157\n\t"
  "v_mfma_f32_16x16x32_bf16 v[164:167], v[196:199], a[144:147], v[164:167]\n\t"
  "ds_read_b128 v[212:215], v232 offset:43520\n\t"
  "v_add_f32_e32 v242, v242, v158\n\t"
  "v_mfma_f32_16x16x32_bf16 v[164:167], v[200:203], a[160:163], v[164:167]\n\t"
  "v_exp_f32_e32 v159, v159\n\t"
  "v_mfma_f32_16x16x32_bf16 v[164:167], v[204:207], a[176:179], v[164:167]\n\t"
  "ds_read_b128 v[216:219], v232 offset:44032\n\t"
  "v_cmp_eq_u32_e32 vcc, 3, v246\n\t"
  "v_mfma_f32_16x16x32_bf16 v[172:175], v[204:207], a[180:183], -4.0\n\t"
  "ds_read_b128 v[220:223], v232 offset:44544\n\t"
  "v_add_f32_e32 v242, v242, v159\n\t"
  "v_mfma_f32_16x16x32_bf16 v[172:175], v[200:203], a[164:167], v[172:175]\n\t"
  "v_cvt_pk_bf16_f32 v155, v158, v159\n\t"
  "s_nop 1\n\t"
  "v_mfma_f32_16x16x32_bf16 v[172:175], v[196:199], a[148:151], v[172:175]\n\t"
  "v_exp_f32_e32 v160, v160\n\t"
  "v_mfma_f32_16x16x32_bf16 v[172:175], v[192:195], a[132:135], v[172:175]\n\t"
  "v_exp_f32_e32 v161, v161\n\t"
  "v_mfma_f32_16x16x32_bf16 v[180:183], v[192:195], a[136:139], -4.0\n\t"
  "v_add_f32_e32 v239, v239, v160\n\t"
  "v_cndmask_b32_e32 v227, v227, v249, vcc\n\t"
  "v_mfma_f32_16x16x32_bf16 v[180:183], v[196:199], a[152:155], v[180:183]\n\t"
  "v_exp_f32_e32 v162, v162\n\t"
  "v_mfma_f32_16x16x32_bf16 v[180:183], v[200:203], a[168:171], v[180:183]\n\t"
  "v_add_f32_e32 v239, v239, v161\n\t"
  "v_cvt_pk_bf16_f32 v160, v160, v161\n\t"
  "v_mfma_f32_16x16x32_bf16 v[180:183], v[204:207], a[184:187], v[180:183]\n\t"
  "v_exp_f32_e32 v163, v163\n\t"
  "v_mfma_f32_16x16x32_bf16 v[188:191], v[204:207], a[188:191], -4.0\n\t"
  "v_add_f32_e32 v239, v239, v162\n\t"
  "s_and_b32 s66, s56, 2\n\t"
  "v_mfma_f32_16x16x32_bf16 v[188:191], v[200:203], a[172:175], v[188:191]\n\t"
  "v_add_f32_e32 v239, v239, v163\n\t"
  "v_cvt_pk_bf16_f32 v161, v162, v163\n\t"
  "v_mfma_f32_16x16x32_bf16 v[188:191], v[196:199], a[156:159], v[188:191]\n\t"
  "s_waitcnt vmcnt(1) lgkmcnt(0)\n\t"
  "s_barrier\n\t"
  "v_mfma_f32_16x16x32_bf16 v[188:191], v[192:195], a[140:143], v[188:191]\n\t"
  "s_cmp_lg_u32 s62, 0\n\t"
  "s_cbranch_scc1 .Larr\n\t"
  "s_lshl_b32 s64, s63, 2\n\t"
  "s_add_u32 s64, s64, 263168\n\t"
  "v_mov_b32_e32 v192, s64\n\t"
  "v_mov_b32_e32 v193, s69\n\t"
  "global_store_dword v192, v193, s[98:99] sc1\n\t"
  ".Larr:\n\t"
  "v_mfma_f32_16x16x32_bf16 v[188:191], v[208:211], a[204:207], v[188:191]\n\t"
  "ds_read_b64_tr_b16 v[192:193], v233\n\t"
  "s_lshl_b32 s66, s66, 3\n\t"
  "v_mfma_f32_16x16x32_bf16 v[188:191], v[212:215], a[220:223], v[188:191]\n\t"
  "ds_read_b64_tr_b16 v[194:195], v233 offset:8192\n\t"
  "s_add_u32 m0, s30, 98304\n\t"
  "v_mfma_f32_16x16x32_bf16 v[188:191], v[216:219], a[236:239], v[188:191]\n\t"
  "buffer_load_dwordx4 v235, s[12:15], s29 offen lds\n\t"
  "v_mfma_f32_16x16x32_bf16 v[188:191], v[220:223], a[252:255], v[188:191]\n\t"
  "ds_read_b64_tr_b16 v[196:197], v234\n\t"
  "v_and_b32_e32 v246, 31, v254\n\t"
  "v_mfma_f32_16x16x32_bf16 v[180:183], v[220:223], a[248:251], v[180:183]\n\t"
  "ds_read_b64_tr_b16 v[198:199], v234 offset:8192\n\t"
  "v_xor_b32_e32 v246, s66, v246\n\t"
  "v_mfma_f32_16x16x32_bf16 v[180:183], v[216:219], a[232:235], v[180:183]\n\t"
  "ds_read_b64_tr_b16 v[200:201], v233 offset:512\n\t"
  "s_lshl_b32 s67, s62, 2\n\t"
  "v_mfma_f32_16x16x32_bf16 v[180:183], v[212:215], a[216:219], v[180:183]\n\t"
  "buffer_load_dwordx4 v236, s[12:15], s29 offen offset:1024 lds\n\t"
  "v_mfma_f32_16x16x32_bf16 v[180:183], v[208:211], a[200:203], v[180:183]\n\t"
  "ds_read_b64_tr_b16 v[202:203], v233 offset:8704\n\t"
  "s_xor_b32 s67, s67, s56\n\t"
  "v_mfma_f32_16x16x32_bf16 v[172:175], v[208:211], a[196:199], v[172:175]\n\t"
  "ds_read_b64_tr_b16 v[204:205], v234 offset:512\n\t"
  "v_lshrrev_b32_e32 v248, 3, v246\n\t"
  "v_mfma_f32_16x16x32_bf16 v[172:175], v[212:215], a[212:215], v[172:175]\n\t"
  "ds_read_b64_tr_b16 v[206:207], v234 offset:8704\n\t"
  "v_xor_b32_e32 v248, s67, v248\n\t"
  "v_mfma_f32_16x16x32_bf16 v[172:175], v[216:219], a[228:231], v[172:175]\n\t"
  "buffer_load_dwordx4 v237, s[12:15], s29 offen offset:2048 lds\n\t"
  "v_mfma_f32_16x16x32_bf16 v[172:175], v[220:223], a[244:247], v[172:175]\n\t"
  "v_exp_f32_e32 v168, v168\n\t"
  "v_mfma_f32_16x16x32_bf16 v[164:167], v[220:223], a[240:243], v[164:167]\n\t"
  "v_exp_f32_e32 v169, v169\n\t"
  "v_mfma_f32_16x16x32_bf16 v[164:167], v[216:219], a[224:227], v[164:167]\n\t"
  "v_add_f32_e32 v240, v240, v168\n\t"
  "v_lshlrev_b32_e32 v248, 2, v248\n\t"
  "v_mfma_f32_16x16x32_bf16 v[164:167], v[212:215], a[208:211], v[164:167]\n\t"
  "buffer_load_dwordx4 v238, s[12:15], s29 offen offset:3072 lds\n\t"
  "v_mfma_f32_16x16x32_bf16 v[164:167], v[208:211], a[192:195], v[164:167]\n\t"
  "s_add_u32 s29, s29, 4096\n\t"
  "s_waitcnt lgkmcnt(0)\n\t"
  "v_mfma_f32_16x16x32_bf16 a[0:3], v[192:195], v[128:131], 0\n\t"
  "ds_read_b64_tr_b16 v[208:209], v233 offset:1024\n\t"
  "v_add_f32_e32 v240, v240, v169\n\t"
  "v_mfma_f32_16x16x32_bf16 a[8:11], v[196:199], v[128:131], 0\n\t"
  "ds_read_b64_tr_b16 v[210:211], v233 offset:9216\n\t"
  "s_add_u32 m0, s30, 102400\n\t"
  "v_mfma_f32_16x16x32_bf16 a[12:15], v[196:199], v[136:139], 0\n\t"
  "buffer_load_dwordx4 v235, s[12:15], s29 offen lds\n\t"
  "v_mfma_f32_16x16x32_bf16 a[4:7], v[192:195], v[136:139], 0\n\t"
  "ds_read_b64_tr_b16 v[212:213], v234 offset:1024\n\t"
  "v_cvt_pk_bf16_f32 v168, v168, v169\n\t"
  "v_mfma_f32_16x16x32_bf16 v[0:3], v[192:195], v[144:147], 0\n\t"
  "ds_read_b64_tr_b16 v[214:215], v234 offset:9216\n\t"
  "v_lshl_add_u32 v243, v246, 6, v248\n\t"
  "v_mfma_f32_16x16x32_bf16 v[4:7], v[196:199], v[144:147], 0\n\t"
  "ds_read_b64_tr_b16 v[216:217], v233 offset:1536\n\t"
  "s_lshl_b32 s58, s61, 13\n\t"
  "v_mfma_f32_16x16x32_bf16 v[12:15], v[196:199], v[152:155], 0\n\t"
  "buffer_load_dwordx4 v236, s[12:15], s29 offen offset:1024 lds\n\t"
  "v_mfma_f32_16x16x32_bf16 v[8:11], v[192:195], v[152:155], 0\n\t"
  "ds_read_b64_tr_b16 v[218:219], v233 offset:9728\n\t"
  "s_lshl_b32 s66, s62, 11\n\t"
  "v_mfma_f32_16x16x32_bf16 v[24:27], v[200:203], v[152:155], 0\n\t"
  "ds_read_b64_tr_b16 v[220:221], v234 offset:1536\n\t"
  "s_add_u32 s58, s58, s66\n\t"
  "v_mfma_f32_16x16x32_bf16 v[28:31], v[204:207], v[152:155], 0\n\t"
  "ds_read_b64_tr_b16 v[222:223], v234 offset:9728\n\t"
  "s_add_u32 s20, s98, 16777216\n\t"
  "s_addc_u32 s21, s99, 0\n\t"
  "v_mfma_f32_16x16x32_bf16 v[20:23], v[204:207], v[144:147], 0\n\t"
  "buffer_load_dwordx4 v237, s[12:15], s29 offen offset:2048 lds\n\t"
  "v_mfma_f32_16x16x32_bf16 v[16:19], v[200:203], v[144:147], 0\n\t"
  "v_exp_f32_e32 v170, v170\n\t"
  "v_mfma_f32_16x16x32_bf16 a[20:23], v[200:203], v[136:139], 0\n\t"
  "v_exp_f32_e32 v171, v171\n\t"
  "v_mfma_f32_16x16x32_bf16 a[28:31], v[204:207], v[136:139], 0\n\t"
  "v_add_f32_e32 v240, v240, v170\n\t"
  "s_and_b32 s21, s21, 0xffff\n\t"
  "v_mfma_f32_16x16x32_bf16 a[24:27], v[204:207], v[128:131], 0\n\t"
  "buffer_load_dwordx4 v238, s[12:15], s29 offen offset:3072 lds\n\t"
  "v_mfma_f32_16x16x32_bf16 a[16:19], v[200:203], v[128:131], 0\n\t"
  "s_add_u32 s29, s29, 28672\n\t"
  "s_waitcnt lgkmcnt(0)\n\t"
  "v_mfma_f32_16x16x32_bf16 a[32:35], v[208:211], v[128:131], 0\n\t"
  "ds_read_b64_tr_b16 v[192:193], v233 offset:2048\n\t"
  "v_add_f32_e32 v240, v240, v171\n\t"
  "v_mfma_f32_16x16x32_bf16 a[40:43], v[212:215], v[128:131], 0\n\t"
  "ds_read_b64_tr_b16 v[194:195], v233 offset:10240\n\t"
  "s_add_u32 m0, s30, 32768\n\t"
  "v_mfma_f32_16x16x32_bf16 a[44:47], v[212:215], v[136:139], 0\n\t"
  "buffer_load_dwordx4 v235, s[8:11], s28 offen lds\n\t"
  "v_mfma_f32_16x16x32_bf16 a[36:39], v[208:211], v[136:139], 0\n\t"
  "ds_read_b64_tr_b16 v[196:197], v234 offset:2048\n\t"
  "v_cvt_pk_bf16_f32 v169, v170, v171\n\t"
  "v_mfma_f32_16x16x32_bf16 v[32:35], v[208:211], v[144:147], 0\n\t"
  "ds_read_b64_tr_b16 v[198:199], v234 offset:10240\n\t"
  "s_mov_b32 s22, 29360128\n\t"
  "v_mfma_f32_16x16x32_bf16 v[36:39], v[212:215], v[144:147], 0\n\t"
  "ds_read_b64_tr_b16 v[200:201], v233 offset:2560\n\t"
  "s_lshl_b32 s66, s61, 4\n\t"
  "v_mfma_f32_16x16x32_bf16 v[44:47], v[212:215], v[152:155], 0\n\t"
  "buffer_load_dwordx4 v236, s[8:11], s28 offen offset:1024 lds\n\t"
  "v_mfma_f32_16x16x32_bf16 v[40:43], v[208:211], v[152:155], 0\n\t"
  "ds_read_b64_tr_b16 v[202:203], v233 offset:10752\n\t"
  "s_lshl_b32 s38, s62, 2\n\t"
  "v_mfma_f32_16x16x32_bf16 v[56:59], v[216:219], v[152:155], 0\n\t"
  "ds_read_b64_tr_b16 v[204:205], v234 offset:2560\n\t"
  "s_add_u32 s66, s66, s38\n\t"
  "v_mfma_f32_16x16x32_bf16 v[60:63], v[220:223], v[152:155], 0\n\t"
  "ds_read_b64_tr_b16 v[206:207], v234 offset:10752\n\t"
  "s_and_b32 s38, s56, 2\n\t"
  "v_mfma_f32_16x16x32_bf16 v[52:55], v[220:223], v[144:147], 0\n\t"
  "buffer_load_dwordx4 v237, s[8:11], s28 offen offset:2048 lds\n\t"
  "v_mfma_f32_16x16x32_bf16 v[48:51], v[216:219], v[144:147], 0\n\t"
  "v_exp_f32_e32 v176, v176\n\t"
  "v_mfma_f32_16x16x32_bf16 a[52:55], v[216:219], v[136:139], 0\n\t"
  "v_exp_f32_e32 v177, v177\n\t"
  "v_mfma_f32_16x16x32_bf16 a[60:63], v[220:223], v[136:139], 0\n\t"
  "v_add_f32_e32 v241, v241, v176\n\t"
  "s_add_u32 s37, s66, s38\n\t"
  "v_mfma_f32_16x16x32_bf16 a[56:59], v[220:223], v[128:131], 0\n\t"
  "buffer_load_dwordx4 v238, s[8:11], s28 offen offset:3072 lds\n\t"
  "v_mfma_f32_16x16x32_bf16 a[48:51], v[216:219], v[128:131], 0\n\t"
  "s_add_u32 s28, s28, 4096\n\t"
  "s_waitcnt lgkmcnt(0)\n\t"
  "v_mfma_f32_16x16x32_bf16 a[64:67], v[192:195], v[128:131], 0\n\t"
  "ds_read_b64_tr_b16 v[208:209], v233 offset:3072\n\t"
  "v_add_f32_e32 v241, v241, v177\n\t"
  "v_mfma_f32_16x16x32_bf16 a[72:75], v[196:199], v[128:131], 0\n\t"
  "ds_read_b64_tr_b16 v[210:211], v233 offset:11264\n\t"
  "s_add_u32 m0, s30, 36864\n\t"
  "v_mfma_f32_16x16x32_bf16 a[76:79], v[196:199], v[136:139], 0\n\t"
  "buffer_load_dwordx4 v235, s[8:11], s28 offen lds\n\t"
  "v_mfma_f32_16x16x32_bf16 a[68:71], v[192:195], v[136:139], 0\n\t"
  "ds_read_b64_tr_b16 v[212:213], v234 offset:3072\n\t"
  "v_cvt_pk_bf16_f32 v176, v176, v177\n\t"
  "v_mfma_f32_16x16x32_bf16 v[64:67], v[192:195], v[144:147], 0\n\t"
  "ds_read_b64_tr_b16 v[214:215], v234 offset:11264\n\t"
  "s_xor_b32 s38, s37, 2\n\t"
  "v_mfma_f32_16x16x32_bf16 v[68:71], v[196:199], v[144:147], 0\n\t"
  "ds_read_b64_tr_b16 v[216:217], v233 offset:3584\n\t"
  "s_mul_i32 s37, s37, 7168\n\t"
  "v_mfma_f32_16x16x32_bf16 v[76:79], v[196:199], v[152:155], 0\n\t"
  "buffer_load_dwordx4 v236, s[8:11], s28 offen offset:1024 lds\n\t"
  "v_mfma_f32_16x16x32_bf16 v[72:75], v[192:195], v[152:155], 0\n\t"
  "ds_read_b64_tr_b16 v[218:219], v233 offset:11776\n\t"
  "s_mul_i32 s38, s38, 7168\n\t"
  "v_mfma_f32_16x16x32_bf16 v[88:91], v[200:203], v[152:155], 0\n\t"
  "ds_read_b64_tr_b16 v[220:221], v234 offset:3584\n\t"
  "v_bfe_u32 v246, v254, 3, 1\n\t"
  "v_mfma_f32_16x16x32_bf16 v[92:95], v[204:207], v[152:155], 0\n\t"
  "ds_read_b64_tr_b16 v[222:223], v234 offset:11776\n\t"
  "v_mul_u32_u24_e32 v249, 7168, v246\n\t"
  "v_mfma_f32_16x16x32_bf16 v[84:87], v[204:207], v[144:147], 0\n\t"
  "buffer_load_dwordx4 v237, s[8:11], s28 offen offset:2048 lds\n\t"
  "v_mfma_f32_16x16x32_bf16 v[80:83], v[200:203], v[144:147], 0\n\t"
  "v_exp_f32_e32 v178, v178\n\t"
  "v_mfma_f32_16x16x32_bf16 a[84:87], v[200:203], v[136:139], 0\n\t"
  "v_exp_f32_e32 v179, v179\n\t"
  "v_mfma_f32_16x16x32_bf16 a[92:95], v[204:207], v[136:139], 0\n\t"
  "v_add_f32_e32 v241, v241, v178\n\t"
  "v_lshrrev_b32_e32 v248, 4, v254\n\t"
  "v_mfma_f32_16x16x32_bf16 a[88:91], v[204:207], v[128:131], 0\n\t"
  "buffer_load_dwordx4 v238, s[8:11], s28 offen offset:3072 lds\n\t"
  "v_mfma_f32_16x16x32_bf16 a[80:83], v[200:203], v[128:131], 0\n\t"
  "s_add_u32 s28, s28, 28672\n\t"
  "s_waitcnt lgkmcnt(0)\n\t"
  "v_mfma_f32_16x16x32_bf16 a[96:99], v[208:211], v[128:131], 0\n\t"
  "ds_read_b128 v[192:195], v232\n\t"
  "v_add_f32_e32 v241, v241, v179\n\t"
  "v_mfma_f32_16x16x32_bf16 a[104:107], v[212:215], v[128:131], 0\n\t"
  "ds_read_b128 v[196:199], v232 offset:512\n\t"
  "v_cvt_pk_bf16_f32 v177, v178, v179\n\t"
  "v_mfma_f32_16x16x32_bf16 a[108:111], v[212:215], v[136:139], 0\n\t"
  "v_lshl_add_u32 v249, v248, 7, v249\n\t"
  "buffer_load_dword v244, v245, s[24:27], s35 offen\n\t"
  "v_mfma_f32_16x16x32_bf16 a[100:103], v[208:211], v[136:139], 0\n\t"
  "ds_read_b128 v[200:203], v232 offset:1024\n\t"
  "s_add_u32 s35, s35, s97\n\t"
  "v_mfma_f32_16x16x32_bf16 v[96:99], v[208:211], v[144:147], 0\n\t"
  "ds_read_b128 v[204:207], v232 offset:1536\n\t"
  "s_min_u32 s35, s35, s96\n\t"
  "v_mfma_f32_16x16x32_bf16 v[100:103], v[212:215], v[144:147], 0\n\t"
  "v_exp_f32_e32 v184, v184\n\t"
  "v_mfma_f32_16x16x32_bf16 v[108:111], v[212:215], v[152:155], 0\n\t"
  "v_exp_f32_e32 v185, v185\n\t"
  "v_mfma_f32_16x16x32_bf16 v[104:107], v[208:211], v[152:155], 0\n\t"
  "v_add_f32_e32 v242, v242, v184\n\t"
  "v_and_b32_e32 v248, 7, v254\n\t"
  "v_mfma_f32_16x16x32_bf16 v[120:123], v[216:219], v[152:155], 0\n\t"
  "v_exp_f32_e32 v186, v186\n\t"
  "v_mfma_f32_16x16x32_bf16 v[124:127], v[220:223], v[152:155], 0\n\t"
  "v_add_f32_e32 v242, v242, v185\n\t"
  "v_cvt_pk_bf16_f32 v184, v184, v185\n\t"
  "v_mfma_f32_16x16x32_bf16 v[116:119], v[220:223], v[144:147], 0\n\t"
  "v_exp_f32_e32 v187, v187\n\t"
  "v_mfma_f32_16x16x32_bf16 v[112:115], v[216:219], v[144:147], 0\n\t"
  "v_add_f32_e32 v242, v242, v186\n\t"
  "v_lshl_add_u32 v249, v248, 4, v249\n\t"
  "v_mfma_f32_16x16x32_bf16 a[116:119], v[216:219], v[136:139], 0\n\t"
  "v_add_f32_e32 v242, v242, v187\n\t"
  "v_cvt_pk_bf16_f32 v185, v186, v187\n\t"
  "v_mfma_f32_16x16x32_bf16 a[124:127], v[220:223], v[136:139], 0\n\t"
  "v_exp_f32_e32 v164, v164\n\t"
  "v_mfma_f32_16x16x32_bf16 a[120:123], v[220:223], v[128:131], 0\n\t"
  "v_exp_f32_e32 v165, v165\n\t"
  "v_mfma_f32_16x16x32_bf16 a[112:115], v[216:219], v[128:131], 0\n\t"
  "v_add_f32_e32 v239, v239, v164\n\t"
  "s_mov_b32 s31, 14\n\t"
  ".p2align 6\n\t"
  ".Lloop:\n\t"
  "s_waitcnt lgkmcnt(0)\n\t"
  "v_mfma_f32_16x16x32_bf16 v[128:131], v[192:195], a[128:131], -4.0\n\t"
  "ds_read_b128 v[208:211], v232 offset:2048\n\t"
  "v_add_f32_e32 v239, v239, v165\n\t"
  "v_mfma_f32_16x16x32_bf16 v[128:131], v[196:199], a[144:147], v[128:131]\n\t"
  "ds_read_b128 v[212:215], v232 offset:2560\n\t"
  "v_cvt_pk_bf16_f32 v162, v164, v165\n\t"
  "v_mfma_f32_16x16x32_bf16 v[128:131], v[200:203], a[160:163], v[128:131]\n\t"
  "v_exp_f32_e32 v166, v166\n\t"
  "v_mfma_f32_16x16x32_bf16 v[128:131], v[204:207], a[176:179], v[128:131]\n\t"
  "ds_read_b128 v[216:219], v232 offset:3072\n\t"
  "v_mfma_f32_16x16x32_bf16 v[136:139], v[204:207], a[180:183], -4.0\n\t"
  "ds_read_b128 v[220:223], v232 offset:3584\n\t"
  "v_add_f32_e32 v239, v239, v166\n\t"
  "v_mfma_f32_16x16x32_bf16 v[136:139], v[200:203], a[164:167], v[136:139]\n\t"
  "v_exp_f32_e32 v167, v167\n\t"
  "v_mfma_f32_16x16x32_bf16 v[136:139], v[196:199], a[148:151], v[136:139]\n\t"
  "v_exp_f32_e32 v172, v172\n\t"
  "v_mfma_f32_16x16x32_bf16 v[136:139], v[192:195], a[132:135], v[136:139]\n\t"
  "v_add_f32_e32 v239, v239, v167\n\t"
  "v_cvt_pk_bf16_f32 v163, v166, v167\n\t"
  "v_mfma_f32_16x16x32_bf16 v[144:147], v[192:195], a[136:139], -4.0\n\t"
  "v_exp_f32_e32 v173, v173\n\t"
  "v_mfma_f32_16x16x32_bf16 v[144:147], v[196:199], a[152:155], v[144:147]\n\t"
  "v_add_f32_e32 v240, v240, v172\n\t"
  "v_mfma_f32_16x16x32_bf16 v[144:147], v[200:203], a[168:171], v[144:147]\n\t"
  "v_exp_f32_e32 v174, v174\n\t"
  "v_mfma_f32_16x16x32_bf16 v[144:147], v[204:207], a[184:187], v[144:147]\n\t"
  "v_add_f32_e32 v240, v240, v173\n\t"
  "v_cvt_pk_bf16_f32 v170, v172, v173\n\t"
  "v_mfma_f32_16x16x32_bf16 v[152:155], v[204:207], a[188:191], -4.0\n\t"
  "v_exp_f32_e32 v175, v175\n\t"
  "v_mfma_f32_16x16x32_bf16 v[152:155], v[200:203], a[172:175], v[152:155]\n\t"
  "v_add_f32_e32 v240, v240, v174\n\t"
  "v_mfma_f32_16x16x32_bf16 v[152:155], v[196:199], a[156:159], v[152:155]\n\t"
  "v_add_f32_e32 v240, v240, v175\n\t"
  "v_cvt_pk_bf16_f32 v171, v174, v175\n\t"
  "v_mfma_f32_16x16x32_bf16 v[152:155], v[192:195], a[140:143], v[152:155]\n\t"
  "s_waitcnt lgkmcnt(0)\n\t"
  "v_mfma_f32_16x16x32_bf16 v[152:155], v[208:211], a[204:207], v[152:155]\n\t"
  "ds_read_b128 v[192:195], v232 offset:8192\n\t"
  "v_mfma_f32_16x16x32_bf16 v[152:155], v[212:215], a[220:223], v[152:155]\n\t"
  "ds_read_b128 v[196:199], v232 offset:8704\n\t"
  "v_mfma_f32_16x16x32_bf16 v[152:155], v[216:219], a[236:239], v[152:155]\n\t"
  "v_exp_f32_e32 v180, v180\n\t"
  "v_mfma_f32_16x16x32_bf16 v[152:155], v[220:223], a[252:255], v[152:155]\n\t"
  "ds_read_b128 v[200:203], v232 offset:9216\n\t"
  "v_mfma_f32_16x16x32_bf16 v[144:147], v[220:223], a[248:251], v[144:147]\n\t"
  "ds_read_b128 v[204:207], v232 offset:9728\n\t"
  "v_add_f32_e32 v241, v241, v180\n\t"
  "v_mfma_f32_16x16x32_bf16 v[144:147], v[216:219], a[232:235], v[144:147]\n\t"
  "v_exp_f32_e32 v181, v181\n\t"
  "v_mfma_f32_16x16x32_bf16 v[144:147], v[212:215], a[216:219], v[144:147]\n\t"
  "v_exp_f32_e32 v182, v182\n\t"
  "v_mfma_f32_16x16x32_bf16 v[144:147], v[208:211], a[200:203], v[144:147]\n\t"
  "v_add_f32_e32 v241, v241, v181\n\t"
  "v_cvt_pk_bf16_f32 v178, v180, v181\n\t"
  "v_mfma_f32_16x16x32_bf16 v[136:139], v[208:211], a[196:199], v[136:139]\n\t"
  "v_exp_f32_e32 v183, v183\n\t"
  "v_mfma_f32_16x16x32_bf16 v[136:139], v[212:215], a[212:215], v[136:139]\n\t"
  "v_add_f32_e32 v241, v241, v182\n\t"
  "v_mfma_f32_16x16x32_bf16 v[136:139], v[216:219], a[228:231], v[136:139]\n\t"
  "v_add_f32_e32 v241, v241, v183\n\t"
  "v_cvt_pk_bf16_f32 v179, v182, v183\n\t"
  "v_mfma_f32_16x16x32_bf16 v[136:139], v[220:223], a[244:247], v[136:139]\n\t"
  "v_exp_f32_e32 v188, v188\n\t"
  "v_mfma_f32_16x16x32_bf16 v[128:131], v[220:223], a[240:243], v[128:131]\n\t"
  "v_exp_f32_e32 v189, v189\n\t"
  "v_mfma_f32_16x16x32_bf16 v[128:131], v[216:219], a[224:227], v[128:131]\n\t"
  "v_add_f32_e32 v242, v242, v188\n\t"
  "v_mfma_f32_16x16x32_bf16 v[128:131], v[212:215], a[208:211], v[128:131]\n\t"
  "v_exp_f32_e32 v190, v190\n\t"
  "v_mfma_f32_16x16x32_bf16 v[128:131], v[208:211], a[192:195], v[128:131]\n\t"
  "v_add_f32_e32 v242, v242, v189\n\t"
  "s_waitcnt lgkmcnt(0)\n\t"
  "v_mfma_f32_16x16x32_bf16 v[132:135], v[192:195], a[128:131], -4.0\n\t"
  "ds_read_b128 v[208:211], v232 offset:10240\n\t"
  "v_cvt_pk_bf16_f32 v186, v188, v189\n\t"
  "v_mfma_f32_16x16x32_bf16 v[132:135], v[196:199], a[144:147], v[132:135]\n\t"
  "ds_read_b128 v[212:215], v232 offset:10752\n\t"
  "v_add_f32_e32 v242, v242, v190\n\t"
  "v_mfma_f32_16x16x32_bf16 v[132:135], v[200:203], a[160:163], v[132:135]\n\t"
  "v_exp_f32_e32 v191, v191\n\t"
  "v_mfma_f32_16x16x32_bf16 v[132:135], v[204:207], a[176:179], v[132:135]\n\t"
  "ds_read_b128 v[216:219], v232 offset:11264\n\t"
  "v_mfma_f32_16x16x32_bf16 v[140:143], v[204:207], a[180:183], -4.0\n\t"
  "ds_read_b128 v[220:223], v232 offset:11776\n\t"
  "v_add_f32_e32 v242, v242, v191\n\t"
  "v_mfma_f32_16x16x32_bf16 v[140:143], v[200:203], a[164:167], v[140:143]\n\t"
  "v_cvt_pk_bf16_f32 v187, v190, v191\n\t"
  "v_mfma_f32_16x16x32_bf16 v[140:143], v[196:199], a[148:151], v[140:143]\n\t"
  "v_exp_f32_e32 v128, v128\n\t"
  "v_mfma_f32_16x16x32_bf16 v[140:143], v[192:195], a[132:135], v[140:143]\n\t"
  "v_exp_f32_e32 v129, v129\n\t"
  "v_mfma_f32_16x16x32_bf16 v[148:151], v[192:195], a[136:139], -4.0\n\t"
  "v_add_f32_e32 v239, v239, v128\n\t"
  "v_mfma_f32_16x16x32_bf16 v[148:151], v[196:199], a[152:155], v[148:151]\n\t"
  "v_exp_f32_e32 v130, v130\n\t"
  "v_mfma_f32_16x16x32_bf16 v[148:151], v[200:203], a[168:171], v[148:151]\n\t"
  "v_add_f32_e32 v239, v239, v129\n\t"
  "v_cvt_pk_bf16_f32 v128, v128, v129\n\t"
  "v_mfma_f32_16x16x32_bf16 v[148:151], v[204:207], a[184:187], v[148:151]\n\t"
  "v_exp_f32_e32 v131, v131\n\t"
  "v_mfma_f32_16x16x32_bf16 v[156:159], v[204:207], a[188:191], -4.0\n\t"
  "v_add_f32_e32 v239, v239, v130\n\t"
  "v_mfma_f32_16x16x32_bf16 v[156:159], v[200:203], a[172:175], v[156:159]\n\t"
  "v_add_f32_e32 v239, v239, v131\n\t"
  "v_cvt_pk_bf16_f32 v129, v130, v131\n\t"
  "v_mfma_f32_16x16x32_bf16 v[156:159], v[196:199], a[156:159], v[156:159]\n\t"
  "s_waitcnt vmcnt(1) lgkmcnt(0)\n\t"
  "s_barrier\n\t"
  "v_mfma_f32_16x16x32_bf16 v[156:159], v[192:195], a[140:143], v[156:159]\n\t"
  "v_mfma_f32_16x16x32_bf16 v[156:159], v[208:211], a[204:207], v[156:159]\n\t"
  "ds_read_b64_tr_b16 v[192:193], v233 offset:32768\n\t"
  "v_mfma_f32_16x16x32_bf16 v[156:159], v[212:215], a[220:223], v[156:159]\n\t"
  "ds_read_b64_tr_b16 v[194:195], v233 offset:40960\n\t"
  "s_add_u32 m0, s30, 65536\n\t"
  "v_mfma_f32_16x16x32_bf16 v[156:159], v[216:219], a[236:239], v[156:159]\n\t"
  "buffer_load_dwordx4 v235, s[12:15], s29 offen lds\n\t"
  "v_mfma_f32_16x16x32_bf16 v[156:159], v[220:223], a[252:255], v[156:159]\n\t"
  "ds_read_b64_tr_b16 v[196:197], v234 offset:32768\n\t"
  "v_mfma_f32_16x16x32_bf16 v[148:151], v[220:223], a[248:251], v[148:151]\n\t"
  "ds_read_b64_tr_b16 v[198:199], v234 offset:40960\n\t"
  "v_mfma_f32_16x16x32_bf16 v[148:151], v[216:219], a[232:235], v[148:151]\n\t"
  "ds_read_b64_tr_b16 v[200:201], v233 offset:33280\n\t"
  "v_mfma_f32_16x16x32_bf16 v[148:151], v[212:215], a[216:219], v[148:151]\n\t"
  "buffer_load_dwordx4 v236, s[12:15], s29 offen offset:1024 lds\n\t"
  "v_mfma_f32_16x16x32_bf16 v[148:151], v[208:211], a[200:203], v[148:151]\n\t"
  "ds_read_b64_tr_b16 v[202:203], v233 offset:41472\n\t"
  "v_mfma_f32_16x16x32_bf16 v[140:143], v[208:211], a[196:199], v[140:143]\n\t"
  "ds_read_b64_tr_b16 v[204:205], v234 offset:33280\n\t"
  "v_mfma_f32_16x16x32_bf16 v[140:143], v[212:215], a[212:215], v[140:143]\n\t"
  "ds_read_b64_tr_b16 v[206:207], v234 offset:41472\n\t"
  "v_mfma_f32_16x16x32_bf16 v[140:143], v[216:219], a[228:231], v[140:143]\n\t"
  "buffer_load_dwordx4 v237, s[12:15], s29 offen offset:2048 lds\n\t"
  "v_mfma_f32_16x16x32_bf16 v[140:143], v[220:223], a[244:247], v[140:143]\n\t"
  "v_exp_f32_e32 v136, v136\n\t"
  "v_mfma_f32_16x16x32_bf16 v[132:135], v[220:223], a[240:243], v[132:135]\n\t"
  "v_exp_f32_e32 v137, v137\n\t"
  "v_mfma_f32_16x16x32_bf16 v[132:135], v[216:219], a[224:227], v[132:135]\n\t"
  "v_add_f32_e32 v240, v240, v136\n\t"
  "v_mfma_f32_16x16x32_bf16 v[132:135], v[212:215], a[208:211], v[132:135]\n\t"
  "buffer_load_dwordx4 v238, s[12:15], s29 offen offset:3072 lds\n\t"
  "v_mfma_f32_16x16x32_bf16 v[132:135], v[208:211], a[192:195], v[132:135]\n\t"
  "s_add_u32 s29, s29, 4096\n\t"
  "s_waitcnt lgkmcnt(0)\n\t"
  "v_mfma_f32_16x16x32_bf16 a[0:3], v[192:195], v[160:163], a[0:3]\n\t"
  "ds_read_b64_tr_b16 v[208:209], v233 offset:33792\n\t"
  "v_add_f32_e32 v240, v240, v137\n\t"
  "v_mfma_f32_16x16x32_bf16 a[8:11], v[196:199], v[160:163], a[8:11]\n\t"
  "ds_read_b64_tr_b16 v[210:211], v233 offset:41984\n\t"
  "s_add_u32 m0, s30, 69632\n\t"
  "v_mfma_f32_16x16x32_bf16 a[12:15], v[196:199], v[168:171], a[12:15]\n\t"
  "buffer_load_dwordx4 v235, s[12:15], s29 offen lds\n\t"
  "v_mfma_f32_16x16x32_bf16 a[4:7], v[192:195], v[168:171], a[4:7]\n\t"
  "ds_read_b64_tr_b16 v[212:213], v234 offset:33792\n\t"
  "v_cvt_pk_bf16_f32 v136, v136, v137\n\t"
  "v_mfma_f32_16x16x32_bf16 v[0:3], v[192:195], v[176:179], v[0:3]\n\t"
  "ds_read_b64_tr_b16 v[214:215], v234 offset:41984\n\t"
  "v_mfma_f32_16x16x32_bf16 v[4:7], v[196:199], v[176:179], v[4:7]\n\t"
  "ds_read_b64_tr_b16 v[216:217], v233 offset:34304\n\t"
  "v_mfma_f32_16x16x32_bf16 v[12:15], v[196:199], v[184:187], v[12:15]\n\t"
  "buffer_load_dwordx4 v236, s[12:15], s29 offen offset:1024 lds\n\t"
  "v_mfma_f32_16x16x32_bf16 v[8:11], v[192:195], v[184:187], v[8:11]\n\t"
  "ds_read_b64_tr_b16 v[218:219], v233 offset:42496\n\t"
  "v_mfma_f32_16x16x32_bf16 v[24:27], v[200:203], v[184:187], v[24:27]\n\t"
  "ds_read_b64_tr_b16 v[220:221], v234 offset:34304\n\t"
  "v_mfma_f32_16x16x32_bf16 v[28:31], v[204:207], v[184:187], v[28:31]\n\t"
  "ds_read_b64_tr_b16 v[222:223], v234 offset:42496\n\t"
  "v_mfma_f32_16x16x32_bf16 v[20:23], v[204:207], v[176:179], v[20:23]\n\t"
  "buffer_load_dwordx4 v237, s[12:15], s29 offen offset:2048 lds\n\t"
  "v_mfma_f32_16x16x32_bf16 v[16:19], v[200:203], v[176:179], v[16:19]\n\t"
  "v_exp_f32_e32 v138, v138\n\t"
  "v_mfma_f32_16x16x32_bf16 a[20:23], v[200:203], v[168:171], a[20:23]\n\t"
  "v_exp_f32_e32 v139, v139\n\t"
  "v_mfma_f32_16x16x32_bf16 a[28:31], v[204:207], v[168:171], a[28:31]\n\t"
  "v_add_f32_e32 v240, v240, v138\n\t"
  "v_mfma_f32_16x16x32_bf16 a[24:27], v[204:207], v[160:163], a[24:27]\n\t"
  "buffer_load_dwordx4 v238, s[12:15], s29 offen offset:3072 lds\n\t"
  "v_mfma_f32_16x16x32_bf16 a[16:19], v[200:203], v[160:163], a[16:19]\n\t"
  "s_add_u32 s29, s29, 28672\n\t"
  "s_waitcnt lgkmcnt(0)\n\t"
  "v_mfma_f32_16x16x32_bf16 a[32:35], v[208:211], v[160:163], a[32:35]\n\t"
  "ds_read_b64_tr_b16 v[192:193], v233 offset:34816\n\t"
  "v_add_f32_e32 v240, v240, v139\n\t"
  "v_mfma_f32_16x16x32_bf16 a[40:43], v[212:215], v[160:163], a[40:43]\n\t"
  "ds_read_b64_tr_b16 v[194:195], v233 offset:43008\n\t"
  "s_add_u32 m0, s30, 0\n\t"
  "v_mfma_f32_16x16x32_bf16 a[44:47], v[212:215], v[168:171], a[44:47]\n\t"
  "buffer_load_dwordx4 v235, s[8:11], s28 offen lds\n\t"
  "v_mfma_f32_16x16x32_bf16 a[36:39], v[208:211], v[168:171], a[36:39]\n\t"
  "ds_read_b64_tr_b16 v[196:197], v234 offset:34816\n\t"
  "v_cvt_pk_bf16_f32 v137, v138, v139\n\t"
  "v_mfma_f32_16x16x32_bf16 v[32:35], v[208:211], v[176:179], v[32:35]\n\t"
  "ds_read_b64_tr_b16 v[198:199], v234 offset:43008\n\t"
  "v_mfma_f32_16x16x32_bf16 v[36:39], v[212:215], v[176:179], v[36:39]\n\t"
  "ds_read_b64_tr_b16 v[200:201], v233 offset:35328\n\t"
  "v_mfma_f32_16x16x32_bf16 v[44:47], v[212:215], v[184:187], v[44:47]\n\t"
  "buffer_load_dwordx4 v236, s[8:11], s28 offen offset:1024 lds\n\t"
  "v_mfma_f32_16x16x32_bf16 v[40:43], v[208:211], v[184:187], v[40:43]\n\t"
  "ds_read_b64_tr_b16 v[202:203], v233 offset:43520\n\t"
  "v_mfma_f32_16x16x32_bf16 v[56:59], v[216:219], v[184:187], v[56:59]\n\t"
  "ds_read_b64_tr_b16 v[204:205], v234 offset:35328\n\t"
  "v_mfma_f32_16x16x32_bf16 v[60:63], v[220:223], v[184:187], v[60:63]\n\t"
  "ds_read_b64_tr_b16 v[206:207], v234 offset:43520\n\t"
  "v_mfma_f32_16x16x32_bf16 v[52:55], v[220:223], v[176:179], v[52:55]\n\t"
  "buffer_load_dwordx4 v237, s[8:11], s28 offen offset:2048 lds\n\t"
  "v_mfma_f32_16x16x32_bf16 v[48:51], v[216:219], v[176:179], v[48:51]\n\t"
  "v_exp_f32_e32 v144, v144\n\t"
  "v_mfma_f32_16x16x32_bf16 a[52:55], v[216:219], v[168:171], a[52:55]\n\t"
  "v_exp_f32_e32 v145, v145\n\t"
  "v_mfma_f32_16x16x32_bf16 a[60:63], v[220:223], v[168:171], a[60:63]\n\t"
  "v_add_f32_e32 v241, v241, v144\n\t"
  "v_mfma_f32_16x16x32_bf16 a[56:59], v[220:223], v[160:163], a[56:59]\n\t"
  "buffer_load_dwordx4 v238, s[8:11], s28 offen offset:3072 lds\n\t"
  "v_mfma_f32_16x16x32_bf16 a[48:51], v[216:219], v[160:163], a[48:51]\n\t"
  "s_add_u32 s28, s28, 4096\n\t"
  "s_waitcnt lgkmcnt(0)\n\t"
  "v_mfma_f32_16x16x32_bf16 a[64:67], v[192:195], v[160:163], a[64:67]\n\t"
  "ds_read_b64_tr_b16 v[208:209], v233 offset:35840\n\t"
  "v_add_f32_e32 v241, v241, v145\n\t"
  "v_mfma_f32_16x16x32_bf16 a[72:75], v[196:199], v[160:163], a[72:75]\n\t"
  "ds_read_b64_tr_b16 v[210:211], v233 offset:44032\n\t"
  "s_add_u32 m0, s30, 4096\n\t"
  "v_mfma_f32_16x16x32_bf16 a[76:79], v[196:199], v[168:171], a[76:79]\n\t"
  "buffer_load_dwordx4 v235, s[8:11], s28 offen lds\n\t"
  "v_mfma_f32_16x16x32_bf16 a[68:71], v[192:195], v[168:171], a[68:71]\n\t"
  "ds_read_b64_tr_b16 v[212:213], v234 offset:35840\n\t"
  "v_cvt_pk_bf16_f32 v144, v144, v145\n\t"
  "v_mfma_f32_16x16x32_bf16 v[64:67], v[192:195], v[176:179], v[64:67]\n\t"
  "ds_read_b64_tr_b16 v[214:215], v234 offset:44032\n\t"
  "v_mfma_f32_16x16x32_bf16 v[68:71], v[196:199], v[176:179], v[68:71]\n\t"
  "ds_read_b64_tr_b16 v[216:217], v233 offset:36352\n\t"
  "v_mfma_f32_16x16x32_bf16 v[76:79], v[196:199], v[184:187], v[76:79]\n\t"
  "buffer_load_dwordx4 v236, s[8:11], s28 offen offset:1024 lds\n\t"
  "v_mfma_f32_16x16x32_bf16 v[72:75], v[192:195], v[184:187], v[72:75]\n\t"
  "ds_read_b64_tr_b16 v[218:219], v233 offset:44544\n\t"
  "v_mfma_f32_16x16x32_bf16 v[88:91], v[200:203], v[184:187], v[88:91]\n\t"
  "ds_read_b64_tr_b16 v[220:221], v234 offset:36352\n\t"
  "v_mfma_f32_16x16x32_bf16 v[92:95], v[204:207], v[184:187], v[92:95]\n\t"
  "ds_read_b64_tr_b16 v[222:223], v234 offset:44544\n\t"
  "v_mfma_f32_16x16x32_bf16 v[84:87], v[204:207], v[176:179], v[84:87]\n\t"
  "buffer_load_dwordx4 v237, s[8:11], s28 offen offset:2048 lds\n\t"
  "v_mfma_f32_16x16x32_bf16 v[80:83], v[200:203], v[176:179], v[80:83]\n\t"
  "v_exp_f32_e32 v146, v146\n\t"
  "v_mfma_f32_16x16x32_bf16 a[84:87], v[200:203], v[168:171], a[84:87]\n\t"
  "v_exp_f32_e32 v147, v147\n\t"
  "v_mfma_f32_16x16x32_bf16 a[92:95], v[204:207], v[168:171], a[92:95]\n\t"
  "v_add_f32_e32 v241, v241, v146\n\t"
  "v_mfma_f32_16x16x32_bf16 a[88:91], v[204:207], v[160:163], a[88:91]\n\t"
  "buffer_load_dwordx4 v238, s[8:11], s28 offen offset:3072 lds\n\t"
  "v_mfma_f32_16x16x32_bf16 a[80:83], v[200:203], v[160:163], a[80:83]\n\t"
  "s_add_u32 s28, s28, 28672\n\t"
  "s_waitcnt lgkmcnt(0)\n\t"
  "v_mfma_f32_16x16x32_bf16 a[96:99], v[208:211], v[160:163], a[96:99]\n\t"
  "ds_read_b128 v[192:195], v232 offset:32768\n\t"
  "v_add_f32_e32 v241, v241, v147\n\t"
  "v_mfma_f32_16x16x32_bf16 a[104:107], v[212:215], v[160:163], a[104:107]\n\t"
  "ds_read_b128 v[196:199], v232 offset:33280\n\t"
  "v_cvt_pk_bf16_f32 v145, v146, v147\n\t"
  "v_mfma_f32_16x16x32_bf16 a[108:111], v[212:215], v[168:171], a[108:111]\n\t"
  "buffer_load_dword v244, v245, s[24:27], s35 offen\n\t"
  "v_mfma_f32_16x16x32_bf16 a[100:103], v[208:211], v[168:171], a[100:103]\n\t"
  "ds_read_b128 v[200:203], v232 offset:33792\n\t"
  "s_add_u32 s35, s35, s97\n\t"
  "v_mfma_f32_16x16x32_bf16 v[96:99], v[208:211], v[176:179], v[96:99]\n\t"
  "ds_read_b128 v[204:207], v232 offset:34304\n\t"
  "s_min_u32 s35, s35, s96\n\t"
  "v_mfma_f32_16x16x32_bf16 v[100:103], v[212:215], v[176:179], v[100:103]\n\t"
  "v_exp_f32_e32 v152, v152\n\t"
  "v_mfma_f32_16x16x32_bf16 v[108:111], v[212:215], v[184:187], v[108:111]\n\t"
  "v_exp_f32_e32 v153, v153\n\t"
  "v_mfma_f32_16x16x32_bf16 v[104:107], v[208:211], v[184:187], v[104:107]\n\t"
  "v_add_f32_e32 v242, v242, v152\n\t"
  "v_mfma_f32_16x16x32_bf16 v[120:123], v[216:219], v[184:187], v[120:123]\n\t"
  "v_exp_f32_e32 v154, v154\n\t"
  "v_mfma_f32_16x16x32_bf16 v[124:127], v[220:223], v[184:187], v[124:127]\n\t"
  "v_add_f32_e32 v242, v242, v153\n\t"
  "v_cvt_pk_bf16_f32 v152, v152, v153\n\t"
  "v_mfma_f32_16x16x32_bf16 v[116:119], v[220:223], v[176:179], v[116:119]\n\t"
  "v_exp_f32_e32 v155, v155\n\t"
  "v_mfma_f32_16x16x32_bf16 v[112:115], v[216:219], v[176:179], v[112:115]\n\t"
  "v_add_f32_e32 v242, v242, v154\n\t"
  "v_mfma_f32_16x16x32_bf16 a[116:119], v[216:219], v[168:171], a[116:119]\n\t"
  "v_add_f32_e32 v242, v242, v155\n\t"
  "v_cvt_pk_bf16_f32 v153, v154, v155\n\t"
  "v_mfma_f32_16x16x32_bf16 a[124:127], v[220:223], v[168:171], a[124:127]\n\t"
  "v_exp_f32_e32 v132, v132\n\t"
  "v_mfma_f32_16x16x32_bf16 a[120:123], v[220:223], v[160:163], a[120:123]\n\t"
  "v_exp_f32_e32 v133, v133\n\t"
  "v_mfma_f32_16x16x32_bf16 a[112:115], v[216:219], v[160:163], a[112:115]\n\t"
  "v_add_f32_e32 v239, v239, v132\n\t"
  "s_waitcnt lgkmcnt(0)\n\t"
  "v_mfma_f32_16x16x32_bf16 v[160:163], v[192:195], a[128:131], -4.0\n\t"
  "ds_read_b128 v[208:211], v232 offset:34816\n\t"
  "v_add_f32_e32 v239, v239, v133\n\t"
  "v_mfma_f32_16x16x32_bf16 v[160:163], v[196:199], a[144:147], v[160:163]\n\t"
  "ds_read_b128 v[212:215], v232 offset:35328\n\t"
  "v_cvt_pk_bf16_f32 v130, v132, v133\n\t"
  "v_mfma_f32_16x16x32_bf16 v[160:163], v[200:203], a[160:163], v[160:163]\n\t"
  "v_exp_f32_e32 v134, v134\n\t"
  "v_mfma_f32_16x16x32_bf16 v[160:163], v[204:207], a[176:179], v[160:163]\n\t"
  "ds_read_b128 v[216:219], v232 offset:35840\n\t"
  "v_mfma_f32_16x16x32_bf16 v[168:171], v[204:207], a[180:183], -4.0\n\t"
  "ds_read_b128 v[220:223], v232 offset:36352\n\t"
  "v_add_f32_e32 v239, v239, v134\n\t"
  "v_mfma_f32_16x16x32_bf16 v[168:171], v[200:203], a[164:167], v[168:171]\n\t"
  "v_exp_f32_e32 v135, v135\n\t"
  "v_mfma_f32_16x16x32_bf16 v[168:171], v[196:199], a[148:151], v[168:171]\n\t"
  "v_exp_f32_e32 v140, v140\n\t"
  "v_mfma_f32_16x16x32_bf16 v[168:171], v[192:195], a[132:135], v[168:171]\n\t"
  "v_add_f32_e32 v239, v239, v135\n\t"
  "v_cvt_pk_bf16_f32 v131, v134, v135\n\t"
  "v_mfma_f32_16x16x32_bf16 v[176:179], v[192:195], a[136:139], -4.0\n\t"
  "v_exp_f32_e32 v141, v141\n\t"
  "v_mfma_f32_16x16x32_bf16 v[176:179], v[196:199], a[152:155], v[176:179]\n\t"
  "v_add_f32_e32 v240, v240, v140\n\t"
  "v_mfma_f32_16x16x32_bf16 v[176:179], v[200:203], a[168:171], v[176:179]\n\t"
  "v_exp_f32_e32 v142, v142\n\t"
  "v_mfma_f32_16x16x32_bf16 v[176:179], v[204:207], a[184:187], v[176:179]\n\t"
  "v_add_f32_e32 v240, v240, v141\n\t"
  "v_cvt_pk_bf16_f32 v138, v140, v141\n\t"
  "v_mfma_f32_16x16x32_bf16 v[184:187], v[204:207], a[188:191], -4.0\n\t"
  "v_exp_f32_e32 v143, v143\n\t"
  "v_mfma_f32_16x16x32_bf16 v[184:187], v[200:203], a[172:175], v[184:187]\n\t"
  "v_add_f32_e32 v240, v240, v142\n\t"
  "v_mfma_f32_16x16x32_bf16 v[184:187], v[196:199], a[156:159], v[184:187]\n\t"
  "v_add_f32_e32 v240, v240, v143\n\t"
  "v_cvt_pk_bf16_f32 v139, v142, v143\n\t"
  "v_mfma_f32_16x16x32_bf16 v[184:187], v[192:195], a[140:143], v[184:187]\n\t"
  "s_waitcnt lgkmcnt(0)\n\t"
  "v_mfma_f32_16x16x32_bf16 v[184:187], v[208:211], a[204:207], v[184:187]\n\t"
  "ds_read_b128 v[192:195], v232 offset:40960\n\t"
  "v_mfma_f32_16x16x32_bf16 v[184:187], v[212:215], a[220:223], v[184:187]\n\t"
  "ds_read_b128 v[196:199], v232 offset:41472\n\t"
  "v_mfma_f32_16x16x32_bf16 v[184:187], v[216:219], a[236:239], v[184:187]\n\t"
  "v_exp_f32_e32 v148, v148\n\t"
  "v_mfma_f32_16x16x32_bf16 v[184:187], v[220:223], a[252:255], v[184:187]\n\t"
  "ds_read_b128 v[200:203], v232 offset:41984\n\t"
  "v_mfma_f32_16x16x32_bf16 v[176:179], v[220:223], a[248:251], v[176:179]\n\t"
  "ds_read_b128 v[204:207], v232 offset:42496\n\t"
  "v_add_f32_e32 v241, v241, v148\n\t"
  "v_mfma_f32_16x16x32_bf16 v[176:179], v[216:219], a[232:235], v[176:179]\n\t"
  "v_exp_f32_e32 v149, v149\n\t"
  "v_mfma_f32_16x16x32_bf16 v[176:179], v[212:215], a[216:219], v[176:179]\n\t"
  "v_exp_f32_e32 v150, v150\n\t"
  "v_mfma_f32_16x16x32_bf16 v[176:179], v[208:211], a[200:203], v[176:179]\n\t"
  "v_add_f32_e32 v241, v241, v149\n\t"
  "v_cvt_pk_bf16_f32 v146, v148, v149\n\t"
  "v_mfma_f32_16x16x32_bf16 v[168:171], v[208:211], a[196:199], v[168:171]\n\t"
  "v_exp_f32_e32 v151, v151\n\t"
  "v_mfma_f32_16x16x32_bf16 v[168:171], v[212:215], a[212:215], v[168:171]\n\t"
  "v_add_f32_e32 v241, v241, v150\n\t"
  "v_mfma_f32_16x16x32_bf16 v[168:171], v[216:219], a[228:231], v[168:171]\n\t"
  "v_add_f32_e32 v241, v241, v151\n\t"
  "v_cvt_pk_bf16_f32 v147, v150, v151\n\t"
  "v_mfma_f32_16x16x32_bf16 v[168:171], v[220:223], a[244:247], v[168:171]\n\t"
  "v_exp_f32_e32 v156, v156\n\t"
  "v_mfma_f32_16x16x32_bf16 v[160:163], v[220:223], a[240:243], v[160:163]\n\t"
  "v_exp_f32_e32 v157, v157\n\t"
  "v_mfma_f32_16x16x32_bf16 v[160:163], v[216:219], a[224:227], v[160:163]\n\t"
  "v_add_f32_e32 v242, v242, v156\n\t"
  "v_mfma_f32_16x16x32_bf16 v[160:163], v[212:215], a[208:211], v[160:163]\n\t"
  "v_exp_f32_e32 v158, v158\n\t"
  "v_mfma_f32_16x16x32_bf16 v[160:163], v[208:211], a[192:195], v[160:163]\n\t"
  "v_add_f32_e32 v242, v242, v157\n\t"
  "s_waitcnt lgkmcnt(0)\n\t"
  "v_mfma_f32_16x16x32_bf16 v[164:167], v[192:195], a[128:131], -4.0\n\t"
  "ds_read_b128 v[208:211], v232 offset:43008\n\t"
  "v_cvt_pk_bf16_f32 v154, v156, v157\n\t"
  "v_mfma_f32_16x16x32_bf16 v[164:167], v[196:199], a[144:147], v[164:167]\n\t"
  "ds_read_b128 v[212:215], v232 offset:43520\n\t"
  "v_add_f32_e32 v242, v242, v158\n\t"
  "v_mfma_f32_16x16x32_bf16 v[164:167], v[200:203], a[160:163], v[164:167]\n\t"
  "v_exp_f32_e32 v159, v159\n\t"
  "v_mfma_f32_16x16x32_bf16 v[164:167], v[204:207], a[176:179], v[164:167]\n\t"
  "ds_read_b128 v[216:219], v232 offset:44032\n\t"
  "v_mfma_f32_16x16x32_bf16 v[172:175], v[204:207], a[180:183], -4.0\n\t"
  "ds_read_b128 v[220:223], v232 offset:44544\n\t"
  "v_add_f32_e32 v242, v242, v159\n\t"
  "v_mfma_f32_16x16x32_bf16 v[172:175], v[200:203], a[164:167], v[172:175]\n\t"
  "v_cvt_pk_bf16_f32 v155, v158, v159\n\t"
  "v_mfma_f32_16x16x32_bf16 v[172:175], v[196:199], a[148:151], v[172:175]\n\t"
  "v_exp_f32_e32 v160, v160\n\t"
  "v_mfma_f32_16x16x32_bf16 v[172:175], v[192:195], a[132:135], v[172:175]\n\t"
  "v_exp_f32_e32 v161, v161\n\t"
  "v_mfma_f32_16x16x32_bf16 v[180:183], v[192:195], a[136:139], -4.0\n\t"
  "v_add_f32_e32 v239, v239, v160\n\t"
  "v_mfma_f32_16x16x32_bf16 v[180:183], v[196:199], a[152:155], v[180:183]\n\t"
  "v_exp_f32_e32 v162, v162\n\t"
  "v_mfma_f32_16x16x32_bf16 v[180:183], v[200:203], a[168:171], v[180:183]\n\t"
  "v_add_f32_e32 v239, v239, v161\n\t"
  "v_cvt_pk_bf16_f32 v160, v160, v161\n\t"
  "v_mfma_f32_16x16x32_bf16 v[180:183], v[204:207], a[184:187], v[180:183]\n\t"
  "v_exp_f32_e32 v163, v163\n\t"
  "v_mfma_f32_16x16x32_bf16 v[188:191], v[204:207], a[188:191], -4.0\n\t"
  "v_add_f32_e32 v239, v239, v162\n\t"
  "v_mfma_f32_16x16x32_bf16 v[188:191], v[200:203], a[172:175], v[188:191]\n\t"
  "v_add_f32_e32 v239, v239, v163\n\t"
  "v_cvt_pk_bf16_f32 v161, v162, v163\n\t"
  "v_mfma_f32_16x16x32_bf16 v[188:191], v[196:199], a[156:159], v[188:191]\n\t"
  "s_waitcnt vmcnt(1) lgkmcnt(0)\n\t"
  "s_barrier\n\t"
  "v_mfma_f32_16x16x32_bf16 v[188:191], v[192:195], a[140:143], v[188:191]\n\t"
  "v_mfma_f32_16x16x32_bf16 v[188:191], v[208:211], a[204:207], v[188:191]\n\t"
  "ds_read_b64_tr_b16 v[192:193], v233\n\t"
  "v_mfma_f32_16x16x32_bf16 v[188:191], v[212:215], a[220:223], v[188:191]\n\t"
  "ds_read_b64_tr_b16 v[194:195], v233 offset:8192\n\t"
  "s_add_u32 m0, s30, 98304\n\t"
  "v_mfma_f32_16x16x32_bf16 v[188:191], v[216:219], a[236:239], v[188:191]\n\t"
  "buffer_load_dwordx4 v235, s[12:15], s29 offen lds\n\t"
  "v_mfma_f32_16x16x32_bf16 v[188:191], v[220:223], a[252:255], v[188:191]\n\t"
  "ds_read_b64_tr_b16 v[196:197], v234\n\t"
  "v_mfma_f32_16x16x32_bf16 v[180:183], v[220:223], a[248:251], v[180:183]\n\t"
  "ds_read_b64_tr_b16 v[198:199], v234 offset:8192\n\t"
  "v_mfma_f32_16x16x32_bf16 v[180:183], v[216:219], a[232:235], v[180:183]\n\t"
  "ds_read_b64_tr_b16 v[200:201], v233 offset:512\n\t"
  "v_mfma_f32_16x16x32_bf16 v[180:183], v[212:215], a[216:219], v[180:183]\n\t"
  "buffer_load_dwordx4 v236, s[12:15], s29 offen offset:1024 lds\n\t"
  "v_mfma_f32_16x16x32_bf16 v[180:183], v[208:211], a[200:203], v[180:183]\n\t"
  "ds_read_b64_tr_b16 v[202:203], v233 offset:8704\n\t"
  "v_mfma_f32_16x16x32_bf16 v[172:175], v[208:211], a[196:199], v[172:175]\n\t"
  "ds_read_b64_tr_b16 v[204:205], v234 offset:512\n\t"
  "v_mfma_f32_16x16x32_bf16 v[172:175], v[212:215], a[212:215], v[172:175]\n\t"
  "ds_read_b64_tr_b16 v[206:207], v234 offset:8704\n\t"
  "v_mfma_f32_16x16x32_bf16 v[172:175], v[216:219], a[228:231], v[172:175]\n\t"
  "buffer_load_dwordx4 v237, s[12:15], s29 offen offset:2048 lds\n\t"
  "v_mfma_f32_16x16x32_bf16 v[172:175], v[220:223], a[244:247], v[172:175]\n\t"
  "v_exp_f32_e32 v168, v168\n\t"
  "v_mfma_f32_16x16x32_bf16 v[164:167], v[220:223], a[240:243], v[164:167]\n\t"
  "v_exp_f32_e32 v169, v169\n\t"
  "v_mfma_f32_16x16x32_bf16 v[164:167], v[216:219], a[224:227], v[164:167]\n\t"
  "v_add_f32_e32 v240, v240, v168\n\t"
  "v_mfma_f32_16x16x32_bf16 v[164:167], v[212:215], a[208:211], v[164:167]\n\t"
  "buffer_load_dwordx4 v238, s[12:15], s29 offen offset:3072 lds\n\t"
  "v_mfma_f32_16x16x32_bf16 v[164:167], v[208:211], a[192:195], v[164:167]\n\t"
  "s_add_u32 s29, s29, 4096\n\t"
  "s_waitcnt lgkmcnt(0)\n\t"
  "v_mfma_f32_16x16x32_bf16 a[0:3], v[192:195], v[128:131], a[0:3]\n\t"
  "ds_read_b64_tr_b16 v[208:209], v233 offset:1024\n\t"
  "v_add_f32_e32 v240, v240, v169\n\t"
  "v_mfma_f32_16x16x32_bf16 a[8:11], v[196:199], v[128:131], a[8:11]\n\t"
  "ds_read_b64_tr_b16 v[210:211], v233 offset:9216\n\t"
  "s_add_u32 m0, s30, 102400\n\t"
  "v_mfma_f32_16x16x32_bf16 a[12:15], v[196:199], v[136:139], a[12:15]\n\t"
  "buffer_load_dwordx4 v235, s[12:15], s29 offen lds\n\t"
  "v_mfma_f32_16x16x32_bf16 a[4:7], v[192:195], v[136:139], a[4:7]\n\t"
  "ds_read_b64_tr_b16 v[212:213], v234 offset:1024\n\t"
  "v_cvt_pk_bf16_f32 v168, v168, v169\n\t"
  "v_mfma_f32_16x16x32_bf16 v[0:3], v[192:195], v[144:147], v[0:3]\n\t"
  "ds_read_b64_tr_b16 v[214:215], v234 offset:9216\n\t"
  "v_mfma_f32_16x16x32_bf16 v[4:7], v[196:199], v[144:147], v[4:7]\n\t"
  "ds_read_b64_tr_b16 v[216:217], v233 offset:1536\n\t"
  "v_mfma_f32_16x16x32_bf16 v[12:15], v[196:199], v[152:155], v[12:15]\n\t"
  "buffer_load_dwordx4 v236, s[12:15], s29 offen offset:1024 lds\n\t"
  "v_mfma_f32_16x16x32_bf16 v[8:11], v[192:195], v[152:155], v[8:11]\n\t"
  "ds_read_b64_tr_b16 v[218:219], v233 offset:9728\n\t"
  "v_mfma_f32_16x16x32_bf16 v[24:27], v[200:203], v[152:155], v[24:27]\n\t"
  "ds_read_b64_tr_b16 v[220:221], v234 offset:1536\n\t"
  "v_mfma_f32_16x16x32_bf16 v[28:31], v[204:207], v[152:155], v[28:31]\n\t"
  "ds_read_b64_tr_b16 v[222:223], v234 offset:9728\n\t"
  "v_mfma_f32_16x16x32_bf16 v[20:23], v[204:207], v[144:147], v[20:23]\n\t"
  "buffer_load_dwordx4 v237, s[12:15], s29 offen offset:2048 lds\n\t"
  "v_mfma_f32_16x16x32_bf16 v[16:19], v[200:203], v[144:147], v[16:19]\n\t"
  "v_exp_f32_e32 v170, v170\n\t"
  "v_mfma_f32_16x16x32_bf16 a[20:23], v[200:203], v[136:139], a[20:23]\n\t"
  "v_exp_f32_e32 v171, v171\n\t"
  "v_mfma_f32_16x16x32_bf16 a[28:31], v[204:207], v[136:139], a[28:31]\n\t"
  "v_add_f32_e32 v240, v240, v170\n\t"
  "v_mfma_f32_16x16x32_bf16 a[24:27], v[204:207], v[128:131], a[24:27]\n\t"
  "buffer_load_dwordx4 v238, s[12:15], s29 offen offset:3072 lds\n\t"
  "v_mfma_f32_16x16x32_bf16 a[16:19], v[200:203], v[128:131], a[16:19]\n\t"
  "s_add_u32 s29, s29, 28672\n\t"
  "s_waitcnt lgkmcnt(0)\n\t"
  "v_mfma_f32_16x16x32_bf16 a[32:35], v[208:211], v[128:131], a[32:35]\n\t"
  "ds_read_b64_tr_b16 v[192:193], v233 offset:2048\n\t"
  "v_add_f32_e32 v240, v240, v171\n\t"
  "v_mfma_f32_16x16x32_bf16 a[40:43], v[212:215], v[128:131], a[40:43]\n\t"
  "ds_read_b64_tr_b16 v[194:195], v233 offset:10240\n\t"
  "s_add_u32 m0, s30, 32768\n\t"
  "v_mfma_f32_16x16x32_bf16 a[44:47], v[212:215], v[136:139], a[44:47]\n\t"
  "buffer_load_dwordx4 v235, s[8:11], s28 offen lds\n\t"
  "v_mfma_f32_16x16x32_bf16 a[36:39], v[208:211], v[136:139], a[36:39]\n\t"
  "ds_read_b64_tr_b16 v[196:197], v234 offset:2048\n\t"
  "v_cvt_pk_bf16_f32 v169, v170, v171\n\t"
  "v_mfma_f32_16x16x32_bf16 v[32:35], v[208:211], v[144:147], v[32:35]\n\t"
  "ds_read_b64_tr_b16 v[198:199], v234 offset:10240\n\t"
  "v_mfma_f32_16x16x32_bf16 v[36:39], v[212:215], v[144:147], v[36:39]\n\t"
  "ds_read_b64_tr_b16 v[200:201], v233 offset:2560\n\t"
  "v_mfma_f32_16x16x32_bf16 v[44:47], v[212:215], v[152:155], v[44:47]\n\t"
  "buffer_load_dwordx4 v236, s[8:11], s28 offen offset:1024 lds\n\t"
  "v_mfma_f32_16x16x32_bf16 v[40:43], v[208:211], v[152:155], v[40:43]\n\t"
  "ds_read_b64_tr_b16 v[202:203], v233 offset:10752\n\t"
  "v_mfma_f32_16x16x32_bf16 v[56:59], v[216:219], v[152:155], v[56:59]\n\t"
  "ds_read_b64_tr_b16 v[204:205], v234 offset:2560\n\t"
  "v_mfma_f32_16x16x32_bf16 v[60:63], v[220:223], v[152:155], v[60:63]\n\t"
  "ds_read_b64_tr_b16 v[206:207], v234 offset:10752\n\t"
  "v_mfma_f32_16x16x32_bf16 v[52:55], v[220:223], v[144:147], v[52:55]\n\t"
  "buffer_load_dwordx4 v237, s[8:11], s28 offen offset:2048 lds\n\t"
  "v_mfma_f32_16x16x32_bf16 v[48:51], v[216:219], v[144:147], v[48:51]\n\t"
  "v_exp_f32_e32 v176, v176\n\t"
  "v_mfma_f32_16x16x32_bf16 a[52:55], v[216:219], v[136:139], a[52:55]\n\t"
  "v_exp_f32_e32 v177, v177\n\t"
  "v_mfma_f32_16x16x32_bf16 a[60:63], v[220:223], v[136:139], a[60:63]\n\t"
  "v_add_f32_e32 v241, v241, v176\n\t"
  "v_mfma_f32_16x16x32_bf16 a[56:59], v[220:223], v[128:131], a[56:59]\n\t"
  "buffer_load_dwordx4 v238, s[8:11], s28 offen offset:3072 lds\n\t"
  "v_mfma_f32_16x16x32_bf16 a[48:51], v[216:219], v[128:131], a[48:51]\n\t"
  "s_add_u32 s28, s28, 4096\n\t"
  "s_waitcnt lgkmcnt(0)\n\t"
  "v_mfma_f32_16x16x32_bf16 a[64:67], v[192:195], v[128:131], a[64:67]\n\t"
  "ds_read_b64_tr_b16 v[208:209], v233 offset:3072\n\t"
  "v_add_f32_e32 v241, v241, v177\n\t"
  "v_mfma_f32_16x16x32_bf16 a[72:75], v[196:199], v[128:131], a[72:75]\n\t"
  "ds_read_b64_tr_b16 v[210:211], v233 offset:11264\n\t"
  "s_add_u32 m0, s30, 36864\n\t"
  "v_mfma_f32_16x16x32_bf16 a[76:79], v[196:199], v[136:139], a[76:79]\n\t"
  "buffer_load_dwordx4 v235, s[8:11], s28 offen lds\n\t"
  "v_mfma_f32_16x16x32_bf16 a[68:71], v[192:195], v[136:139], a[68:71]\n\t"
  "ds_read_b64_tr_b16 v[212:213], v234 offset:3072\n\t"
  "v_cvt_pk_bf16_f32 v176, v176, v177\n\t"
  "v_mfma_f32_16x16x32_bf16 v[64:67], v[192:195], v[144:147], v[64:67]\n\t"
  "ds_read_b64_tr_b16 v[214:215], v234 offset:11264\n\t"
  "v_mfma_f32_16x16x32_bf16 v[68:71], v[196:199], v[144:147], v[68:71]\n\t"
  "ds_read_b64_tr_b16 v[216:217], v233 offset:3584\n\t"
  "v_mfma_f32_16x16x32_bf16 v[76:79], v[196:199], v[152:155], v[76:79]\n\t"
  "buffer_load_dwordx4 v236, s[8:11], s28 offen offset:1024 lds\n\t"
  "v_mfma_f32_16x16x32_bf16 v[72:75], v[192:195], v[152:155], v[72:75]\n\t"
  "ds_read_b64_tr_b16 v[218:219], v233 offset:11776\n\t"
  "v_mfma_f32_16x16x32_bf16 v[88:91], v[200:203], v[152:155], v[88:91]\n\t"
  "ds_read_b64_tr_b16 v[220:221], v234 offset:3584\n\t"
  "v_mfma_f32_16x16x32_bf16 v[92:95], v[204:207], v[152:155], v[92:95]\n\t"
  "ds_read_b64_tr_b16 v[222:223], v234 offset:11776\n\t"
  "v_mfma_f32_16x16x32_bf16 v[84:87], v[204:207], v[144:147], v[84:87]\n\t"
  "buffer_load_dwordx4 v237, s[8:11], s28 offen offset:2048 lds\n\t"
  "v_mfma_f32_16x16x32_bf16 v[80:83], v[200:203], v[144:147], v[80:83]\n\t"
  "v_exp_f32_e32 v178, v178\n\t"
  "v_mfma_f32_16x16x32_bf16 a[84:87], v[200:203], v[136:139], a[84:87]\n\t"
  "v_exp_f32_e32 v179, v179\n\t"
  "v_mfma_f32_16x16x32_bf16 a[92:95], v[204:207], v[136:139], a[92:95]\n\t"
  "v_add_f32_e32 v241, v241, v178\n\t"
  "v_mfma_f32_16x16x32_bf16 a[88:91], v[204:207], v[128:131], a[88:91]\n\t"
  "buffer_load_dwordx4 v238, s[8:11], s28 offen offset:3072 lds\n\t"
  "v_mfma_f32_16x16x32_bf16 a[80:83], v[200:203], v[128:131], a[80:83]\n\t"
  "s_add_u32 s28, s28, 28672\n\t"
  "s_waitcnt lgkmcnt(0)\n\t"
  "v_mfma_f32_16x16x32_bf16 a[96:99], v[208:211], v[128:131], a[96:99]\n\t"
  "ds_read_b128 v[192:195], v232\n\t"
  "v_add_f32_e32 v241, v241, v179\n\t"
  "v_mfma_f32_16x16x32_bf16 a[104:107], v[212:215], v[128:131], a[104:107]\n\t"
  "ds_read_b128 v[196:199], v232 offset:512\n\t"
  "v_cvt_pk_bf16_f32 v177, v178, v179\n\t"
  "v_mfma_f32_16x16x32_bf16 a[108:111], v[212:215], v[136:139], a[108:111]\n\t"
  "buffer_load_dword v244, v245, s[24:27], s35 offen\n\t"
  "v_mfma_f32_16x16x32_bf16 a[100:103], v[208:211], v[136:139], a[100:103]\n\t"
  "ds_read_b128 v[200:203], v232 offset:1024\n\t"
  "s_add_u32 s35, s35, s97\n\t"
  "v_mfma_f32_16x16x32_bf16 v[96:99], v[208:211], v[144:147], v[96:99]\n\t"
  "ds_read_b128 v[204:207], v232 offset:1536\n\t"
  "s_min_u32 s35, s35, s96\n\t"
  "v_mfma_f32_16x16x32_bf16 v[100:103], v[212:215], v[144:147], v[100:103]\n\t"
  "v_exp_f32_e32 v184, v184\n\t"
  "v_mfma_f32_16x16x32_bf16 v[108:111], v[212:215], v[152:155], v[108:111]\n\t"
  "v_exp_f32_e32 v185, v185\n\t"
  "v_mfma_f32_16x16x32_bf16 v[104:107], v[208:211], v[152:155], v[104:107]\n\t"
  "v_add_f32_e32 v242, v242, v184\n\t"
  "v_mfma_f32_16x16x32_bf16 v[120:123], v[216:219], v[152:155], v[120:123]\n\t"
  "v_exp_f32_e32 v186, v186\n\t"
  "v_mfma_f32_16x16x32_bf16 v[124:127], v[220:223], v[152:155], v[124:127]\n\t"
  "v_add_f32_e32 v242, v242, v185\n\t"
  "v_cvt_pk_bf16_f32 v184, v184, v185\n\t"
  "v_mfma_f32_16x16x32_bf16 v[116:119], v[220:223], v[144:147], v[116:119]\n\t"
  "v_exp_f32_e32 v187, v187\n\t"
  "v_mfma_f32_16x16x32_bf16 v[112:115], v[216:219], v[144:147], v[112:115]\n\t"
  "v_add_f32_e32 v242, v242, v186\n\t"
  "v_mfma_f32_16x16x32_bf16 a[116:119], v[216:219], v[136:139], a[116:119]\n\t"
  "v_add_f32_e32 v242, v242, v187\n\t"
  "v_cvt_pk_bf16_f32 v185, v186, v187\n\t"
  "v_mfma_f32_16x16x32_bf16 a[124:127], v[220:223], v[136:139], a[124:127]\n\t"
  "v_exp_f32_e32 v164, v164\n\t"
  "v_mfma_f32_16x16x32_bf16 a[120:123], v[220:223], v[128:131], a[120:123]\n\t"
  "v_exp_f32_e32 v165, v165\n\t"
  "v_mfma_f32_16x16x32_bf16 a[112:115], v[216:219], v[128:131], a[112:115]\n\t"
  "v_add_f32_e32 v239, v239, v164\n\t"
  "s_sub_u32 s31, s31, 1\n\t"
  "s_cmp_lg_u32 s31, 0\n\t"
  "s_cbranch_scc1 .Lloop\n\t"
  "s_waitcnt lgkmcnt(0)\n\t"
  "v_mfma_f32_16x16x32_bf16 v[128:131], v[192:195], a[128:131], -4.0\n\t"
  "ds_read_b128 v[208:211], v232 offset:2048\n\t"
  "v_add_f32_e32 v239, v239, v165\n\t"
  "v_mfma_f32_16x16x32_bf16 v[128:131], v[196:199], a[144:147], v[128:131]\n\t"
  "ds_read_b128 v[212:215], v232 offset:2560\n\t"
  "v_cvt_pk_bf16_f32 v162, v164, v165\n\t"
  "v_mfma_f32_16x16x32_bf16 v[128:131], v[200:203], a[160:163], v[128:131]\n\t"
  "v_exp_f32_e32 v166, v166\n\t"
  "v_mfma_f32_16x16x32_bf16 v[128:131], v[204:207], a[176:179], v[128:131]\n\t"
  "ds_read_b128 v[216:219], v232 offset:3072\n\t"
  "s_andn2_b32 s67, s67, 2\n\t"
  "v_mfma_f32_16x16x32_bf16 v[136:139], v[204:207], a[180:183], -4.0\n\t"
  "ds_read_b128 v[220:223], v232 offset:3584\n\t"
  "v_add_f32_e32 v239, v239, v166\n\t"
  "v_mfma_f32_16x16x32_bf16 v[136:139], v[200:203], a[164:167], v[136:139]\n\t"
  "v_exp_f32_e32 v167, v167\n\t"
  "v_mfma_f32_16x16x32_bf16 v[136:139], v[196:199], a[148:151], v[136:139]\n\t"
  "v_exp_f32_e32 v172, v172\n\t"
  "v_mfma_f32_16x16x32_bf16 v[136:139], v[192:195], a[132:135], v[136:139]\n\t"
  "v_add_f32_e32 v239, v239, v167\n\t"
  "v_cvt_pk_bf16_f32 v163, v166, v167\n\t"
  "v_mfma_f32_16x16x32_bf16 v[144:147], v[192:195], a[136:139], -4.0\n\t"
  "v_exp_f32_e32 v173, v173\n\t"
  "v_mfma_f32_16x16x32_bf16 v[144:147], v[196:199], a[152:155], v[144:147]\n\t"
  "v_add_f32_e32 v240, v240, v172\n\t"
  "v_xor_b32_e32 v246, s67, v246\n\t"
  "v_mfma_f32_16x16x32_bf16 v[144:147], v[200:203], a[168:171], v[144:147]\n\t"
  "v_exp_f32_e32 v174, v174\n\t"
  "v_mfma_f32_16x16x32_bf16 v[144:147], v[204:207], a[184:187], v[144:147]\n\t"
  "v_add_f32_e32 v240, v240, v173\n\t"
  "v_cvt_pk_bf16_f32 v170, v172, v173\n\t"
  "v_mfma_f32_16x16x32_bf16 v[152:155], v[204:207], a[188:191], -4.0\n\t"
  "v_exp_f32_e32 v175, v175\n\t"
  "v_mfma_f32_16x16x32_bf16 v[152:155], v[200:203], a[172:175], v[152:155]\n\t"
  "v_add_f32_e32 v240, v240, v174\n\t"
  "v_mul_u32_u24_e32 v247, 1835008, v246\n\t"
  "v_mfma_f32_16x16x32_bf16 v[152:155], v[196:199], a[156:159], v[152:155]\n\t"
  "v_add_f32_e32 v240, v240, v175\n\t"
  "v_cvt_pk_bf16_f32 v171, v174, v175\n\t"
  "v_mfma_f32_16x16x32_bf16 v[152:155], v[192:195], a[140:143], v[152:155]\n\t"
  "v_add_u32_e32 v249, v249, v247\n\t"
  "s_waitcnt lgkmcnt(0)\n\t"
  "v_mfma_f32_16x16x32_bf16 v[152:155], v[208:211], a[204:207], v[152:155]\n\t"
  "ds_read_b128 v[192:195], v232 offset:8192\n\t"
  "v_cmp_eq_u32_e32 vcc, 0, v246\n\t"
  "v_mfma_f32_16x16x32_bf16 v[152:155], v[212:215], a[220:223], v[152:155]\n\t"
  "ds_read_b128 v[196:199], v232 offset:8704\n\t"
  "v_mov_b32_e32 v246, 0x7f000000\n\t"
  "v_mfma_f32_16x16x32_bf16 v[152:155], v[216:219], a[236:239], v[152:155]\n\t"
  "v_exp_f32_e32 v180, v180\n\t"
  "v_mfma_f32_16x16x32_bf16 v[152:155], v[220:223], a[252:255], v[152:155]\n\t"
  "ds_read_b128 v[200:203], v232 offset:9216\n\t"
  "s_nop 0\n\t"
  "v_mfma_f32_16x16x32_bf16 v[144:147], v[220:223], a[248:251], v[144:147]\n\t"
  "ds_read_b128 v[204:207], v232 offset:9728\n\t"
  "v_add_f32_e32 v241, v241, v180\n\t"
  "v_mfma_f32_16x16x32_bf16 v[144:147], v[216:219], a[232:235], v[144:147]\n\t"
  "v_exp_f32_e32 v181, v181\n\t"
  "v_mfma_f32_16x16x32_bf16 v[144:147], v[212:215], a[216:219], v[144:147]\n\t"
  "v_exp_f32_e32 v182, v182\n\t"
  "v_mfma_f32_16x16x32_bf16 v[144:147], v[208:211], a[200:203], v[144:147]\n\t"
  "v_add_f32_e32 v241, v241, v181\n\t"
  "v_cvt_pk_bf16_f32 v178, v180, v181\n\t"
  "v_mfma_f32_16x16x32_bf16 v[136:139], v[208:211], a[196:199], v[136:139]\n\t"
  "v_exp_f32_e32 v183, v183\n\t"
  "v_mfma_f32_16x16x32_bf16 v[136:139], v[212:215], a[212:215], v[136:139]\n\t"
  "v_add_f32_e32 v241, v241, v182\n\t"
  "v_cndmask_b32_e32 v247, v249, v246, vcc\n\t"
  "v_mfma_f32_16x16x32_bf16 v[136:139], v[216:219], a[228:231], v[136:139]\n\t"
  "v_add_f32_e32 v241, v241, v183\n\t"
  "v_cvt_pk_bf16_f32 v179, v182, v183\n\t"
  "v_mfma_f32_16x16x32_bf16 v[136:139], v[220:223], a[244:247], v[136:139]\n\t"
  "v_exp_f32_e32 v188, v188\n\t"
  "v_mfma_f32_16x16x32_bf16 v[128:131], v[220:223], a[240:243], v[128:131]\n\t"
  "v_exp_f32_e32 v189, v189\n\t"
  "v_mfma_f32_16x16x32_bf16 v[128:131], v[216:219], a[224:227], v[128:131]\n\t"
  "v_add_f32_e32 v242, v242, v188\n\t"
  "v_add_u32_e32 v248, 3670016, v249\n\t"
  "v_mfma_f32_16x16x32_bf16 v[128:131], v[212:215], a[208:211], v[128:131]\n\t"
  "v_exp_f32_e32 v190, v190\n\t"
  "v_mfma_f32_16x16x32_bf16 v[128:131], v[208:211], a[192:195], v[128:131]\n\t"
  "v_add_f32_e32 v242, v242, v189\n\t"
  "s_waitcnt lgkmcnt(0)\n\t"
  "v_mfma_f32_16x16x32_bf16 v[132:135], v[192:195], a[128:131], -4.0\n\t"
  "ds_read_b128 v[208:211], v232 offset:10240\n\t"
  "v_cvt_pk_bf16_f32 v186, v188, v189\n\t"
  "v_mfma_f32_16x16x32_bf16 v[132:135], v[196:199], a[144:147], v[132:135]\n\t"
  "ds_read_b128 v[212:215], v232 offset:10752\n\t"
  "v_add_f32_e32 v242, v242, v190\n\t"
  "v_mfma_f32_16x16x32_bf16 v[132:135], v[200:203], a[160:163], v[132:135]\n\t"
  "v_exp_f32_e32 v191, v191\n\t"
  "v_mfma_f32_16x16x32_bf16 v[132:135], v[204:207], a[176:179], v[132:135]\n\t"
  "ds_read_b128 v[216:219], v232 offset:11264\n\t"
  "s_add_u32 s24, s98, 65536\n\t"
  "s_addc_u32 s25, s99, 0\n\t"
  "v_mfma_f32_16x16x32_bf16 v[140:143], v[204:207], a[180:183], -4.0\n\t"
  "ds_read_b128 v[220:223], v232 offset:11776\n\t"
  "v_add_f32_e32 v242, v242, v191\n\t"
  "v_mfma_f32_16x16x32_bf16 v[140:143], v[200:203], a[164:167], v[140:143]\n\t"
  "v_cvt_pk_bf16_f32 v187, v190, v191\n\t"
  "s_and_b32 s25, s25, 0xffff\n\t"
  "v_mfma_f32_16x16x32_bf16 v[140:143], v[196:199], a[148:151], v[140:143]\n\t"
  "v_exp_f32_e32 v128, v128\n\t"
  "v_mfma_f32_16x16x32_bf16 v[140:143], v[192:195], a[132:135], v[140:143]\n\t"
  "v_exp_f32_e32 v129, v129\n\t"
  "v_mfma_f32_16x16x32_bf16 v[148:151], v[192:195], a[136:139], -4.0\n\t"
  "v_add_f32_e32 v239, v239, v128\n\t"
  "s_mov_b32 s26, 131072\n\t"
  "v_mfma_f32_16x16x32_bf16 v[148:151], v[196:199], a[152:155], v[148:151]\n\t"
  "v_exp_f32_e32 v130, v130\n\t"
  "v_mfma_f32_16x16x32_bf16 v[148:151], v[200:203], a[168:171], v[148:151]\n\t"
  "v_add_f32_e32 v239, v239, v129\n\t"
  "v_cvt_pk_bf16_f32 v128, v128, v129\n\t"
  "v_mfma_f32_16x16x32_bf16 v[148:151], v[204:207], a[184:187], v[148:151]\n\t"
  "v_exp_f32_e32 v131, v131\n\t"
  "v_mfma_f32_16x16x32_bf16 v[156:159], v[204:207], a[188:191], -4.0\n\t"
  "v_add_f32_e32 v239, v239, v130\n\t"
  "v_mfma_f32_16x16x32_bf16 v[156:159], v[200:203], a[172:175], v[156:159]\n\t"
  "v_add_f32_e32 v239, v239, v131\n\t"
  "v_cvt_pk_bf16_f32 v129, v130, v131\n\t"
  "v_mfma_f32_16x16x32_bf16 v[156:159], v[196:199], a[156:159], v[156:159]\n\t"
  "s_waitcnt vmcnt(1) lgkmcnt(0)\n\t"
  "s_barrier\n\t"
  "v_mfma_f32_16x16x32_bf16 v[156:159], v[192:195], a[140:143], v[156:159]\n\t"
  "v_mfma_f32_16x16x32_bf16 v[156:159], v[208:211], a[204:207], v[156:159]\n\t"
  "ds_read_b64_tr_b16 v[192:193], v233 offset:32768\n\t"
  "v_mfma_f32_16x16x32_bf16 v[156:159], v[212:215], a[220:223], v[156:159]\n\t"
  "ds_read_b64_tr_b16 v[194:195], v233 offset:40960\n\t"
  "s_add_u32 m0, s30, 65536\n\t"
  "v_mfma_f32_16x16x32_bf16 v[156:159], v[216:219], a[236:239], v[156:159]\n\t"
  "buffer_load_dwordx4 v235, s[12:15], s29 offen lds\n\t"
  "v_mfma_f32_16x16x32_bf16 v[156:159], v[220:223], a[252:255], v[156:159]\n\t"
  "ds_read_b64_tr_b16 v[196:197], v234 offset:32768\n\t"
  "v_mfma_f32_16x16x32_bf16 v[148:151], v[220:223], a[248:251], v[148:151]\n\t"
  "ds_read_b64_tr_b16 v[198:199], v234 offset:40960\n\t"
  "v_mfma_f32_16x16x32_bf16 v[148:151], v[216:219], a[232:235], v[148:151]\n\t"
  "ds_read_b64_tr_b16 v[200:201], v233 offset:33280\n\t"
  "v_mfma_f32_16x16x32_bf16 v[148:151], v[212:215], a[216:219], v[148:151]\n\t"
  "buffer_load_dwordx4 v236, s[12:15], s29 offen offset:1024 lds\n\t"
  "v_mfma_f32_16x16x32_bf16 v[148:151], v[208:211], a[200:203], v[148:151]\n\t"
  "ds_read_b64_tr_b16 v[202:203], v233 offset:41472\n\t"
  "v_mfma_f32_16x16x32_bf16 v[140:143], v[208:211], a[196:199], v[140:143]\n\t"
  "ds_read_b64_tr_b16 v[204:205], v234 offset:33280\n\t"
  "v_mfma_f32_16x16x32_bf16 v[140:143], v[212:215], a[212:215], v[140:143]\n\t"
  "ds_read_b64_tr_b16 v[206:207], v234 offset:41472\n\t"
  "v_mfma_f32_16x16x32_bf16 v[140:143], v[216:219], a[228:231], v[140:143]\n\t"
  "buffer_load_dwordx4 v237, s[12:15], s29 offen offset:2048 lds\n\t"
  "v_mfma_f32_16x16x32_bf16 v[140:143], v[220:223], a[244:247], v[140:143]\n\t"
  "v_exp_f32_e32 v136, v136\n\t"
  "v_mfma_f32_16x16x32_bf16 v[132:135], v[220:223], a[240:243], v[132:135]\n\t"
  "v_exp_f32_e32 v137, v137\n\t"
  "v_mfma_f32_16x16x32_bf16 v[132:135], v[216:219], a[224:227], v[132:135]\n\t"
  "v_add_f32_e32 v240, v240, v136\n\t"
  "v_mfma_f32_16x16x32_bf16 v[132:135], v[212:215], a[208:211], v[132:135]\n\t"
  "buffer_load_dwordx4 v238, s[12:15], s29 offen offset:3072 lds\n\t"
  "v_mfma_f32_16x16x32_bf16 v[132:135], v[208:211], a[192:195], v[132:135]\n\t"
  "s_add_u32 s29, s29, 4096\n\t"
  "s_waitcnt lgkmcnt(0)\n\t"
  "v_mfma_f32_16x16x32_bf16 a[0:3], v[192:195], v[160:163], a[0:3]\n\t"
  "ds_read_b64_tr_b16 v[208:209], v233 offset:33792\n\t"
  "v_add_f32_e32 v240, v240, v137\n\t"
  "v_mfma_f32_16x16x32_bf16 a[8:11], v[196:199], v[160:163], a[8:11]\n\t"
  "ds_read_b64_tr_b16 v[210:211], v233 offset:41984\n\t"
  "s_add_u32 m0, s30, 69632\n\t"
  "v_mfma_f32_16x16x32_bf16 a[12:15], v[196:199], v[168:171], a[12:15]\n\t"
  "buffer_load_dwordx4 v235, s[12:15], s29 offen lds\n\t"
  "v_mfma_f32_16x16x32_bf16 a[4:7], v[192:195], v[168:171], a[4:7]\n\t"
  "ds_read_b64_tr_b16 v[212:213], v234 offset:33792\n\t"
  "v_cvt_pk_bf16_f32 v136, v136, v137\n\t"
  "v_mfma_f32_16x16x32_bf16 v[0:3], v[192:195], v[176:179], v[0:3]\n\t"
  "ds_read_b64_tr_b16 v[214:215], v234 offset:41984\n\t"
  "v_mfma_f32_16x16x32_bf16 v[4:7], v[196:199], v[176:179], v[4:7]\n\t"
  "ds_read_b64_tr_b16 v[216:217], v233 offset:34304\n\t"
  "v_mfma_f32_16x16x32_bf16 v[12:15], v[196:199], v[184:187], v[12:15]\n\t"
  "buffer_load_dwordx4 v236, s[12:15], s29 offen offset:1024 lds\n\t"
  "v_mfma_f32_16x16x32_bf16 v[8:11], v[192:195], v[184:187], v[8:11]\n\t"
  "ds_read_b64_tr_b16 v[218:219], v233 offset:42496\n\t"
  "v_mfma_f32_16x16x32_bf16 v[24:27], v[200:203], v[184:187], v[24:27]\n\t"
  "ds_read_b64_tr_b16 v[220:221], v234 offset:34304\n\t"
  "v_mfma_f32_16x16x32_bf16 v[28:31], v[204:207], v[184:187], v[28:31]\n\t"
  "ds_read_b64_tr_b16 v[222:223], v234 offset:42496\n\t"
  "v_mfma_f32_16x16x32_bf16 v[20:23], v[204:207], v[176:179], v[20:23]\n\t"
  "buffer_load_dwordx4 v237, s[12:15], s29 offen offset:2048 lds\n\t"
  "v_mfma_f32_16x16x32_bf16 v[16:19], v[200:203], v[176:179], v[16:19]\n\t"
  "v_exp_f32_e32 v138, v138\n\t"
  "v_mfma_f32_16x16x32_bf16 a[20:23], v[200:203], v[168:171], a[20:23]\n\t"
  "v_exp_f32_e32 v139, v139\n\t"
  "v_mfma_f32_16x16x32_bf16 a[28:31], v[204:207], v[168:171], a[28:31]\n\t"
  "v_add_f32_e32 v240, v240, v138\n\t"
  "v_mfma_f32_16x16x32_bf16 a[24:27], v[204:207], v[160:163], a[24:27]\n\t"
  "buffer_load_dwordx4 v238, s[12:15], s29 offen offset:3072 lds\n\t"
  "v_mfma_f32_16x16x32_bf16 a[16:19], v[200:203], v[160:163], a[16:19]\n\t"
  "s_add_u32 s29, s29, 28672\n\t"
  "s_waitcnt lgkmcnt(0)\n\t"
  "v_mfma_f32_16x16x32_bf16 a[32:35], v[208:211], v[160:163], a[32:35]\n\t"
  "ds_read_b64_tr_b16 v[192:193], v233 offset:34816\n\t"
  "v_add_f32_e32 v240, v240, v139\n\t"
  "v_mfma_f32_16x16x32_bf16 a[40:43], v[212:215], v[160:163], a[40:43]\n\t"
  "ds_read_b64_tr_b16 v[194:195], v233 offset:43008\n\t"
  "s_add_u32 m0, s30, 0\n\t"
  "v_mfma_f32_16x16x32_bf16 a[44:47], v[212:215], v[168:171], a[44:47]\n\t"
  "buffer_load_dwordx4 v235, s[12:15], s29 offen lds\n\t"
  "v_mfma_f32_16x16x32_bf16 a[36:39], v[208:211], v[168:171], a[36:39]\n\t"
  "ds_read_b64_tr_b16 v[196:197], v234 offset:34816\n\t"
  "v_cvt_pk_bf16_f32 v137, v138, v139\n\t"
  "v_mfma_f32_16x16x32_bf16 v[32:35], v[208:211], v[176:179], v[32:35]\n\t"
  "ds_read_b64_tr_b16 v[198:199], v234 offset:43008\n\t"
  "v_mfma_f32_16x16x32_bf16 v[36:39], v[212:215], v[176:179], v[36:39]\n\t"
  "ds_read_b64_tr_b16 v[200:201], v233 offset:35328\n\t"
  "v_mfma_f32_16x16x32_bf16 v[44:47], v[212:215], v[184:187], v[44:47]\n\t"
  "buffer_load_dwordx4 v236, s[12:15], s29 offen offset:1024 lds\n\t"
  "v_mfma_f32_16x16x32_bf16 v[40:43], v[208:211], v[184:187], v[40:43]\n\t"
  "ds_read_b64_tr_b16 v[202:203], v233 offset:43520\n\t"
  "v_mfma_f32_16x16x32_bf16 v[56:59], v[216:219], v[184:187], v[56:59]\n\t"
  "ds_read_b64_tr_b16 v[204:205], v234 offset:35328\n\t"
  "v_mfma_f32_16x16x32_bf16 v[60:63], v[220:223], v[184:187], v[60:63]\n\t"
  "ds_read_b64_tr_b16 v[206:207], v234 offset:43520\n\t"
  "v_mfma_f32_16x16x32_bf16 v[52:55], v[220:223], v[176:179], v[52:55]\n\t"
  "buffer_load_dwordx4 v237, s[12:15], s29 offen offset:2048 lds\n\t"
  "v_mfma_f32_16x16x32_bf16 v[48:51], v[216:219], v[176:179], v[48:51]\n\t"
  "v_exp_f32_e32 v144, v144\n\t"
  "v_mfma_f32_16x16x32_bf16 a[52:55], v[216:219], v[168:171], a[52:55]\n\t"
  "v_exp_f32_e32 v145, v145\n\t"
  "v_mfma_f32_16x16x32_bf16 a[60:63], v[220:223], v[168:171], a[60:63]\n\t"
  "v_add_f32_e32 v241, v241, v144\n\t"
  "v_mfma_f32_16x16x32_bf16 a[56:59], v[220:223], v[160:163], a[56:59]\n\t"
  "buffer_load_dwordx4 v238, s[12:15], s29 offen offset:3072 lds\n\t"
  "v_mfma_f32_16x16x32_bf16 a[48:51], v[216:219], v[160:163], a[48:51]\n\t"
  "s_add_u32 s29, s29, 4096\n\t"
  "s_waitcnt lgkmcnt(0)\n\t"
  "v_mfma_f32_16x16x32_bf16 a[64:67], v[192:195], v[160:163], a[64:67]\n\t"
  "ds_read_b64_tr_b16 v[208:209], v233 offset:35840\n\t"
  "v_add_f32_e32 v241, v241, v145\n\t"
  "v_mfma_f32_16x16x32_bf16 a[72:75], v[196:199], v[160:163], a[72:75]\n\t"
  "ds_read_b64_tr_b16 v[210:211], v233 offset:44032\n\t"
  "s_add_u32 m0, s30, 4096\n\t"
  "v_mfma_f32_16x16x32_bf16 a[76:79], v[196:199], v[168:171], a[76:79]\n\t"
  "buffer_load_dwordx4 v235, s[12:15], s29 offen lds\n\t"
  "v_mfma_f32_16x16x32_bf16 a[68:71], v[192:195], v[168:171], a[68:71]\n\t"
  "ds_read_b64_tr_b16 v[212:213], v234 offset:35840\n\t"
  "v_cvt_pk_bf16_f32 v144, v144, v145\n\t"
  "v_mfma_f32_16x16x32_bf16 v[64:67], v[192:195], v[176:179], v[64:67]\n\t"
  "ds_read_b64_tr_b16 v[214:215], v234 offset:44032\n\t"
  "v_mfma_f32_16x16x32_bf16 v[68:71], v[196:199], v[176:179], v[68:71]\n\t"
  "ds_read_b64_tr_b16 v[216:217], v233 offset:36352\n\t"
  "v_mfma_f32_16x16x32_bf16 v[76:79], v[196:199], v[184:187], v[76:79]\n\t"
  "buffer_load_dwordx4 v236, s[12:15], s29 offen offset:1024 lds\n\t"
  "v_mfma_f32_16x16x32_bf16 v[72:75], v[192:195], v[184:187], v[72:75]\n\t"
  "ds_read_b64_tr_b16 v[218:219], v233 offset:44544\n\t"
  "v_mfma_f32_16x16x32_bf16 v[88:91], v[200:203], v[184:187], v[88:91]\n\t"
  "ds_read_b64_tr_b16 v[220:221], v234 offset:36352\n\t"
  "v_mfma_f32_16x16x32_bf16 v[92:95], v[204:207], v[184:187], v[92:95]\n\t"
  "ds_read_b64_tr_b16 v[222:223], v234 offset:44544\n\t"
  "v_mfma_f32_16x16x32_bf16 v[84:87], v[204:207], v[176:179], v[84:87]\n\t"
  "buffer_load_dwordx4 v237, s[12:15], s29 offen offset:2048 lds\n\t"
  "v_mfma_f32_16x16x32_bf16 v[80:83], v[200:203], v[176:179], v[80:83]\n\t"
  "v_exp_f32_e32 v146, v146\n\t"
  "v_mfma_f32_16x16x32_bf16 a[84:87], v[200:203], v[168:171], a[84:87]\n\t"
  "v_exp_f32_e32 v147, v147\n\t"
  "v_mfma_f32_16x16x32_bf16 a[92:95], v[204:207], v[168:171], a[92:95]\n\t"
  "v_add_f32_e32 v241, v241, v146\n\t"
  "v_mfma_f32_16x16x32_bf16 a[88:91], v[204:207], v[160:163], a[88:91]\n\t"
  "buffer_load_dwordx4 v238, s[12:15], s29 offen offset:3072 lds\n\t"
  "v_mfma_f32_16x16x32_bf16 a[80:83], v[200:203], v[160:163], a[80:83]\n\t"
  "s_add_u32 s29, s29, 28672\n\t"
  "s_waitcnt lgkmcnt(0)\n\t"
  "v_mfma_f32_16x16x32_bf16 a[96:99], v[208:211], v[160:163], a[96:99]\n\t"
  "ds_read_b128 v[192:195], v232 offset:32768\n\t"
  "v_add_f32_e32 v241, v241, v147\n\t"
  "v_mfma_f32_16x16x32_bf16 a[104:107], v[212:215], v[160:163], a[104:107]\n\t"
  "ds_read_b128 v[196:199], v232 offset:33280\n\t"
  "v_cvt_pk_bf16_f32 v145, v146, v147\n\t"
  "v_mfma_f32_16x16x32_bf16 a[108:111], v[212:215], v[168:171], a[108:111]\n\t"
  "v_exp_f32_e32 v152, v152\n\t"
  "v_mfma_f32_16x16x32_bf16 a[100:103], v[208:211], v[168:171], a[100:103]\n\t"
  "ds_read_b128 v[200:203], v232 offset:33792\n\t"
  "v_mfma_f32_16x16x32_bf16 v[96:99], v[208:211], v[176:179], v[96:99]\n\t"
  "ds_read_b128 v[204:207], v232 offset:34304\n\t"
  "v_add_f32_e32 v242, v242, v152\n\t"
  "v_mfma_f32_16x16x32_bf16 v[100:103], v[212:215], v[176:179], v[100:103]\n\t"
  "v_exp_f32_e32 v153, v153\n\t"
  "v_mfma_f32_16x16x32_bf16 v[108:111], v[212:215], v[184:187], v[108:111]\n\t"
  "v_exp_f32_e32 v154, v154\n\t"
  "v_mfma_f32_16x16x32_bf16 v[104:107], v[208:211], v[184:187], v[104:107]\n\t"
  "v_add_f32_e32 v242, v242, v153\n\t"
  "v_cvt_pk_bf16_f32 v152, v152, v153\n\t"
  "v_mfma_f32_16x16x32_bf16 v[120:123], v[216:219], v[184:187], v[120:123]\n\t"
  "v_exp_f32_e32 v155, v155\n\t"
  "v_mfma_f32_16x16x32_bf16 v[124:127], v[220:223], v[184:187], v[124:127]\n\t"
  "v_add_f32_e32 v242, v242, v154\n\t"
  "v_mfma_f32_16x16x32_bf16 v[116:119], v[220:223], v[176:179], v[116:119]\n\t"
  "v_add_f32_e32 v242, v242, v155\n\t"
  "v_cvt_pk_bf16_f32 v153, v154, v155\n\t"
  "v_mfma_f32_16x16x32_bf16 v[112:115], v[216:219], v[176:179], v[112:115]\n\t"
  "v_exp_f32_e32 v132, v132\n\t"
  "v_mfma_f32_16x16x32_bf16 a[116:119], v[216:219], v[168:171], a[116:119]\n\t"
  "s_lshr_b32 s64, s63, 3\n\t"
  "s_lshl_b32 s64, s64, 2\n\t"
  "s_add_u32 s64, s64, s62\n\t"
  "s_mul_i32 s64, s64, 384\n\t"
  "s_min_u32 s64, s64, 36480\n\t"
  "v_min_u32_e32 v246, 2, v254\n\t"
  "v_lshlrev_b32_e32 v246, 7, v246\n\t"
  "v_add_u32_e32 v246, s64, v246\n\t"
  "s_getpc_b64 s[92:93]\n\t"
  "global_load_dword v255, v246, s[92:93]\n\t"
  "v_mfma_f32_16x16x32_bf16 a[124:127], v[220:223], v[168:171], a[124:127]\n\t"
  "v_exp_f32_e32 v133, v133\n\t"
  "v_mfma_f32_16x16x32_bf16 a[120:123], v[220:223], v[160:163], a[120:123]\n\t"
  "v_add_f32_e32 v239, v239, v132\n\t"
  "v_mfma_f32_16x16x32_bf16 a[112:115], v[216:219], v[160:163], a[112:115]\n\t"
  "v_add_f32_e32 v239, v239, v133\n\t"
  "s_waitcnt lgkmcnt(0)\n\t"
  "v_mfma_f32_16x16x32_bf16 v[160:163], v[192:195], a[128:131], -4.0\n\t"
  "ds_read_b128 v[208:211], v232 offset:34816\n\t"
  "s_mul_i32 s64, s60, 229376\n\t"
  "s_add_u32 s16, s48, s64\n\t"
  "s_addc_u32 s17, s49, 0\n\t"
  "v_mfma_f32_16x16x32_bf16 v[160:163], v[196:199], a[144:147], v[160:163]\n\t"
  "ds_read_b128 v[212:215], v232 offset:35328\n\t"
  "s_and_b32 s17, s17, 0xffff\n\t"
  "v_mfma_f32_16x16x32_bf16 v[160:163], v[200:203], a[160:163], v[160:163]\n\t"
  "v_cvt_pk_bf16_f32 v130, v132, v133\n\t"
  "s_mov_b32 s18, 229376\n\t"
  "v_mfma_f32_16x16x32_bf16 v[160:163], v[204:207], a[176:179], v[160:163]\n\t"
  "ds_read_b128 v[216:219], v232 offset:35840\n\t"
  "s_mul_i32 s36, s62, 57344\n\t"
  "v_mfma_f32_16x16x32_bf16 v[168:171], v[204:207], a[180:183], -4.0\n\t"
  "ds_read_b128 v[220:223], v232 offset:36352\n\t"
  "v_bfe_u32 v246, v254, 2, 3\n\t"
  "v_mfma_f32_16x16x32_bf16 v[168:171], v[200:203], a[164:167], v[168:171]\n\t"
  "v_mul_u32_u24_e32 v246, 384, v246\n\t"
  "v_mfma_f32_16x16x32_bf16 v[168:171], v[196:199], a[148:151], v[168:171]\n\t"
  "v_add_u32_e32 v246, v246, v235\n\t"
  "v_mfma_f32_16x16x32_bf16 v[168:171], v[192:195], a[132:135], v[168:171]\n\t"
  "v_subrev_u32_e32 v246, 3072, v246\n\t"
  "v_mfma_f32_16x16x32_bf16 v[176:179], v[192:195], a[136:139], -4.0\n\t"
  "v_add_u32_e32 v235, 0, v246\n\t"
  "v_mfma_f32_16x16x32_bf16 v[176:179], v[196:199], a[152:155], v[176:179]\n\t"
  "v_add_u32_e32 v236, 6144, v246\n\t"
  "v_mfma_f32_16x16x32_bf16 v[176:179], v[200:203], a[168:171], v[176:179]\n\t"
  "v_add_u32_e32 v237, 12288, v246\n\t"
  "v_mfma_f32_16x16x32_bf16 v[176:179], v[204:207], a[184:187], v[176:179]\n\t"
  "v_add_u32_e32 v238, 18432, v246\n\t"
  "v_mfma_f32_16x16x32_bf16 v[184:187], v[204:207], a[188:191], -4.0\n\t"
  "v_exp_f32_e32 v134, v134\n\t"
  "v_mfma_f32_16x16x32_bf16 v[184:187], v[200:203], a[172:175], v[184:187]\n\t"
  "v_exp_f32_e32 v135, v135\n\t"
  "v_mfma_f32_16x16x32_bf16 v[184:187], v[196:199], a[156:159], v[184:187]\n\t"
  "v_add_f32_e32 v239, v239, v134\n\t"
  "v_mfma_f32_16x16x32_bf16 v[184:187], v[192:195], a[140:143], v[184:187]\n\t"
  "v_add_f32_e32 v239, v239, v135\n\t"
  "s_waitcnt lgkmcnt(0)\n\t"
  "v_mfma_f32_16x16x32_bf16 v[184:187], v[208:211], a[204:207], v[184:187]\n\t"
  "ds_read_b128 v[192:195], v232 offset:40960\n\t"
  "v_cvt_pk_bf16_f32 v131, v134, v135\n\t"
  "v_mfma_f32_16x16x32_bf16 v[184:187], v[212:215], a[220:223], v[184:187]\n\t"
  "ds_read_b128 v[196:199], v232 offset:41472\n\t"
  "v_mfma_f32_16x16x32_bf16 v[184:187], v[216:219], a[236:239], v[184:187]\n\t"
  "v_exp_f32_e32 v140, v140\n\t"
  "v_mfma_f32_16x16x32_bf16 v[184:187], v[220:223], a[252:255], v[184:187]\n\t"
  "ds_read_b128 v[200:203], v232 offset:41984\n\t"
  "v_mfma_f32_16x16x32_bf16 v[176:179], v[220:223], a[248:251], v[176:179]\n\t"
  "ds_read_b128 v[204:207], v232 offset:42496\n\t"
  "v_add_f32_e32 v240, v240, v140\n\t"
  "v_mfma_f32_16x16x32_bf16 v[176:179], v[216:219], a[232:235], v[176:179]\n\t"
  "v_exp_f32_e32 v141, v141\n\t"
  "v_mfma_f32_16x16x32_bf16 v[176:179], v[212:215], a[216:219], v[176:179]\n\t"
  "v_exp_f32_e32 v142, v142\n\t"
  "v_mfma_f32_16x16x32_bf16 v[176:179], v[208:211], a[200:203], v[176:179]\n\t"
  "v_add_f32_e32 v240, v240, v141\n\t"
  "v_cvt_pk_bf16_f32 v138, v140, v141\n\t"
  "v_mfma_f32_16x16x32_bf16 v[168:171], v[208:211], a[196:199], v[168:171]\n\t"
  "v_exp_f32_e32 v143, v143\n\t"
  "v_mfma_f32_16x16x32_bf16 v[168:171], v[212:215], a[212:215], v[168:171]\n\t"
  "v_add_f32_e32 v240, v240, v142\n\t"
  "v_mfma_f32_16x16x32_bf16 v[168:171], v[216:219], a[228:231], v[168:171]\n\t"
  "v_add_f32_e32 v240, v240, v143\n\t"
  "v_cvt_pk_bf16_f32 v139, v142, v143\n\t"
  "v_mfma_f32_16x16x32_bf16 v[168:171], v[220:223], a[244:247], v[168:171]\n\t"
  "v_exp_f32_e32 v148, v148\n\t"
  "v_mfma_f32_16x16x32_bf16 v[160:163], v[220:223], a[240:243], v[160:163]\n\t"
  "v_exp_f32_e32 v149, v149\n\t"
  "v_mfma_f32_16x16x32_bf16 v[160:163], v[216:219], a[224:227], v[160:163]\n\t"
  "v_add_f32_e32 v241, v241, v148\n\t"
  "v_mfma_f32_16x16x32_bf16 v[160:163], v[212:215], a[208:211], v[160:163]\n\t"
  "v_exp_f32_e32 v150, v150\n\t"
  "v_mfma_f32_16x16x32_bf16 v[160:163], v[208:211], a[192:195], v[160:163]\n\t"
  "v_add_f32_e32 v241, v241, v149\n\t"
  "s_waitcnt lgkmcnt(0)\n\t"
  "v_mfma_f32_16x16x32_bf16 v[164:167], v[192:195], a[128:131], -4.0\n\t"
  "ds_read_b128 v[208:211], v232 offset:43008\n\t"
  "v_cvt_pk_bf16_f32 v146, v148, v149\n\t"
  "v_mfma_f32_16x16x32_bf16 v[164:167], v[196:199], a[144:147], v[164:167]\n\t"
  "ds_read_b128 v[212:215], v232 offset:43520\n\t"
  "v_add_f32_e32 v241, v241, v150\n\t"
  "v_mfma_f32_16x16x32_bf16 v[164:167], v[200:203], a[160:163], v[164:167]\n\t"
  "v_exp_f32_e32 v151, v151\n\t"
  "v_mfma_f32_16x16x32_bf16 v[164:167], v[204:207], a[176:179], v[164:167]\n\t"
  "ds_read_b128 v[216:219], v232 offset:44032\n\t"
  "v_mfma_f32_16x16x32_bf16 v[172:175], v[204:207], a[180:183], -4.0\n\t"
  "ds_read_b128 v[220:223], v232 offset:44544\n\t"
  "v_add_f32_e32 v241, v241, v151\n\t"
  "v_mfma_f32_16x16x32_bf16 v[172:175], v[200:203], a[164:167], v[172:175]\n\t"
  "v_cvt_pk_bf16_f32 v147, v150, v151\n\t"
  "v_mfma_f32_16x16x32_bf16 v[172:175], v[196:199], a[148:151], v[172:175]\n\t"
  "v_exp_f32_e32 v156, v156\n\t"
  "v_mfma_f32_16x16x32_bf16 v[172:175], v[192:195], a[132:135], v[172:175]\n\t"
  "v_exp_f32_e32 v157, v157\n\t"
  "v_mfma_f32_16x16x32_bf16 v[180:183], v[192:195], a[136:139], -4.0\n\t"
  "v_add_f32_e32 v242, v242, v156\n\t"
  "v_mfma_f32_16x16x32_bf16 v[180:183], v[196:199], a[152:155], v[180:183]\n\t"
  "v_exp_f32_e32 v158, v158\n\t"
  "v_mfma_f32_16x16x32_bf16 v[180:183], v[200:203], a[168:171], v[180:183]\n\t"
  "v_add_f32_e32 v242, v242, v157\n\t"
  "v_cvt_pk_bf16_f32 v154, v156, v157\n\t"
  "v_mfma_f32_16x16x32_bf16 v[180:183], v[204:207], a[184:187], v[180:183]\n\t"
  "v_exp_f32_e32 v159, v159\n\t"
  "v_mfma_f32_16x16x32_bf16 v[188:191], v[204:207], a[188:191], -4.0\n\t"
  "v_add_f32_e32 v242, v242, v158\n\t"
  "v_mfma_f32_16x16x32_bf16 v[188:191], v[200:203], a[172:175], v[188:191]\n\t"
  "v_add_f32_e32 v242, v242, v159\n\t"
  "v_cvt_pk_bf16_f32 v155, v158, v159\n\t"
  "v_mfma_f32_16x16x32_bf16 v[188:191], v[196:199], a[156:159], v[188:191]\n\t"
  "s_waitcnt vmcnt(1) lgkmcnt(0)\n\t"
  "s_barrier\n\t"
  "v_mfma_f32_16x16x32_bf16 v[188:191], v[192:195], a[140:143], v[188:191]\n\t"
  "v_mfma_f32_16x16x32_bf16 v[188:191], v[208:211], a[204:207], v[188:191]\n\t"
  "ds_read_b64_tr_b16 v[192:193], v233\n\t"
  "v_mfma_f32_16x16x32_bf16 v[188:191], v[212:215], a[220:223], v[188:191]\n\t"
  "ds_read_b64_tr_b16 v[194:195], v233 offset:8192\n\t"
  "s_add_u32 m0, s30, 131072\n\t"
  "v_mfma_f32_16x16x32_bf16 v[188:191], v[216:219], a[236:239], v[188:191]\n\t"
  "buffer_load_dwordx4 v235, s[16:19], s36 offen lds\n\t"
  "v_mfma_f32_16x16x32_bf16 v[188:191], v[220:223], a[252:255], v[188:191]\n\t"
  "ds_read_b64_tr_b16 v[196:197], v234\n\t"
  "v_mfma_f32_16x16x32_bf16 v[180:183], v[220:223], a[248:251], v[180:183]\n\t"
  "ds_read_b64_tr_b16 v[198:199], v234 offset:8192\n\t"
  "v_mfma_f32_16x16x32_bf16 v[180:183], v[216:219], a[232:235], v[180:183]\n\t"
  "ds_read_b64_tr_b16 v[200:201], v233 offset:512\n\t"
  "v_mfma_f32_16x16x32_bf16 v[180:183], v[212:215], a[216:219], v[180:183]\n\t"
  "buffer_load_dwordx4 v236, s[16:19], s36 offen offset:1024 lds\n\t"
  "v_mfma_f32_16x16x32_bf16 v[180:183], v[208:211], a[200:203], v[180:183]\n\t"
  "ds_read_b64_tr_b16 v[202:203], v233 offset:8704\n\t"
  "v_mfma_f32_16x16x32_bf16 v[172:175], v[208:211], a[196:199], v[172:175]\n\t"
  "ds_read_b64_tr_b16 v[204:205], v234 offset:512\n\t"
  "v_mfma_f32_16x16x32_bf16 v[172:175], v[212:215], a[212:215], v[172:175]\n\t"
  "ds_read_b64_tr_b16 v[206:207], v234 offset:8704\n\t"
  "v_mfma_f32_16x16x32_bf16 v[172:175], v[216:219], a[228:231], v[172:175]\n\t"
  "buffer_load_dwordx4 v237, s[16:19], s36 offen offset:2048 lds\n\t"
  "v_mfma_f32_16x16x32_bf16 v[172:175], v[220:223], a[244:247], v[172:175]\n\t"
  "v_exp_f32_e32 v160, v160\n\t"
  "v_mfma_f32_16x16x32_bf16 v[164:167], v[220:223], a[240:243], v[164:167]\n\t"
  "v_exp_f32_e32 v161, v161\n\t"
  "v_mfma_f32_16x16x32_bf16 v[164:167], v[216:219], a[224:227], v[164:167]\n\t"
  "v_add_f32_e32 v239, v239, v160\n\t"
  "v_mfma_f32_16x16x32_bf16 v[164:167], v[212:215], a[208:211], v[164:167]\n\t"
  "buffer_load_dwordx4 v238, s[16:19], s36 offen offset:3072 lds\n\t"
  "v_mfma_f32_16x16x32_bf16 v[164:167], v[208:211], a[192:195], v[164:167]\n\t"
  "s_add_u32 s36, s36, 28672\n\t"
  "s_waitcnt lgkmcnt(0)\n\t"
  "v_mfma_f32_16x16x32_bf16 a[0:3], v[192:195], v[128:131], a[0:3]\n\t"
  "ds_read_b64_tr_b16 v[208:209], v233 offset:1024\n\t"
  "v_add_f32_e32 v239, v239, v161\n\t"
  "v_mfma_f32_16x16x32_bf16 a[8:11], v[196:199], v[128:131], a[8:11]\n\t"
  "ds_read_b64_tr_b16 v[210:211], v233 offset:9216\n\t"
  "s_add_u32 m0, s30, 135168\n\t"
  "v_mfma_f32_16x16x32_bf16 a[12:15], v[196:199], v[136:139], a[12:15]\n\t"
  "buffer_load_dwordx4 v235, s[16:19], s36 offen lds\n\t"
  "v_mfma_f32_16x16x32_bf16 a[4:7], v[192:195], v[136:139], a[4:7]\n\t"
  "ds_read_b64_tr_b16 v[212:213], v234 offset:1024\n\t"
  "v_cvt_pk_bf16_f32 v160, v160, v161\n\t"
  "v_mfma_f32_16x16x32_bf16 v[0:3], v[192:195], v[144:147], v[0:3]\n\t"
  "ds_read_b64_tr_b16 v[214:215], v234 offset:9216\n\t"
  "v_mfma_f32_16x16x32_bf16 v[4:7], v[196:199], v[144:147], v[4:7]\n\t"
  "ds_read_b64_tr_b16 v[216:217], v233 offset:1536\n\t"
  "v_mfma_f32_16x16x32_bf16 v[12:15], v[196:199], v[152:155], v[12:15]\n\t"
  "buffer_load_dwordx4 v236, s[16:19], s36 offen offset:1024 lds\n\t"
  "v_mfma_f32_16x16x32_bf16 v[8:11], v[192:195], v[152:155], v[8:11]\n\t"
  "ds_read_b64_tr_b16 v[218:219], v233 offset:9728\n\t"
  "v_mfma_f32_16x16x32_bf16 v[24:27], v[200:203], v[152:155], v[24:27]\n\t"
  "ds_read_b64_tr_b16 v[220:221], v234 offset:1536\n\t"
  "v_mfma_f32_16x16x32_bf16 v[28:31], v[204:207], v[152:155], v[28:31]\n\t"
  "ds_read_b64_tr_b16 v[222:223], v234 offset:9728\n\t"
  "v_mfma_f32_16x16x32_bf16 v[20:23], v[204:207], v[144:147], v[20:23]\n\t"
  "buffer_load_dwordx4 v237, s[16:19], s36 offen offset:2048 lds\n\t"
  "v_mfma_f32_16x16x32_bf16 v[16:19], v[200:203], v[144:147], v[16:19]\n\t"
  "v_exp_f32_e32 v162, v162\n\t"
  "v_mfma_f32_16x16x32_bf16 a[20:23], v[200:203], v[136:139], a[20:23]\n\t"
  "v_exp_f32_e32 v163, v163\n\t"
  "v_mfma_f32_16x16x32_bf16 a[28:31], v[204:207], v[136:139], a[28:31]\n\t"
  "v_add_f32_e32 v239, v239, v162\n\t"
  "v_mfma_f32_16x16x32_bf16 a[24:27], v[204:207], v[128:131], a[24:27]\n\t"
  "buffer_load_dwordx4 v238, s[16:19], s36 offen offset:3072 lds\n\t"
  "v_mfma_f32_16x16x32_bf16 a[16:19], v[200:203], v[128:131], a[16:19]\n\t"
  "s_add_u32 s36, s36, 4294938752\n\t"
  "s_waitcnt lgkmcnt(0)\n\t"
  "v_mfma_f32_16x16x32_bf16 a[32:35], v[208:211], v[128:131], a[32:35]\n\t"
  "ds_read_b64_tr_b16 v[192:193], v233 offset:2048\n\t"
  "v_add_f32_e32 v239, v239, v163\n\t"
  "v_mfma_f32_16x16x32_bf16 a[40:43], v[212:215], v[128:131], a[40:43]\n\t"
  "ds_read_b64_tr_b16 v[194:195], v233 offset:10240\n\t"
  "s_add_u32 m0, s30, 32768\n\t"
  "v_mfma_f32_16x16x32_bf16 a[44:47], v[212:215], v[136:139], a[44:47]\n\t"
  "buffer_load_dwordx4 v235, s[16:19], s36 offen lds\n\t"
  "v_mfma_f32_16x16x32_bf16 a[36:39], v[208:211], v[136:139], a[36:39]\n\t"
  "ds_read_b64_tr_b16 v[196:197], v234 offset:2048\n\t"
  "v_cvt_pk_bf16_f32 v161, v162, v163\n\t"
  "v_mfma_f32_16x16x32_bf16 v[32:35], v[208:211], v[144:147], v[32:35]\n\t"
  "ds_read_b64_tr_b16 v[198:199], v234 offset:10240\n\t"
  "v_mfma_f32_16x16x32_bf16 v[36:39], v[212:215], v[144:147], v[36:39]\n\t"
  "ds_read_b64_tr_b16 v[200:201], v233 offset:2560\n\t"
  "v_mfma_f32_16x16x32_bf16 v[44:47], v[212:215], v[152:155], v[44:47]\n\t"
  "buffer_load_dwordx4 v236, s[16:19], s36 offen offset:1024 lds\n\t"
  "v_mfma_f32_16x16x32_bf16 v[40:43], v[208:211], v[152:155], v[40:43]\n\t"
  "ds_read_b64_tr_b16 v[202:203], v233 offset:10752\n\t"
  "v_mfma_f32_16x16x32_bf16 v[56:59], v[216:219], v[152:155], v[56:59]\n\t"
  "ds_read_b64_tr_b16 v[204:205], v234 offset:2560\n\t"
  "v_mfma_f32_16x16x32_bf16 v[60:63], v[220:223], v[152:155], v[60:63]\n\t"
  "ds_read_b64_tr_b16 v[206:207], v234 offset:10752\n\t"
  "v_mfma_f32_16x16x32_bf16 v[52:55], v[220:223], v[144:147], v[52:55]\n\t"
  "buffer_load_dwordx4 v237, s[16:19], s36 offen offset:2048 lds\n\t"
  "v_mfma_f32_16x16x32_bf16 v[48:51], v[216:219], v[144:147], v[48:51]\n\t"
  "v_exp_f32_e32 v168, v168\n\t"
  "v_mfma_f32_16x16x32_bf16 a[52:55], v[216:219], v[136:139], a[52:55]\n\t"
  "v_exp_f32_e32 v169, v169\n\t"
  "v_mfma_f32_16x16x32_bf16 a[60:63], v[220:223], v[136:139], a[60:63]\n\t"
  "v_add_f32_e32 v240, v240, v168\n\t"
  "v_mfma_f32_16x16x32_bf16 a[56:59], v[220:223], v[128:131], a[56:59]\n\t"
  "buffer_load_dwordx4 v238, s[16:19], s36 offen offset:3072 lds\n\t"
  "v_mfma_f32_16x16x32_bf16 a[48:51], v[216:219], v[128:131], a[48:51]\n\t"
  "s_add_u32 s36, s36, 28672\n\t"
  "s_waitcnt lgkmcnt(0)\n\t"
  "v_mfma_f32_16x16x32_bf16 a[64:67], v[192:195], v[128:131], a[64:67]\n\t"
  "ds_read_b64_tr_b16 v[208:209], v233 offset:3072\n\t"
  "v_add_f32_e32 v240, v240, v169\n\t"
  "v_mfma_f32_16x16x32_bf16 a[72:75], v[196:199], v[128:131], a[72:75]\n\t"
  "ds_read_b64_tr_b16 v[210:211], v233 offset:11264\n\t"
  "s_add_u32 m0, s30, 36864\n\t"
  "v_mfma_f32_16x16x32_bf16 a[76:79], v[196:199], v[136:139], a[76:79]\n\t"
  "buffer_load_dwordx4 v235, s[16:19], s36 offen lds\n\t"
  "v_mfma_f32_16x16x32_bf16 a[68:71], v[192:195], v[136:139], a[68:71]\n\t"
  "ds_read_b64_tr_b16 v[212:213], v234 offset:3072\n\t"
  "v_cvt_pk_bf16_f32 v168, v168, v169\n\t"
  "v_mfma_f32_16x16x32_bf16 v[64:67], v[192:195], v[144:147], v[64:67]\n\t"
  "ds_read_b64_tr_b16 v[214:215], v234 offset:11264\n\t"
  "v_mfma_f32_16x16x32_bf16 v[68:71], v[196:199], v[144:147], v[68:71]\n\t"
  "ds_read_b64_tr_b16 v[216:217], v233 offset:3584\n\t"
  "v_mfma_f32_16x16x32_bf16 v[76:79], v[196:199], v[152:155], v[76:79]\n\t"
  "buffer_load_dwordx4 v236, s[16:19], s36 offen offset:1024 lds\n\t"
  "v_mfma_f32_16x16x32_bf16 v[72:75], v[192:195], v[152:155], v[72:75]\n\t"
  "ds_read_b64_tr_b16 v[218:219], v233 offset:11776\n\t"
  "v_mfma_f32_16x16x32_bf16 v[88:91], v[200:203], v[152:155], v[88:91]\n\t"
  "ds_read_b64_tr_b16 v[220:221], v234 offset:3584\n\t"
  "v_mfma_f32_16x16x32_bf16 v[92:95], v[204:207], v[152:155], v[92:95]\n\t"
  "ds_read_b64_tr_b16 v[222:223], v234 offset:11776\n\t"
  "v_mfma_f32_16x16x32_bf16 v[84:87], v[204:207], v[144:147], v[84:87]\n\t"
  "buffer_load_dwordx4 v237, s[16:19], s36 offen offset:2048 lds\n\t"
  "v_mfma_f32_16x16x32_bf16 v[80:83], v[200:203], v[144:147], v[80:83]\n\t"
  "v_exp_f32_e32 v170, v170\n\t"
  "v_mfma_f32_16x16x32_bf16 a[84:87], v[200:203], v[136:139], a[84:87]\n\t"
  "v_exp_f32_e32 v171, v171\n\t"
  "v_mfma_f32_16x16x32_bf16 a[92:95], v[204:207], v[136:139], a[92:95]\n\t"
  "v_add_f32_e32 v240, v240, v170\n\t"
  "v_mfma_f32_16x16x32_bf16 a[88:91], v[204:207], v[128:131], a[88:91]\n\t"
  "buffer_load_dwordx4 v238, s[16:19], s36 offen offset:3072 lds\n\t"
  "v_mfma_f32_16x16x32_bf16 a[80:83], v[200:203], v[128:131], a[80:83]\n\t"
  "s_add_u32 s36, s36, 4294938752\n\t"
  "s_waitcnt lgkmcnt(0)\n\t"
  "v_mfma_f32_16x16x32_bf16 a[96:99], v[208:211], v[128:131], a[96:99]\n\t"
  "v_add_f32_e32 v240, v240, v171\n\t"
  "v_cvt_pk_bf16_f32 v169, v170, v171\n\t"
  "v_mfma_f32_16x16x32_bf16 a[104:107], v[212:215], v[128:131], a[104:107]\n\t"
  "v_exp_f32_e32 v176, v176\n\t"
  "v_mfma_f32_16x16x32_bf16 a[108:111], v[212:215], v[136:139], a[108:111]\n\t"
  "v_exp_f32_e32 v177, v177\n\t"
  "v_mfma_f32_16x16x32_bf16 a[100:103], v[208:211], v[136:139], a[100:103]\n\t"
  "v_add_f32_e32 v241, v241, v176\n\t"
  "v_mfma_f32_16x16x32_bf16 v[96:99], v[208:211], v[144:147], v[96:99]\n\t"
  "v_exp_f32_e32 v178, v178\n\t"
  "v_mfma_f32_16x16x32_bf16 v[100:103], v[212:215], v[144:147], v[100:103]\n\t"
  "v_add_f32_e32 v241, v241, v177\n\t"
  "v_cvt_pk_bf16_f32 v176, v176, v177\n\t"
  "v_mfma_f32_16x16x32_bf16 v[108:111], v[212:215], v[152:155], v[108:111]\n\t"
  "v_exp_f32_e32 v179, v179\n\t"
  "v_mfma_f32_16x16x32_bf16 v[104:107], v[208:211], v[152:155], v[104:107]\n\t"
  "v_add_f32_e32 v241, v241, v178\n\t"
  "v_mfma_f32_16x16x32_bf16 v[120:123], v[216:219], v[152:155], v[120:123]\n\t"
  "v_add_f32_e32 v241, v241, v179\n\t"
  "v_cvt_pk_bf16_f32 v177, v178, v179\n\t"
  "v_mfma_f32_16x16x32_bf16 v[124:127], v[220:223], v[152:155], v[124:127]\n\t"
  "v_exp_f32_e32 v184, v184\n\t"
  "v_mfma_f32_16x16x32_bf16 v[116:119], v[220:223], v[144:147], v[116:119]\n\t"
  "v_exp_f32_e32 v185, v185\n\t"
  "v_mfma_f32_16x16x32_bf16 v[112:115], v[216:219], v[144:147], v[112:115]\n\t"
  "v_add_f32_e32 v242, v242, v184\n\t"
  "v_mfma_f32_16x16x32_bf16 a[116:119], v[216:219], v[136:139], a[116:119]\n\t"
  "v_exp_f32_e32 v186, v186\n\t"
  "v_mfma_f32_16x16x32_bf16 a[124:127], v[220:223], v[136:139], a[124:127]\n\t"
  "v_add_f32_e32 v242, v242, v185\n\t"
  "v_cvt_pk_bf16_f32 v184, v184, v185\n\t"
  "v_mfma_f32_16x16x32_bf16 a[120:123], v[220:223], v[128:131], a[120:123]\n\t"
  "v_exp_f32_e32 v187, v187\n\t"
  "v_mfma_f32_16x16x32_bf16 a[112:115], v[216:219], v[128:131], a[112:115]\n\t"
  "v_add_f32_e32 v242, v242, v186\n\t"
  "v_add_f32_e32 v242, v242, v187\n\t"
  "v_cvt_pk_bf16_f32 v185, v186, v187\n\t"
  "v_exp_f32_e32 v164, v164\n\t"
  "v_exp_f32_e32 v165, v165\n\t"
  "v_add_f32_e32 v239, v239, v164\n\t"
  "v_exp_f32_e32 v166, v166\n\t"
  "v_add_f32_e32 v239, v239, v165\n\t"
  "v_cvt_pk_bf16_f32 v162, v164, v165\n\t"
  "v_exp_f32_e32 v167, v167\n\t"
  "v_add_f32_e32 v239, v239, v166\n\t"
  "v_add_f32_e32 v239, v239, v167\n\t"
  "v_cvt_pk_bf16_f32 v163, v166, v167\n\t"
  "v_exp_f32_e32 v172, v172\n\t"
  "v_exp_f32_e32 v173, v173\n\t"
  "v_add_f32_e32 v240, v240, v172\n\t"
  "v_exp_f32_e32 v174, v174\n\t"
  "v_add_f32_e32 v240, v240, v173\n\t"
  "v_cvt_pk_bf16_f32 v170, v172, v173\n\t"
  "v_exp_f32_e32 v175, v175\n\t"
  "v_add_f32_e32 v240, v240, v174\n\t"
  "v_add_f32_e32 v240, v240, v175\n\t"
  "v_cvt_pk_bf16_f32 v171, v174, v175\n\t"
  "v_exp_f32_e32 v180, v180\n\t"
  "v_exp_f32_e32 v181, v181\n\t"
  "v_add_f32_e32 v241, v241, v180\n\t"
  "v_exp_f32_e32 v182, v182\n\t"
  "v_add_f32_e32 v241, v241, v181\n\t"
  "v_cvt_pk_bf16_f32 v178, v180, v181\n\t"
  "v_exp_f32_e32 v183, v183\n\t"
  "v_add_f32_e32 v241, v241, v182\n\t"
  "v_add_f32_e32 v241, v241, v183\n\t"
  "v_cvt_pk_bf16_f32 v179, v182, v183\n\t"
  "v_exp_f32_e32 v188, v188\n\t"
  "v_exp_f32_e32 v189, v189\n\t"
  "v_add_f32_e32 v242, v242, v188\n\t"
  "v_exp_f32_e32 v190, v190\n\t"
  "v_add_f32_e32 v242, v242, v189\n\t"
  "v_cvt_pk_bf16_f32 v186, v188, v189\n\t"
  "v_exp_f32_e32 v191, v191\n\t"
  "v_add_f32_e32 v242, v242, v190\n\t"
  "v_add_f32_e32 v242, v242, v191\n\t"
  "v_cvt_pk_bf16_f32 v187, v190, v191\n\t"
  ".Ldrain:\n\t"
  "ds_write2_b32 v250, v241, v242 offset1:1\n\t"
  "v_subrev_u32_e32 v244, 65536, v233\n\t"
  "v_subrev_u32_e32 v245, 65536, v234\n\t"
  "s_waitcnt lgkmcnt(0)\n\t"
  "ds_read_b64_tr_b16 v[192:193], v244\n\t"
  "ds_read_b64_tr_b16 v[194:195], v244 offset:8192\n\t"
  "ds_read_b64_tr_b16 v[196:197], v245\n\t"
  "ds_read_b64_tr_b16 v[198:199], v245 offset:8192\n\t"
  "ds_read_b64_tr_b16 v[200:201], v244 offset:512\n\t"
  "ds_read_b64_tr_b16 v[202:203], v244 offset:8704\n\t"
  "ds_read_b64_tr_b16 v[204:205], v245 offset:512\n\t"
  "ds_read_b64_tr_b16 v[206:207], v245 offset:8704\n\t"
  "s_barrier\n\t"
  "ds_read_b64 v[140:141], v251\n\t"
  "s_waitcnt lgkmcnt(1)\n\t"
  "v_mfma_f32_16x16x32_bf16 a[0:3], v[192:195], v[160:163], a[0:3]\n\t"
  "ds_read_b64_tr_b16 v[208:209], v244 offset:1024\n\t"
  "s_waitcnt lgkmcnt(1)\n\t"
  "v_add_f32_e32 v140, v140, v239\n\t"
  "v_mfma_f32_16x16x32_bf16 a[8:11], v[196:199], v[160:163], a[8:11]\n\t"
  "ds_read_b64_tr_b16 v[210:211], v244 offset:9216\n\t"
  "v_add_f32_e32 v141, v141, v240\n\t"
  "v_mfma_f32_16x16x32_bf16 a[12:15], v[196:199], v[168:171], a[12:15]\n\t"
  "ds_read_b64_tr_b16 v[212:213], v245 offset:1024\n\t"
  "s_mov_b32 s64, 0x21800000\n\t"
  "v_mfma_f32_16x16x32_bf16 a[4:7], v[192:195], v[168:171], a[4:7]\n\t"
  "ds_read_b64_tr_b16 v[214:215], v245 offset:9216\n\t"
  "v_mov_b32_e32 v151, 0x5d800000\n\t"
  "v_mfma_f32_16x16x32_bf16 v[0:3], v[192:195], v[176:179], v[0:3]\n\t"
  "ds_read_b64_tr_b16 v[216:217], v244 offset:1536\n\t"
  "v_permlane16_swap_b32 v140, v141\n\t"
  "v_mfma_f32_16x16x32_bf16 v[4:7], v[196:199], v[176:179], v[4:7]\n\t"
  "ds_read_b64_tr_b16 v[218:219], v244 offset:9728\n\t"
  "v_add_f32_e32 v140, v140, v141\n\t"
  "v_mfma_f32_16x16x32_bf16 v[12:15], v[196:199], v[184:187], v[12:15]\n\t"
  "ds_read_b64_tr_b16 v[220:221], v245 offset:1536\n\t"
  "v_mov_b32_e32 v141, v140\n\t"
  "v_mfma_f32_16x16x32_bf16 v[8:11], v[192:195], v[184:187], v[8:11]\n\t"
  "ds_read_b64_tr_b16 v[222:223], v245 offset:9728\n\t"
  "s_nop 0\n\t"
  "v_mfma_f32_16x16x32_bf16 v[24:27], v[200:203], v[184:187], v[24:27]\n\t"
  "v_permlane32_swap_b32 v141, v140\n\t"
  "v_add_f32_e32 v140, v140, v141\n\t"
  "v_mfma_f32_16x16x32_bf16 v[28:31], v[204:207], v[184:187], v[28:31]\n\t"
  "v_med3_f32 v150, v140, s64, v151\n\t"
  "v_cmp_neq_f32_e32 vcc, v150, v140\n\t"
  "v_mfma_f32_16x16x32_bf16 v[20:23], v[204:207], v[176:179], v[20:23]\n\t"
  "v_rcp_f32_e32 v140, v140\n\t"
  "v_mfma_f32_16x16x32_bf16 v[16:19], v[200:203], v[176:179], v[16:19]\n\t"
  "s_or_b32 s64, vcc_lo, vcc_hi\n\t"
  "v_mov_b32_e32 v151, s64\n\t"
  "v_mfma_f32_16x16x32_bf16 a[20:23], v[200:203], v[168:171], a[20:23]\n\t"
  "ds_write_b32 v252, v151\n\t"
  "v_mfma_f32_16x16x32_bf16 a[28:31], v[204:207], v[168:171], a[28:31]\n\t"
  "v_mfma_f32_16x16x32_bf16 a[24:27], v[204:207], v[160:163], a[24:27]\n\t"
  "s_waitcnt lgkmcnt(0)\n\t"
  "s_barrier\n\t"
  "ds_read_b128 v[144:147], v253\n\t"
  "v_mfma_f32_16x16x32_bf16 a[16:19], v[200:203], v[160:163], a[16:19]\n\t"
  "v_mfma_f32_16x16x32_bf16 a[32:35], v[208:211], v[160:163], a[32:35]\n\t"
  "ds_read_b64_tr_b16 v[192:193], v244 offset:2048\n\t"
  "v_mfma_f32_16x16x32_bf16 a[40:43], v[212:215], v[160:163], a[40:43]\n\t"
  "ds_read_b64_tr_b16 v[194:195], v244 offset:10240\n\t"
  "s_waitcnt lgkmcnt(2)\n\t"
  "v_or3_b32 v151, v144, v145, v146\n\t"
  "v_mfma_f32_16x16x32_bf16 a[44:47], v[212:215], v[168:171], a[44:47]\n\t"
  "ds_read_b64_tr_b16 v[196:197], v245 offset:2048\n\t"
  "v_or_b32_e32 v151, v151, v147\n\t"
  "v_mfma_f32_16x16x32_bf16 a[36:39], v[208:211], v[168:171], a[36:39]\n\t"
  "ds_read_b64_tr_b16 v[198:199], v245 offset:10240\n\t"
  "s_nop 0\n\t"
  "v_mfma_f32_16x16x32_bf16 v[32:35], v[208:211], v[176:179], v[32:35]\n\t"
  "ds_read_b64_tr_b16 v[200:201], v244 offset:2560\n\t"
  "v_readfirstlane_b32 s64, v151\n\t"
  "v_mfma_f32_16x16x32_bf16 v[36:39], v[212:215], v[176:179], v[36:39]\n\t"
  "ds_read_b64_tr_b16 v[202:203], v244 offset:10752\n\t"
  "buffer_store_dword v140, v243, s[24:27], s58 offen sc1\n\t"
  "v_mfma_f32_16x16x32_bf16 v[44:47], v[212:215], v[184:187], v[44:47]\n\t"
  "ds_read_b64_tr_b16 v[204:205], v245 offset:2560\n\t"
  "s_andn2_b32 s64, s64, s68\n\t"
  "v_mfma_f32_16x16x32_bf16 v[40:43], v[208:211], v[184:187], v[40:43]\n\t"
  "ds_read_b64_tr_b16 v[206:207], v245 offset:10752\n\t"
  "s_cmp_lg_u32 s64, 0\n\t"
  "s_cbranch_scc1 .Lfallback\n\t"
  "v_mfma_f32_16x16x32_bf16 v[56:59], v[216:219], v[184:187], v[56:59]\n\t"
  "v_lshrrev_b32_e32 v150, 5, v254\n\t"
  "v_mul_u32_u24_e32 v150, 3072, v150\n\t"
  "v_mfma_f32_16x16x32_bf16 v[60:63], v[220:223], v[184:187], v[60:63]\n\t"
  "v_add_u32_e32 v150, s39, v150\n\t"
  "v_cvt_pk_bf16_f32 v0, v0, v1\n\t"
  "v_mfma_f32_16x16x32_bf16 v[52:55], v[220:223], v[176:179], v[52:55]\n\t"
  "v_cvt_pk_bf16_f32 v1, v2, v3\n\t"
  "v_cvt_pk_bf16_f32 v2, v4, v5\n\t"
  "v_mfma_f32_16x16x32_bf16 v[48:51], v[216:219], v[176:179], v[48:51]\n\t"
  "v_cvt_pk_bf16_f32 v3, v6, v7\n\t"
  "ds_write_b128 v230, v[0:3]\n\t"
  "v_mfma_f32_16x16x32_bf16 a[52:55], v[216:219], v[168:171], a[52:55]\n\t"
  "v_cvt_pk_bf16_f32 v8, v8, v9\n\t"
  "v_cvt_pk_bf16_f32 v9, v10, v11\n\t"
  "v_mfma_f32_16x16x32_bf16 a[60:63], v[220:223], v[168:171], a[60:63]\n\t"
  "v_cvt_pk_bf16_f32 v10, v12, v13\n\t"
  "v_cvt_pk_bf16_f32 v11, v14, v15\n\t"
  "v_mfma_f32_16x16x32_bf16 a[56:59], v[220:223], v[160:163], a[56:59]\n\t"
  "ds_write_b128 v230, v[8:11] offset:1024\n\t"
  "v_cvt_pk_bf16_f32 v16, v16, v17\n\t"
  "v_mfma_f32_16x16x32_bf16 a[48:51], v[216:219], v[160:163], a[48:51]\n\t"
  "v_cvt_pk_bf16_f32 v17, v18, v19\n\t"
  "s_waitcnt lgkmcnt(2)\n\t"
  "v_mfma_f32_16x16x32_bf16 a[64:67], v[192:195], v[160:163], a[64:67]\n\t"
  "ds_read_b64_tr_b16 v[208:209], v244 offset:3072\n\t"
  "v_cvt_pk_bf16_f32 v18, v20, v21\n\t"
  "v_mfma_f32_16x16x32_bf16 a[72:75], v[196:199], v[160:163], a[72:75]\n\t"
  "ds_read_b64_tr_b16 v[210:211], v244 offset:11264\n\t"
  "v_cvt_pk_bf16_f32 v19, v22, v23\n\t"
  "v_mfma_f32_16x16x32_bf16 a[76:79], v[196:199], v[168:171], a[76:79]\n\t"
  "ds_read_b64_tr_b16 v[212:213], v245 offset:3072\n\t"
  "ds_write_b128 v230, v[16:19] offset:2048\n\t"
  "v_mfma_f32_16x16x32_bf16 a[68:71], v[192:195], v[168:171], a[68:71]\n\t"
  "ds_read_b64_tr_b16 v[214:215], v245 offset:11264\n\t"
  "v_cvt_pk_bf16_f32 v24, v24, v25\n\t"
  "v_mfma_f32_16x16x32_bf16 v[64:67], v[192:195], v[176:179], v[64:67]\n\t"
  "ds_read_b64_tr_b16 v[216:217], v244 offset:3584\n\t"
  "v_cvt_pk_bf16_f32 v25, v26, v27\n\t"
  "v_mfma_f32_16x16x32_bf16 v[68:71], v[196:199], v[176:179], v[68:71]\n\t"
  "ds_read_b64_tr_b16 v[218:219], v244 offset:11776\n\t"
  "v_cvt_pk_bf16_f32 v26, v28, v29\n\t"
  "v_mfma_f32_16x16x32_bf16 v[76:79], v[196:199], v[184:187], v[76:79]\n\t"
  "ds_read_b64_tr_b16 v[220:221], v245 offset:3584\n\t"
  "v_cvt_pk_bf16_f32 v27, v30, v31\n\t"
  "v_mfma_f32_16x16x32_bf16 v[72:75], v[192:195], v[184:187], v[72:75]\n\t"
  "ds_read_b64_tr_b16 v[222:223], v245 offset:11776\n\t"
  "ds_write_b128 v230, v[24:27] offset:3072\n\t"
  "v_mfma_f32_16x16x32_bf16 v[88:91], v[200:203], v[184:187], v[88:91]\n\t"
  "v_cvt_pk_bf16_f32 v32, v32, v33\n\t"
  "v_cvt_pk_bf16_f32 v33, v34, v35\n\t"
  "v_mfma_f32_16x16x32_bf16 v[92:95], v[204:207], v[184:187], v[92:95]\n\t"
  "v_cvt_pk_bf16_f32 v34, v36, v37\n\t"
  "v_cvt_pk_bf16_f32 v35, v38, v39\n\t"
  "v_mfma_f32_16x16x32_bf16 v[84:87], v[204:207], v[176:179], v[84:87]\n\t"
  "ds_write_b128 v230, v[32:35] offset:4096\n\t"
  "v_cvt_pk_bf16_f32 v40, v40, v41\n\t"
  "v_mfma_f32_16x16x32_bf16 v[80:83], v[200:203], v[176:179], v[80:83]\n\t"
  "v_cvt_pk_bf16_f32 v41, v42, v43\n\t"
  "v_cvt_pk_bf16_f32 v42, v44, v45\n\t"
  "v_mfma_f32_16x16x32_bf16 a[84:87], v[200:203], v[168:171], a[84:87]\n\t"
  "v_cvt_pk_bf16_f32 v43, v46, v47\n\t"
  "ds_write_b128 v230, v[40:43] offset:5120\n\t"
  "v_mfma_f32_16x16x32_bf16 a[92:95], v[204:207], v[168:171], a[92:95]\n\t"
  "v_cvt_pk_bf16_f32 v48, v48, v49\n\t"
  "v_cvt_pk_bf16_f32 v49, v50, v51\n\t"
  "v_mfma_f32_16x16x32_bf16 a[88:91], v[204:207], v[160:163], a[88:91]\n\t"
  "v_cvt_pk_bf16_f32 v50, v52, v53\n\t"
  "v_cvt_pk_bf16_f32 v51, v54, v55\n\t"
  "v_mfma_f32_16x16x32_bf16 a[80:83], v[200:203], v[160:163], a[80:83]\n\t"
  "ds_write_b128 v230, v[48:51] offset:6144\n\t"
  "s_waitcnt lgkmcnt(4)\n\t"
  "v_mfma_f32_16x16x32_bf16 a[96:99], v[208:211], v[160:163], a[96:99]\n\t"
  "v_cvt_pk_bf16_f32 v56, v56, v57\n\t"
  "v_cvt_pk_bf16_f32 v57, v58, v59\n\t"
  "v_mfma_f32_16x16x32_bf16 a[104:107], v[212:215], v[160:163], a[104:107]\n\t"
  "v_cvt_pk_bf16_f32 v58, v60, v61\n\t"
  "v_cvt_pk_bf16_f32 v59, v62, v63\n\t"
  "v_mfma_f32_16x16x32_bf16 a[108:111], v[212:215], v[168:171], a[108:111]\n\t"
  "ds_write_b128 v230, v[56:59] offset:7168\n\t"
  "v_cvt_pk_bf16_f32 v64, v64, v65\n\t"
  "v_mfma_f32_16x16x32_bf16 a[100:103], v[208:211], v[168:171], a[100:103]\n\t"
  "v_cvt_pk_bf16_f32 v65, v66, v67\n\t"
  "v_cvt_pk_bf16_f32 v66, v68, v69\n\t"
  "v_mfma_f32_16x16x32_bf16 v[96:99], v[208:211], v[176:179], v[96:99]\n\t"
  "v_cvt_pk_bf16_f32 v67, v70, v71\n\t"
  "ds_write_b128 v230, v[64:67] offset:8192\n\t"
  "v_mfma_f32_16x16x32_bf16 v[100:103], v[212:215], v[176:179], v[100:103]\n\t"
  "v_cvt_pk_bf16_f32 v72, v72, v73\n\t"
  "v_cvt_pk_bf16_f32 v73, v74, v75\n\t"
  "v_mfma_f32_16x16x32_bf16 v[108:111], v[212:215], v[184:187], v[108:111]\n\t"
  "v_cvt_pk_bf16_f32 v74, v76, v77\n\t"
  "v_cvt_pk_bf16_f32 v75, v78, v79\n\t"
  "v_mfma_f32_16x16x32_bf16 v[104:107], v[208:211], v[184:187], v[104:107]\n\t"
  "ds_write_b128 v230, v[72:75] offset:9216\n\t"
  "v_cvt_pk_bf16_f32 v80, v80, v81\n\t"
  "v_mfma_f32_16x16x32_bf16 v[120:123], v[216:219], v[184:187], v[120:123]\n\t"
  "v_cvt_pk_bf16_f32 v81, v82, v83\n\t"
  "v_cvt_pk_bf16_f32 v82, v84, v85\n\t"
  "v_mfma_f32_16x16x32_bf16 v[124:127], v[220:223], v[184:187], v[124:127]\n\t"
  "v_cvt_pk_bf16_f32 v83, v86, v87\n\t"
  "ds_write_b128 v230, v[80:83] offset:10240\n\t"
  "v_mfma_f32_16x16x32_bf16 v[116:119], v[220:223], v[176:179], v[116:119]\n\t"
  "v_cvt_pk_bf16_f32 v88, v88, v89\n\t"
  "v_cvt_pk_bf16_f32 v89, v90, v91\n\t"
  "v_mfma_f32_16x16x32_bf16 v[112:115], v[216:219], v[176:179], v[112:115]\n\t"
  "v_cvt_pk_bf16_f32 v90, v92, v93\n\t"
  "v_cvt_pk_bf16_f32 v91, v94, v95\n\t"
  "v_mfma_f32_16x16x32_bf16 a[116:119], v[216:219], v[168:171], a[116:119]\n\t"
  "ds_write_b128 v230, v[88:91] offset:11264\n\t"
  "v_cvt_pk_bf16_f32 v96, v96, v97\n\t"
  "v_mfma_f32_16x16x32_bf16 a[124:127], v[220:223], v[168:171], a[124:127]\n\t"
  "v_cvt_pk_bf16_f32 v97, v98, v99\n\t"
  "v_cvt_pk_bf16_f32 v98, v100, v101\n\t"
  "v_mfma_f32_16x16x32_bf16 a[120:123], v[220:223], v[160:163], a[120:123]\n\t"
  "v_cvt_pk_bf16_f32 v99, v102, v103\n\t"
  "ds_write_b128 v230, v[96:99] offset:12288\n\t"
  "v_mfma_f32_16x16x32_bf16 a[112:115], v[216:219], v[160:163], a[112:115]\n\t"
  "v_cvt_pk_bf16_f32 v104, v104, v105\n\t"
  "v_sub_u32_e32 v192, v244, v150\n\t"
  "v_add_u32_e32 v194, 65536, v192\n\t"
  "v_add_u32_e32 v196, 131072, v192\n\t"
  "v_sub_u32_e32 v193, v245, v150\n\t"
  "v_add_u32_e32 v195, 65536, v193\n\t"
  "v_add_u32_e32 v197, 131072, v193\n\t"
  "v_cvt_pk_bf16_f32 v105, v106, v107\n\t"
  "v_cvt_pk_bf16_f32 v106, v108, v109\n\t"
  "v_cvt_pk_bf16_f32 v107, v110, v111\n\t"
  "ds_write_b128 v230, v[104:107] offset:13312\n\t"
  "v_cvt_pk_bf16_f32 v112, v112, v113\n\t"
  "v_cvt_pk_bf16_f32 v113, v114, v115\n\t"
  "v_cvt_pk_bf16_f32 v114, v116, v117\n\t"
  "v_cvt_pk_bf16_f32 v115, v118, v119\n\t"
  "ds_write_b128 v230, v[112:115] offset:14336\n\t"
  "v_cvt_pk_bf16_f32 v120, v120, v121\n\t"
  "v_cvt_pk_bf16_f32 v121, v122, v123\n\t"
  "v_cvt_pk_bf16_f32 v122, v124, v125\n\t"
  "v_cvt_pk_bf16_f32 v123, v126, v127\n\t"
  "ds_write_b128 v230, v[120:123] offset:15360\n\t"
  "s_waitcnt vmcnt(9) lgkmcnt(0)\n\t"
  "s_barrier\n\t"
  "ds_read_b128 v[64:67], v231\n\t"
  "ds_read_b128 v[68:71], v231 offset:1024\n\t"
  "ds_read_b128 v[72:75], v231 offset:2048\n\t"
  "ds_read_b128 v[76:79], v231 offset:3072\n\t"
  "ds_read_b128 v[80:83], v231 offset:4096\n\t"
  "ds_read_b128 v[84:87], v231 offset:5120\n\t"
  "ds_read_b128 v[88:91], v231 offset:6144\n\t"
  "ds_read_b128 v[92:95], v231 offset:7168\n\t"
  "ds_read_b128 v[96:99], v231 offset:8192\n\t"
  "ds_read_b128 v[100:103], v231 offset:9216\n\t"
  "ds_read_b128 v[104:107], v231 offset:10240\n\t"
  "ds_read_b128 v[108:111], v231 offset:11264\n\t"
  "s_waitcnt lgkmcnt(11)\n\t"
  "v_mfma_f32_16x16x32_bf16 a[0:3], v[226:229], v[64:67], a[0:3]\n\t"
  "ds_read_b128 v[112:115], v231 offset:12288\n\t"
  "v_mfma_f32_16x16x32_bf16 a[8:11], v[224:227], v[64:67], a[8:11]\n\t"
  "ds_read_b128 v[116:119], v231 offset:13312\n\t"
  "s_add_u32 m0, s30, 0\n\t"
  "s_nop 0\n\t"
  "buffer_load_dwordx4 v235, s[16:19], s36 offen lds\n\t"
  "s_waitcnt lgkmcnt(12)\n\t"
  "v_mfma_f32_16x16x32_bf16 a[4:7], v[226:229], v[68:71], a[4:7]\n\t"
  "ds_read_b128 v[120:123], v231 offset:14336\n\t"
  "v_mfma_f32_16x16x32_bf16 a[12:15], v[224:227], v[68:71], a[12:15]\n\t"
  "ds_read_b128 v[124:127], v231 offset:15360\n\t"
  "s_waitcnt lgkmcnt(13)\n\t"
  "v_mfma_f32_16x16x32_bf16 a[16:19], v[226:229], v[72:75], a[16:19]\n\t"
  "v_mfma_f32_16x16x32_bf16 a[24:27], v[224:227], v[72:75], a[24:27]\n\t"
  "buffer_load_dwordx4 v236, s[16:19], s36 offen offset:1024 lds\n\t"
  "s_waitcnt lgkmcnt(12)\n\t"
  "v_mfma_f32_16x16x32_bf16 a[20:23], v[226:229], v[76:79], a[20:23]\n\t"
  "v_accvgpr_read_b32 v160, a0\n\t"
  "v_accvgpr_read_b32 v161, a1\n\t"
  "v_mfma_f32_16x16x32_bf16 a[28:31], v[224:227], v[76:79], a[28:31]\n\t"
  "v_accvgpr_read_b32 v162, a2\n\t"
  "v_accvgpr_read_b32 v163, a3\n\t"
  "s_waitcnt lgkmcnt(11)\n\t"
  "v_mfma_f32_16x16x32_bf16 a[32:35], v[226:229], v[80:83], a[32:35]\n\t"
  "v_accvgpr_read_b32 v164, a8\n\t"
  "v_accvgpr_read_b32 v165, a9\n\t"
  "v_mfma_f32_16x16x32_bf16 a[40:43], v[224:227], v[80:83], a[40:43]\n\t"
  "buffer_load_dwordx4 v237, s[16:19], s36 offen offset:2048 lds\n\t"
  "s_waitcnt lgkmcnt(10)\n\t"
  "v_mfma_f32_16x16x32_bf16 a[36:39], v[226:229], v[84:87], a[36:39]\n\t"
  "v_accvgpr_read_b32 v166, a10\n\t"
  "v_accvgpr_read_b32 v167, a11\n\t"
  "v_mfma_f32_16x16x32_bf16 a[44:47], v[224:227], v[84:87], a[44:47]\n\t"
  "v_cvt_pk_bf16_f32 v0, v160, v161\n\t"
  "v_cvt_pk_bf16_f32 v1, v162, v163\n\t"
  "s_waitcnt lgkmcnt(9)\n\t"
  "v_mfma_f32_16x16x32_bf16 a[48:51], v[226:229], v[88:91], a[48:51]\n\t"
  "v_cvt_pk_bf16_f32 v2, v164, v165\n\t"
  "v_cvt_pk_bf16_f32 v3, v166, v167\n\t"
  "v_mfma_f32_16x16x32_bf16 a[56:59], v[224:227], v[88:91], a[56:59]\n\t"
  "buffer_load_dwordx4 v238, s[16:19], s36 offen offset:3072 lds\n\t"
  "s_add_u32 s36, s36, 28672\n\t"
  "s_waitcnt lgkmcnt(8)\n\t"
  "v_mfma_f32_16x16x32_bf16 a[52:55], v[226:229], v[92:95], a[52:55]\n\t"
  "v_accvgpr_read_b32 v168, a4\n\t"
  "v_accvgpr_read_b32 v169, a5\n\t"
  "v_mfma_f32_16x16x32_bf16 a[60:63], v[224:227], v[92:95], a[60:63]\n\t"
  "v_accvgpr_read_b32 v170, a6\n\t"
  "v_accvgpr_read_b32 v171, a7\n\t"
  "s_waitcnt lgkmcnt(7)\n\t"
  "v_mfma_f32_16x16x32_bf16 a[64:67], v[226:229], v[96:99], a[64:67]\n\t"
  "v_accvgpr_read_b32 v172, a12\n\t"
  "v_accvgpr_read_b32 v173, a13\n\t"
  "v_mfma_f32_16x16x32_bf16 a[72:75], v[224:227], v[96:99], a[72:75]\n\t"
  "s_add_u32 m0, s30, 4096\n\t"
  "s_nop 0\n\t"
  "buffer_load_dwordx4 v235, s[16:19], s36 offen lds\n\t"
  "s_waitcnt lgkmcnt(6)\n\t"
  "v_mfma_f32_16x16x32_bf16 a[68:71], v[226:229], v[100:103], a[68:71]\n\t"
  "v_accvgpr_read_b32 v174, a14\n\t"
  "v_accvgpr_read_b32 v175, a15\n\t"
  "v_mfma_f32_16x16x32_bf16 a[76:79], v[224:227], v[100:103], a[76:79]\n\t"
  "v_cvt_pk_bf16_f32 v4, v168, v169\n\t"
  "v_cvt_pk_bf16_f32 v5, v170, v171\n\t"
  "s_waitcnt lgkmcnt(5)\n\t"
  "v_mfma_f32_16x16x32_bf16 a[80:83], v[226:229], v[104:107], a[80:83]\n\t"
  "v_cvt_pk_bf16_f32 v6, v172, v173\n\t"
  "v_cvt_pk_bf16_f32 v7, v174, v175\n\t"
  "v_mfma_f32_16x16x32_bf16 a[88:91], v[224:227], v[104:107], a[88:91]\n\t"
  "buffer_load_dwordx4 v236, s[16:19], s36 offen offset:1024 lds\n\t"
  "s_waitcnt lgkmcnt(4)\n\t"
  "v_mfma_f32_16x16x32_bf16 a[84:87], v[226:229], v[108:111], a[84:87]\n\t"
  "v_accvgpr_read_b32 v176, a16\n\t"
  "v_accvgpr_read_b32 v177, a17\n\t"
  "v_mfma_f32_16x16x32_bf16 a[92:95], v[224:227], v[108:111], a[92:95]\n\t"
  "v_accvgpr_read_b32 v178, a18\n\t"
  "v_accvgpr_read_b32 v179, a19\n\t"
  "s_waitcnt lgkmcnt(3)\n\t"
  "v_mfma_f32_16x16x32_bf16 a[96:99], v[226:229], v[112:115], a[96:99]\n\t"
  "v_accvgpr_read_b32 v180, a24\n\t"
  "v_accvgpr_read_b32 v181, a25\n\t"
  "v_mfma_f32_16x16x32_bf16 a[104:107], v[224:227], v[112:115], a[104:107]\n\t"
  "buffer_load_dwordx4 v237, s[16:19], s36 offen offset:2048 lds\n\t"
  "s_waitcnt lgkmcnt(2)\n\t"
  "v_mfma_f32_16x16x32_bf16 a[100:103], v[226:229], v[116:119], a[100:103]\n\t"
  "v_accvgpr_read_b32 v182, a26\n\t"
  "v_accvgpr_read_b32 v183, a27\n\t"
  "v_mfma_f32_16x16x32_bf16 a[108:111], v[224:227], v[116:119], a[108:111]\n\t"
  "v_cvt_pk_bf16_f32 v8, v176, v177\n\t"
  "v_cvt_pk_bf16_f32 v9, v178, v179\n\t"
  "s_waitcnt lgkmcnt(1)\n\t"
  "v_mfma_f32_16x16x32_bf16 a[112:115], v[226:229], v[120:123], a[112:115]\n\t"
  "v_cvt_pk_bf16_f32 v10, v180, v181\n\t"
  "v_cvt_pk_bf16_f32 v11, v182, v183\n\t"
  "v_mfma_f32_16x16x32_bf16 a[120:123], v[224:227], v[120:123], a[120:123]\n\t"
  "buffer_load_dwordx4 v238, s[16:19], s36 offen offset:3072 lds\n\t"
  "s_add_u32 s36, s36, 4294938752\n\t"
  "s_waitcnt lgkmcnt(0)\n\t"
  "v_mfma_f32_16x16x32_bf16 a[116:119], v[226:229], v[124:127], a[116:119]\n\t"
  "v_accvgpr_read_b32 v184, a20\n\t"
  "v_accvgpr_read_b32 v185, a21\n\t"
  "v_mfma_f32_16x16x32_bf16 a[124:127], v[224:227], v[124:127], a[124:127]\n\t"
  "v_accvgpr_read_b32 v186, a22\n\t"
  "v_accvgpr_read_b32 v187, a23\n\t"
  "v_accvgpr_read_b32 v188, a28\n\t"
  "v_accvgpr_read_b32 v189, a29\n\t"
  "v_accvgpr_read_b32 v190, a30\n\t"
  "v_accvgpr_read_b32 v191, a31\n\t"
  "v_cvt_pk_bf16_f32 v12, v184, v185\n\t"
  "v_cvt_pk_bf16_f32 v13, v186, v187\n\t"
  "v_cvt_pk_bf16_f32 v14, v188, v189\n\t"
  "v_cvt_pk_bf16_f32 v15, v190, v191\n\t"
  "v_accvgpr_read_b32 v160, a32\n\t"
  "v_accvgpr_read_b32 v161, a33\n\t"
  "v_accvgpr_read_b32 v162, a34\n\t"
  "v_accvgpr_read_b32 v163, a35\n\t"
  "v_accvgpr_read_b32 v164, a40\n\t"
  "v_accvgpr_read_b32 v165, a41\n\t"
  "v_accvgpr_read_b32 v166, a42\n\t"
  "v_accvgpr_read_b32 v167, a43\n\t"
  "v_cvt_pk_bf16_f32 v16, v160, v161\n\t"
  "v_cvt_pk_bf16_f32 v17, v162, v163\n\t"
  "v_cvt_pk_bf16_f32 v18, v164, v165\n\t"
  "v_cvt_pk_bf16_f32 v19, v166, v167\n\t"
  "v_accvgpr_read_b32 v168, a36\n\t"
  "v_accvgpr_read_b32 v169, a37\n\t"
  "v_accvgpr_read_b32 v170, a38\n\t"
  "v_accvgpr_read_b32 v171, a39\n\t"
  "v_accvgpr_read_b32 v172, a44\n\t"
  "v_accvgpr_read_b32 v173, a45\n\t"
  "v_accvgpr_read_b32 v174, a46\n\t"
  "v_accvgpr_read_b32 v175, a47\n\t"
  "v_cvt_pk_bf16_f32 v20, v168, v169\n\t"
  "v_cvt_pk_bf16_f32 v21, v170, v171\n\t"
  "v_cvt_pk_bf16_f32 v22, v172, v173\n\t"
  "v_cvt_pk_bf16_f32 v23, v174, v175\n\t"
  "v_accvgpr_read_b32 v176, a48\n\t"
  "v_accvgpr_read_b32 v177, a49\n\t"
  "v_accvgpr_read_b32 v178, a50\n\t"
  "v_accvgpr_read_b32 v179, a51\n\t"
  "v_accvgpr_read_b32 v180, a56\n\t"
  "v_accvgpr_read_b32 v181, a57\n\t"
  "v_accvgpr_read_b32 v182, a58\n\t"
  "v_accvgpr_read_b32 v183, a59\n\t"
  "v_cvt_pk_bf16_f32 v24, v176, v177\n\t"
  "v_cvt_pk_bf16_f32 v25, v178, v179\n\t"
  "v_cvt_pk_bf16_f32 v26, v180, v181\n\t"
  "v_cvt_pk_bf16_f32 v27, v182, v183\n\t"
  "v_accvgpr_read_b32 v184, a52\n\t"
  "v_accvgpr_read_b32 v185, a53\n\t"
  "v_accvgpr_read_b32 v186, a54\n\t"
  "v_accvgpr_read_b32 v187, a55\n\t"
  "v_accvgpr_read_b32 v188, a60\n\t"
  "v_accvgpr_read_b32 v189, a61\n\t"
  "v_accvgpr_read_b32 v190, a62\n\t"
  "v_accvgpr_read_b32 v191, a63\n\t"
  "v_cvt_pk_bf16_f32 v28, v184, v185\n\t"
  "v_cvt_pk_bf16_f32 v29, v186, v187\n\t"
  "v_cvt_pk_bf16_f32 v30, v188, v189\n\t"
  "v_cvt_pk_bf16_f32 v31, v190, v191\n\t"
  "v_accvgpr_read_b32 v160, a64\n\t"
  "v_accvgpr_read_b32 v161, a65\n\t"
  "v_accvgpr_read_b32 v162, a66\n\t"
  "v_accvgpr_read_b32 v163, a67\n\t"
  "v_accvgpr_read_b32 v164, a72\n\t"
  "v_accvgpr_read_b32 v165, a73\n\t"
  "v_accvgpr_read_b32 v166, a74\n\t"
  "v_accvgpr_read_b32 v167, a75\n\t"
  "v_cvt_pk_bf16_f32 v32, v160, v161\n\t"
  "v_cvt_pk_bf16_f32 v33, v162, v163\n\t"
  "v_cvt_pk_bf16_f32 v34, v164, v165\n\t"
  "v_cvt_pk_bf16_f32 v35, v166, v167\n\t"
  "v_accvgpr_read_b32 v168, a68\n\t"
  "v_accvgpr_read_b32 v169, a69\n\t"
  "v_accvgpr_read_b32 v170, a70\n\t"
  "v_accvgpr_read_b32 v171, a71\n\t"
  "v_accvgpr_read_b32 v172, a76\n\t"
  "v_accvgpr_read_b32 v173, a77\n\t"
  "v_accvgpr_read_b32 v174, a78\n\t"
  "v_accvgpr_read_b32 v175, a79\n\t"
  "v_cvt_pk_bf16_f32 v36, v168, v169\n\t"
  "v_cvt_pk_bf16_f32 v37, v170, v171\n\t"
  "v_cvt_pk_bf16_f32 v38, v172, v173\n\t"
  "v_cvt_pk_bf16_f32 v39, v174, v175\n\t"
  "v_accvgpr_read_b32 v176, a80\n\t"
  "v_accvgpr_read_b32 v177, a81\n\t"
  "v_accvgpr_read_b32 v178, a82\n\t"
  "v_accvgpr_read_b32 v179, a83\n\t"
  "v_accvgpr_read_b32 v180, a88\n\t"
  "v_accvgpr_read_b32 v181, a89\n\t"
  "v_accvgpr_read_b32 v182, a90\n\t"
  "v_accvgpr_read_b32 v183, a91\n\t"
  "v_cvt_pk_bf16_f32 v40, v176, v177\n\t"
  "v_cvt_pk_bf16_f32 v41, v178, v179\n\t"
  "v_cvt_pk_bf16_f32 v42, v180, v181\n\t"
  "v_cvt_pk_bf16_f32 v43, v182, v183\n\t"
  "v_accvgpr_read_b32 v184, a84\n\t"
  "v_accvgpr_read_b32 v185, a85\n\t"
  "v_accvgpr_read_b32 v186, a86\n\t"
  "v_accvgpr_read_b32 v187, a87\n\t"
  "v_accvgpr_read_b32 v188, a92\n\t"
  "v_accvgpr_read_b32 v189, a93\n\t"
  "v_accvgpr_read_b32 v190, a94\n\t"
  "v_accvgpr_read_b32 v191, a95\n\t"
  "v_cvt_pk_bf16_f32 v44, v184, v185\n\t"
  "v_cvt_pk_bf16_f32 v45, v186, v187\n\t"
  "v_cvt_pk_bf16_f32 v46, v188, v189\n\t"
  "v_cvt_pk_bf16_f32 v47, v190, v191\n\t"
  "v_accvgpr_read_b32 v160, a96\n\t"
  "v_accvgpr_read_b32 v161, a97\n\t"
  "v_accvgpr_read_b32 v162, a98\n\t"
  "v_accvgpr_read_b32 v163, a99\n\t"
  "v_accvgpr_read_b32 v164, a104\n\t"
  "v_accvgpr_read_b32 v165, a105\n\t"
  "v_accvgpr_read_b32 v166, a106\n\t"
  "v_accvgpr_read_b32 v167, a107\n\t"
  "v_cvt_pk_bf16_f32 v48, v160, v161\n\t"
  "v_cvt_pk_bf16_f32 v49, v162, v163\n\t"
  "v_cvt_pk_bf16_f32 v50, v164, v165\n\t"
  "v_cvt_pk_bf16_f32 v51, v166, v167\n\t"
  "v_accvgpr_read_b32 v168, a100\n\t"
  "v_accvgpr_read_b32 v169, a101\n\t"
  "v_accvgpr_read_b32 v170, a102\n\t"
  "v_accvgpr_read_b32 v171, a103\n\t"
  "v_accvgpr_read_b32 v172, a108\n\t"
  "v_accvgpr_read_b32 v173, a109\n\t"
  "v_accvgpr_read_b32 v174, a110\n\t"
  "v_accvgpr_read_b32 v175, a111\n\t"
  "v_cvt_pk_bf16_f32 v52, v168, v169\n\t"
  "v_cvt_pk_bf16_f32 v53, v170, v171\n\t"
  "v_cvt_pk_bf16_f32 v54, v172, v173\n\t"
  "v_cvt_pk_bf16_f32 v55, v174, v175\n\t"
  "v_accvgpr_read_b32 v176, a112\n\t"
  "v_accvgpr_read_b32 v177, a113\n\t"
  "v_accvgpr_read_b32 v178, a114\n\t"
  "v_accvgpr_read_b32 v179, a115\n\t"
  "v_accvgpr_read_b32 v180, a120\n\t"
  "v_accvgpr_read_b32 v181, a121\n\t"
  "v_accvgpr_read_b32 v182, a122\n\t"
  "v_accvgpr_read_b32 v183, a123\n\t"
  "v_cvt_pk_bf16_f32 v56, v176, v177\n\t"
  "v_cvt_pk_bf16_f32 v57, v178, v179\n\t"
  "v_cvt_pk_bf16_f32 v58, v180, v181\n\t"
  "v_cvt_pk_bf16_f32 v59, v182, v183\n\t"
  "v_accvgpr_read_b32 v184, a116\n\t"
  "v_accvgpr_read_b32 v185, a117\n\t"
  "v_accvgpr_read_b32 v186, a118\n\t"
  "v_accvgpr_read_b32 v187, a119\n\t"
  "v_accvgpr_read_b32 v188, a124\n\t"
  "v_accvgpr_read_b32 v189, a125\n\t"
  "v_accvgpr_read_b32 v190, a126\n\t"
  "v_accvgpr_read_b32 v191, a127\n\t"
  "v_cvt_pk_bf16_f32 v60, v184, v185\n\t"
  "v_cvt_pk_bf16_f32 v61, v186, v187\n\t"
  "v_cvt_pk_bf16_f32 v62, v188, v189\n\t"
  "v_cvt_pk_bf16_f32 v63, v190, v191\n\t"
  "ds_read_b64_tr_b16 v[128:129], v196\n\t"
  "ds_read_b64_tr_b16 v[130:131], v196 offset:2048\n\t"
  "ds_read_b64_tr_b16 v[132:133], v196 offset:4096\n\t"
  "ds_read_b64_tr_b16 v[134:135], v196 offset:6144\n\t"
  "ds_read_b64_tr_b16 v[136:137], v196 offset:8192\n\t"
  "ds_read_b64_tr_b16 v[138:139], v196 offset:10240\n\t"
  "ds_read_b64_tr_b16 v[140:141], v196 offset:12288\n\t"
  "ds_read_b64_tr_b16 v[142:143], v196 offset:14336\n\t"
  "ds_read_b64_tr_b16 v[144:145], v196 offset:16384\n\t"
  "ds_read_b64_tr_b16 v[146:147], v196 offset:18432\n\t"
  "ds_read_b64_tr_b16 v[148:149], v196 offset:20480\n\t"
  "ds_read_b64_tr_b16 v[150:151], v196 offset:22528\n\t"
  "ds_read_b64_tr_b16 v[152:153], v196 offset:24576\n\t"
  "ds_read_b64_tr_b16 v[154:155], v196 offset:26624\n\t"
  "s_waitcnt lgkmcnt(6)\n\t"
  "v_mfma_f32_16x16x32_bf16 v[64:67], v[128:131], v[0:3], 0\n\t"
  "ds_read_b64_tr_b16 v[156:157], v196 offset:28672\n\t"
  "v_mfma_f32_16x16x32_bf16 v[68:71], v[128:131], v[4:7], 0\n\t"
  "ds_read_b64_tr_b16 v[158:159], v196 offset:30720\n\t"
  "v_mfma_f32_16x16x32_bf16 v[68:71], v[132:135], v[12:15], v[68:71]\n\t"
  "ds_read_b64_tr_b16 v[160:161], v197\n\t"
  "s_barrier\n\t"
  "v_mfma_f32_16x16x32_bf16 v[64:67], v[132:135], v[8:11], v[64:67]\n\t"
  "ds_read_b64_tr_b16 v[162:163], v197 offset:2048\n\t"
  "s_add_u32 m0, s30, 65536\n\t"
  "v_mfma_f32_16x16x32_bf16 v[64:67], v[136:139], v[16:19], v[64:67]\n\t"
  "ds_read_b64_tr_b16 v[164:165], v197 offset:4096\n\t"
  "buffer_load_dwordx4 v235, s[16:19], s36 offen lds\n\t"
  "v_mfma_f32_16x16x32_bf16 v[68:71], v[136:139], v[20:23], v[68:71]\n\t"
  "ds_read_b64_tr_b16 v[166:167], v197 offset:6144\n\t"
  "v_mfma_f32_16x16x32_bf16 v[68:71], v[140:143], v[28:31], v[68:71]\n\t"
  "ds_read_b64_tr_b16 v[168:169], v197 offset:8192\n\t"
  "v_mfma_f32_16x16x32_bf16 v[64:67], v[140:143], v[24:27], v[64:67]\n\t"
  "ds_read_b64_tr_b16 v[170:171], v197 offset:10240\n\t"
  "s_waitcnt lgkmcnt(6)\n\t"
  "v_mfma_f32_16x16x32_bf16 v[64:67], v[144:147], v[32:35], v[64:67]\n\t"
  "ds_read_b64_tr_b16 v[172:173], v197 offset:12288\n\t"
  "buffer_load_dwordx4 v236, s[16:19], s36 offen offset:1024 lds\n\t"
  "v_mfma_f32_16x16x32_bf16 v[68:71], v[144:147], v[36:39], v[68:71]\n\t"
  "ds_read_b64_tr_b16 v[174:175], v197 offset:14336\n\t"
  "v_mfma_f32_16x16x32_bf16 v[68:71], v[148:151], v[44:47], v[68:71]\n\t"
  "ds_read_b64_tr_b16 v[176:177], v197 offset:16384\n\t"
  "v_mfma_f32_16x16x32_bf16 v[64:67], v[148:151], v[40:43], v[64:67]\n\t"
  "ds_read_b64_tr_b16 v[178:179], v197 offset:18432\n\t"
  "v_mfma_f32_16x16x32_bf16 v[64:67], v[152:155], v[48:51], v[64:67]\n\t"
  "ds_read_b64_tr_b16 v[180:181], v197 offset:20480\n\t"
  "buffer_load_dwordx4 v237, s[16:19], s36 offen offset:2048 lds\n\t"
  "v_mfma_f32_16x16x32_bf16 v[68:71], v[152:155], v[52:55], v[68:71]\n\t"
  "ds_read_b64_tr_b16 v[182:183], v197 offset:22528\n\t"
  "v_mfma_f32_16x16x32_bf16 v[68:71], v[156:159], v[60:63], v[68:71]\n\t"
  "ds_read_b64_tr_b16 v[184:185], v197 offset:24576\n\t"
  "v_mfma_f32_16x16x32_bf16 v[64:67], v[156:159], v[56:59], v[64:67]\n\t"
  "ds_read_b64_tr_b16 v[186:187], v197 offset:26624\n\t"
  "s_waitcnt lgkmcnt(6)\n\t"
  "v_mfma_f32_16x16x32_bf16 v[72:75], v[160:163], v[0:3], 0\n\t"
  "ds_read_b64_tr_b16 v[188:189], v197 offset:28672\n\t"
  "buffer_load_dwordx4 v238, s[16:19], s36 offen offset:3072 lds\n\t"
  "v_mfma_f32_16x16x32_bf16 v[76:79], v[160:163], v[4:7], 0\n\t"
  "ds_read_b64_tr_b16 v[190:191], v197 offset:30720\n\t"
  "s_add_u32 s36, s36, 28672\n\t"
  "v_mfma_f32_16x16x32_bf16 v[76:79], v[164:167], v[12:15], v[76:79]\n\t"
  "ds_read_b64_tr_b16 v[128:129], v196 offset:512\n\t"
  "v_mfma_f32_16x16x32_bf16 v[72:75], v[164:167], v[8:11], v[72:75]\n\t"
  "ds_read_b64_tr_b16 v[130:131], v196 offset:2560\n\t"
  "s_add_u32 m0, s30, 69632\n\t"
  "v_mfma_f32_16x16x32_bf16 v[72:75], v[168:171], v[16:19], v[72:75]\n\t"
  "ds_read_b64_tr_b16 v[132:133], v196 offset:4608\n\t"
  "buffer_load_dwordx4 v235, s[16:19], s36 offen lds\n\t"
  "v_mfma_f32_16x16x32_bf16 v[76:79], v[168:171], v[20:23], v[76:79]\n\t"
  "ds_read_b64_tr_b16 v[134:135], v196 offset:6656\n\t"
  "v_mfma_f32_16x16x32_bf16 v[76:79], v[172:175], v[28:31], v[76:79]\n\t"
  "ds_read_b64_tr_b16 v[136:137], v196 offset:8704\n\t"
  "v_mfma_f32_16x16x32_bf16 v[72:75], v[172:175], v[24:27], v[72:75]\n\t"
  "ds_read_b64_tr_b16 v[138:139], v196 offset:10752\n\t"
  "s_waitcnt lgkmcnt(6)\n\t"
  "v_mfma_f32_16x16x32_bf16 v[72:75], v[176:179], v[32:35], v[72:75]\n\t"
  "ds_read_b64_tr_b16 v[140:141], v196 offset:12800\n\t"
  "buffer_load_dwordx4 v236, s[16:19], s36 offen offset:1024 lds\n\t"
  "v_mfma_f32_16x16x32_bf16 v[76:79], v[176:179], v[36:39], v[76:79]\n\t"
  "ds_read_b64_tr_b16 v[142:143], v196 offset:14848\n\t"
  "v_mfma_f32_16x16x32_bf16 v[76:79], v[180:183], v[44:47], v[76:79]\n\t"
  "ds_read_b64_tr_b16 v[144:145], v196 offset:16896\n\t"
  "v_mfma_f32_16x16x32_bf16 v[72:75], v[180:183], v[40:43], v[72:75]\n\t"
  "ds_read_b64_tr_b16 v[146:147], v196 offset:18944\n\t"
  "v_mfma_f32_16x16x32_bf16 v[72:75], v[184:187], v[48:51], v[72:75]\n\t"
  "ds_read_b64_tr_b16 v[148:149], v196 offset:20992\n\t"
  "buffer_load_dwordx4 v237, s[16:19], s36 offen offset:2048 lds\n\t"
  "v_mfma_f32_16x16x32_bf16 v[76:79], v[184:187], v[52:55], v[76:79]\n\t"
  "ds_read_b64_tr_b16 v[150:151], v196 offset:23040\n\t"
  "v_mfma_f32_16x16x32_bf16 v[76:79], v[188:191], v[60:63], v[76:79]\n\t"
  "ds_read_b64_tr_b16 v[152:153], v196 offset:25088\n\t"
  "s_waitcnt vmcnt(16)\n\t"
  "s_barrier\n\t"
  "v_mfma_f32_16x16x32_bf16 v[72:75], v[188:191], v[56:59], v[72:75]\n\t"
  "ds_read_b64_tr_b16 v[154:155], v196 offset:27136\n\t"
  "s_waitcnt lgkmcnt(6)\n\t"
  "v_mfma_f32_16x16x32_bf16 v[80:83], v[128:131], v[0:3], 0\n\t"
  "ds_read_b64_tr_b16 v[156:157], v196 offset:29184\n\t"
  "buffer_load_dwordx4 v238, s[16:19], s36 offen offset:3072 lds\n\t"
  "v_mfma_f32_16x16x32_bf16 v[84:87], v[128:131], v[4:7], 0\n\t"
  "ds_read_b64_tr_b16 v[158:159], v196 offset:31232\n\t"
  "s_add_u32 s36, s36, 4294938752\n\t"
  "v_mfma_f32_16x16x32_bf16 v[84:87], v[132:135], v[12:15], v[84:87]\n\t"
  "ds_read_b64_tr_b16 v[160:161], v197 offset:512\n\t"
  "v_mfma_f32_16x16x32_bf16 v[80:83], v[132:135], v[8:11], v[80:83]\n\t"
  "ds_read_b64_tr_b16 v[162:163], v197 offset:2560\n\t"
  "s_add_u32 m0, s30, 98304\n\t"
  "v_mfma_f32_16x16x32_bf16 v[80:83], v[136:139], v[16:19], v[80:83]\n\t"
  "ds_read_b64_tr_b16 v[164:165], v197 offset:4608\n\t"
  "buffer_load_dwordx4 v235, s[16:19], s36 offen lds\n\t"
  "v_cvt_pk_bf16_f32 v208, v64, v65\n\t"
  "v_mfma_f32_16x16x32_bf16 v[84:87], v[136:139], v[20:23], v[84:87]\n\t"
  "ds_read_b64_tr_b16 v[166:167], v197 offset:6656\n\t"
  "v_cvt_pk_bf16_f32 v209, v66, v67\n\t"
  "v_mfma_f32_16x16x32_bf16 v[84:87], v[140:143], v[28:31], v[84:87]\n\t"
  "ds_read_b64_tr_b16 v[168:169], v197 offset:8704\n\t"
  "v_cvt_pk_bf16_f32 v210, v72, v73\n\t"
  "v_mfma_f32_16x16x32_bf16 v[80:83], v[140:143], v[24:27], v[80:83]\n\t"
  "ds_read_b64_tr_b16 v[170:171], v197 offset:10752\n\t"
  "s_waitcnt lgkmcnt(6)\n\t"
  "v_mfma_f32_16x16x32_bf16 v[80:83], v[144:147], v[32:35], v[80:83]\n\t"
  "ds_read_b64_tr_b16 v[172:173], v197 offset:12800\n\t"
  "buffer_load_dwordx4 v236, s[16:19], s36 offen offset:1024 lds\n\t"
  "v_cvt_pk_bf16_f32 v211, v74, v75\n\t"
  "v_mfma_f32_16x16x32_bf16 v[84:87], v[144:147], v[36:39], v[84:87]\n\t"
  "ds_read_b64_tr_b16 v[174:175], v197 offset:14848\n\t"
  "v_cvt_pk_bf16_f32 v212, v68, v69\n\t"
  "v_mfma_f32_16x16x32_bf16 v[84:87], v[148:151], v[44:47], v[84:87]\n\t"
  "ds_read_b64_tr_b16 v[176:177], v197 offset:16896\n\t"
  "v_cvt_pk_bf16_f32 v213, v70, v71\n\t"
  "v_mfma_f32_16x16x32_bf16 v[80:83], v[148:151], v[40:43], v[80:83]\n\t"
  "ds_read_b64_tr_b16 v[178:179], v197 offset:18944\n\t"
  "v_mfma_f32_16x16x32_bf16 v[80:83], v[152:155], v[48:51], v[80:83]\n\t"
  "ds_read_b64_tr_b16 v[180:181], v197 offset:20992\n\t"
  "buffer_load_dwordx4 v237, s[16:19], s36 offen offset:2048 lds\n\t"
  "v_cvt_pk_bf16_f32 v214, v76, v77\n\t"
  "v_mfma_f32_16x16x32_bf16 v[84:87], v[152:155], v[52:55], v[84:87]\n\t"
  "ds_read_b64_tr_b16 v[182:183], v197 offset:23040\n\t"
  "v_cvt_pk_bf16_f32 v215, v78, v79\n\t"
  "v_mfma_f32_16x16x32_bf16 v[84:87], v[156:159], v[60:63], v[84:87]\n\t"
  "ds_read_b64_tr_b16 v[184:185], v197 offset:25088\n\t"
  "v_mfma_f32_16x16x32_bf16 v[80:83], v[156:159], v[56:59], v[80:83]\n\t"
  "ds_read_b64_tr_b16 v[186:187], v197 offset:27136\n\t"
  "s_waitcnt lgkmcnt(6)\n\t"
  "v_mfma_f32_16x16x32_bf16 v[88:91], v[160:163], v[0:3], 0\n\t"
  "ds_read_b64_tr_b16 v[188:189], v197 offset:29184\n\t"
  "buffer_load_dwordx4 v238, s[16:19], s36 offen offset:3072 lds\n\t"
  "v_mfma_f32_16x16x32_bf16 v[92:95], v[160:163], v[4:7], 0\n\t"
  "ds_read_b64_tr_b16 v[190:191], v197 offset:31232\n\t"
  "s_add_u32 s36, s36, 28672\n\t"
  "v_accvgpr_write_b32 a0, v208\n\t"
  "v_mfma_f32_16x16x32_bf16 v[92:95], v[164:167], v[12:15], v[92:95]\n\t"
  "ds_read_b64_tr_b16 v[128:129], v192 offset:32768\n\t"
  "v_accvgpr_write_b32 a1, v209\n\t"
  "v_mfma_f32_16x16x32_bf16 v[88:91], v[164:167], v[8:11], v[88:91]\n\t"
  "ds_read_b64_tr_b16 v[130:131], v192 offset:34816\n\t"
  "s_add_u32 m0, s30, 102400\n\t"
  "v_mfma_f32_16x16x32_bf16 v[88:91], v[168:171], v[16:19], v[88:91]\n\t"
  "ds_read_b64_tr_b16 v[132:133], v192 offset:36864\n\t"
  "buffer_load_dwordx4 v235, s[16:19], s36 offen lds\n\t"
  "v_accvgpr_write_b32 a2, v210\n\t"
  "v_mfma_f32_16x16x32_bf16 v[92:95], v[168:171], v[20:23], v[92:95]\n\t"
  "ds_read_b64_tr_b16 v[134:135], v192 offset:38912\n\t"
  "v_accvgpr_write_b32 a3, v211\n\t"
  "v_mfma_f32_16x16x32_bf16 v[92:95], v[172:175], v[28:31], v[92:95]\n\t"
  "ds_read_b64_tr_b16 v[136:137], v192 offset:40960\n\t"
  "v_mfma_f32_16x16x32_bf16 v[88:91], v[172:175], v[24:27], v[88:91]\n\t"
  "ds_read_b64_tr_b16 v[138:139], v192 offset:43008\n\t"
  "s_waitcnt lgkmcnt(6)\n\t"
  "v_mfma_f32_16x16x32_bf16 v[88:91], v[176:179], v[32:35], v[88:91]\n\t"
  "ds_read_b64_tr_b16 v[140:141], v192 offset:45056\n\t"
  "buffer_load_dwordx4 v236, s[16:19], s36 offen offset:1024 lds\n\t"
  "v_mfma_f32_16x16x32_bf16 v[92:95], v[176:179], v[36:39], v[92:95]\n\t"
  "ds_read_b64_tr_b16 v[142:143], v192 offset:47104\n\t"
  "v_mfma_f32_16x16x32_bf16 v[92:95], v[180:183], v[44:47], v[92:95]\n\t"
  "ds_read_b64_tr_b16 v[144:145], v192 offset:49152\n\t"
  "v_mfma_f32_16x16x32_bf16 v[88:91], v[180:183], v[40:43], v[88:91]\n\t"
  "ds_read_b64_tr_b16 v[146:147], v192 offset:51200\n\t"
  "v_mfma_f32_16x16x32_bf16 v[88:91], v[184:187], v[48:51], v[88:91]\n\t"
  "ds_read_b64_tr_b16 v[148:149], v192 offset:53248\n\t"
  "buffer_load_dwordx4 v237, s[16:19], s36 offen offset:2048 lds\n\t"
  "v_mfma_f32_16x16x32_bf16 v[92:95], v[184:187], v[52:55], v[92:95]\n\t"
  "ds_read_b64_tr_b16 v[150:151], v192 offset:55296\n\t"
  "v_mfma_f32_16x16x32_bf16 v[92:95], v[188:191], v[60:63], v[92:95]\n\t"
  "ds_read_b64_tr_b16 v[152:153], v192 offset:57344\n\t"
  "v_mfma_f32_16x16x32_bf16 v[88:91], v[188:191], v[56:59], v[88:91]\n\t"
  "ds_read_b64_tr_b16 v[154:155], v192 offset:59392\n\t"
  "s_waitcnt lgkmcnt(6)\n\t"
  "v_mfma_f32_16x16x32_bf16 v[96:99], v[128:131], v[0:3], 0\n\t"
  "ds_read_b64_tr_b16 v[156:157], v192 offset:61440\n\t"
  "buffer_load_dwordx4 v238, s[16:19], s36 offen offset:3072 lds\n\t"
  "v_mfma_f32_16x16x32_bf16 v[100:103], v[128:131], v[4:7], 0\n\t"
  "ds_read_b64_tr_b16 v[158:159], v192 offset:63488\n\t"
  "s_add_u32 s36, s36, 4294938752\n\t"
  "v_mfma_f32_16x16x32_bf16 v[100:103], v[132:135], v[12:15], v[100:103]\n\t"
  "ds_read_b64_tr_b16 v[160:161], v193 offset:32768\n\t"
  "v_mfma_f32_16x16x32_bf16 v[96:99], v[132:135], v[8:11], v[96:99]\n\t"
  "ds_read_b64_tr_b16 v[162:163], v193 offset:34816\n\t"
  "v_mfma_f32_16x16x32_bf16 v[96:99], v[136:139], v[16:19], v[96:99]\n\t"
  "ds_read_b64_tr_b16 v[164:165], v193 offset:36864\n\t"
  "buffer_store_dwordx4 v[208:211], v247, s[20:23], s37 offen sc1\n\t"
  "v_cvt_pk_bf16_f32 v216, v80, v81\n\t"
  "v_mfma_f32_16x16x32_bf16 v[100:103], v[136:139], v[20:23], v[100:103]\n\t"
  "ds_read_b64_tr_b16 v[166:167], v193 offset:38912\n\t"
  "v_cvt_pk_bf16_f32 v217, v82, v83\n\t"
  "v_mfma_f32_16x16x32_bf16 v[100:103], v[140:143], v[28:31], v[100:103]\n\t"
  "ds_read_b64_tr_b16 v[168:169], v193 offset:40960\n\t"
  "v_cvt_pk_bf16_f32 v218, v88, v89\n\t"
  "v_mfma_f32_16x16x32_bf16 v[96:99], v[140:143], v[24:27], v[96:99]\n\t"
  "ds_read_b64_tr_b16 v[170:171], v193 offset:43008\n\t"
  "s_waitcnt lgkmcnt(6)\n\t"
  "v_mfma_f32_16x16x32_bf16 v[96:99], v[144:147], v[32:35], v[96:99]\n\t"
  "ds_read_b64_tr_b16 v[172:173], v193 offset:45056\n\t"
  "buffer_store_dwordx4 v[212:215], v248, s[20:23], s38 offen sc1\n\t"
  "v_cvt_pk_bf16_f32 v219, v90, v91\n\t"
  "v_mfma_f32_16x16x32_bf16 v[100:103], v[144:147], v[36:39], v[100:103]\n\t"
  "ds_read_b64_tr_b16 v[174:175], v193 offset:47104\n\t"
  "v_cvt_pk_bf16_f32 v220, v84, v85\n\t"
  "v_mfma_f32_16x16x32_bf16 v[100:103], v[148:151], v[44:47], v[100:103]\n\t"
  "ds_read_b64_tr_b16 v[176:177], v193 offset:49152\n\t"
  "v_cvt_pk_bf16_f32 v221, v86, v87\n\t"
  "v_mfma_f32_16x16x32_bf16 v[96:99], v[148:151], v[40:43], v[96:99]\n\t"
  "ds_read_b64_tr_b16 v[178:179], v193 offset:51200\n\t"
  "v_mfma_f32_16x16x32_bf16 v[96:99], v[152:155], v[48:51], v[96:99]\n\t"
  "ds_read_b64_tr_b16 v[180:181], v193 offset:53248\n\t"
  "buffer_store_dwordx4 v[216:219], v247, s[20:23], s37 offen offset:512 sc1\n\t"
  "v_cvt_pk_bf16_f32 v222, v92, v93\n\t"
  "v_mfma_f32_16x16x32_bf16 v[100:103], v[152:155], v[52:55], v[100:103]\n\t"
  "ds_read_b64_tr_b16 v[182:183], v193 offset:55296\n\t"
  "s_add_u32 s37, s37, 1024\n\t"
  "v_cvt_pk_bf16_f32 v223, v94, v95\n\t"
  "v_mfma_f32_16x16x32_bf16 v[100:103], v[156:159], v[60:63], v[100:103]\n\t"
  "ds_read_b64_tr_b16 v[184:185], v193 offset:57344\n\t"
  "v_mfma_f32_16x16x32_bf16 v[96:99], v[156:159], v[56:59], v[96:99]\n\t"
  "ds_read_b64_tr_b16 v[186:187], v193 offset:59392\n\t"
  "s_waitcnt lgkmcnt(6)\n\t"
  "v_mfma_f32_16x16x32_bf16 v[104:107], v[160:163], v[0:3], 0\n\t"
  "ds_read_b64_tr_b16 v[188:189], v193 offset:61440\n\t"
  "buffer_store_dwordx4 v[220:223], v248, s[20:23], s38 offen offset:512 sc1\n\t"
  "v_mfma_f32_16x16x32_bf16 v[108:111], v[160:163], v[4:7], 0\n\t"
  "ds_read_b64_tr_b16 v[190:191], v193 offset:63488\n\t"
  "v_accvgpr_write_b32 a4, v216\n\t"
  "s_add_u32 s38, s38, 1024\n\t"
  "v_mfma_f32_16x16x32_bf16 v[108:111], v[164:167], v[12:15], v[108:111]\n\t"
  "ds_read_b64_tr_b16 v[128:129], v192 offset:33280\n\t"
  "v_accvgpr_write_b32 a5, v217\n\t"
  "v_mfma_f32_16x16x32_bf16 v[104:107], v[164:167], v[8:11], v[104:107]\n\t"
  "ds_read_b64_tr_b16 v[130:131], v192 offset:35328\n\t"
  "v_mfma_f32_16x16x32_bf16 v[104:107], v[168:171], v[16:19], v[104:107]\n\t"
  "ds_read_b64_tr_b16 v[132:133], v192 offset:37376\n\t"
  "v_accvgpr_write_b32 a6, v218\n\t"
  "v_mfma_f32_16x16x32_bf16 v[108:111], v[168:171], v[20:23], v[108:111]\n\t"
  "ds_read_b64_tr_b16 v[134:135], v192 offset:39424\n\t"
  "v_accvgpr_write_b32 a7, v219\n\t"
  "v_mfma_f32_16x16x32_bf16 v[108:111], v[172:175], v[28:31], v[108:111]\n\t"
  "ds_read_b64_tr_b16 v[136:137], v192 offset:41472\n\t"
  "v_mfma_f32_16x16x32_bf16 v[104:107], v[172:175], v[24:27], v[104:107]\n\t"
  "ds_read_b64_tr_b16 v[138:139], v192 offset:43520\n\t"
  "s_waitcnt lgkmcnt(6)\n\t"
  "v_mfma_f32_16x16x32_bf16 v[104:107], v[176:179], v[32:35], v[104:107]\n\t"
  "ds_read_b64_tr_b16 v[140:141], v192 offset:45568\n\t"
  "v_mfma_f32_16x16x32_bf16 v[108:111], v[176:179], v[36:39], v[108:111]\n\t"
  "ds_read_b64_tr_b16 v[142:143], v192 offset:47616\n\t"
  "v_mfma_f32_16x16x32_bf16 v[108:111], v[180:183], v[44:47], v[108:111]\n\t"
  "ds_read_b64_tr_b16 v[144:145], v192 offset:49664\n\t"
  "v_mfma_f32_16x16x32_bf16 v[104:107], v[180:183], v[40:43], v[104:107]\n\t"
  "ds_read_b64_tr_b16 v[146:147], v192 offset:51712\n\t"
  "v_mfma_f32_16x16x32_bf16 v[104:107], v[184:187], v[48:51], v[104:107]\n\t"
  "ds_read_b64_tr_b16 v[148:149], v192 offset:53760\n\t"
  "v_mfma_f32_16x16x32_bf16 v[108:111], v[184:187], v[52:55], v[108:111]\n\t"
  "ds_read_b64_tr_b16 v[150:151], v192 offset:55808\n\t"
  "v_mfma_f32_16x16x32_bf16 v[108:111], v[188:191], v[60:63], v[108:111]\n\t"
  "ds_read_b64_tr_b16 v[152:153], v192 offset:57856\n\t"
  "s_waitcnt vmcnt(20)\n\t"
  "s_barrier\n\t"
  "v_mfma_f32_16x16x32_bf16 v[104:107], v[188:191], v[56:59], v[104:107]\n\t"
  "ds_read_b64_tr_b16 v[154:155], v192 offset:59904\n\t"
  "s_add_u32 m0, s30, 131072\n\t"
  "s_waitcnt lgkmcnt(6)\n\t"
  "v_mfma_f32_16x16x32_bf16 v[112:115], v[128:131], v[0:3], 0\n\t"
  "ds_read_b64_tr_b16 v[156:157], v192 offset:61952\n\t"
  "buffer_load_dwordx4 v235, s[16:19], s36 offen lds\n\t"
  "v_mfma_f32_16x16x32_bf16 v[116:119], v[128:131], v[4:7], 0\n\t"
  "ds_read_b64_tr_b16 v[158:159], v192 offset:64000\n\t"
  "v_mfma_f32_16x16x32_bf16 v[116:119], v[132:135], v[12:15], v[116:119]\n\t"
  "ds_read_b64_tr_b16 v[160:161], v193 offset:33280\n\t"
  "v_mfma_f32_16x16x32_bf16 v[112:115], v[132:135], v[8:11], v[112:115]\n\t"
  "ds_read_b64_tr_b16 v[162:163], v193 offset:35328\n\t"
  "v_mfma_f32_16x16x32_bf16 v[112:115], v[136:139], v[16:19], v[112:115]\n\t"
  "ds_read_b64_tr_b16 v[164:165], v193 offset:37376\n\t"
  "buffer_load_dwordx4 v236, s[16:19], s36 offen offset:1024 lds\n\t"
  "v_cvt_pk_bf16_f32 v208, v96, v97\n\t"
  "v_mfma_f32_16x16x32_bf16 v[116:119], v[136:139], v[20:23], v[116:119]\n\t"
  "ds_read_b64_tr_b16 v[166:167], v193 offset:39424\n\t"
  "v_cvt_pk_bf16_f32 v209, v98, v99\n\t"
  "v_mfma_f32_16x16x32_bf16 v[116:119], v[140:143], v[28:31], v[116:119]\n\t"
  "ds_read_b64_tr_b16 v[168:169], v193 offset:41472\n\t"
  "v_cvt_pk_bf16_f32 v210, v104, v105\n\t"
  "v_mfma_f32_16x16x32_bf16 v[112:115], v[140:143], v[24:27], v[112:115]\n\t"
  "ds_read_b64_tr_b16 v[170:171], v193 offset:43520\n\t"
  "s_waitcnt lgkmcnt(6)\n\t"
  "v_mfma_f32_16x16x32_bf16 v[112:115], v[144:147], v[32:35], v[112:115]\n\t"
  "ds_read_b64_tr_b16 v[172:173], v193 offset:45568\n\t"
  "buffer_load_dwordx4 v237, s[16:19], s36 offen offset:2048 lds\n\t"
  "v_cvt_pk_bf16_f32 v211, v106, v107\n\t"
  "v_mfma_f32_16x16x32_bf16 v[116:119], v[144:147], v[36:39], v[116:119]\n\t"
  "ds_read_b64_tr_b16 v[174:175], v193 offset:47616\n\t"
  "v_cvt_pk_bf16_f32 v212, v100, v101\n\t"
  "v_mfma_f32_16x16x32_bf16 v[116:119], v[148:151], v[44:47], v[116:119]\n\t"
  "ds_read_b64_tr_b16 v[176:177], v193 offset:49664\n\t"
  "v_cvt_pk_bf16_f32 v213, v102, v103\n\t"
  "v_mfma_f32_16x16x32_bf16 v[112:115], v[148:151], v[40:43], v[112:115]\n\t"
  "ds_read_b64_tr_b16 v[178:179], v193 offset:51712\n\t"
  "v_mfma_f32_16x16x32_bf16 v[112:115], v[152:155], v[48:51], v[112:115]\n\t"
  "ds_read_b64_tr_b16 v[180:181], v193 offset:53760\n\t"
  "buffer_load_dwordx4 v238, s[16:19], s36 offen offset:3072 lds\n\t"
  "v_cvt_pk_bf16_f32 v214, v108, v109\n\t"
  "v_mfma_f32_16x16x32_bf16 v[116:119], v[152:155], v[52:55], v[116:119]\n\t"
  "ds_read_b64_tr_b16 v[182:183], v193 offset:55808\n\t"
  "s_add_u32 s36, s36, 28672\n\t"
  "v_cvt_pk_bf16_f32 v215, v110, v111\n\t"
  "v_mfma_f32_16x16x32_bf16 v[116:119], v[156:159], v[60:63], v[116:119]\n\t"
  "ds_read_b64_tr_b16 v[184:185], v193 offset:57856\n\t"
  "v_mfma_f32_16x16x32_bf16 v[112:115], v[156:159], v[56:59], v[112:115]\n\t"
  "ds_read_b64_tr_b16 v[186:187], v193 offset:59904\n\t"
  "s_add_u32 m0, s30, 135168\n\t"
  "s_waitcnt lgkmcnt(6)\n\t"
  "v_mfma_f32_16x16x32_bf16 v[120:123], v[160:163], v[0:3], 0\n\t"
  "ds_read_b64_tr_b16 v[188:189], v193 offset:61952\n\t"
  "buffer_load_dwordx4 v235, s[16:19], s36 offen lds\n\t"
  "v_mfma_f32_16x16x32_bf16 v[124:127], v[160:163], v[4:7], 0\n\t"
  "ds_read_b64_tr_b16 v[190:191], v193 offset:64000\n\t"
  "v_accvgpr_write_b32 a8, v208\n\t"
  "v_mfma_f32_16x16x32_bf16 v[124:127], v[164:167], v[12:15], v[124:127]\n\t"
  "ds_read_b64_tr_b16 v[128:129], v192\n\t"
  "v_accvgpr_write_b32 a9, v209\n\t"
  "v_mfma_f32_16x16x32_bf16 v[120:123], v[164:167], v[8:11], v[120:123]\n\t"
  "ds_read_b64_tr_b16 v[130:131], v192 offset:2048\n\t"
  "v_mfma_f32_16x16x32_bf16 v[120:123], v[168:171], v[16:19], v[120:123]\n\t"
  "ds_read_b64_tr_b16 v[132:133], v192 offset:4096\n\t"
  "buffer_load_dwordx4 v236, s[16:19], s36 offen offset:1024 lds\n\t"
  "v_accvgpr_write_b32 a10, v210\n\t"
  "v_mfma_f32_16x16x32_bf16 v[124:127], v[168:171], v[20:23], v[124:127]\n\t"
  "ds_read_b64_tr_b16 v[134:135], v192 offset:6144\n\t"
  "v_accvgpr_write_b32 a11, v211\n\t"
  "v_mfma_f32_16x16x32_bf16 v[124:127], v[172:175], v[28:31], v[124:127]\n\t"
  "ds_read_b64_tr_b16 v[136:137], v192 offset:8192\n\t"
  "v_mfma_f32_16x16x32_bf16 v[120:123], v[172:175], v[24:27], v[120:123]\n\t"
  "ds_read_b64_tr_b16 v[138:139], v192 offset:10240\n\t"
  "s_waitcnt lgkmcnt(6)\n\t"
  "v_mfma_f32_16x16x32_bf16 v[120:123], v[176:179], v[32:35], v[120:123]\n\t"
  "ds_read_b64_tr_b16 v[140:141], v192 offset:12288\n\t"
  "buffer_load_dwordx4 v237, s[16:19], s36 offen offset:2048 lds\n\t"
  "v_mfma_f32_16x16x32_bf16 v[124:127], v[176:179], v[36:39], v[124:127]\n\t"
  "ds_read_b64_tr_b16 v[142:143], v192 offset:14336\n\t"
  "v_mfma_f32_16x16x32_bf16 v[124:127], v[180:183], v[44:47], v[124:127]\n\t"
  "ds_read_b64_tr_b16 v[144:145], v192 offset:16384\n\t"
  "v_mfma_f32_16x16x32_bf16 v[120:123], v[180:183], v[40:43], v[120:123]\n\t"
  "ds_read_b64_tr_b16 v[146:147], v192 offset:18432\n\t"
  "v_mfma_f32_16x16x32_bf16 v[120:123], v[184:187], v[48:51], v[120:123]\n\t"
  "ds_read_b64_tr_b16 v[148:149], v192 offset:20480\n\t"
  "buffer_load_dwordx4 v238, s[16:19], s36 offen offset:3072 lds\n\t"
  "v_mfma_f32_16x16x32_bf16 v[124:127], v[184:187], v[52:55], v[124:127]\n\t"
  "ds_read_b64_tr_b16 v[150:151], v192 offset:22528\n\t"
  "s_add_u32 s36, s36, 4294938752\n\t"
  "v_mfma_f32_16x16x32_bf16 v[124:127], v[188:191], v[60:63], v[124:127]\n\t"
  "ds_read_b64_tr_b16 v[152:153], v192 offset:24576\n\t"
  "v_mfma_f32_16x16x32_bf16 v[120:123], v[188:191], v[56:59], v[120:123]\n\t"
  "ds_read_b64_tr_b16 v[154:155], v192 offset:26624\n\t"
  "s_waitcnt lgkmcnt(6)\n\t"
  "v_mfma_f32_16x16x32_bf16 v[64:67], v[128:131], v[0:3], 0\n\t"
  "ds_read_b64_tr_b16 v[156:157], v192 offset:28672\n\t"
  "buffer_store_dwordx4 v[208:211], v247, s[20:23], s37 offen sc1\n\t"
  "v_mfma_f32_16x16x32_bf16 v[68:71], v[128:131], v[4:7], 0\n\t"
  "ds_read_b64_tr_b16 v[158:159], v192 offset:30720\n\t"
  "s_lshl_b32 s59, s61, 4\n\t"
  "v_mfma_f32_16x16x32_bf16 v[68:71], v[132:135], v[12:15], v[68:71]\n\t"
  "ds_read_b64_tr_b16 v[160:161], v193\n\t"
  "s_or_b32 s59, s59, s56\n\t"
  "v_mfma_f32_16x16x32_bf16 v[64:67], v[132:135], v[8:11], v[64:67]\n\t"
  "ds_read_b64_tr_b16 v[162:163], v193 offset:2048\n\t"
  "v_mfma_f32_16x16x32_bf16 v[64:67], v[136:139], v[16:19], v[64:67]\n\t"
  "ds_read_b64_tr_b16 v[164:165], v193 offset:4096\n\t"
  "buffer_store_dwordx4 v[212:215], v248, s[20:23], s38 offen sc1\n\t"
  "v_cvt_pk_bf16_f32 v216, v112, v113\n\t"
  "s_lshr_b32 s58, s59, 3\n\t"
  "v_mfma_f32_16x16x32_bf16 v[68:71], v[136:139], v[20:23], v[68:71]\n\t"
  "ds_read_b64_tr_b16 v[166:167], v193 offset:6144\n\t"
  "v_cvt_pk_bf16_f32 v217, v114, v115\n\t"
  "s_and_b32 s57, s59, 7\n\t"
  "v_mfma_f32_16x16x32_bf16 v[68:71], v[140:143], v[28:31], v[68:71]\n\t"
  "ds_read_b64_tr_b16 v[168:169], v193 offset:8192\n\t"
  "v_cvt_pk_bf16_f32 v218, v120, v121\n\t"
  "v_mfma_f32_16x16x32_bf16 v[64:67], v[140:143], v[24:27], v[64:67]\n\t"
  "ds_read_b64_tr_b16 v[170:171], v193 offset:10240\n\t"
  "s_waitcnt lgkmcnt(6)\n\t"
  "v_mfma_f32_16x16x32_bf16 v[64:67], v[144:147], v[32:35], v[64:67]\n\t"
  "ds_read_b64_tr_b16 v[172:173], v193 offset:12288\n\t"
  "v_cvt_pk_bf16_f32 v219, v122, v123\n\t"
  "v_mfma_f32_16x16x32_bf16 v[68:71], v[144:147], v[36:39], v[68:71]\n\t"
  "ds_read_b64_tr_b16 v[174:175], v193 offset:14336\n\t"
  "v_cvt_pk_bf16_f32 v220, v116, v117\n\t"
  "v_mfma_f32_16x16x32_bf16 v[68:71], v[148:151], v[44:47], v[68:71]\n\t"
  "ds_read_b64_tr_b16 v[176:177], v193 offset:16384\n\t"
  "buffer_store_dwordx4 v[216:219], v247, s[20:23], s37 offen offset:512 sc1\n\t"
  "v_cvt_pk_bf16_f32 v221, v118, v119\n\t"
  "v_mfma_f32_16x16x32_bf16 v[64:67], v[148:151], v[40:43], v[64:67]\n\t"
  "ds_read_b64_tr_b16 v[178:179], v193 offset:18432\n\t"
  "s_add_u32 s37, s37, 1024\n\t"
  "v_mfma_f32_16x16x32_bf16 v[64:67], v[152:155], v[48:51], v[64:67]\n\t"
  "ds_read_b64_tr_b16 v[180:181], v193 offset:20480\n\t"
  "v_cvt_pk_bf16_f32 v222, v124, v125\n\t"
  "v_mfma_f32_16x16x32_bf16 v[68:71], v[152:155], v[52:55], v[68:71]\n\t"
  "ds_read_b64_tr_b16 v[182:183], v193 offset:22528\n\t"
  "v_cvt_pk_bf16_f32 v223, v126, v127\n\t"
  "v_mfma_f32_16x16x32_bf16 v[68:71], v[156:159], v[60:63], v[68:71]\n\t"
  "ds_read_b64_tr_b16 v[184:185], v193 offset:24576\n\t"
  "v_mfma_f32_16x16x32_bf16 v[64:67], v[156:159], v[56:59], v[64:67]\n\t"
  "ds_read_b64_tr_b16 v[186:187], v193 offset:26624\n\t"
  "s_waitcnt lgkmcnt(6)\n\t"
  "v_mfma_f32_16x16x32_bf16 v[72:75], v[160:163], v[0:3], 0\n\t"
  "ds_read_b64_tr_b16 v[188:189], v193 offset:28672\n\t"
  "buffer_store_dwordx4 v[220:223], v248, s[20:23], s38 offen offset:512 sc1\n\t"
  "v_mfma_f32_16x16x32_bf16 v[76:79], v[160:163], v[4:7], 0\n\t"
  "ds_read_b64_tr_b16 v[190:191], v193 offset:30720\n\t"
  "v_accvgpr_write_b32 a12, v216\n\t"
  "s_add_u32 s38, s38, 1024\n\t"
  "v_mfma_f32_16x16x32_bf16 v[76:79], v[164:167], v[12:15], v[76:79]\n\t"
  "ds_read_b64_tr_b16 v[128:129], v192 offset:512\n\t"
  "v_accvgpr_write_b32 a13, v217\n\t"
  "v_mfma_f32_16x16x32_bf16 v[72:75], v[164:167], v[8:11], v[72:75]\n\t"
  "ds_read_b64_tr_b16 v[130:131], v192 offset:2560\n\t"
  "v_mfma_f32_16x16x32_bf16 v[72:75], v[168:171], v[16:19], v[72:75]\n\t"
  "ds_read_b64_tr_b16 v[132:133], v192 offset:4608\n\t"
  "v_accvgpr_write_b32 a14, v218\n\t"
  "v_mfma_f32_16x16x32_bf16 v[76:79], v[168:171], v[20:23], v[76:79]\n\t"
  "ds_read_b64_tr_b16 v[134:135], v192 offset:6656\n\t"
  "v_accvgpr_write_b32 a15, v219\n\t"
  "v_mfma_f32_16x16x32_bf16 v[76:79], v[172:175], v[28:31], v[76:79]\n\t"
  "ds_read_b64_tr_b16 v[136:137], v192 offset:8704\n\t"
  "v_mfma_f32_16x16x32_bf16 v[72:75], v[172:175], v[24:27], v[72:75]\n\t"
  "ds_read_b64_tr_b16 v[138:139], v192 offset:10752\n\t"
  "s_waitcnt lgkmcnt(6)\n\t"
  "v_mfma_f32_16x16x32_bf16 v[72:75], v[176:179], v[32:35], v[72:75]\n\t"
  "ds_read_b64_tr_b16 v[140:141], v192 offset:12800\n\t"
  "v_mfma_f32_16x16x32_bf16 v[76:79], v[176:179], v[36:39], v[76:79]\n\t"
  "ds_read_b64_tr_b16 v[142:143], v192 offset:14848\n\t"
  "v_mfma_f32_16x16x32_bf16 v[76:79], v[180:183], v[44:47], v[76:79]\n\t"
  "ds_read_b64_tr_b16 v[144:145], v192 offset:16896\n\t"
  "v_mfma_f32_16x16x32_bf16 v[72:75], v[180:183], v[40:43], v[72:75]\n\t"
  "ds_read_b64_tr_b16 v[146:147], v192 offset:18944\n\t"
  "v_mfma_f32_16x16x32_bf16 v[72:75], v[184:187], v[48:51], v[72:75]\n\t"
  "ds_read_b64_tr_b16 v[148:149], v192 offset:20992\n\t"
  "v_mfma_f32_16x16x32_bf16 v[76:79], v[184:187], v[52:55], v[76:79]\n\t"
  "ds_read_b64_tr_b16 v[150:151], v192 offset:23040\n\t"
  "v_mfma_f32_16x16x32_bf16 v[76:79], v[188:191], v[60:63], v[76:79]\n\t"
  "ds_read_b64_tr_b16 v[152:153], v192 offset:25088\n\t"
  "s_waitcnt vmcnt(24)\n\t"
  "s_barrier\n\t"
  "v_mfma_f32_16x16x32_bf16 v[72:75], v[188:191], v[56:59], v[72:75]\n\t"
  "ds_read_b64_tr_b16 v[154:155], v192 offset:27136\n\t"
  "s_add_u32 m0, s30, 32768\n\t"
  "s_waitcnt lgkmcnt(6)\n\t"
  "v_mfma_f32_16x16x32_bf16 v[80:83], v[128:131], v[0:3], 0\n\t"
  "ds_read_b64_tr_b16 v[156:157], v192 offset:29184\n\t"
  "buffer_load_dwordx4 v235, s[16:19], s36 offen lds\n\t"
  "v_mfma_f32_16x16x32_bf16 v[84:87], v[128:131], v[4:7], 0\n\t"
  "ds_read_b64_tr_b16 v[158:159], v192 offset:31232\n\t"
  "v_mfma_f32_16x16x32_bf16 v[84:87], v[132:135], v[12:15], v[84:87]\n\t"
  "ds_read_b64_tr_b16 v[160:161], v193 offset:512\n\t"
  "v_mfma_f32_16x16x32_bf16 v[80:83], v[132:135], v[8:11], v[80:83]\n\t"
  "ds_read_b64_tr_b16 v[162:163], v193 offset:2560\n\t"
  "v_mfma_f32_16x16x32_bf16 v[80:83], v[136:139], v[16:19], v[80:83]\n\t"
  "ds_read_b64_tr_b16 v[164:165], v193 offset:4608\n\t"
  "buffer_load_dwordx4 v236, s[16:19], s36 offen offset:1024 lds\n\t"
  "v_cvt_pk_bf16_f32 v208, v64, v65\n\t"
  "v_mfma_f32_16x16x32_bf16 v[84:87], v[136:139], v[20:23], v[84:87]\n\t"
  "ds_read_b64_tr_b16 v[166:167], v193 offset:6656\n\t"
  "v_cvt_pk_bf16_f32 v209, v66, v67\n\t"
  "v_mfma_f32_16x16x32_bf16 v[84:87], v[140:143], v[28:31], v[84:87]\n\t"
  "ds_read_b64_tr_b16 v[168:169], v193 offset:8704\n\t"
  "v_cvt_pk_bf16_f32 v210, v72, v73\n\t"
  "v_mfma_f32_16x16x32_bf16 v[80:83], v[140:143], v[24:27], v[80:83]\n\t"
  "ds_read_b64_tr_b16 v[170:171], v193 offset:10752\n\t"
  "s_waitcnt lgkmcnt(6)\n\t"
  "v_mfma_f32_16x16x32_bf16 v[80:83], v[144:147], v[32:35], v[80:83]\n\t"
  "ds_read_b64_tr_b16 v[172:173], v193 offset:12800\n\t"
  "buffer_load_dwordx4 v237, s[16:19], s36 offen offset:2048 lds\n\t"
  "v_cvt_pk_bf16_f32 v211, v74, v75\n\t"
  "v_mfma_f32_16x16x32_bf16 v[84:87], v[144:147], v[36:39], v[84:87]\n\t"
  "ds_read_b64_tr_b16 v[174:175], v193 offset:14848\n\t"
  "v_cvt_pk_bf16_f32 v212, v68, v69\n\t"
  "v_mfma_f32_16x16x32_bf16 v[84:87], v[148:151], v[44:47], v[84:87]\n\t"
  "ds_read_b64_tr_b16 v[176:177], v193 offset:16896\n\t"
  "v_cvt_pk_bf16_f32 v213, v70, v71\n\t"
  "v_mfma_f32_16x16x32_bf16 v[80:83], v[148:151], v[40:43], v[80:83]\n\t"
  "ds_read_b64_tr_b16 v[178:179], v193 offset:18944\n\t"
  "v_mfma_f32_16x16x32_bf16 v[80:83], v[152:155], v[48:51], v[80:83]\n\t"
  "ds_read_b64_tr_b16 v[180:181], v193 offset:20992\n\t"
  "buffer_load_dwordx4 v238, s[16:19], s36 offen offset:3072 lds\n\t"
  "v_cvt_pk_bf16_f32 v214, v76, v77\n\t"
  "v_mfma_f32_16x16x32_bf16 v[84:87], v[152:155], v[52:55], v[84:87]\n\t"
  "ds_read_b64_tr_b16 v[182:183], v193 offset:23040\n\t"
  "s_add_u32 s36, s36, 28672\n\t"
  "v_cvt_pk_bf16_f32 v215, v78, v79\n\t"
  "v_mfma_f32_16x16x32_bf16 v[84:87], v[156:159], v[60:63], v[84:87]\n\t"
  "ds_read_b64_tr_b16 v[184:185], v193 offset:25088\n\t"
  "v_mfma_f32_16x16x32_bf16 v[80:83], v[156:159], v[56:59], v[80:83]\n\t"
  "ds_read_b64_tr_b16 v[186:187], v193 offset:27136\n\t"
  "s_add_u32 m0, s30, 36864\n\t"
  "s_waitcnt lgkmcnt(6)\n\t"
  "v_mfma_f32_16x16x32_bf16 v[88:91], v[160:163], v[0:3], 0\n\t"
  "ds_read_b64_tr_b16 v[188:189], v193 offset:29184\n\t"
  "buffer_load_dwordx4 v235, s[16:19], s36 offen lds\n\t"
  "v_mfma_f32_16x16x32_bf16 v[92:95], v[160:163], v[4:7], 0\n\t"
  "ds_read_b64_tr_b16 v[190:191], v193 offset:31232\n\t"
  "v_accvgpr_write_b32 a16, v208\n\t"
  "v_mfma_f32_16x16x32_bf16 v[92:95], v[164:167], v[12:15], v[92:95]\n\t"
  "ds_read_b64_tr_b16 v[128:129], v194\n\t"
  "v_accvgpr_write_b32 a17, v209\n\t"
  "v_mfma_f32_16x16x32_bf16 v[88:91], v[164:167], v[8:11], v[88:91]\n\t"
  "ds_read_b64_tr_b16 v[130:131], v194 offset:2048\n\t"
  "v_mfma_f32_16x16x32_bf16 v[88:91], v[168:171], v[16:19], v[88:91]\n\t"
  "ds_read_b64_tr_b16 v[132:133], v194 offset:4096\n\t"
  "buffer_load_dwordx4 v236, s[16:19], s36 offen offset:1024 lds\n\t"
  "v_accvgpr_write_b32 a18, v210\n\t"
  "v_mfma_f32_16x16x32_bf16 v[92:95], v[168:171], v[20:23], v[92:95]\n\t"
  "ds_read_b64_tr_b16 v[134:135], v194 offset:6144\n\t"
  "v_accvgpr_write_b32 a19, v211\n\t"
  "v_mfma_f32_16x16x32_bf16 v[92:95], v[172:175], v[28:31], v[92:95]\n\t"
  "ds_read_b64_tr_b16 v[136:137], v194 offset:8192\n\t"
  "v_mfma_f32_16x16x32_bf16 v[88:91], v[172:175], v[24:27], v[88:91]\n\t"
  "ds_read_b64_tr_b16 v[138:139], v194 offset:10240\n\t"
  "s_waitcnt lgkmcnt(6)\n\t"
  "v_mfma_f32_16x16x32_bf16 v[88:91], v[176:179], v[32:35], v[88:91]\n\t"
  "ds_read_b64_tr_b16 v[140:141], v194 offset:12288\n\t"
  "buffer_load_dwordx4 v237, s[16:19], s36 offen offset:2048 lds\n\t"
  "v_mfma_f32_16x16x32_bf16 v[92:95], v[176:179], v[36:39], v[92:95]\n\t"
  "ds_read_b64_tr_b16 v[142:143], v194 offset:14336\n\t"
  "v_mfma_f32_16x16x32_bf16 v[92:95], v[180:183], v[44:47], v[92:95]\n\t"
  "ds_read_b64_tr_b16 v[144:145], v194 offset:16384\n\t"
  "v_mfma_f32_16x16x32_bf16 v[88:91], v[180:183], v[40:43], v[88:91]\n\t"
  "ds_read_b64_tr_b16 v[146:147], v194 offset:18432\n\t"
  "v_mfma_f32_16x16x32_bf16 v[88:91], v[184:187], v[48:51], v[88:91]\n\t"
  "ds_read_b64_tr_b16 v[148:149], v194 offset:20480\n\t"
  "buffer_load_dwordx4 v238, s[16:19], s36 offen offset:3072 lds\n\t"
  "v_mfma_f32_16x16x32_bf16 v[92:95], v[184:187], v[52:55], v[92:95]\n\t"
  "ds_read_b64_tr_b16 v[150:151], v194 offset:22528\n\t"
  "s_add_u32 s36, s36, 4294938752\n\t"
  "v_mfma_f32_16x16x32_bf16 v[92:95], v[188:191], v[60:63], v[92:95]\n\t"
  "ds_read_b64_tr_b16 v[152:153], v194 offset:24576\n\t"
  "v_mfma_f32_16x16x32_bf16 v[88:91], v[188:191], v[56:59], v[88:91]\n\t"
  "ds_read_b64_tr_b16 v[154:155], v194 offset:26624\n\t"
  "s_waitcnt lgkmcnt(6)\n\t"
  "v_mfma_f32_16x16x32_bf16 v[96:99], v[128:131], v[0:3], 0\n\t"
  "ds_read_b64_tr_b16 v[156:157], v194 offset:28672\n\t"
  "buffer_store_dwordx4 v[208:211], v247, s[20:23], s37 offen sc1\n\t"
  "v_mfma_f32_16x16x32_bf16 v[100:103], v[128:131], v[4:7], 0\n\t"
  "ds_read_b64_tr_b16 v[158:159], v194 offset:30720\n\t"
  "v_mfma_f32_16x16x32_bf16 v[100:103], v[132:135], v[12:15], v[100:103]\n\t"
  "ds_read_b64_tr_b16 v[160:161], v195\n\t"
  "v_mfma_f32_16x16x32_bf16 v[96:99], v[132:135], v[8:11], v[96:99]\n\t"
  "ds_read_b64_tr_b16 v[162:163], v195 offset:2048\n\t"
  "v_mfma_f32_16x16x32_bf16 v[96:99], v[136:139], v[16:19], v[96:99]\n\t"
  "ds_read_b64_tr_b16 v[164:165], v195 offset:4096\n\t"
  "buffer_store_dwordx4 v[212:215], v248, s[20:23], s38 offen sc1\n\t"
  "v_cvt_pk_bf16_f32 v216, v80, v81\n\t"
  "v_mfma_f32_16x16x32_bf16 v[100:103], v[136:139], v[20:23], v[100:103]\n\t"
  "ds_read_b64_tr_b16 v[166:167], v195 offset:6144\n\t"
  "v_cvt_pk_bf16_f32 v217, v82, v83\n\t"
  "v_mfma_f32_16x16x32_bf16 v[100:103], v[140:143], v[28:31], v[100:103]\n\t"
  "ds_read_b64_tr_b16 v[168:169], v195 offset:8192\n\t"
  "v_cvt_pk_bf16_f32 v218, v88, v89\n\t"
  "v_mfma_f32_16x16x32_bf16 v[96:99], v[140:143], v[24:27], v[96:99]\n\t"
  "ds_read_b64_tr_b16 v[170:171], v195 offset:10240\n\t"
  "s_waitcnt lgkmcnt(6)\n\t"
  "v_mfma_f32_16x16x32_bf16 v[96:99], v[144:147], v[32:35], v[96:99]\n\t"
  "ds_read_b64_tr_b16 v[172:173], v195 offset:12288\n\t"
  "v_cvt_pk_bf16_f32 v219, v90, v91\n\t"
  "v_mfma_f32_16x16x32_bf16 v[100:103], v[144:147], v[36:39], v[100:103]\n\t"
  "ds_read_b64_tr_b16 v[174:175], v195 offset:14336\n\t"
  "v_cvt_pk_bf16_f32 v220, v84, v85\n\t"
  "v_mfma_f32_16x16x32_bf16 v[100:103], v[148:151], v[44:47], v[100:103]\n\t"
  "ds_read_b64_tr_b16 v[176:177], v195 offset:16384\n\t"
  "buffer_store_dwordx4 v[216:219], v247, s[20:23], s37 offen offset:512 sc1\n\t"
  "v_cvt_pk_bf16_f32 v221, v86, v87\n\t"
  "v_mfma_f32_16x16x32_bf16 v[96:99], v[148:151], v[40:43], v[96:99]\n\t"
  "ds_read_b64_tr_b16 v[178:179], v195 offset:18432\n\t"
  "s_add_u32 s37, s37, 1024\n\t"
  "v_mfma_f32_16x16x32_bf16 v[96:99], v[152:155], v[48:51], v[96:99]\n\t"
  "ds_read_b64_tr_b16 v[180:181], v195 offset:20480\n\t"
  "v_cvt_pk_bf16_f32 v222, v92, v93\n\t"
  "v_mfma_f32_16x16x32_bf16 v[100:103], v[152:155], v[52:55], v[100:103]\n\t"
  "ds_read_b64_tr_b16 v[182:183], v195 offset:22528\n\t"
  "v_cvt_pk_bf16_f32 v223, v94, v95\n\t"
  "v_mfma_f32_16x16x32_bf16 v[100:103], v[156:159], v[60:63], v[100:103]\n\t"
  "ds_read_b64_tr_b16 v[184:185], v195 offset:24576\n\t"
  "v_mfma_f32_16x16x32_bf16 v[96:99], v[156:159], v[56:59], v[96:99]\n\t"
  "ds_read_b64_tr_b16 v[186:187], v195 offset:26624\n\t"
  "s_waitcnt lgkmcnt(6)\n\t"
  "v_mfma_f32_16x16x32_bf16 v[104:107], v[160:163], v[0:3], 0\n\t"
  "ds_read_b64_tr_b16 v[188:189], v195 offset:28672\n\t"
  "buffer_store_dwordx4 v[220:223], v248, s[20:23], s38 offen offset:512 sc1\n\t"
  "v_mfma_f32_16x16x32_bf16 v[108:111], v[160:163], v[4:7], 0\n\t"
  "ds_read_b64_tr_b16 v[190:191], v195 offset:30720\n\t"
  "v_accvgpr_write_b32 a20, v216\n\t"
  "s_add_u32 s38, s38, 1024\n\t"
  "v_mfma_f32_16x16x32_bf16 v[108:111], v[164:167], v[12:15], v[108:111]\n\t"
  "ds_read_b64_tr_b16 v[128:129], v194 offset:512\n\t"
  "v_accvgpr_write_b32 a21, v217\n\t"
  "v_mfma_f32_16x16x32_bf16 v[104:107], v[164:167], v[8:11], v[104:107]\n\t"
  "ds_read_b64_tr_b16 v[130:131], v194 offset:2560\n\t"
  "v_mfma_f32_16x16x32_bf16 v[104:107], v[168:171], v[16:19], v[104:107]\n\t"
  "ds_read_b64_tr_b16 v[132:133], v194 offset:4608\n\t"
  "v_accvgpr_write_b32 a22, v218\n\t"
  "v_mfma_f32_16x16x32_bf16 v[108:111], v[168:171], v[20:23], v[108:111]\n\t"
  "ds_read_b64_tr_b16 v[134:135], v194 offset:6656\n\t"
  "v_accvgpr_write_b32 a23, v219\n\t"
  "v_mfma_f32_16x16x32_bf16 v[108:111], v[172:175], v[28:31], v[108:111]\n\t"
  "ds_read_b64_tr_b16 v[136:137], v194 offset:8704\n\t"
  "v_mfma_f32_16x16x32_bf16 v[104:107], v[172:175], v[24:27], v[104:107]\n\t"
  "ds_read_b64_tr_b16 v[138:139], v194 offset:10752\n\t"
  "s_waitcnt lgkmcnt(6)\n\t"
  "v_mfma_f32_16x16x32_bf16 v[104:107], v[176:179], v[32:35], v[104:107]\n\t"
  "ds_read_b64_tr_b16 v[140:141], v194 offset:12800\n\t"
  "v_mfma_f32_16x16x32_bf16 v[108:111], v[176:179], v[36:39], v[108:111]\n\t"
  "ds_read_b64_tr_b16 v[142:143], v194 offset:14848\n\t"
  "v_mfma_f32_16x16x32_bf16 v[108:111], v[180:183], v[44:47], v[108:111]\n\t"
  "ds_read_b64_tr_b16 v[144:145], v194 offset:16896\n\t"
  "v_mfma_f32_16x16x32_bf16 v[104:107], v[180:183], v[40:43], v[104:107]\n\t"
  "ds_read_b64_tr_b16 v[146:147], v194 offset:18944\n\t"
  "v_mfma_f32_16x16x32_bf16 v[104:107], v[184:187], v[48:51], v[104:107]\n\t"
  "ds_read_b64_tr_b16 v[148:149], v194 offset:20992\n\t"
  "v_mfma_f32_16x16x32_bf16 v[108:111], v[184:187], v[52:55], v[108:111]\n\t"
  "ds_read_b64_tr_b16 v[150:151], v194 offset:23040\n\t"
  "v_mfma_f32_16x16x32_bf16 v[108:111], v[188:191], v[60:63], v[108:111]\n\t"
  "ds_read_b64_tr_b16 v[152:153], v194 offset:25088\n\t"
  "s_waitcnt vmcnt(28)\n\t"
  "s_barrier\n\t"
  "v_mfma_f32_16x16x32_bf16 v[104:107], v[188:191], v[56:59], v[104:107]\n\t"
  "ds_read_b64_tr_b16 v[154:155], v194 offset:27136\n\t"
  "s_waitcnt lgkmcnt(6)\n\t"
  "v_mfma_f32_16x16x32_bf16 v[112:115], v[128:131], v[0:3], 0\n\t"
  "ds_read_b64_tr_b16 v[156:157], v194 offset:29184\n\t"
  "v_mfma_f32_16x16x32_bf16 v[116:119], v[128:131], v[4:7], 0\n\t"
  "ds_read_b64_tr_b16 v[158:159], v194 offset:31232\n\t"
  "v_mfma_f32_16x16x32_bf16 v[116:119], v[132:135], v[12:15], v[116:119]\n\t"
  "ds_read_b64_tr_b16 v[160:161], v195 offset:512\n\t"
  "v_mfma_f32_16x16x32_bf16 v[112:115], v[132:135], v[8:11], v[112:115]\n\t"
  "ds_read_b64_tr_b16 v[162:163], v195 offset:2560\n\t"
  "v_mfma_f32_16x16x32_bf16 v[112:115], v[136:139], v[16:19], v[112:115]\n\t"
  "ds_read_b64_tr_b16 v[164:165], v195 offset:4608\n\t"
  "v_cvt_pk_bf16_f32 v208, v96, v97\n\t"
  "v_mfma_f32_16x16x32_bf16 v[116:119], v[136:139], v[20:23], v[116:119]\n\t"
  "ds_read_b64_tr_b16 v[166:167], v195 offset:6656\n\t"
  "v_cvt_pk_bf16_f32 v209, v98, v99\n\t"
  "v_mfma_f32_16x16x32_bf16 v[116:119], v[140:143], v[28:31], v[116:119]\n\t"
  "ds_read_b64_tr_b16 v[168:169], v195 offset:8704\n\t"
  "v_cvt_pk_bf16_f32 v210, v104, v105\n\t"
  "v_mfma_f32_16x16x32_bf16 v[112:115], v[140:143], v[24:27], v[112:115]\n\t"
  "ds_read_b64_tr_b16 v[170:171], v195 offset:10752\n\t"
  "s_waitcnt lgkmcnt(6)\n\t"
  "v_mfma_f32_16x16x32_bf16 v[112:115], v[144:147], v[32:35], v[112:115]\n\t"
  "ds_read_b64_tr_b16 v[172:173], v195 offset:12800\n\t"
  "v_cvt_pk_bf16_f32 v211, v106, v107\n\t"
  "v_mfma_f32_16x16x32_bf16 v[116:119], v[144:147], v[36:39], v[116:119]\n\t"
  "ds_read_b64_tr_b16 v[174:175], v195 offset:14848\n\t"
  "v_cvt_pk_bf16_f32 v212, v100, v101\n\t"
  "v_mfma_f32_16x16x32_bf16 v[116:119], v[148:151], v[44:47], v[116:119]\n\t"
  "ds_read_b64_tr_b16 v[176:177], v195 offset:16896\n\t"
  "buffer_store_dwordx4 v[208:211], v247, s[20:23], s37 offen sc1\n\t"
  "v_cvt_pk_bf16_f32 v213, v102, v103\n\t"
  "v_mfma_f32_16x16x32_bf16 v[112:115], v[148:151], v[40:43], v[112:115]\n\t"
  "ds_read_b64_tr_b16 v[178:179], v195 offset:18944\n\t"
  "v_mfma_f32_16x16x32_bf16 v[112:115], v[152:155], v[48:51], v[112:115]\n\t"
  "ds_read_b64_tr_b16 v[180:181], v195 offset:20992\n\t"
  "v_cvt_pk_bf16_f32 v214, v108, v109\n\t"
  "v_mfma_f32_16x16x32_bf16 v[116:119], v[152:155], v[52:55], v[116:119]\n\t"
  "ds_read_b64_tr_b16 v[182:183], v195 offset:23040\n\t"
  "v_cvt_pk_bf16_f32 v215, v110, v111\n\t"
  "v_mfma_f32_16x16x32_bf16 v[116:119], v[156:159], v[60:63], v[116:119]\n\t"
  "ds_read_b64_tr_b16 v[184:185], v195 offset:25088\n\t"
  "v_mfma_f32_16x16x32_bf16 v[112:115], v[156:159], v[56:59], v[112:115]\n\t"
  "ds_read_b64_tr_b16 v[186:187], v195 offset:27136\n\t"
  "s_waitcnt lgkmcnt(6)\n\t"
  "v_mfma_f32_16x16x32_bf16 v[120:123], v[160:163], v[0:3], 0\n\t"
  "ds_read_b64_tr_b16 v[188:189], v195 offset:29184\n\t"
  "buffer_store_dwordx4 v[212:215], v248, s[20:23], s38 offen sc1\n\t"
  "v_mfma_f32_16x16x32_bf16 v[124:127], v[160:163], v[4:7], 0\n\t"
  "ds_read_b64_tr_b16 v[190:191], v195 offset:31232\n\t"
  "v_accvgpr_write_b32 a24, v208\n\t"
  "v_mfma_f32_16x16x32_bf16 v[124:127], v[164:167], v[12:15], v[124:127]\n\t"
  "ds_read_b64_tr_b16 v[128:129], v194 offset:32768\n\t"
  "v_accvgpr_write_b32 a25, v209\n\t"
  "v_mfma_f32_16x16x32_bf16 v[120:123], v[164:167], v[8:11], v[120:123]\n\t"
  "ds_read_b64_tr_b16 v[130:131], v194 offset:34816\n\t"
  "v_mfma_f32_16x16x32_bf16 v[120:123], v[168:171], v[16:19], v[120:123]\n\t"
  "ds_read_b64_tr_b16 v[132:133], v194 offset:36864\n\t"
  "v_accvgpr_write_b32 a26, v210\n\t"
  "v_mfma_f32_16x16x32_bf16 v[124:127], v[168:171], v[20:23], v[124:127]\n\t"
  "ds_read_b64_tr_b16 v[134:135], v194 offset:38912\n\t"
  "v_accvgpr_write_b32 a27, v211\n\t"
  "v_mfma_f32_16x16x32_bf16 v[124:127], v[172:175], v[28:31], v[124:127]\n\t"
  "ds_read_b64_tr_b16 v[136:137], v194 offset:40960\n\t"
  "v_mfma_f32_16x16x32_bf16 v[120:123], v[172:175], v[24:27], v[120:123]\n\t"
  "ds_read_b64_tr_b16 v[138:139], v194 offset:43008\n\t"
  "s_waitcnt lgkmcnt(6)\n\t"
  "v_mfma_f32_16x16x32_bf16 v[120:123], v[176:179], v[32:35], v[120:123]\n\t"
  "ds_read_b64_tr_b16 v[140:141], v194 offset:45056\n\t"
  "v_mfma_f32_16x16x32_bf16 v[124:127], v[176:179], v[36:39], v[124:127]\n\t"
  "ds_read_b64_tr_b16 v[142:143], v194 offset:47104\n\t"
  "v_mfma_f32_16x16x32_bf16 v[124:127], v[180:183], v[44:47], v[124:127]\n\t"
  "ds_read_b64_tr_b16 v[144:145], v194 offset:49152\n\t"
  "v_mfma_f32_16x16x32_bf16 v[120:123], v[180:183], v[40:43], v[120:123]\n\t"
  "ds_read_b64_tr_b16 v[146:147], v194 offset:51200\n\t"
  "v_mfma_f32_16x16x32_bf16 v[120:123], v[184:187], v[48:51], v[120:123]\n\t"
  "ds_read_b64_tr_b16 v[148:149], v194 offset:53248\n\t"
  "v_mfma_f32_16x16x32_bf16 v[124:127], v[184:187], v[52:55], v[124:127]\n\t"
  "ds_read_b64_tr_b16 v[150:151], v194 offset:55296\n\t"
  "v_mfma_f32_16x16x32_bf16 v[124:127], v[188:191], v[60:63], v[124:127]\n\t"
  "ds_read_b64_tr_b16 v[152:153], v194 offset:57344\n\t"
  "v_mfma_f32_16x16x32_bf16 v[120:123], v[188:191], v[56:59], v[120:123]\n\t"
  "ds_read_b64_tr_b16 v[154:155], v194 offset:59392\n\t"
  "s_waitcnt lgkmcnt(6)\n\t"
  "v_mfma_f32_16x16x32_bf16 v[64:67], v[128:131], v[0:3], 0\n\t"
  "ds_read_b64_tr_b16 v[156:157], v194 offset:61440\n\t"
  "v_mfma_f32_16x16x32_bf16 v[68:71], v[128:131], v[4:7], 0\n\t"
  "ds_read_b64_tr_b16 v[158:159], v194 offset:63488\n\t"
  "v_mfma_f32_16x16x32_bf16 v[68:71], v[132:135], v[12:15], v[68:71]\n\t"
  "ds_read_b64_tr_b16 v[160:161], v195 offset:32768\n\t"
  "v_mfma_f32_16x16x32_bf16 v[64:67], v[132:135], v[8:11], v[64:67]\n\t"
  "ds_read_b64_tr_b16 v[162:163], v195 offset:34816\n\t"
  "v_mfma_f32_16x16x32_bf16 v[64:67], v[136:139], v[16:19], v[64:67]\n\t"
  "ds_read_b64_tr_b16 v[164:165], v195 offset:36864\n\t"
  "v_cvt_pk_bf16_f32 v216, v112, v113\n\t"
  "v_mfma_f32_16x16x32_bf16 v[68:71], v[136:139], v[20:23], v[68:71]\n\t"
  "ds_read_b64_tr_b16 v[166:167], v195 offset:38912\n\t"
  "v_cvt_pk_bf16_f32 v217, v114, v115\n\t"
  "v_mfma_f32_16x16x32_bf16 v[68:71], v[140:143], v[28:31], v[68:71]\n\t"
  "ds_read_b64_tr_b16 v[168:169], v195 offset:40960\n\t"
  "v_cvt_pk_bf16_f32 v218, v120, v121\n\t"
  "v_mfma_f32_16x16x32_bf16 v[64:67], v[140:143], v[24:27], v[64:67]\n\t"
  "ds_read_b64_tr_b16 v[170:171], v195 offset:43008\n\t"
  "s_waitcnt lgkmcnt(6)\n\t"
  "v_mfma_f32_16x16x32_bf16 v[64:67], v[144:147], v[32:35], v[64:67]\n\t"
  "ds_read_b64_tr_b16 v[172:173], v195 offset:45056\n\t"
  "v_cvt_pk_bf16_f32 v219, v122, v123\n\t"
  "v_mfma_f32_16x16x32_bf16 v[68:71], v[144:147], v[36:39], v[68:71]\n\t"
  "ds_read_b64_tr_b16 v[174:175], v195 offset:47104\n\t"
  "v_cvt_pk_bf16_f32 v220, v116, v117\n\t"
  "v_mfma_f32_16x16x32_bf16 v[68:71], v[148:151], v[44:47], v[68:71]\n\t"
  "ds_read_b64_tr_b16 v[176:177], v195 offset:49152\n\t"
  "buffer_store_dwordx4 v[216:219], v247, s[20:23], s37 offen offset:512 sc1\n\t"
  "v_cvt_pk_bf16_f32 v221, v118, v119\n\t"
  "v_mfma_f32_16x16x32_bf16 v[64:67], v[148:151], v[40:43], v[64:67]\n\t"
  "ds_read_b64_tr_b16 v[178:179], v195 offset:51200\n\t"
  "s_add_u32 s37, s37, 1024\n\t"
  "v_mfma_f32_16x16x32_bf16 v[64:67], v[152:155], v[48:51], v[64:67]\n\t"
  "ds_read_b64_tr_b16 v[180:181], v195 offset:53248\n\t"
  "v_cvt_pk_bf16_f32 v222, v124, v125\n\t"
  "v_mfma_f32_16x16x32_bf16 v[68:71], v[152:155], v[52:55], v[68:71]\n\t"
  "ds_read_b64_tr_b16 v[182:183], v195 offset:55296\n\t"
  "v_cvt_pk_bf16_f32 v223, v126, v127\n\t"
  "v_mfma_f32_16x16x32_bf16 v[68:71], v[156:159], v[60:63], v[68:71]\n\t"
  "ds_read_b64_tr_b16 v[184:185], v195 offset:57344\n\t"
  "v_mfma_f32_16x16x32_bf16 v[64:67], v[156:159], v[56:59], v[64:67]\n\t"
  "ds_read_b64_tr_b16 v[186:187], v195 offset:59392\n\t"
  "s_waitcnt lgkmcnt(6)\n\t"
  "v_mfma_f32_16x16x32_bf16 v[72:75], v[160:163], v[0:3], 0\n\t"
  "ds_read_b64_tr_b16 v[188:189], v195 offset:61440\n\t"
  "buffer_store_dwordx4 v[220:223], v248, s[20:23], s38 offen offset:512 sc1\n\t"
  "v_mfma_f32_16x16x32_bf16 v[76:79], v[160:163], v[4:7], 0\n\t"
  "ds_read_b64_tr_b16 v[190:191], v195 offset:63488\n\t"
  "v_accvgpr_write_b32 a28, v216\n\t"
  "s_add_u32 s38, s38, 1024\n\t"
  "v_mfma_f32_16x16x32_bf16 v[76:79], v[164:167], v[12:15], v[76:79]\n\t"
  "ds_read_b64_tr_b16 v[128:129], v194 offset:33280\n\t"
  "v_accvgpr_write_b32 a29, v217\n\t"
  "v_mfma_f32_16x16x32_bf16 v[72:75], v[164:167], v[8:11], v[72:75]\n\t"
  "ds_read_b64_tr_b16 v[130:131], v194 offset:35328\n\t"
  "v_mfma_f32_16x16x32_bf16 v[72:75], v[168:171], v[16:19], v[72:75]\n\t"
  "ds_read_b64_tr_b16 v[132:133], v194 offset:37376\n\t"
  "v_accvgpr_write_b32 a30, v218\n\t"
  "v_mfma_f32_16x16x32_bf16 v[76:79], v[168:171], v[20:23], v[76:79]\n\t"
  "ds_read_b64_tr_b16 v[134:135], v194 offset:39424\n\t"
  "v_accvgpr_write_b32 a31, v219\n\t"
  "v_mfma_f32_16x16x32_bf16 v[76:79], v[172:175], v[28:31], v[76:79]\n\t"
  "ds_read_b64_tr_b16 v[136:137], v194 offset:41472\n\t"
  "v_mfma_f32_16x16x32_bf16 v[72:75], v[172:175], v[24:27], v[72:75]\n\t"
  "ds_read_b64_tr_b16 v[138:139], v194 offset:43520\n\t"
  "s_waitcnt lgkmcnt(6)\n\t"
  "v_mfma_f32_16x16x32_bf16 v[72:75], v[176:179], v[32:35], v[72:75]\n\t"
  "ds_read_b64_tr_b16 v[140:141], v194 offset:45568\n\t"
  "v_mfma_f32_16x16x32_bf16 v[76:79], v[176:179], v[36:39], v[76:79]\n\t"
  "ds_read_b64_tr_b16 v[142:143], v194 offset:47616\n\t"
  "v_mfma_f32_16x16x32_bf16 v[76:79], v[180:183], v[44:47], v[76:79]\n\t"
  "ds_read_b64_tr_b16 v[144:145], v194 offset:49664\n\t"
  "v_mfma_f32_16x16x32_bf16 v[72:75], v[180:183], v[40:43], v[72:75]\n\t"
  "ds_read_b64_tr_b16 v[146:147], v194 offset:51712\n\t"
  "v_mfma_f32_16x16x32_bf16 v[72:75], v[184:187], v[48:51], v[72:75]\n\t"
  "ds_read_b64_tr_b16 v[148:149], v194 offset:53760\n\t"
  "v_mfma_f32_16x16x32_bf16 v[76:79], v[184:187], v[52:55], v[76:79]\n\t"
  "ds_read_b64_tr_b16 v[150:151], v194 offset:55808\n\t"
  "v_mfma_f32_16x16x32_bf16 v[76:79], v[188:191], v[60:63], v[76:79]\n\t"
  "ds_read_b64_tr_b16 v[152:153], v194 offset:57856\n\t"
  "s_waitcnt vmcnt(20)\n\t"
  "s_waitcnt vmcnt(4)\n\t"
  "s_barrier\n\t"
  "s_cmp_lg_u32 s62, 3\n\t"
  "s_cbranch_scc1 .Lpub266240\n\t"
  "s_andn2_b32 s64, s59, 15\n\t"
  "s_lshl_b32 s64, s64, 5\n\t"
  "s_and_b32 s65, s59, 15\n\t"
  "s_add_u32 s64, s64, s65\n\t"
  "s_lshl_b32 s64, s64, 2\n\t"
  "s_add_u32 s64, s64, 266240\n\t"
  "v_lshlrev_b32_e32 v198, 7, v254\n\t"
  "v_add_u32_e32 v198, s64, v198\n\t"
  "v_mov_b32_e32 v199, s69\n\t"
  "s_mov_b64 exec, 65535\n\t"
  "global_store_dword v198, v199, s[98:99] sc1\n\t"
  "s_mov_b64 exec, -1\n\t"
  ".Lpub266240:\n\t"
  "v_mfma_f32_16x16x32_bf16 v[72:75], v[188:191], v[56:59], v[72:75]\n\t"
  "ds_read_b64_tr_b16 v[154:155], v194 offset:59904\n\t"
  "s_waitcnt lgkmcnt(6)\n\t"
  "v_mfma_f32_16x16x32_bf16 v[80:83], v[128:131], v[0:3], 0\n\t"
  "ds_read_b64_tr_b16 v[156:157], v194 offset:61952\n\t"
  "v_mfma_f32_16x16x32_bf16 v[84:87], v[128:131], v[4:7], 0\n\t"
  "ds_read_b64_tr_b16 v[158:159], v194 offset:64000\n\t"
  "v_mfma_f32_16x16x32_bf16 v[84:87], v[132:135], v[12:15], v[84:87]\n\t"
  "ds_read_b64_tr_b16 v[160:161], v195 offset:33280\n\t"
  "v_mfma_f32_16x16x32_bf16 v[80:83], v[132:135], v[8:11], v[80:83]\n\t"
  "ds_read_b64_tr_b16 v[162:163], v195 offset:35328\n\t"
  "v_mfma_f32_16x16x32_bf16 v[80:83], v[136:139], v[16:19], v[80:83]\n\t"
  "ds_read_b64_tr_b16 v[164:165], v195 offset:37376\n\t"
  "v_cvt_pk_bf16_f32 v208, v64, v65\n\t"
  "v_mfma_f32_16x16x32_bf16 v[84:87], v[136:139], v[20:23], v[84:87]\n\t"
  "ds_read_b64_tr_b16 v[166:167], v195 offset:39424\n\t"
  "v_cvt_pk_bf16_f32 v209, v66, v67\n\t"
  "v_mfma_f32_16x16x32_bf16 v[84:87], v[140:143], v[28:31], v[84:87]\n\t"
  "ds_read_b64_tr_b16 v[168:169], v195 offset:41472\n\t"
  "v_cvt_pk_bf16_f32 v210, v72, v73\n\t"
  "v_mfma_f32_16x16x32_bf16 v[80:83], v[140:143], v[24:27], v[80:83]\n\t"
  "ds_read_b64_tr_b16 v[170:171], v195 offset:43520\n\t"
  "s_waitcnt lgkmcnt(6)\n\t"
  "v_mfma_f32_16x16x32_bf16 v[80:83], v[144:147], v[32:35], v[80:83]\n\t"
  "ds_read_b64_tr_b16 v[172:173], v195 offset:45568\n\t"
  "v_cvt_pk_bf16_f32 v211, v74, v75\n\t"
  "v_mfma_f32_16x16x32_bf16 v[84:87], v[144:147], v[36:39], v[84:87]\n\t"
  "ds_read_b64_tr_b16 v[174:175], v195 offset:47616\n\t"
  "v_cvt_pk_bf16_f32 v212, v68, v69\n\t"
  "v_mfma_f32_16x16x32_bf16 v[84:87], v[148:151], v[44:47], v[84:87]\n\t"
  "ds_read_b64_tr_b16 v[176:177], v195 offset:49664\n\t"
  "buffer_store_dwordx4 v[208:211], v247, s[20:23], s37 offen sc1\n\t"
  "v_cvt_pk_bf16_f32 v213, v70, v71\n\t"
  "v_mfma_f32_16x16x32_bf16 v[80:83], v[148:151], v[40:43], v[80:83]\n\t"
  "ds_read_b64_tr_b16 v[178:179], v195 offset:51712\n\t"
  "v_mfma_f32_16x16x32_bf16 v[80:83], v[152:155], v[48:51], v[80:83]\n\t"
  "ds_read_b64_tr_b16 v[180:181], v195 offset:53760\n\t"
  "v_cvt_pk_bf16_f32 v214, v76, v77\n\t"
  "v_mfma_f32_16x16x32_bf16 v[84:87], v[152:155], v[52:55], v[84:87]\n\t"
  "ds_read_b64_tr_b16 v[182:183], v195 offset:55808\n\t"
  "v_cvt_pk_bf16_f32 v215, v78, v79\n\t"
  "v_mfma_f32_16x16x32_bf16 v[84:87], v[156:159], v[60:63], v[84:87]\n\t"
  "ds_read_b64_tr_b16 v[184:185], v195 offset:57856\n\t"
  "v_mfma_f32_16x16x32_bf16 v[80:83], v[156:159], v[56:59], v[80:83]\n\t"
  "ds_read_b64_tr_b16 v[186:187], v195 offset:59904\n\t"
  "s_waitcnt lgkmcnt(6)\n\t"
  "v_mfma_f32_16x16x32_bf16 v[88:91], v[160:163], v[0:3], 0\n\t"
  "ds_read_b64_tr_b16 v[188:189], v195 offset:61952\n\t"
  "buffer_store_dwordx4 v[212:215], v248, s[20:23], s38 offen sc1\n\t"
  "v_mfma_f32_16x16x32_bf16 v[92:95], v[160:163], v[4:7], 0\n\t"
  "ds_read_b64_tr_b16 v[190:191], v195 offset:64000\n\t"
  "v_accvgpr_write_b32 a32, v208\n\t"
  "v_mfma_f32_16x16x32_bf16 v[92:95], v[164:167], v[12:15], v[92:95]\n\t"
  "ds_read_b64_tr_b16 v[128:129], v196\n\t"
  "v_accvgpr_write_b32 a33, v209\n\t"
  "v_mfma_f32_16x16x32_bf16 v[88:91], v[164:167], v[8:11], v[88:91]\n\t"
  "ds_read_b64_tr_b16 v[130:131], v196 offset:2048\n\t"
  "v_mfma_f32_16x16x32_bf16 v[88:91], v[168:171], v[16:19], v[88:91]\n\t"
  "ds_read_b64_tr_b16 v[132:133], v196 offset:4096\n\t"
  "v_accvgpr_write_b32 a34, v210\n\t"
  "v_mfma_f32_16x16x32_bf16 v[92:95], v[168:171], v[20:23], v[92:95]\n\t"
  "ds_read_b64_tr_b16 v[134:135], v196 offset:6144\n\t"
  "v_accvgpr_write_b32 a35, v211\n\t"
  "v_mfma_f32_16x16x32_bf16 v[92:95], v[172:175], v[28:31], v[92:95]\n\t"
  "ds_read_b64_tr_b16 v[136:137], v196 offset:8192\n\t"
  "v_mfma_f32_16x16x32_bf16 v[88:91], v[172:175], v[24:27], v[88:91]\n\t"
  "ds_read_b64_tr_b16 v[138:139], v196 offset:10240\n\t"
  "s_waitcnt lgkmcnt(6)\n\t"
  "v_mfma_f32_16x16x32_bf16 v[88:91], v[176:179], v[32:35], v[88:91]\n\t"
  "ds_read_b64_tr_b16 v[140:141], v196 offset:12288\n\t"
  "v_mfma_f32_16x16x32_bf16 v[92:95], v[176:179], v[36:39], v[92:95]\n\t"
  "ds_read_b64_tr_b16 v[142:143], v196 offset:14336\n\t"
  "v_mfma_f32_16x16x32_bf16 v[92:95], v[180:183], v[44:47], v[92:95]\n\t"
  "ds_read_b64_tr_b16 v[144:145], v196 offset:16384\n\t"
  "v_mfma_f32_16x16x32_bf16 v[88:91], v[180:183], v[40:43], v[88:91]\n\t"
  "ds_read_b64_tr_b16 v[146:147], v196 offset:18432\n\t"
  "v_mfma_f32_16x16x32_bf16 v[88:91], v[184:187], v[48:51], v[88:91]\n\t"
  "ds_read_b64_tr_b16 v[148:149], v196 offset:20480\n\t"
  "v_mfma_f32_16x16x32_bf16 v[92:95], v[184:187], v[52:55], v[92:95]\n\t"
  "ds_read_b64_tr_b16 v[150:151], v196 offset:22528\n\t"
  "v_mfma_f32_16x16x32_bf16 v[92:95], v[188:191], v[60:63], v[92:95]\n\t"
  "ds_read_b64_tr_b16 v[152:153], v196 offset:24576\n\t"
  "v_mfma_f32_16x16x32_bf16 v[88:91], v[188:191], v[56:59], v[88:91]\n\t"
  "ds_read_b64_tr_b16 v[154:155], v196 offset:26624\n\t"
  "s_waitcnt lgkmcnt(6)\n\t"
  "v_mfma_f32_16x16x32_bf16 v[96:99], v[128:131], v[0:3], 0\n\t"
  "ds_read_b64_tr_b16 v[156:157], v196 offset:28672\n\t"
  "v_mfma_f32_16x16x32_bf16 v[100:103], v[128:131], v[4:7], 0\n\t"
  "ds_read_b64_tr_b16 v[158:159], v196 offset:30720\n\t"
  "v_mfma_f32_16x16x32_bf16 v[100:103], v[132:135], v[12:15], v[100:103]\n\t"
  "ds_read_b64_tr_b16 v[160:161], v197\n\t"
  "v_mfma_f32_16x16x32_bf16 v[96:99], v[132:135], v[8:11], v[96:99]\n\t"
  "ds_read_b64_tr_b16 v[162:163], v197 offset:2048\n\t"
  "v_mfma_f32_16x16x32_bf16 v[96:99], v[136:139], v[16:19], v[96:99]\n\t"
  "ds_read_b64_tr_b16 v[164:165], v197 offset:4096\n\t"
  "v_cvt_pk_bf16_f32 v216, v80, v81\n\t"
  "v_mfma_f32_16x16x32_bf16 v[100:103], v[136:139], v[20:23], v[100:103]\n\t"
  "ds_read_b64_tr_b16 v[166:167], v197 offset:6144\n\t"
  "v_cvt_pk_bf16_f32 v217, v82, v83\n\t"
  "v_mfma_f32_16x16x32_bf16 v[100:103], v[140:143], v[28:31], v[100:103]\n\t"
  "ds_read_b64_tr_b16 v[168:169], v197 offset:8192\n\t"
  "v_cvt_pk_bf16_f32 v218, v88, v89\n\t"
  "v_mfma_f32_16x16x32_bf16 v[96:99], v[140:143], v[24:27], v[96:99]\n\t"
  "ds_read_b64_tr_b16 v[170:171], v197 offset:10240\n\t"
  "s_waitcnt lgkmcnt(6)\n\t"
  "v_mfma_f32_16x16x32_bf16 v[96:99], v[144:147], v[32:35], v[96:99]\n\t"
  "ds_read_b64_tr_b16 v[172:173], v197 offset:12288\n\t"
  "v_cvt_pk_bf16_f32 v219, v90, v91\n\t"
  "v_mfma_f32_16x16x32_bf16 v[100:103], v[144:147], v[36:39], v[100:103]\n\t"
  "ds_read_b64_tr_b16 v[174:175], v197 offset:14336\n\t"
  "v_cvt_pk_bf16_f32 v220, v84, v85\n\t"
  "v_mfma_f32_16x16x32_bf16 v[100:103], v[148:151], v[44:47], v[100:103]\n\t"
  "ds_read_b64_tr_b16 v[176:177], v197 offset:16384\n\t"
  "buffer_store_dwordx4 v[216:219], v247, s[20:23], s37 offen offset:512 sc1\n\t"
  "v_cvt_pk_bf16_f32 v221, v86, v87\n\t"
  "v_mfma_f32_16x16x32_bf16 v[96:99], v[148:151], v[40:43], v[96:99]\n\t"
  "ds_read_b64_tr_b16 v[178:179], v197 offset:18432\n\t"
  "s_add_u32 s37, s37, 1024\n\t"
  "v_mfma_f32_16x16x32_bf16 v[96:99], v[152:155], v[48:51], v[96:99]\n\t"
  "ds_read_b64_tr_b16 v[180:181], v197 offset:20480\n\t"
  "v_cvt_pk_bf16_f32 v222, v92, v93\n\t"
  "v_mfma_f32_16x16x32_bf16 v[100:103], v[152:155], v[52:55], v[100:103]\n\t"
  "ds_read_b64_tr_b16 v[182:183], v197 offset:22528\n\t"
  "v_cvt_pk_bf16_f32 v223, v94, v95\n\t"
  "v_mfma_f32_16x16x32_bf16 v[100:103], v[156:159], v[60:63], v[100:103]\n\t"
  "ds_read_b64_tr_b16 v[184:185], v197 offset:24576\n\t"
  "v_mfma_f32_16x16x32_bf16 v[96:99], v[156:159], v[56:59], v[96:99]\n\t"
  "ds_read_b64_tr_b16 v[186:187], v197 offset:26624\n\t"
  "s_waitcnt lgkmcnt(6)\n\t"
  "v_mfma_f32_16x16x32_bf16 v[104:107], v[160:163], v[0:3], 0\n\t"
  "ds_read_b64_tr_b16 v[188:189], v197 offset:28672\n\t"
  "buffer_store_dwordx4 v[220:223], v248, s[20:23], s38 offen offset:512 sc1\n\t"
  "v_mfma_f32_16x16x32_bf16 v[108:111], v[160:163], v[4:7], 0\n\t"
  "ds_read_b64_tr_b16 v[190:191], v197 offset:30720\n\t"
  "v_accvgpr_write_b32 a36, v216\n\t"
  "s_add_u32 s38, s38, 1024\n\t"
  "v_mfma_f32_16x16x32_bf16 v[108:111], v[164:167], v[12:15], v[108:111]\n\t"
  "ds_read_b64_tr_b16 v[128:129], v196 offset:512\n\t"
  "v_accvgpr_write_b32 a37, v217\n\t"
  "v_mfma_f32_16x16x32_bf16 v[104:107], v[164:167], v[8:11], v[104:107]\n\t"
  "ds_read_b64_tr_b16 v[130:131], v196 offset:2560\n\t"
  "v_mfma_f32_16x16x32_bf16 v[104:107], v[168:171], v[16:19], v[104:107]\n\t"
  "ds_read_b64_tr_b16 v[132:133], v196 offset:4608\n\t"
  "v_accvgpr_write_b32 a38, v218\n\t"
  "v_mfma_f32_16x16x32_bf16 v[108:111], v[168:171], v[20:23], v[108:111]\n\t"
  "ds_read_b64_tr_b16 v[134:135], v196 offset:6656\n\t"
  "v_accvgpr_write_b32 a39, v219\n\t"
  "v_mfma_f32_16x16x32_bf16 v[108:111], v[172:175], v[28:31], v[108:111]\n\t"
  "ds_read_b64_tr_b16 v[136:137], v196 offset:8704\n\t"
  "v_mfma_f32_16x16x32_bf16 v[104:107], v[172:175], v[24:27], v[104:107]\n\t"
  "ds_read_b64_tr_b16 v[138:139], v196 offset:10752\n\t"
  "s_waitcnt lgkmcnt(6)\n\t"
  "v_mfma_f32_16x16x32_bf16 v[104:107], v[176:179], v[32:35], v[104:107]\n\t"
  "ds_read_b64_tr_b16 v[140:141], v196 offset:12800\n\t"
  "v_mfma_f32_16x16x32_bf16 v[108:111], v[176:179], v[36:39], v[108:111]\n\t"
  "ds_read_b64_tr_b16 v[142:143], v196 offset:14848\n\t"
  "v_mfma_f32_16x16x32_bf16 v[108:111], v[180:183], v[44:47], v[108:111]\n\t"
  "ds_read_b64_tr_b16 v[144:145], v196 offset:16896\n\t"
  "v_mfma_f32_16x16x32_bf16 v[104:107], v[180:183], v[40:43], v[104:107]\n\t"
  "ds_read_b64_tr_b16 v[146:147], v196 offset:18944\n\t"
  "v_mfma_f32_16x16x32_bf16 v[104:107], v[184:187], v[48:51], v[104:107]\n\t"
  "ds_read_b64_tr_b16 v[148:149], v196 offset:20992\n\t"
  "v_mfma_f32_16x16x32_bf16 v[108:111], v[184:187], v[52:55], v[108:111]\n\t"
  "ds_read_b64_tr_b16 v[150:151], v196 offset:23040\n\t"
  "v_mfma_f32_16x16x32_bf16 v[108:111], v[188:191], v[60:63], v[108:111]\n\t"
  "ds_read_b64_tr_b16 v[152:153], v196 offset:25088\n\t"
  "s_barrier\n\t"
  "v_mfma_f32_16x16x32_bf16 v[104:107], v[188:191], v[56:59], v[104:107]\n\t"
  "ds_read_b64_tr_b16 v[154:155], v196 offset:27136\n\t"
  "s_waitcnt lgkmcnt(6)\n\t"
  "v_mfma_f32_16x16x32_bf16 v[112:115], v[128:131], v[0:3], 0\n\t"
  "ds_read_b64_tr_b16 v[156:157], v196 offset:29184\n\t"
  "v_mfma_f32_16x16x32_bf16 v[116:119], v[128:131], v[4:7], 0\n\t"
  "ds_read_b64_tr_b16 v[158:159], v196 offset:31232\n\t"
  "v_mfma_f32_16x16x32_bf16 v[116:119], v[132:135], v[12:15], v[116:119]\n\t"
  "ds_read_b64_tr_b16 v[160:161], v197 offset:512\n\t"
  "v_mfma_f32_16x16x32_bf16 v[112:115], v[132:135], v[8:11], v[112:115]\n\t"
  "ds_read_b64_tr_b16 v[162:163], v197 offset:2560\n\t"
  "v_mfma_f32_16x16x32_bf16 v[112:115], v[136:139], v[16:19], v[112:115]\n\t"
  "ds_read_b64_tr_b16 v[164:165], v197 offset:4608\n\t"
  "v_cvt_pk_bf16_f32 v208, v96, v97\n\t"
  "v_mfma_f32_16x16x32_bf16 v[116:119], v[136:139], v[20:23], v[116:119]\n\t"
  "ds_read_b64_tr_b16 v[166:167], v197 offset:6656\n\t"
  "v_cvt_pk_bf16_f32 v209, v98, v99\n\t"
  "v_mfma_f32_16x16x32_bf16 v[116:119], v[140:143], v[28:31], v[116:119]\n\t"
  "ds_read_b64_tr_b16 v[168:169], v197 offset:8704\n\t"
  "v_cvt_pk_bf16_f32 v210, v104, v105\n\t"
  "v_mfma_f32_16x16x32_bf16 v[112:115], v[140:143], v[24:27], v[112:115]\n\t"
  "ds_read_b64_tr_b16 v[170:171], v197 offset:10752\n\t"
  "s_waitcnt lgkmcnt(6)\n\t"
  "v_mfma_f32_16x16x32_bf16 v[112:115], v[144:147], v[32:35], v[112:115]\n\t"
  "ds_read_b64_tr_b16 v[172:173], v197 offset:12800\n\t"
  "v_cvt_pk_bf16_f32 v211, v106, v107\n\t"
  "v_mfma_f32_16x16x32_bf16 v[116:119], v[144:147], v[36:39], v[116:119]\n\t"
  "ds_read_b64_tr_b16 v[174:175], v197 offset:14848\n\t"
  "v_cvt_pk_bf16_f32 v212, v100, v101\n\t"
  "v_mfma_f32_16x16x32_bf16 v[116:119], v[148:151], v[44:47], v[116:119]\n\t"
  "ds_read_b64_tr_b16 v[176:177], v197 offset:16896\n\t"
  "buffer_store_dwordx4 v[208:211], v247, s[20:23], s37 offen sc1\n\t"
  "v_cvt_pk_bf16_f32 v213, v102, v103\n\t"
  "v_mfma_f32_16x16x32_bf16 v[112:115], v[148:151], v[40:43], v[112:115]\n\t"
  "ds_read_b64_tr_b16 v[178:179], v197 offset:18944\n\t"
  "v_mfma_f32_16x16x32_bf16 v[112:115], v[152:155], v[48:51], v[112:115]\n\t"
  "ds_read_b64_tr_b16 v[180:181], v197 offset:20992\n\t"
  "v_cvt_pk_bf16_f32 v214, v108, v109\n\t"
  "v_mfma_f32_16x16x32_bf16 v[116:119], v[152:155], v[52:55], v[116:119]\n\t"
  "ds_read_b64_tr_b16 v[182:183], v197 offset:23040\n\t"
  "v_cvt_pk_bf16_f32 v215, v110, v111\n\t"
  "v_mfma_f32_16x16x32_bf16 v[116:119], v[156:159], v[60:63], v[116:119]\n\t"
  "ds_read_b64_tr_b16 v[184:185], v197 offset:25088\n\t"
  "v_mfma_f32_16x16x32_bf16 v[112:115], v[156:159], v[56:59], v[112:115]\n\t"
  "ds_read_b64_tr_b16 v[186:187], v197 offset:27136\n\t"
  "s_waitcnt lgkmcnt(6)\n\t"
  "v_mfma_f32_16x16x32_bf16 v[120:123], v[160:163], v[0:3], 0\n\t"
  "ds_read_b64_tr_b16 v[188:189], v197 offset:29184\n\t"
  "buffer_store_dwordx4 v[212:215], v248, s[20:23], s38 offen sc1\n\t"
  "v_mfma_f32_16x16x32_bf16 v[124:127], v[160:163], v[4:7], 0\n\t"
  "ds_read_b64_tr_b16 v[190:191], v197 offset:31232\n\t"
  "v_accvgpr_write_b32 a40, v208\n\t"
  "v_mfma_f32_16x16x32_bf16 v[124:127], v[164:167], v[12:15], v[124:127]\n\t"
  "ds_read_b64_tr_b16 v[128:129], v192 offset:32768\n\t"
  "v_accvgpr_write_b32 a41, v209\n\t"
  "v_mfma_f32_16x16x32_bf16 v[120:123], v[164:167], v[8:11], v[120:123]\n\t"
  "ds_read_b64_tr_b16 v[130:131], v192 offset:34816\n\t"
  "v_mfma_f32_16x16x32_bf16 v[120:123], v[168:171], v[16:19], v[120:123]\n\t"
  "ds_read_b64_tr_b16 v[132:133], v192 offset:36864\n\t"
  "v_accvgpr_write_b32 a42, v210\n\t"
  "v_mfma_f32_16x16x32_bf16 v[124:127], v[168:171], v[20:23], v[124:127]\n\t"
  "ds_read_b64_tr_b16 v[134:135], v192 offset:38912\n\t"
  "v_accvgpr_write_b32 a43, v211\n\t"
  "v_mfma_f32_16x16x32_bf16 v[124:127], v[172:175], v[28:31], v[124:127]\n\t"
  "ds_read_b64_tr_b16 v[136:137], v192 offset:40960\n\t"
  "v_mfma_f32_16x16x32_bf16 v[120:123], v[172:175], v[24:27], v[120:123]\n\t"
  "ds_read_b64_tr_b16 v[138:139], v192 offset:43008\n\t"
  "s_waitcnt lgkmcnt(6)\n\t"
  "v_mfma_f32_16x16x32_bf16 v[120:123], v[176:179], v[32:35], v[120:123]\n\t"
  "ds_read_b64_tr_b16 v[140:141], v192 offset:45056\n\t"
  "v_mfma_f32_16x16x32_bf16 v[124:127], v[176:179], v[36:39], v[124:127]\n\t"
  "ds_read_b64_tr_b16 v[142:143], v192 offset:47104\n\t"
  "v_mfma_f32_16x16x32_bf16 v[124:127], v[180:183], v[44:47], v[124:127]\n\t"
  "ds_read_b64_tr_b16 v[144:145], v192 offset:49152\n\t"
  "v_mfma_f32_16x16x32_bf16 v[120:123], v[180:183], v[40:43], v[120:123]\n\t"
  "ds_read_b64_tr_b16 v[146:147], v192 offset:51200\n\t"
  "v_mfma_f32_16x16x32_bf16 v[120:123], v[184:187], v[48:51], v[120:123]\n\t"
  "ds_read_b64_tr_b16 v[148:149], v192 offset:53248\n\t"
  "v_mfma_f32_16x16x32_bf16 v[124:127], v[184:187], v[52:55], v[124:127]\n\t"
  "ds_read_b64_tr_b16 v[150:151], v192 offset:55296\n\t"
  "v_mfma_f32_16x16x32_bf16 v[124:127], v[188:191], v[60:63], v[124:127]\n\t"
  "ds_read_b64_tr_b16 v[152:153], v192 offset:57344\n\t"
  "v_mfma_f32_16x16x32_bf16 v[120:123], v[188:191], v[56:59], v[120:123]\n\t"
  "ds_read_b64_tr_b16 v[154:155], v192 offset:59392\n\t"
  "s_waitcnt lgkmcnt(6)\n\t"
  "v_mfma_f32_16x16x32_bf16 v[64:67], v[128:131], v[0:3], 0\n\t"
  "ds_read_b64_tr_b16 v[156:157], v192 offset:61440\n\t"
  "v_mfma_f32_16x16x32_bf16 v[68:71], v[128:131], v[4:7], 0\n\t"
  "ds_read_b64_tr_b16 v[158:159], v192 offset:63488\n\t"
  "v_mfma_f32_16x16x32_bf16 v[68:71], v[132:135], v[12:15], v[68:71]\n\t"
  "ds_read_b64_tr_b16 v[160:161], v193 offset:32768\n\t"
  "v_mfma_f32_16x16x32_bf16 v[64:67], v[132:135], v[8:11], v[64:67]\n\t"
  "ds_read_b64_tr_b16 v[162:163], v193 offset:34816\n\t"
  "v_mfma_f32_16x16x32_bf16 v[64:67], v[136:139], v[16:19], v[64:67]\n\t"
  "ds_read_b64_tr_b16 v[164:165], v193 offset:36864\n\t"
  "v_cvt_pk_bf16_f32 v216, v112, v113\n\t"
  "v_mfma_f32_16x16x32_bf16 v[68:71], v[136:139], v[20:23], v[68:71]\n\t"
  "ds_read_b64_tr_b16 v[166:167], v193 offset:38912\n\t"
  "v_cvt_pk_bf16_f32 v217, v114, v115\n\t"
  "v_mfma_f32_16x16x32_bf16 v[68:71], v[140:143], v[28:31], v[68:71]\n\t"
  "ds_read_b64_tr_b16 v[168:169], v193 offset:40960\n\t"
  "v_cvt_pk_bf16_f32 v218, v120, v121\n\t"
  "v_mfma_f32_16x16x32_bf16 v[64:67], v[140:143], v[24:27], v[64:67]\n\t"
  "ds_read_b64_tr_b16 v[170:171], v193 offset:43008\n\t"
  "s_waitcnt lgkmcnt(6)\n\t"
  "v_mfma_f32_16x16x32_bf16 v[64:67], v[144:147], v[32:35], v[64:67]\n\t"
  "ds_read_b64_tr_b16 v[172:173], v193 offset:45056\n\t"
  "v_cvt_pk_bf16_f32 v219, v122, v123\n\t"
  "v_mfma_f32_16x16x32_bf16 v[68:71], v[144:147], v[36:39], v[68:71]\n\t"
  "ds_read_b64_tr_b16 v[174:175], v193 offset:47104\n\t"
  "v_cvt_pk_bf16_f32 v220, v116, v117\n\t"
  "v_mfma_f32_16x16x32_bf16 v[68:71], v[148:151], v[44:47], v[68:71]\n\t"
  "ds_read_b64_tr_b16 v[176:177], v193 offset:49152\n\t"
  "buffer_store_dwordx4 v[216:219], v247, s[20:23], s37 offen offset:512 sc1\n\t"
  "v_cvt_pk_bf16_f32 v221, v118, v119\n\t"
  "v_mfma_f32_16x16x32_bf16 v[64:67], v[148:151], v[40:43], v[64:67]\n\t"
  "ds_read_b64_tr_b16 v[178:179], v193 offset:51200\n\t"
  "s_add_u32 s37, s37, 1024\n\t"
  "v_mfma_f32_16x16x32_bf16 v[64:67], v[152:155], v[48:51], v[64:67]\n\t"
  "ds_read_b64_tr_b16 v[180:181], v193 offset:53248\n\t"
  "v_cvt_pk_bf16_f32 v222, v124, v125\n\t"
  "v_mfma_f32_16x16x32_bf16 v[68:71], v[152:155], v[52:55], v[68:71]\n\t"
  "ds_read_b64_tr_b16 v[182:183], v193 offset:55296\n\t"
  "v_cvt_pk_bf16_f32 v223, v126, v127\n\t"
  "v_mfma_f32_16x16x32_bf16 v[68:71], v[156:159], v[60:63], v[68:71]\n\t"
  "ds_read_b64_tr_b16 v[184:185], v193 offset:57344\n\t"
  "v_mfma_f32_16x16x32_bf16 v[64:67], v[156:159], v[56:59], v[64:67]\n\t"
  "ds_read_b64_tr_b16 v[186:187], v193 offset:59392\n\t"
  "s_waitcnt lgkmcnt(6)\n\t"
  "v_mfma_f32_16x16x32_bf16 v[72:75], v[160:163], v[0:3], 0\n\t"
  "ds_read_b64_tr_b16 v[188:189], v193 offset:61440\n\t"
  "buffer_store_dwordx4 v[220:223], v248, s[20:23], s38 offen offset:512 sc1\n\t"
  "v_mfma_f32_16x16x32_bf16 v[76:79], v[160:163], v[4:7], 0\n\t"
  "ds_read_b64_tr_b16 v[190:191], v193 offset:63488\n\t"
  "v_accvgpr_write_b32 a44, v216\n\t"
  "s_add_u32 s38, s38, 1024\n\t"
  "v_mfma_f32_16x16x32_bf16 v[76:79], v[164:167], v[12:15], v[76:79]\n\t"
  "ds_read_b64_tr_b16 v[128:129], v192 offset:33280\n\t"
  "v_accvgpr_write_b32 a45, v217\n\t"
  "v_mfma_f32_16x16x32_bf16 v[72:75], v[164:167], v[8:11], v[72:75]\n\t"
  "ds_read_b64_tr_b16 v[130:131], v192 offset:35328\n\t"
  "v_mfma_f32_16x16x32_bf16 v[72:75], v[168:171], v[16:19], v[72:75]\n\t"
  "ds_read_b64_tr_b16 v[132:133], v192 offset:37376\n\t"
  "v_accvgpr_write_b32 a46, v218\n\t"
  "v_mfma_f32_16x16x32_bf16 v[76:79], v[168:171], v[20:23], v[76:79]\n\t"
  "ds_read_b64_tr_b16 v[134:135], v192 offset:39424\n\t"
  "v_accvgpr_write_b32 a47, v219\n\t"
  "v_mfma_f32_16x16x32_bf16 v[76:79], v[172:175], v[28:31], v[76:79]\n\t"
  "ds_read_b64_tr_b16 v[136:137], v192 offset:41472\n\t"
  "v_mfma_f32_16x16x32_bf16 v[72:75], v[172:175], v[24:27], v[72:75]\n\t"
  "ds_read_b64_tr_b16 v[138:139], v192 offset:43520\n\t"
  "s_waitcnt lgkmcnt(6)\n\t"
  "v_mfma_f32_16x16x32_bf16 v[72:75], v[176:179], v[32:35], v[72:75]\n\t"
  "ds_read_b64_tr_b16 v[140:141], v192 offset:45568\n\t"
  "v_mfma_f32_16x16x32_bf16 v[76:79], v[176:179], v[36:39], v[76:79]\n\t"
  "ds_read_b64_tr_b16 v[142:143], v192 offset:47616\n\t"
  "v_mfma_f32_16x16x32_bf16 v[76:79], v[180:183], v[44:47], v[76:79]\n\t"
  "ds_read_b64_tr_b16 v[144:145], v192 offset:49664\n\t"
  "v_mfma_f32_16x16x32_bf16 v[72:75], v[180:183], v[40:43], v[72:75]\n\t"
  "ds_read_b64_tr_b16 v[146:147], v192 offset:51712\n\t"
  "v_mfma_f32_16x16x32_bf16 v[72:75], v[184:187], v[48:51], v[72:75]\n\t"
  "ds_read_b64_tr_b16 v[148:149], v192 offset:53760\n\t"
  "v_mfma_f32_16x16x32_bf16 v[76:79], v[184:187], v[52:55], v[76:79]\n\t"
  "ds_read_b64_tr_b16 v[150:151], v192 offset:55808\n\t"
  "v_mfma_f32_16x16x32_bf16 v[76:79], v[188:191], v[60:63], v[76:79]\n\t"
  "ds_read_b64_tr_b16 v[152:153], v192 offset:57856\n\t"
  "v_mfma_f32_16x16x32_bf16 v[72:75], v[188:191], v[56:59], v[72:75]\n\t"
  "ds_read_b64_tr_b16 v[154:155], v192 offset:59904\n\t"
  "s_waitcnt lgkmcnt(6)\n\t"
  "v_mfma_f32_16x16x32_bf16 v[80:83], v[128:131], v[0:3], 0\n\t"
  "ds_read_b64_tr_b16 v[156:157], v192 offset:61952\n\t"
  "v_mfma_f32_16x16x32_bf16 v[84:87], v[128:131], v[4:7], 0\n\t"
  "ds_read_b64_tr_b16 v[158:159], v192 offset:64000\n\t"
  "v_mfma_f32_16x16x32_bf16 v[84:87], v[132:135], v[12:15], v[84:87]\n\t"
  "ds_read_b64_tr_b16 v[160:161], v193 offset:33280\n\t"
  "v_mfma_f32_16x16x32_bf16 v[80:83], v[132:135], v[8:11], v[80:83]\n\t"
  "ds_read_b64_tr_b16 v[162:163], v193 offset:35328\n\t"
  "v_mfma_f32_16x16x32_bf16 v[80:83], v[136:139], v[16:19], v[80:83]\n\t"
  "ds_read_b64_tr_b16 v[164:165], v193 offset:37376\n\t"
  "v_cvt_pk_bf16_f32 v208, v64, v65\n\t"
  "v_mfma_f32_16x16x32_bf16 v[84:87], v[136:139], v[20:23], v[84:87]\n\t"
  "ds_read_b64_tr_b16 v[166:167], v193 offset:39424\n\t"
  "v_cvt_pk_bf16_f32 v209, v66, v67\n\t"
  "v_mfma_f32_16x16x32_bf16 v[84:87], v[140:143], v[28:31], v[84:87]\n\t"
  "ds_read_b64_tr_b16 v[168:169], v193 offset:41472\n\t"
  "v_cvt_pk_bf16_f32 v210, v72, v73\n\t"
  "v_mfma_f32_16x16x32_bf16 v[80:83], v[140:143], v[24:27], v[80:83]\n\t"
  "ds_read_b64_tr_b16 v[170:171], v193 offset:43520\n\t"
  "s_waitcnt lgkmcnt(6)\n\t"
  "v_mfma_f32_16x16x32_bf16 v[80:83], v[144:147], v[32:35], v[80:83]\n\t"
  "ds_read_b64_tr_b16 v[172:173], v193 offset:45568\n\t"
  "v_cvt_pk_bf16_f32 v211, v74, v75\n\t"
  "v_mfma_f32_16x16x32_bf16 v[84:87], v[144:147], v[36:39], v[84:87]\n\t"
  "ds_read_b64_tr_b16 v[174:175], v193 offset:47616\n\t"
  "v_cvt_pk_bf16_f32 v212, v68, v69\n\t"
  "v_mfma_f32_16x16x32_bf16 v[84:87], v[148:151], v[44:47], v[84:87]\n\t"
  "ds_read_b64_tr_b16 v[176:177], v193 offset:49664\n\t"
  "buffer_store_dwordx4 v[208:211], v247, s[20:23], s37 offen sc1\n\t"
  "v_cvt_pk_bf16_f32 v213, v70, v71\n\t"
  "v_mfma_f32_16x16x32_bf16 v[80:83], v[148:151], v[40:43], v[80:83]\n\t"
  "ds_read_b64_tr_b16 v[178:179], v193 offset:51712\n\t"
  "v_mfma_f32_16x16x32_bf16 v[80:83], v[152:155], v[48:51], v[80:83]\n\t"
  "ds_read_b64_tr_b16 v[180:181], v193 offset:53760\n\t"
  "v_cvt_pk_bf16_f32 v214, v76, v77\n\t"
  "v_mfma_f32_16x16x32_bf16 v[84:87], v[152:155], v[52:55], v[84:87]\n\t"
  "ds_read_b64_tr_b16 v[182:183], v193 offset:55808\n\t"
  "v_cvt_pk_bf16_f32 v215, v78, v79\n\t"
  "v_mfma_f32_16x16x32_bf16 v[84:87], v[156:159], v[60:63], v[84:87]\n\t"
  "ds_read_b64_tr_b16 v[184:185], v193 offset:57856\n\t"
  "v_mfma_f32_16x16x32_bf16 v[80:83], v[156:159], v[56:59], v[80:83]\n\t"
  "ds_read_b64_tr_b16 v[186:187], v193 offset:59904\n\t"
  "s_waitcnt lgkmcnt(6)\n\t"
  "v_mfma_f32_16x16x32_bf16 v[88:91], v[160:163], v[0:3], 0\n\t"
  "ds_read_b64_tr_b16 v[188:189], v193 offset:61952\n\t"
  "buffer_store_dwordx4 v[212:215], v248, s[20:23], s38 offen sc1\n\t"
  "v_mfma_f32_16x16x32_bf16 v[92:95], v[160:163], v[4:7], 0\n\t"
  "ds_read_b64_tr_b16 v[190:191], v193 offset:64000\n\t"
  "v_accvgpr_write_b32 a48, v208\n\t"
  "v_mfma_f32_16x16x32_bf16 v[92:95], v[164:167], v[12:15], v[92:95]\n\t"
  "v_accvgpr_write_b32 a49, v209\n\t"
  "v_mfma_f32_16x16x32_bf16 v[88:91], v[164:167], v[8:11], v[88:91]\n\t"
  "v_mfma_f32_16x16x32_bf16 v[88:91], v[168:171], v[16:19], v[88:91]\n\t"
  "v_accvgpr_write_b32 a50, v210\n\t"
  "v_mfma_f32_16x16x32_bf16 v[92:95], v[168:171], v[20:23], v[92:95]\n\t"
  "v_accvgpr_write_b32 a51, v211\n\t"
  "v_mfma_f32_16x16x32_bf16 v[92:95], v[172:175], v[28:31], v[92:95]\n\t"
  "v_mfma_f32_16x16x32_bf16 v[88:91], v[172:175], v[24:27], v[88:91]\n\t"
  "s_waitcnt lgkmcnt(0)\n\t"
  "v_mfma_f32_16x16x32_bf16 v[88:91], v[176:179], v[32:35], v[88:91]\n\t"
  "v_mfma_f32_16x16x32_bf16 v[92:95], v[176:179], v[36:39], v[92:95]\n\t"
  "v_mfma_f32_16x16x32_bf16 v[92:95], v[180:183], v[44:47], v[92:95]\n\t"
  "v_mfma_f32_16x16x32_bf16 v[88:91], v[180:183], v[40:43], v[88:91]\n\t"
  "v_mfma_f32_16x16x32_bf16 v[88:91], v[184:187], v[48:51], v[88:91]\n\t"
  "v_mfma_f32_16x16x32_bf16 v[92:95], v[184:187], v[52:55], v[92:95]\n\t"
  "v_mfma_f32_16x16x32_bf16 v[92:95], v[188:191], v[60:63], v[92:95]\n\t"
  "v_mfma_f32_16x16x32_bf16 v[88:91], v[188:191], v[56:59], v[88:91]\n\t"
  "s_nop 7\n\t"
  "v_cvt_pk_bf16_f32 v216, v80, v81\n\t"
  "v_cvt_pk_bf16_f32 v217, v82, v83\n\t"
  "v_cvt_pk_bf16_f32 v218, v88, v89\n\t"
  "v_cvt_pk_bf16_f32 v219, v90, v91\n\t"
  "buffer_store_dwordx4 v[216:219], v247, s[20:23], s37 offen offset:512 sc1\n\t"
  "s_add_u32 s37, s37, 1024\n\t"
  "v_cvt_pk_bf16_f32 v220, v84, v85\n\t"
  "v_cvt_pk_bf16_f32 v221, v86, v87\n\t"
  "v_cvt_pk_bf16_f32 v222, v92, v93\n\t"
  "v_cvt_pk_bf16_f32 v223, v94, v95\n\t"
  "buffer_store_dwordx4 v[220:223], v248, s[20:23], s38 offen offset:512 sc1\n\t"
  "s_add_u32 s38, s38, 1024\n\t"
  "v_accvgpr_write_b32 a52, v216\n\t"
  "v_accvgpr_write_b32 a53, v217\n\t"
  "v_accvgpr_write_b32 a54, v218\n\t"
  "v_accvgpr_write_b32 a55, v219\n\t"
  "s_mul_i32 s39, s62, 7168\n\t"
  "s_lshr_b32 s66, s62, 1\n\t"
  "s_and_b32 s67, s62, 1\n\t"
  "v_bfe_u32 v239, v254, 2, 3\n\t"
  "v_and_b32_e32 v240, 3, v254\n\t"
  "v_lshrrev_b32_e32 v241, 5, v254\n\t"
  "v_bfe_u32 v242, v254, 4, 1\n\t"
  "v_lshlrev_b32_e32 v242, 1, v242\n\t"
  "v_xor_b32_e32 v240, v240, v242\n\t"
  "v_lshlrev_b32_e32 v240, 4, v240\n\t"
  "v_mul_u32_u24_e32 v239, 3584, v239\n\t"
  "v_add_u32_e32 v239, v239, v240\n\t"
  "v_mul_u32_u24_e32 v243, 64, v241\n\t"
  "v_add_u32_e32 v243, v243, v239\n\t"
  "v_add_u32_e32 v225, 3072, v243\n\t"
  "v_mul_u32_u24_e32 v243, 64, v241\n\t"
  "v_add_u32_e32 v243, v243, v239\n\t"
  "v_add_u32_e32 v226, 2176, v243\n\t"
  "v_mul_u32_u24_e32 v243, 64, v241\n\t"
  "v_add_u32_e32 v243, v243, v239\n\t"
  "v_add_u32_e32 v227, 1280, v243\n\t"
  "v_mul_u32_u24_e32 v243, 28288, v241\n\t"
  "v_add_u32_e32 v243, v243, v239\n\t"
  "v_add_u32_e32 v228, 384, v243\n\t"
  "v_mul_u32_u24_e32 v243, 64, v241\n\t"
  "v_add_u32_e32 v243, v243, v239\n\t"
  "v_add_u32_e32 v229, 31808, v243\n\t"
  "v_mul_u32_u24_e32 v243, 64, v241\n\t"
  "v_add_u32_e32 v243, v243, v239\n\t"
  "v_add_u32_e32 v230, 30912, v243\n\t"
  "v_mul_u32_u24_e32 v243, 64, v241\n\t"
  "v_add_u32_e32 v243, v243, v239\n\t"
  "v_add_u32_e32 v231, 30016, v243\n\t"
  "s_mul_i32 s64, s57, 448\n\t"
  "s_add_u32 s8, s50, s64\n\t"
  "s_addc_u32 s9, s51, 0\n\t"
  "s_sub_u32 s8, s8, 3072\n\t"
  "s_subb_u32 s9, s9, 0\n\t"
  "s_and_b32 s9, s9, 0xffff\n\t"
  "s_mov_b32 s10, 1605568\n\t"
  "s_mov_b32 s11, 0x20000\n\t"
  "s_mul_i32 s28, s62, 57344\n\t"
  "s_lshl_b32 s37, s58, 3\n\t"
  "s_add_u32 s37, s37, s57\n\t"
  "s_add_u32 s38, s62, 3\n\t"
  "s_cmp_eq_u32 s62, 3\n\t"
  "s_cselect_b32 s68, 6, s62\n\t"
  "s_min_u32 s38, s38, 6\n\t"
  "v_and_b32_e32 v216, 7, v254\n\t"
  "v_bfe_u32 v217, v254, 3, 2\n\t"
  "v_lshrrev_b32_e32 v218, 5, v254\n\t"
  "v_lshlrev_b32_e32 v219, 4, v217\n\t"
  "v_lshl_add_u32 v219, v218, 7, v219\n\t"
  "v_lshrrev_b32_e32 v220, 1, v219\n\t"
  "v_mul_u32_u24_e32 v221, 896, v216\n\t"
  "v_add_u32_e32 v220, v220, v221\n\t"
  "v_lshlrev_b32_e32 v251, 4, v254\n\t"
  "v_lshlrev_b32_e32 v221, 6, v216\n\t"
  "v_lshlrev_b32_e32 v222, 8, v217\n\t"
  "v_lshl_add_u32 v222, v218, 10, v222\n\t"
  "v_lshl_add_u32 v222, v216, 4, v222\n\t"
  "s_mov_b32 s31, 0xffff0000\n\t"
  "s_add_u32 s20, s98, 16777216\n\t"
  "s_addc_u32 s21, s99, 0\n\t"
  "s_and_b32 s21, s21, 0xffff\n\t"
  "s_mov_b32 s22, 29360128\n\t"
  "s_mov_b32 s23, 0x20000\n\t"
  "s_mul_i32 s60, s37, 7168\n\t"
  "s_lshl_b32 s65, s38, 10\n\t"
  "s_add_u32 s61, s60, s65\n\t"
  "s_lshl_b32 s65, s68, 10\n\t"
  "s_add_u32 s60, s60, s65\n\t"
  "s_add_u32 s12, s90, 0\n\t"
  "s_addc_u32 s13, s91, 0\n\t"
  "s_and_b32 s13, s13, 0xffff\n\t"
  "s_mov_b32 s14, 1835008\n\t"
  "s_mov_b32 s15, 0x20000\n\t"
  "s_add_u32 s16, s98, 0\n\t"
  "s_addc_u32 s17, s99, 0\n\t"
  "s_and_b32 s17, s17, 0xffff\n\t"
  "s_mov_b32 s18, 10752\n\t"
  "s_mov_b32 s19, 0x20000\n\t"
  "s_mul_i32 s92, s37, 7168\n\t"
  "s_lshl_b32 s65, s38, 7\n\t"
  "s_add_u32 s93, s92, s65\n\t"
  "s_lshl_b32 s65, s68, 7\n\t"
  "s_add_u32 s92, s92, s65\n\t"
  "s_lshl_b32 s96, s68, 8\n\t"
  "s_lshr_b32 s64, s56, 2\n\t"
  "s_cmp_eq_u32 s64, s62\n\t"
  "s_cselect_b32 s46, 0x00ff00ff, 0\n\t"
  "s_and_b32 s65, s56, 1\n\t"
  "s_lshl_b32 s65, s65, 3\n\t"
  "s_lshl_b32 s46, s46, s65\n\t"
  "s_mov_b32 s47, s46\n\t"
  "v_lshrrev_b32_e32 v223, 4, v254\n\t"
  "v_lshlrev_b32_e32 v223, 7, v223\n\t"
  "v_and_b32_e32 v224, 7, v254\n\t"
  "v_lshl_add_u32 v223, v224, 4, v223\n\t"
  "v_add_u32_e32 v223, 94208, v223\n\t"
  "s_mov_b64 exec, s[46:47]\n\t"
  "s_cbranch_execz .Lkw14\n\t"
  "ds_write_b128 v223, a[0:3]\n\t"
  "ds_write_b128 v223, a[4:7] offset:512\n\t"
  "ds_write_b128 v223, a[8:11] offset:1024\n\t"
  "ds_write_b128 v223, a[12:15] offset:1536\n\t"
  "ds_write_b128 v223, a[16:19] offset:2048\n\t"
  "ds_write_b128 v223, a[20:23] offset:2560\n\t"
  "ds_write_b128 v223, a[24:27] offset:3072\n\t"
  "ds_write_b128 v223, a[28:31] offset:3584\n\t"
  "ds_write_b128 v223, a[32:35] offset:32768\n\t"
  "ds_write_b128 v223, a[36:39] offset:33280\n\t"
  "ds_write_b128 v223, a[40:43] offset:33792\n\t"
  "ds_write_b128 v223, a[44:47] offset:34304\n\t"
  "ds_write_b128 v223, a[48:51] offset:34816\n\t"
  "ds_write_b128 v223, a[52:55] offset:35328\n\t"
  ".Lkw14:\n\t"
  "s_mov_b64 exec, -1\n\t"
  "s_waitcnt lgkmcnt(0)\n\t"
  "s_cmp_eq_u32 s62, 3\n\t"
  "s_cbranch_scc1 .LqR\n\t"
  "buffer_load_dwordx2 v[144:145], v220, s[12:15], s92 offen\n\t"
  "buffer_load_dwordx2 v[146:147], v220, s[12:15], s92 offen offset:32\n\t"
  "buffer_load_dwordx2 v[148:149], v220, s[12:15], s93 offen\n\t"
  "buffer_load_dwordx2 v[150:151], v220, s[12:15], s93 offen offset:32\n\t"
  "s_add_u32 m0, s39, 0\n\t"
  "s_nop 0\n\t"
  "buffer_load_dwordx4 v225, s[8:11], s28 offen lds\n\t"
  "buffer_load_dwordx4 v226, s[8:11], s28 offen offset:1024 lds\n\t"
  "buffer_load_dwordx4 v227, s[8:11], s28 offen offset:2048 lds\n\t"
  "buffer_load_dwordx4 v228, s[8:11], s28 offen offset:3072 lds\n\t"
  "s_add_u32 m0, s39, 4096\n\t"
  "s_nop 0\n\t"
  "buffer_load_dwordx4 v229, s[8:11], s28 offen lds\n\t"
  "buffer_load_dwordx4 v230, s[8:11], s28 offen offset:1024 lds\n\t"
  "buffer_load_dwordx4 v231, s[8:11], s28 offen offset:2048 lds\n\t"
  "s_add_u32 s28, s28, 229376\n\t"
  "s_add_u32 m0, s39, 65536\n\t"
  "s_nop 0\n\t"
  "buffer_load_dwordx4 v225, s[8:11], s28 offen lds\n\t"
  "buffer_load_dwordx4 v226, s[8:11], s28 offen offset:1024 lds\n\t"
  "buffer_load_dwordx4 v227, s[8:11], s28 offen offset:2048 lds\n\t"
  "buffer_load_dwordx4 v228, s[8:11], s28 offen offset:3072 lds\n\t"
  "s_add_u32 m0, s39, 69632\n\t"
  "s_nop 0\n\t"
  "buffer_load_dwordx4 v229, s[8:11], s28 offen lds\n\t"
  "buffer_load_dwordx4 v230, s[8:11], s28 offen offset:1024 lds\n\t"
  "buffer_load_dwordx4 v231, s[8:11], s28 offen offset:2048 lds\n\t"
  "s_add_u32 s28, s28, 229376\n\t"
  "s_add_u32 m0, s39, 98304\n\t"
  "s_nop 0\n\t"
  "buffer_load_dwordx4 v225, s[8:11], s28 offen lds\n\t"
  "buffer_load_dwordx4 v226, s[8:11], s28 offen offset:1024 lds\n\t"
  "buffer_load_dwordx4 v227, s[8:11], s28 offen offset:2048 lds\n\t"
  "buffer_load_dwordx4 v228, s[8:11], s28 offen offset:3072 lds\n\t"
  "s_add_u32 m0, s39, 102400\n\t"
  "s_nop 0\n\t"
  "buffer_load_dwordx4 v229, s[8:11], s28 offen lds\n\t"
  "buffer_load_dwordx4 v230, s[8:11], s28 offen offset:1024 lds\n\t"
  "buffer_load_dwordx4 v231, s[8:11], s28 offen offset:2048 lds\n\t"
  "s_add_u32 s28, s28, 229376\n\t"
  "s_lshl_b32 s97, s38, 8\n\t"
  "v_bfe_u32 v216, v254, 2, 3\n\t"
  "v_and_b32_e32 v217, 3, v254\n\t"
  "v_bfe_u32 v223, v254, 4, 1\n\t"
  "v_lshlrev_b32_e32 v223, 1, v223\n\t"
  "v_xor_b32_e32 v217, v217, v223\n\t"
  "v_lshlrev_b32_e32 v217, 4, v217\n\t"
  "v_mul_u32_u24_e32 v224, 896, v216\n\t"
  "v_add_u32_e32 v224, v224, v217\n\t"
  "v_lshl_add_u32 v224, v218, 6, v224\n\t"
  "v_add_u32_e32 v232, 0, v224\n\t"
  "v_add_u32_e32 v233, 6144, v224\n\t"
  "v_add_u32_e32 v234, 12288, v224\n\t"
  "v_add_u32_e32 v235, 18432, v224\n\t"
  "v_bfe_u32 v217, v254, 1, 1\n\t"
  "v_xor_b32_e32 v217, v217, v223\n\t"
  "v_lshlrev_b32_e32 v217, 4, v217\n\t"
  "v_and_b32_e32 v223, 1, v254\n\t"
  "v_lshlrev_b32_e32 v223, 3, v223\n\t"
  "v_lshl_add_u32 v223, v216, 6, v223\n\t"
  "v_add_u32_e32 v223, v223, v217\n\t"
  "v_mul_u32_u24_e32 v224, 3584, v218\n\t"
  "v_add_u32_e32 v224, v224, v223\n\t"
  "v_lshl_add_u32 v223, v218, 10, v223\n\t"
  "s_lshl_b32 s64, s67, 5\n\t"
  "v_xor_b32_e32 v224, s64, v224\n\t"
  "s_mul_i32 s64, s67, 1536\n\t"
  "s_lshl_b32 s65, s67, 11\n\t"
  "v_add_u32_e32 v236, s64, v224\n\t"
  "v_xor_b32_e32 v224, 32, v224\n\t"
  "v_add_u32_e32 v237, s65, v224\n\t"
  "s_lshl_b32 s64, s67, 9\n\t"
  "v_add_u32_e32 v242, s64, v223\n\t"
  "v_xor_b32_e32 v243, 32, v242\n\t"
  "v_add_u32_e32 v238, 65536, v236\n\t"
  "v_add_u32_e32 v244, 65536, v242\n\t"
  "v_add_u32_e32 v239, 65536, v237\n\t"
  "v_add_u32_e32 v245, 65536, v243\n\t"
  "v_add_u32_e32 v240, 131072, v236\n\t"
  "v_add_u32_e32 v246, 131072, v242\n\t"
  "v_add_u32_e32 v241, 131072, v237\n\t"
  "v_add_u32_e32 v247, 131072, v243\n\t"
  "s_mul_i32 s64, s66, 14336\n\t"
  "s_add_u32 s64, s64, 0\n\t"
  "v_add_u32_e32 v250, s64, v251\n\t"
  "s_mul_i32 s64, s67, 6152\n\t"
  "s_mul_i32 s65, s67, 8184\n\t"
  "s_add_u32 s65, s65, 8\n\t"
  "v_add_u32_e32 v248, s64, v250\n\t"
  "v_add_u32_e32 v249, s65, v250\n\t"
  "v_bfe_u32 v223, v254, 3, 1\n\t"
  "v_mul_u32_u24_e32 v223, 7168, v223\n\t"
  "v_and_b32_e32 v224, 7, v254\n\t"
  "v_lshl_add_u32 v223, v224, 4, v223\n\t"
  "v_lshrrev_b32_e32 v224, 4, v254\n\t"
  "v_lshl_add_u32 v223, v224, 7, v223\n\t"
  "s_lshl_b32 s64, s66, 2\n\t"
  "s_xor_b32 s64, s64, s57\n\t"
  "s_andn2_b32 s64, s64, 2\n\t"
  "v_bfe_u32 v224, v254, 3, 1\n\t"
  "v_xor_b32_e32 v224, s64, v224\n\t"
  "v_mul_u32_u24_e32 v252, 1835008, v224\n\t"
  "v_add_u32_e32 v223, v223, v252\n\t"
  "v_cmp_eq_u32_e32 vcc, 0, v224\n\t"
  "v_mov_b32_e32 v224, 0x7f000000\n\t"
  "v_add_u32_e32 v253, 3670016, v223\n\t"
  "v_cndmask_b32_e32 v252, v223, v224, vcc\n\t"
  "s_mul_i32 s64, s57, 200704\n\t"
  "s_add_u32 s12, s52, s64\n\t"
  "s_addc_u32 s13, s53, 0\n\t"
  "s_and_b32 s13, s13, 0xffff\n\t"
  "s_mov_b32 s14, 200704\n\t"
  "s_mov_b32 s15, 0x20000\n\t"
  "s_mul_i32 s29, s62, 50176\n\t"
  "s_waitcnt vmcnt(21)\n\t"
  "s_barrier\n\t"
  "s_add_u32 s64, s96, 3584\n\t"
  "s_add_u32 s65, s97, 3584\n\t"
  "buffer_load_dwordx4 v[152:155], v219, s[16:19], s64 offen sc1\n\t"
  "buffer_load_dwordx4 v[156:159], v219, s[16:19], s64 offen offset:64 sc1\n\t"
  "buffer_load_dwordx4 v[160:163], v219, s[16:19], s65 offen sc1\n\t"
  "buffer_load_dwordx4 v[164:167], v219, s[16:19], s65 offen offset:64 sc1\n\t"
  "s_add_u32 s64, s96, 5376\n\t"
  "s_add_u32 s65, s97, 5376\n\t"
  "buffer_load_dwordx4 v[168:171], v219, s[16:19], s64 offen sc1\n\t"
  "buffer_load_dwordx4 v[172:175], v219, s[16:19], s64 offen offset:64 sc1\n\t"
  "buffer_load_dwordx4 v[176:179], v219, s[16:19], s65 offen sc1\n\t"
  "buffer_load_dwordx4 v[180:183], v219, s[16:19], s65 offen offset:64 sc1\n\t"
  "s_add_u32 s64, s96, 7168\n\t"
  "s_add_u32 s65, s97, 7168\n\t"
  "buffer_load_dwordx4 v[184:187], v219, s[16:19], s64 offen sc1\n\t"
  "buffer_load_dwordx4 v[188:191], v219, s[16:19], s64 offen offset:64 sc1\n\t"
  "buffer_load_dwordx4 v[192:195], v219, s[16:19], s65 offen sc1\n\t"
  "buffer_load_dwordx4 v[196:199], v219, s[16:19], s65 offen offset:64 sc1\n\t"
  "s_add_u32 s64, s96, 8960\n\t"
  "s_add_u32 s65, s97, 8960\n\t"
  "buffer_load_dwordx4 a[112:115], v219, s[16:19], s64 offen sc1\n\t"
  "buffer_load_dwordx4 a[116:119], v219, s[16:19], s64 offen offset:64 sc1\n\t"
  "buffer_load_dwordx4 a[120:123], v219, s[16:19], s65 offen sc1\n\t"
  "buffer_load_dwordx4 a[124:127], v219, s[16:19], s65 offen offset:64 sc1\n\t"
  "s_add_u32 m0, s39, 131072\n\t"
  "s_nop 0\n\t"
  "buffer_load_dwordx4 v225, s[8:11], s28 offen lds\n\t"
  "buffer_load_dwordx4 v226, s[8:11], s28 offen offset:1024 lds\n\t"
  "buffer_load_dwordx4 v227, s[8:11], s28 offen offset:2048 lds\n\t"
  "buffer_load_dwordx4 v228, s[8:11], s28 offen offset:3072 lds\n\t"
  "s_add_u32 m0, s39, 135168\n\t"
  "s_nop 0\n\t"
  "buffer_load_dwordx4 v229, s[8:11], s28 offen lds\n\t"
  "buffer_load_dwordx4 v230, s[8:11], s28 offen offset:1024 lds\n\t"
  "buffer_load_dwordx4 v231, s[8:11], s28 offen offset:2048 lds\n\t"
  "s_add_u32 s28, s28, 229376\n\t"
  "s_add_u32 m0, s39, 32768\n\t"
  "s_nop 0\n\t"
  "buffer_load_dwordx4 v225, s[8:11], s28 offen lds\n\t"
  "buffer_load_dwordx4 v226, s[8:11], s28 offen offset:1024 lds\n\t"
  "buffer_load_dwordx4 v227, s[8:11], s28 offen offset:2048 lds\n\t"
  "buffer_load_dwordx4 v228, s[8:11], s28 offen offset:3072 lds\n\t"
  "s_add_u32 m0, s39, 36864\n\t"
  "s_nop 0\n\t"
  "buffer_load_dwordx4 v229, s[8:11], s28 offen lds\n\t"
  "buffer_load_dwordx4 v230, s[8:11], s28 offen offset:1024 lds\n\t"
  "buffer_load_dwordx4 v231, s[8:11], s28 offen offset:2048 lds\n\t"
  "s_add_u32 s28, s28, 229376\n\t"
  "v_mov_b32_e32 v224, 0xbfb8aa3b\n\t"
  "s_waitcnt vmcnt(14)\n\t"
  "v_pk_add_f32 v[184:185], v[184:185], 1.0 op_sel_hi:[1,0]\n\t"
  "v_pk_mul_f32 v[168:169], v[168:169], v[224:225] op_sel_hi:[1,0]\n\t"
  "v_pk_add_f32 v[186:187], v[186:187], 1.0 op_sel_hi:[1,0]\n\t"
  "v_pk_mul_f32 v[170:171], v[170:171], v[224:225] op_sel_hi:[1,0]\n\t"
  "v_pk_add_f32 v[188:189], v[188:189], 1.0 op_sel_hi:[1,0]\n\t"
  "v_pk_mul_f32 v[172:173], v[172:173], v[224:225] op_sel_hi:[1,0]\n\t"
  "v_pk_add_f32 v[190:191], v[190:191], 1.0 op_sel_hi:[1,0]\n\t"
  "v_pk_mul_f32 v[174:175], v[174:175], v[224:225] op_sel_hi:[1,0]\n\t"
  "v_pk_add_f32 v[192:193], v[192:193], 1.0 op_sel_hi:[1,0]\n\t"
  "v_pk_mul_f32 v[176:177], v[176:177], v[224:225] op_sel_hi:[1,0]\n\t"
  "v_pk_add_f32 v[194:195], v[194:195], 1.0 op_sel_hi:[1,0]\n\t"
  "v_pk_mul_f32 v[178:179], v[178:179], v[224:225] op_sel_hi:[1,0]\n\t"
  "v_pk_add_f32 v[196:197], v[196:197], 1.0 op_sel_hi:[1,0]\n\t"
  "v_pk_mul_f32 v[180:181], v[180:181], v[224:225] op_sel_hi:[1,0]\n\t"
  "v_pk_add_f32 v[198:199], v[198:199], 1.0 op_sel_hi:[1,0]\n\t"
  "v_pk_mul_f32 v[182:183], v[182:183], v[224:225] op_sel_hi:[1,0]\n\t"
  "s_barrier\n\t"
  "s_waitcnt vmcnt(7)\n\t"
  "s_lshl_b32 s65, s37, 9\n\t"
  "buffer_load_dwordx4 v[128:131], v221, s[24:27], s65 offen sc1\n\t"
  "buffer_load_dwordx4 v[132:135], v221, s[24:27], s65 offen offset:16 sc1\n\t"
  "buffer_load_dwordx4 v[136:139], v221, s[24:27], s65 offen offset:32 sc1\n\t"
  "buffer_load_dwordx4 v[140:143], v221, s[24:27], s65 offen offset:48 sc1\n\t"
  "s_lshr_b32 s64, s68, 2\n\t"
  "s_mul_i32 s64, s64, 28672\n\t"
  "s_lshl_b32 s65, s68, 10\n\t"
  "s_add_u32 s64, s64, s65\n\t"
  "s_add_u32 s64, s64, 94208\n\t"
  "v_add_u32_e32 v224, s64, v251\n\t"
  "ds_read_b128 v[0:3], v224\n\t"
  "s_add_u32 s60, s60, 1835008\n\t"
  "buffer_load_dwordx4 v[4:7], v251, s[20:23], s60 offen sc1\n\t"
  "s_add_u32 s60, s60, 1835008\n\t"
  "buffer_load_dwordx4 v[8:11], v251, s[20:23], s60 offen sc1\n\t"
  "s_add_u32 s60, s60, 1835008\n\t"
  "buffer_load_dwordx4 v[12:15], v251, s[20:23], s60 offen sc1\n\t"
  "s_add_u32 s60, s60, 1835008\n\t"
  "buffer_load_dwordx4 v[16:19], v251, s[20:23], s60 offen sc1\n\t"
  "s_add_u32 s60, s60, 1835008\n\t"
  "buffer_load_dwordx4 v[20:23], v251, s[20:23], s60 offen sc1\n\t"
  "s_add_u32 s60, s60, 1835008\n\t"
  "buffer_load_dwordx4 v[24:27], v251, s[20:23], s60 offen sc1\n\t"
  "s_add_u32 s60, s60, 1835008\n\t"
  "buffer_load_dwordx4 v[28:31], v251, s[20:23], s60 offen sc1\n\t"
  "s_add_u32 s60, s60, 1835008\n\t"
  "buffer_load_dwordx4 v[32:35], v251, s[20:23], s60 offen sc1\n\t"
  "s_add_u32 s60, s60, 1835008\n\t"
  "buffer_load_dwordx4 v[36:39], v251, s[20:23], s60 offen sc1\n\t"
  "s_add_u32 s60, s60, 1835008\n\t"
  "buffer_load_dwordx4 v[40:43], v251, s[20:23], s60 offen sc1\n\t"
  "s_add_u32 s60, s60, 1835008\n\t"
  "buffer_load_dwordx4 v[44:47], v251, s[20:23], s60 offen sc1\n\t"
  "s_add_u32 s60, s60, 1835008\n\t"
  "buffer_load_dwordx4 v[48:51], v251, s[20:23], s60 offen sc1\n\t"
  "s_add_u32 s60, s60, 1835008\n\t"
  "buffer_load_dwordx4 v[52:55], v251, s[20:23], s60 offen sc1\n\t"
  "s_add_u32 s60, s60, 1835008\n\t"
  "buffer_load_dwordx4 v[56:59], v251, s[20:23], s60 offen sc1\n\t"
  "s_add_u32 s60, s60, 1835008\n\t"
  "buffer_load_dwordx4 v[60:63], v251, s[20:23], s60 offen sc1\n\t"
  "s_waitcnt vmcnt(15)\n\t"
  "s_waitcnt lgkmcnt(0)\n\t"
  "v_lshlrev_b32_e32 v216, 16, v0\n\t"
  "v_and_b32_e32 v217, s31, v0\n\t"
  "v_pk_mul_f32 v[200:201], v[216:217], v[128:129] op_sel_hi:[1,0]\n\t"
  "v_lshlrev_b32_e32 v216, 16, v1\n\t"
  "v_and_b32_e32 v217, s31, v1\n\t"
  "v_pk_mul_f32 v[202:203], v[216:217], v[128:129] op_sel_hi:[1,0]\n\t"
  "v_lshlrev_b32_e32 v216, 16, v2\n\t"
  "v_and_b32_e32 v217, s31, v2\n\t"
  "v_pk_mul_f32 v[204:205], v[216:217], v[128:129] op_sel_hi:[1,0]\n\t"
  "v_lshlrev_b32_e32 v216, 16, v3\n\t"
  "v_and_b32_e32 v217, s31, v3\n\t"
  "v_pk_mul_f32 v[206:207], v[216:217], v[128:129] op_sel_hi:[1,0]\n\t"
  "s_waitcnt vmcnt(14)\n\t"
  "v_lshlrev_b32_e32 v216, 16, v4\n\t"
  "v_and_b32_e32 v217, s31, v4\n\t"
  "v_pk_fma_f32 v[200:201], v[216:217], v[128:129], v[200:201] op_sel:[0,1,0] op_sel_hi:[1,1,1]\n\t"
  "v_lshlrev_b32_e32 v216, 16, v5\n\t"
  "v_and_b32_e32 v217, s31, v5\n\t"
  "v_pk_fma_f32 v[202:203], v[216:217], v[128:129], v[202:203] op_sel:[0,1,0] op_sel_hi:[1,1,1]\n\t"
  "v_lshlrev_b32_e32 v216, 16, v6\n\t"
  "v_and_b32_e32 v217, s31, v6\n\t"
  "v_pk_fma_f32 v[204:205], v[216:217], v[128:129], v[204:205] op_sel:[0,1,0] op_sel_hi:[1,1,1]\n\t"
  "v_lshlrev_b32_e32 v216, 16, v7\n\t"
  "v_and_b32_e32 v217, s31, v7\n\t"
  "v_pk_fma_f32 v[206:207], v[216:217], v[128:129], v[206:207] op_sel:[0,1,0] op_sel_hi:[1,1,1]\n\t"
  "s_waitcnt vmcnt(13)\n\t"
  "v_lshlrev_b32_e32 v216, 16, v8\n\t"
  "v_and_b32_e32 v217, s31, v8\n\t"
  "v_pk_fma_f32 v[200:201], v[216:217], v[130:131], v[200:201] op_sel:[0,0,0] op_sel_hi:[1,0,1]\n\t"
  "v_lshlrev_b32_e32 v216, 16, v9\n\t"
  "v_and_b32_e32 v217, s31, v9\n\t"
  "v_pk_fma_f32 v[202:203], v[216:217], v[130:131], v[202:203] op_sel:[0,0,0] op_sel_hi:[1,0,1]\n\t"
  "v_lshlrev_b32_e32 v216, 16, v10\n\t"
  "v_and_b32_e32 v217, s31, v10\n\t"
  "v_pk_fma_f32 v[204:205], v[216:217], v[130:131], v[204:205] op_sel:[0,0,0] op_sel_hi:[1,0,1]\n\t"
  "v_lshlrev_b32_e32 v216, 16, v11\n\t"
  "v_and_b32_e32 v217, s31, v11\n\t"
  "v_pk_fma_f32 v[206:207], v[216:217], v[130:131], v[206:207] op_sel:[0,0,0] op_sel_hi:[1,0,1]\n\t"
  "s_waitcnt vmcnt(12)\n\t"
  "v_lshlrev_b32_e32 v216, 16, v12\n\t"
  "v_and_b32_e32 v217, s31, v12\n\t"
  "v_pk_fma_f32 v[200:201], v[216:217], v[130:131], v[200:201] op_sel:[0,1,0] op_sel_hi:[1,1,1]\n\t"
  "v_lshlrev_b32_e32 v216, 16, v13\n\t"
  "v_and_b32_e32 v217, s31, v13\n\t"
  "v_pk_fma_f32 v[202:203], v[216:217], v[130:131], v[202:203] op_sel:[0,1,0] op_sel_hi:[1,1,1]\n\t"
  "v_lshlrev_b32_e32 v216, 16, v14\n\t"
  "v_and_b32_e32 v217, s31, v14\n\t"
  "v_pk_fma_f32 v[204:205], v[216:217], v[130:131], v[204:205] op_sel:[0,1,0] op_sel_hi:[1,1,1]\n\t"
  "v_lshlrev_b32_e32 v216, 16, v15\n\t"
  "v_and_b32_e32 v217, s31, v15\n\t"
  "v_pk_fma_f32 v[206:207], v[216:217], v[130:131], v[206:207] op_sel:[0,1,0] op_sel_hi:[1,1,1]\n\t"
  "s_waitcnt vmcnt(11)\n\t"
  "v_lshlrev_b32_e32 v216, 16, v16\n\t"
  "v_and_b32_e32 v217, s31, v16\n\t"
  "v_pk_fma_f32 v[200:201], v[216:217], v[132:133], v[200:201] op_sel:[0,0,0] op_sel_hi:[1,0,1]\n\t"
  "v_lshlrev_b32_e32 v216, 16, v17\n\t"
  "v_and_b32_e32 v217, s31, v17\n\t"
  "v_pk_fma_f32 v[202:203], v[216:217], v[132:133], v[202:203] op_sel:[0,0,0] op_sel_hi:[1,0,1]\n\t"
  "v_lshlrev_b32_e32 v216, 16, v18\n\t"
  "v_and_b32_e32 v217, s31, v18\n\t"
  "v_pk_fma_f32 v[204:205], v[216:217], v[132:133], v[204:205] op_sel:[0,0,0] op_sel_hi:[1,0,1]\n\t"
  "v_lshlrev_b32_e32 v216, 16, v19\n\t"
  "v_and_b32_e32 v217, s31, v19\n\t"
  "v_pk_fma_f32 v[206:207], v[216:217], v[132:133], v[206:207] op_sel:[0,0,0] op_sel_hi:[1,0,1]\n\t"
  "s_waitcnt vmcnt(10)\n\t"
  "v_lshlrev_b32_e32 v216, 16, v20\n\t"
  "v_and_b32_e32 v217, s31, v20\n\t"
  "v_pk_fma_f32 v[200:201], v[216:217], v[132:133], v[200:201] op_sel:[0,1,0] op_sel_hi:[1,1,1]\n\t"
  "v_lshlrev_b32_e32 v216, 16, v21\n\t"
  "v_and_b32_e32 v217, s31, v21\n\t"
  "v_pk_fma_f32 v[202:203], v[216:217], v[132:133], v[202:203] op_sel:[0,1,0] op_sel_hi:[1,1,1]\n\t"
  "v_lshlrev_b32_e32 v216, 16, v22\n\t"
  "v_and_b32_e32 v217, s31, v22\n\t"
  "v_pk_fma_f32 v[204:205], v[216:217], v[132:133], v[204:205] op_sel:[0,1,0] op_sel_hi:[1,1,1]\n\t"
  "v_lshlrev_b32_e32 v216, 16, v23\n\t"
  "v_and_b32_e32 v217, s31, v23\n\t"
  "v_pk_fma_f32 v[206:207], v[216:217], v[132:133], v[206:207] op_sel:[0,1,0] op_sel_hi:[1,1,1]\n\t"
  "s_waitcnt vmcnt(9)\n\t"
  "v_lshlrev_b32_e32 v216, 16, v24\n\t"
  "v_and_b32_e32 v217, s31, v24\n\t"
  "v_pk_fma_f32 v[200:201], v[216:217], v[134:135], v[200:201] op_sel:[0,0,0] op_sel_hi:[1,0,1]\n\t"
  "v_lshlrev_b32_e32 v216, 16, v25\n\t"
  "v_and_b32_e32 v217, s31, v25\n\t"
  "v_pk_fma_f32 v[202:203], v[216:217], v[134:135], v[202:203] op_sel:[0,0,0] op_sel_hi:[1,0,1]\n\t"
  "v_lshlrev_b32_e32 v216, 16, v26\n\t"
  "v_and_b32_e32 v217, s31, v26\n\t"
  "v_pk_fma_f32 v[204:205], v[216:217], v[134:135], v[204:205] op_sel:[0,0,0] op_sel_hi:[1,0,1]\n\t"
  "v_lshlrev_b32_e32 v216, 16, v27\n\t"
  "v_and_b32_e32 v217, s31, v27\n\t"
  "v_pk_fma_f32 v[206:207], v[216:217], v[134:135], v[206:207] op_sel:[0,0,0] op_sel_hi:[1,0,1]\n\t"
  "s_waitcnt vmcnt(8)\n\t"
  "v_lshlrev_b32_e32 v216, 16, v28\n\t"
  "v_and_b32_e32 v217, s31, v28\n\t"
  "v_pk_fma_f32 v[200:201], v[216:217], v[134:135], v[200:201] op_sel:[0,1,0] op_sel_hi:[1,1,1]\n\t"
  "v_lshlrev_b32_e32 v216, 16, v29\n\t"
  "v_and_b32_e32 v217, s31, v29\n\t"
  "v_pk_fma_f32 v[202:203], v[216:217], v[134:135], v[202:203] op_sel:[0,1,0] op_sel_hi:[1,1,1]\n\t"
  "v_lshlrev_b32_e32 v216, 16, v30\n\t"
  "v_and_b32_e32 v217, s31, v30\n\t"
  "v_pk_fma_f32 v[204:205], v[216:217], v[134:135], v[204:205] op_sel:[0,1,0] op_sel_hi:[1,1,1]\n\t"
  "v_lshlrev_b32_e32 v216, 16, v31\n\t"
  "v_and_b32_e32 v217, s31, v31\n\t"
  "v_pk_fma_f32 v[206:207], v[216:217], v[134:135], v[206:207] op_sel:[0,1,0] op_sel_hi:[1,1,1]\n\t"
  "s_waitcnt vmcnt(7)\n\t"
  "v_lshlrev_b32_e32 v216, 16, v32\n\t"
  "v_and_b32_e32 v217, s31, v32\n\t"
  "v_pk_fma_f32 v[200:201], v[216:217], v[136:137], v[200:201] op_sel:[0,0,0] op_sel_hi:[1,0,1]\n\t"
  "v_lshlrev_b32_e32 v216, 16, v33\n\t"
  "v_and_b32_e32 v217, s31, v33\n\t"
  "v_pk_fma_f32 v[202:203], v[216:217], v[136:137], v[202:203] op_sel:[0,0,0] op_sel_hi:[1,0,1]\n\t"
  "v_lshlrev_b32_e32 v216, 16, v34\n\t"
  "v_and_b32_e32 v217, s31, v34\n\t"
  "v_pk_fma_f32 v[204:205], v[216:217], v[136:137], v[204:205] op_sel:[0,0,0] op_sel_hi:[1,0,1]\n\t"
  "v_lshlrev_b32_e32 v216, 16, v35\n\t"
  "v_and_b32_e32 v217, s31, v35\n\t"
  "v_pk_fma_f32 v[206:207], v[216:217], v[136:137], v[206:207] op_sel:[0,0,0] op_sel_hi:[1,0,1]\n\t"
  "s_waitcnt vmcnt(6)\n\t"
  "v_lshlrev_b32_e32 v216, 16, v36\n\t"
  "v_and_b32_e32 v217, s31, v36\n\t"
  "v_pk_fma_f32 v[200:201], v[216:217], v[136:137], v[200:201] op_sel:[0,1,0] op_sel_hi:[1,1,1]\n\t"
  "v_lshlrev_b32_e32 v216, 16, v37\n\t"
  "v_and_b32_e32 v217, s31, v37\n\t"
  "v_pk_fma_f32 v[202:203], v[216:217], v[136:137], v[202:203] op_sel:[0,1,0] op_sel_hi:[1,1,1]\n\t"
  "v_lshlrev_b32_e32 v216, 16, v38\n\t"
  "v_and_b32_e32 v217, s31, v38\n\t"
  "v_pk_fma_f32 v[204:205], v[216:217], v[136:137], v[204:205] op_sel:[0,1,0] op_sel_hi:[1,1,1]\n\t"
  "v_lshlrev_b32_e32 v216, 16, v39\n\t"
  "v_and_b32_e32 v217, s31, v39\n\t"
  "v_pk_fma_f32 v[206:207], v[216:217], v[136:137], v[206:207] op_sel:[0,1,0] op_sel_hi:[1,1,1]\n\t"
  "s_waitcnt vmcnt(5)\n\t"
  "v_lshlrev_b32_e32 v216, 16, v40\n\t"
  "v_and_b32_e32 v217, s31, v40\n\t"
  "v_pk_fma_f32 v[200:201], v[216:217], v[138:139], v[200:201] op_sel:[0,0,0] op_sel_hi:[1,0,1]\n\t"
  "v_lshlrev_b32_e32 v216, 16, v41\n\t"
  "v_and_b32_e32 v217, s31, v41\n\t"
  "v_pk_fma_f32 v[202:203], v[216:217], v[138:139], v[202:203] op_sel:[0,0,0] op_sel_hi:[1,0,1]\n\t"
  "v_lshlrev_b32_e32 v216, 16, v42\n\t"
  "v_and_b32_e32 v217, s31, v42\n\t"
  "v_pk_fma_f32 v[204:205], v[216:217], v[138:139], v[204:205] op_sel:[0,0,0] op_sel_hi:[1,0,1]\n\t"
  "v_lshlrev_b32_e32 v216, 16, v43\n\t"
  "v_and_b32_e32 v217, s31, v43\n\t"
  "v_pk_fma_f32 v[206:207], v[216:217], v[138:139], v[206:207] op_sel:[0,0,0] op_sel_hi:[1,0,1]\n\t"
  "s_waitcnt vmcnt(4)\n\t"
  "v_lshlrev_b32_e32 v216, 16, v44\n\t"
  "v_and_b32_e32 v217, s31, v44\n\t"
  "v_pk_fma_f32 v[200:201], v[216:217], v[138:139], v[200:201] op_sel:[0,1,0] op_sel_hi:[1,1,1]\n\t"
  "v_lshlrev_b32_e32 v216, 16, v45\n\t"
  "v_and_b32_e32 v217, s31, v45\n\t"
  "v_pk_fma_f32 v[202:203], v[216:217], v[138:139], v[202:203] op_sel:[0,1,0] op_sel_hi:[1,1,1]\n\t"
  "v_lshlrev_b32_e32 v216, 16, v46\n\t"
  "v_and_b32_e32 v217, s31, v46\n\t"
  "v_pk_fma_f32 v[204:205], v[216:217], v[138:139], v[204:205] op_sel:[0,1,0] op_sel_hi:[1,1,1]\n\t"
  "v_lshlrev_b32_e32 v216, 16, v47\n\t"
  "v_and_b32_e32 v217, s31, v47\n\t"
  "v_pk_fma_f32 v[206:207], v[216:217], v[138:139], v[206:207] op_sel:[0,1,0] op_sel_hi:[1,1,1]\n\t"
  "s_waitcnt vmcnt(3)\n\t"
  "v_lshlrev_b32_e32 v216, 16, v48\n\t"
  "v_and_b32_e32 v217, s31, v48\n\t"
  "v_pk_fma_f32 v[200:201], v[216:217], v[140:141], v[200:201] op_sel:[0,0,0] op_sel_hi:[1,0,1]\n\t"
  "v_lshlrev_b32_e32 v216, 16, v49\n\t"
  "v_and_b32_e32 v217, s31, v49\n\t"
  "v_pk_fma_f32 v[202:203], v[216:217], v[140:141], v[202:203] op_sel:[0,0,0] op_sel_hi:[1,0,1]\n\t"
  "v_lshlrev_b32_e32 v216, 16, v50\n\t"
  "v_and_b32_e32 v217, s31, v50\n\t"
  "v_pk_fma_f32 v[204:205], v[216:217], v[140:141], v[204:205] op_sel:[0,0,0] op_sel_hi:[1,0,1]\n\t"
  "v_lshlrev_b32_e32 v216, 16, v51\n\t"
  "v_and_b32_e32 v217, s31, v51\n\t"
  "v_pk_fma_f32 v[206:207], v[216:217], v[140:141], v[206:207] op_sel:[0,0,0] op_sel_hi:[1,0,1]\n\t"
  "s_waitcnt vmcnt(2)\n\t"
  "v_lshlrev_b32_e32 v216, 16, v52\n\t"
  "v_and_b32_e32 v217, s31, v52\n\t"
  "v_pk_fma_f32 v[200:201], v[216:217], v[140:141], v[200:201] op_sel:[0,1,0] op_sel_hi:[1,1,1]\n\t"
  "v_lshlrev_b32_e32 v216, 16, v53\n\t"
  "v_and_b32_e32 v217, s31, v53\n\t"
  "v_pk_fma_f32 v[202:203], v[216:217], v[140:141], v[202:203] op_sel:[0,1,0] op_sel_hi:[1,1,1]\n\t"
  "v_lshlrev_b32_e32 v216, 16, v54\n\t"
  "v_and_b32_e32 v217, s31, v54\n\t"
  "v_pk_fma_f32 v[204:205], v[216:217], v[140:141], v[204:205] op_sel:[0,1,0] op_sel_hi:[1,1,1]\n\t"
  "v_lshlrev_b32_e32 v216, 16, v55\n\t"
  "v_and_b32_e32 v217, s31, v55\n\t"
  "v_pk_fma_f32 v[206:207], v[216:217], v[140:141], v[206:207] op_sel:[0,1,0] op_sel_hi:[1,1,1]\n\t"
  "s_waitcnt vmcnt(1)\n\t"
  "v_lshlrev_b32_e32 v216, 16, v56\n\t"
  "v_and_b32_e32 v217, s31, v56\n\t"
  "v_pk_fma_f32 v[200:201], v[216:217], v[142:143], v[200:201] op_sel:[0,0,0] op_sel_hi:[1,0,1]\n\t"
  "v_lshlrev_b32_e32 v216, 16, v57\n\t"
  "v_and_b32_e32 v217, s31, v57\n\t"
  "v_pk_fma_f32 v[202:203], v[216:217], v[142:143], v[202:203] op_sel:[0,0,0] op_sel_hi:[1,0,1]\n\t"
  "v_lshlrev_b32_e32 v216, 16, v58\n\t"
  "v_and_b32_e32 v217, s31, v58\n\t"
  "v_pk_fma_f32 v[204:205], v[216:217], v[142:143], v[204:205] op_sel:[0,0,0] op_sel_hi:[1,0,1]\n\t"
  "v_lshlrev_b32_e32 v216, 16, v59\n\t"
  "v_and_b32_e32 v217, s31, v59\n\t"
  "v_pk_fma_f32 v[206:207], v[216:217], v[142:143], v[206:207] op_sel:[0,0,0] op_sel_hi:[1,0,1]\n\t"
  "s_waitcnt vmcnt(0)\n\t"
  "v_lshlrev_b32_e32 v216, 16, v60\n\t"
  "v_and_b32_e32 v217, s31, v60\n\t"
  "v_pk_fma_f32 v[200:201], v[216:217], v[142:143], v[200:201] op_sel:[0,1,0] op_sel_hi:[1,1,1]\n\t"
  "v_lshlrev_b32_e32 v216, 16, v61\n\t"
  "v_and_b32_e32 v217, s31, v61\n\t"
  "v_pk_fma_f32 v[202:203], v[216:217], v[142:143], v[202:203] op_sel:[0,1,0] op_sel_hi:[1,1,1]\n\t"
  "v_lshlrev_b32_e32 v216, 16, v62\n\t"
  "v_and_b32_e32 v217, s31, v62\n\t"
  "v_pk_fma_f32 v[204:205], v[216:217], v[142:143], v[204:205] op_sel:[0,1,0] op_sel_hi:[1,1,1]\n\t"
  "v_lshlrev_b32_e32 v216, 16, v63\n\t"
  "v_and_b32_e32 v217, s31, v63\n\t"
  "v_pk_fma_f32 v[206:207], v[216:217], v[142:143], v[206:207] op_sel:[0,1,0] op_sel_hi:[1,1,1]\n\t"
  "s_barrier\n\t"
  "s_lshr_b32 s64, s38, 2\n\t"
  "s_mul_i32 s64, s64, 28672\n\t"
  "s_lshl_b32 s65, s38, 10\n\t"
  "s_add_u32 s64, s64, s65\n\t"
  "s_add_u32 s64, s64, 94208\n\t"
  "v_add_u32_e32 v224, s64, v251\n\t"
  "ds_read_b128 v[64:67], v224\n\t"
  "s_add_u32 s61, s61, 1835008\n\t"
  "buffer_load_dwordx4 v[68:71], v251, s[20:23], s61 offen sc1\n\t"
  "s_add_u32 s61, s61, 1835008\n\t"
  "buffer_load_dwordx4 v[72:75], v251, s[20:23], s61 offen sc1\n\t"
  "s_add_u32 s61, s61, 1835008\n\t"
  "buffer_load_dwordx4 v[76:79], v251, s[20:23], s61 offen sc1\n\t"
  "s_add_u32 s61, s61, 1835008\n\t"
  "buffer_load_dwordx4 v[80:83], v251, s[20:23], s61 offen sc1\n\t"
  "s_add_u32 s61, s61, 1835008\n\t"
  "buffer_load_dwordx4 v[84:87], v251, s[20:23], s61 offen sc1\n\t"
  "s_add_u32 s61, s61, 1835008\n\t"
  "buffer_load_dwordx4 v[88:91], v251, s[20:23], s61 offen sc1\n\t"
  "s_add_u32 s61, s61, 1835008\n\t"
  "buffer_load_dwordx4 v[92:95], v251, s[20:23], s61 offen sc1\n\t"
  "s_add_u32 s61, s61, 1835008\n\t"
  "buffer_load_dwordx4 v[96:99], v251, s[20:23], s61 offen sc1\n\t"
  "s_add_u32 s61, s61, 1835008\n\t"
  "buffer_load_dwordx4 v[100:103], v251, s[20:23], s61 offen sc1\n\t"
  "s_add_u32 s61, s61, 1835008\n\t"
  "buffer_load_dwordx4 v[104:107], v251, s[20:23], s61 offen sc1\n\t"
  "s_add_u32 s61, s61, 1835008\n\t"
  "buffer_load_dwordx4 v[108:111], v251, s[20:23], s61 offen sc1\n\t"
  "s_add_u32 s61, s61, 1835008\n\t"
  "buffer_load_dwordx4 v[112:115], v251, s[20:23], s61 offen sc1\n\t"
  "s_add_u32 s61, s61, 1835008\n\t"
  "buffer_load_dwordx4 v[116:119], v251, s[20:23], s61 offen sc1\n\t"
  "s_add_u32 s61, s61, 1835008\n\t"
  "buffer_load_dwordx4 v[120:123], v251, s[20:23], s61 offen sc1\n\t"
  "s_add_u32 s61, s61, 1835008\n\t"
  "buffer_load_dwordx4 v[124:127], v251, s[20:23], s61 offen sc1\n\t"
  "s_add_u32 s16, s98, 3145728\n\t"
  "s_addc_u32 s17, s99, 0\n\t"
  "s_and_b32 s17, s17, 0xffff\n\t"
  "s_mov_b32 s18, 1835008\n\t"
  "s_mov_b32 s19, 0x20000\n\t"
  "s_lshr_b32 s64, s37, 1\n\t"
  "s_mul_i32 s64, s64, 14336\n\t"
  "s_and_b32 s65, s37, 1\n\t"
  "s_lshl_b32 s65, s65, 7\n\t"
  "s_add_u32 s64, s64, s65\n\t"
  "s_lshl_b32 s65, s38, 11\n\t"
  "s_add_u32 s97, s64, s65\n\t"
  "s_lshl_b32 s65, s68, 11\n\t"
  "s_add_u32 s96, s64, s65\n\t"
  "s_lshl_b32 s64, s58, 2\n\t"
  "s_lshl_b32 s65, s66, 1\n\t"
  "s_add_u32 s64, s64, s65\n\t"
  "s_bfe_u32 s65, s57, 65537\n\t"
  "s_add_u32 s64, s64, s65\n\t"
  "s_xor_b32 s34, s64, 1\n\t"
  "s_mul_i32 s45, s64, 14336\n\t"
  "s_mul_i32 s34, s34, 14336\n\t"
  "s_lshl_b32 s65, s67, 11\n\t"
  "s_add_u32 s45, s45, s65\n\t"
  "s_add_u32 s34, s34, s65\n\t"
  "s_sub_u32 s44, 4096, s65\n\t"
  "s_lshl_b32 s64, s66, 16\n\t"
  "s_add_u32 s64, s64, 61440\n\t"
  "v_add_u32_e32 v57, s64, v251\n\t"
  "s_lshl_b32 s65, s67, 15\n\t"
  "v_add_u32_e32 v56, s65, v57\n\t"
  "s_add_u32 s20, s98, 50331648\n\t"
  "s_addc_u32 s21, s99, 0\n\t"
  "s_and_b32 s21, s21, 0xffff\n\t"
  "s_mov_b32 s22, 14680064\n\t"
  "s_mov_b32 s23, 0x20000\n\t"
  "s_lshl_b32 s64, s58, 3\n\t"
  "s_lshl_b32 s65, s66, 2\n\t"
  "s_add_u32 s64, s64, s65\n\t"
  "s_and_b32 s65, s57, 2\n\t"
  "s_add_u32 s64, s64, s65\n\t"
  "s_xor_b32 s36, s64, 2\n\t"
  "s_mul_i32 s35, s64, 7168\n\t"
  "s_mul_i32 s36, s36, 7168\n\t"
  "s_lshl_b32 s65, s67, 9\n\t"
  "s_add_u32 s35, s35, s65\n\t"
  "s_add_u32 s36, s36, s65\n\t"
  "v_and_b32_e32 v14, 7, v254\n\t"
  "v_lshlrev_b32_e32 v14, 5, v14\n\t"
  "v_add_u32_e32 v14, 28672, v14\n\t"
  "s_lshl_b32 s64, s62, 3\n\t"
  "v_add_u32_e32 v15, s64, v14\n\t"
  "v_mov_b32_e32 v12, 0xbfb8aa3b\n\t"
  "v_lshlrev_b32_e32 v16, 16, v144\n\t"
  "v_and_b32_e32 v17, s31, v144\n\t"
  "v_lshlrev_b32_e32 v18, 16, v145\n\t"
  "v_and_b32_e32 v19, s31, v145\n\t"
  "v_lshlrev_b32_e32 v20, 16, v146\n\t"
  "v_and_b32_e32 v21, s31, v146\n\t"
  "v_lshlrev_b32_e32 v22, 16, v147\n\t"
  "v_and_b32_e32 v23, s31, v147\n\t"
  "v_lshlrev_b32_e32 v24, 16, v148\n\t"
  "v_and_b32_e32 v25, s31, v148\n\t"
  "v_lshlrev_b32_e32 v26, 16, v149\n\t"
  "v_and_b32_e32 v27, s31, v149\n\t"
  "v_lshlrev_b32_e32 v28, 16, v150\n\t"
  "v_and_b32_e32 v29, s31, v150\n\t"
  "v_lshlrev_b32_e32 v30, 16, v151\n\t"
  "v_and_b32_e32 v31, s31, v151\n\t"
  "v_pk_fma_f32 v[200:201], v[200:201], v[152:153], v[16:17]\n\t"
  "v_pk_fma_f32 v[202:203], v[202:203], v[154:155], v[18:19]\n\t"
  "v_pk_fma_f32 v[204:205], v[204:205], v[156:157], v[20:21]\n\t"
  "v_pk_fma_f32 v[206:207], v[206:207], v[158:159], v[22:23]\n\t"
  "v_pk_add_f32 v[0:1], v[200:201], v[202:203]\n\t"
  "v_pk_mul_f32 v[2:3], v[200:201], v[200:201]\n\t"
  "v_pk_add_f32 v[0:1], v[0:1], v[204:205]\n\t"
  "v_pk_fma_f32 v[2:3], v[202:203], v[202:203], v[2:3]\n\t"
  "v_pk_add_f32 v[0:1], v[0:1], v[206:207]\n\t"
  "v_pk_fma_f32 v[2:3], v[204:205], v[204:205], v[2:3]\n\t"
  "v_add_f32_e32 v0, v0, v1\n\t"
  "v_pk_fma_f32 v[2:3], v[206:207], v[206:207], v[2:3]\n\t"
  "v_add_f32_e32 v1, v2, v3\n\t"
  "s_waitcnt lgkmcnt(0)\n\t"
  "v_lshlrev_b32_e32 v216, 16, v64\n\t"
  "v_and_b32_e32 v217, s31, v64\n\t"
  "v_pk_mul_f32 v[208:209], v[216:217], v[128:129] op_sel_hi:[1,0]\n\t"
  "v_lshlrev_b32_e32 v216, 16, v65\n\t"
  "v_and_b32_e32 v217, s31, v65\n\t"
  "v_pk_mul_f32 v[210:211], v[216:217], v[128:129] op_sel_hi:[1,0]\n\t"
  "v_lshlrev_b32_e32 v216, 16, v66\n\t"
  "v_and_b32_e32 v217, s31, v66\n\t"
  "v_pk_mul_f32 v[212:213], v[216:217], v[128:129] op_sel_hi:[1,0]\n\t"
  "v_lshlrev_b32_e32 v216, 16, v67\n\t"
  "v_and_b32_e32 v217, s31, v67\n\t"
  "v_pk_mul_f32 v[214:215], v[216:217], v[128:129] op_sel_hi:[1,0]\n\t"
  "s_waitcnt vmcnt(14)\n\t"
  "v_lshlrev_b32_e32 v216, 16, v68\n\t"
  "v_and_b32_e32 v217, s31, v68\n\t"
  "v_pk_fma_f32 v[208:209], v[216:217], v[128:129], v[208:209] op_sel:[0,1,0] op_sel_hi:[1,1,1]\n\t"
  "v_lshlrev_b32_e32 v216, 16, v69\n\t"
  "v_and_b32_e32 v217, s31, v69\n\t"
  "v_pk_fma_f32 v[210:211], v[216:217], v[128:129], v[210:211] op_sel:[0,1,0] op_sel_hi:[1,1,1]\n\t"
  "v_lshlrev_b32_e32 v216, 16, v70\n\t"
  "v_and_b32_e32 v217, s31, v70\n\t"
  "v_pk_fma_f32 v[212:213], v[216:217], v[128:129], v[212:213] op_sel:[0,1,0] op_sel_hi:[1,1,1]\n\t"
  "v_lshlrev_b32_e32 v216, 16, v71\n\t"
  "v_and_b32_e32 v217, s31, v71\n\t"
  "v_pk_fma_f32 v[214:215], v[216:217], v[128:129], v[214:215] op_sel:[0,1,0] op_sel_hi:[1,1,1]\n\t"
  "s_waitcnt vmcnt(13)\n\t"
  "v_lshlrev_b32_e32 v216, 16, v72\n\t"
  "v_and_b32_e32 v217, s31, v72\n\t"
  "v_pk_fma_f32 v[208:209], v[216:217], v[130:131], v[208:209] op_sel:[0,0,0] op_sel_hi:[1,0,1]\n\t"
  "v_lshlrev_b32_e32 v216, 16, v73\n\t"
  "v_and_b32_e32 v217, s31, v73\n\t"
  "v_pk_fma_f32 v[210:211], v[216:217], v[130:131], v[210:211] op_sel:[0,0,0] op_sel_hi:[1,0,1]\n\t"
  "v_lshlrev_b32_e32 v216, 16, v74\n\t"
  "v_and_b32_e32 v217, s31, v74\n\t"
  "v_pk_fma_f32 v[212:213], v[216:217], v[130:131], v[212:213] op_sel:[0,0,0] op_sel_hi:[1,0,1]\n\t"
  "v_lshlrev_b32_e32 v216, 16, v75\n\t"
  "v_and_b32_e32 v217, s31, v75\n\t"
  "v_pk_fma_f32 v[214:215], v[216:217], v[130:131], v[214:215] op_sel:[0,0,0] op_sel_hi:[1,0,1]\n\t"
  "s_waitcnt vmcnt(12)\n\t"
  "v_lshlrev_b32_e32 v216, 16, v76\n\t"
  "v_and_b32_e32 v217, s31, v76\n\t"
  "v_pk_fma_f32 v[208:209], v[216:217], v[130:131], v[208:209] op_sel:[0,1,0] op_sel_hi:[1,1,1]\n\t"
  "v_lshlrev_b32_e32 v216, 16, v77\n\t"
  "v_and_b32_e32 v217, s31, v77\n\t"
  "v_pk_fma_f32 v[210:211], v[216:217], v[130:131], v[210:211] op_sel:[0,1,0] op_sel_hi:[1,1,1]\n\t"
  "v_lshlrev_b32_e32 v216, 16, v78\n\t"
  "v_and_b32_e32 v217, s31, v78\n\t"
  "v_pk_fma_f32 v[212:213], v[216:217], v[130:131], v[212:213] op_sel:[0,1,0] op_sel_hi:[1,1,1]\n\t"
  "v_lshlrev_b32_e32 v216, 16, v79\n\t"
  "v_and_b32_e32 v217, s31, v79\n\t"
  "v_pk_fma_f32 v[214:215], v[216:217], v[130:131], v[214:215] op_sel:[0,1,0] op_sel_hi:[1,1,1]\n\t"
  "s_waitcnt vmcnt(11)\n\t"
  "v_lshlrev_b32_e32 v216, 16, v80\n\t"
  "v_and_b32_e32 v217, s31, v80\n\t"
  "v_pk_fma_f32 v[208:209], v[216:217], v[132:133], v[208:209] op_sel:[0,0,0] op_sel_hi:[1,0,1]\n\t"
  "v_lshlrev_b32_e32 v216, 16, v81\n\t"
  "v_and_b32_e32 v217, s31, v81\n\t"
  "v_pk_fma_f32 v[210:211], v[216:217], v[132:133], v[210:211] op_sel:[0,0,0] op_sel_hi:[1,0,1]\n\t"
  "v_lshlrev_b32_e32 v216, 16, v82\n\t"
  "v_and_b32_e32 v217, s31, v82\n\t"
  "v_pk_fma_f32 v[212:213], v[216:217], v[132:133], v[212:213] op_sel:[0,0,0] op_sel_hi:[1,0,1]\n\t"
  "v_lshlrev_b32_e32 v216, 16, v83\n\t"
  "v_and_b32_e32 v217, s31, v83\n\t"
  "v_pk_fma_f32 v[214:215], v[216:217], v[132:133], v[214:215] op_sel:[0,0,0] op_sel_hi:[1,0,1]\n\t"
  "s_waitcnt vmcnt(10)\n\t"
  "v_lshlrev_b32_e32 v216, 16, v84\n\t"
  "v_and_b32_e32 v217, s31, v84\n\t"
  "v_pk_fma_f32 v[208:209], v[216:217], v[132:133], v[208:209] op_sel:[0,1,0] op_sel_hi:[1,1,1]\n\t"
  "v_lshlrev_b32_e32 v216, 16, v85\n\t"
  "v_and_b32_e32 v217, s31, v85\n\t"
  "v_pk_fma_f32 v[210:211], v[216:217], v[132:133], v[210:211] op_sel:[0,1,0] op_sel_hi:[1,1,1]\n\t"
  "v_lshlrev_b32_e32 v216, 16, v86\n\t"
  "v_and_b32_e32 v217, s31, v86\n\t"
  "v_pk_fma_f32 v[212:213], v[216:217], v[132:133], v[212:213] op_sel:[0,1,0] op_sel_hi:[1,1,1]\n\t"
  "v_lshlrev_b32_e32 v216, 16, v87\n\t"
  "v_and_b32_e32 v217, s31, v87\n\t"
  "v_pk_fma_f32 v[214:215], v[216:217], v[132:133], v[214:215] op_sel:[0,1,0] op_sel_hi:[1,1,1]\n\t"
  "s_waitcnt vmcnt(9)\n\t"
  "v_lshlrev_b32_e32 v216, 16, v88\n\t"
  "v_and_b32_e32 v217, s31, v88\n\t"
  "v_pk_fma_f32 v[208:209], v[216:217], v[134:135], v[208:209] op_sel:[0,0,0] op_sel_hi:[1,0,1]\n\t"
  "v_lshlrev_b32_e32 v216, 16, v89\n\t"
  "v_and_b32_e32 v217, s31, v89\n\t"
  "v_pk_fma_f32 v[210:211], v[216:217], v[134:135], v[210:211] op_sel:[0,0,0] op_sel_hi:[1,0,1]\n\t"
  "v_lshlrev_b32_e32 v216, 16, v90\n\t"
  "v_and_b32_e32 v217, s31, v90\n\t"
  "v_pk_fma_f32 v[212:213], v[216:217], v[134:135], v[212:213] op_sel:[0,0,0] op_sel_hi:[1,0,1]\n\t"
  "v_lshlrev_b32_e32 v216, 16, v91\n\t"
  "v_and_b32_e32 v217, s31, v91\n\t"
  "v_pk_fma_f32 v[214:215], v[216:217], v[134:135], v[214:215] op_sel:[0,0,0] op_sel_hi:[1,0,1]\n\t"
  "s_waitcnt vmcnt(8)\n\t"
  "v_lshlrev_b32_e32 v216, 16, v92\n\t"
  "v_and_b32_e32 v217, s31, v92\n\t"
  "v_pk_fma_f32 v[208:209], v[216:217], v[134:135], v[208:209] op_sel:[0,1,0] op_sel_hi:[1,1,1]\n\t"
  "v_lshlrev_b32_e32 v216, 16, v93\n\t"
  "v_and_b32_e32 v217, s31, v93\n\t"
  "v_pk_fma_f32 v[210:211], v[216:217], v[134:135], v[210:211] op_sel:[0,1,0] op_sel_hi:[1,1,1]\n\t"
  "v_lshlrev_b32_e32 v216, 16, v94\n\t"
  "v_and_b32_e32 v217, s31, v94\n\t"
  "v_pk_fma_f32 v[212:213], v[216:217], v[134:135], v[212:213] op_sel:[0,1,0] op_sel_hi:[1,1,1]\n\t"
  "v_lshlrev_b32_e32 v216, 16, v95\n\t"
  "v_and_b32_e32 v217, s31, v95\n\t"
  "v_pk_fma_f32 v[214:215], v[216:217], v[134:135], v[214:215] op_sel:[0,1,0] op_sel_hi:[1,1,1]\n\t"
  "s_waitcnt vmcnt(7)\n\t"
  "v_lshlrev_b32_e32 v216, 16, v96\n\t"
  "v_and_b32_e32 v217, s31, v96\n\t"
  "v_pk_fma_f32 v[208:209], v[216:217], v[136:137], v[208:209] op_sel:[0,0,0] op_sel_hi:[1,0,1]\n\t"
  "v_lshlrev_b32_e32 v216, 16, v97\n\t"
  "v_and_b32_e32 v217, s31, v97\n\t"
  "v_pk_fma_f32 v[210:211], v[216:217], v[136:137], v[210:211] op_sel:[0,0,0] op_sel_hi:[1,0,1]\n\t"
  "v_lshlrev_b32_e32 v216, 16, v98\n\t"
  "v_and_b32_e32 v217, s31, v98\n\t"
  "v_pk_fma_f32 v[212:213], v[216:217], v[136:137], v[212:213] op_sel:[0,0,0] op_sel_hi:[1,0,1]\n\t"
  "v_lshlrev_b32_e32 v216, 16, v99\n\t"
  "v_and_b32_e32 v217, s31, v99\n\t"
  "v_pk_fma_f32 v[214:215], v[216:217], v[136:137], v[214:215] op_sel:[0,0,0] op_sel_hi:[1,0,1]\n\t"
  "s_waitcnt vmcnt(6)\n\t"
  "v_lshlrev_b32_e32 v216, 16, v100\n\t"
  "v_and_b32_e32 v217, s31, v100\n\t"
  "v_pk_fma_f32 v[208:209], v[216:217], v[136:137], v[208:209] op_sel:[0,1,0] op_sel_hi:[1,1,1]\n\t"
  "v_lshlrev_b32_e32 v216, 16, v101\n\t"
  "v_and_b32_e32 v217, s31, v101\n\t"
  "v_pk_fma_f32 v[210:211], v[216:217], v[136:137], v[210:211] op_sel:[0,1,0] op_sel_hi:[1,1,1]\n\t"
  "v_lshlrev_b32_e32 v216, 16, v102\n\t"
  "v_and_b32_e32 v217, s31, v102\n\t"
  "v_pk_fma_f32 v[212:213], v[216:217], v[136:137], v[212:213] op_sel:[0,1,0] op_sel_hi:[1,1,1]\n\t"
  "v_lshlrev_b32_e32 v216, 16, v103\n\t"
  "v_and_b32_e32 v217, s31, v103\n\t"
  "v_pk_fma_f32 v[214:215], v[216:217], v[136:137], v[214:215] op_sel:[0,1,0] op_sel_hi:[1,1,1]\n\t"
  "s_waitcnt vmcnt(5)\n\t"
  "v_lshlrev_b32_e32 v216, 16, v104\n\t"
  "v_and_b32_e32 v217, s31, v104\n\t"
  "v_pk_fma_f32 v[208:209], v[216:217], v[138:139], v[208:209] op_sel:[0,0,0] op_sel_hi:[1,0,1]\n\t"
  "v_lshlrev_b32_e32 v216, 16, v105\n\t"
  "v_and_b32_e32 v217, s31, v105\n\t"
  "v_pk_fma_f32 v[210:211], v[216:217], v[138:139], v[210:211] op_sel:[0,0,0] op_sel_hi:[1,0,1]\n\t"
  "v_lshlrev_b32_e32 v216, 16, v106\n\t"
  "v_and_b32_e32 v217, s31, v106\n\t"
  "v_pk_fma_f32 v[212:213], v[216:217], v[138:139], v[212:213] op_sel:[0,0,0] op_sel_hi:[1,0,1]\n\t"
  "v_lshlrev_b32_e32 v216, 16, v107\n\t"
  "v_and_b32_e32 v217, s31, v107\n\t"
  "v_pk_fma_f32 v[214:215], v[216:217], v[138:139], v[214:215] op_sel:[0,0,0] op_sel_hi:[1,0,1]\n\t"
  "s_waitcnt vmcnt(4)\n\t"
  "v_lshlrev_b32_e32 v216, 16, v108\n\t"
  "v_and_b32_e32 v217, s31, v108\n\t"
  "v_pk_fma_f32 v[208:209], v[216:217], v[138:139], v[208:209] op_sel:[0,1,0] op_sel_hi:[1,1,1]\n\t"
  "v_lshlrev_b32_e32 v216, 16, v109\n\t"
  "v_and_b32_e32 v217, s31, v109\n\t"
  "v_pk_fma_f32 v[210:211], v[216:217], v[138:139], v[210:211] op_sel:[0,1,0] op_sel_hi:[1,1,1]\n\t"
  "v_lshlrev_b32_e32 v216, 16, v110\n\t"
  "v_and_b32_e32 v217, s31, v110\n\t"
  "v_pk_fma_f32 v[212:213], v[216:217], v[138:139], v[212:213] op_sel:[0,1,0] op_sel_hi:[1,1,1]\n\t"
  "v_lshlrev_b32_e32 v216, 16, v111\n\t"
  "v_and_b32_e32 v217, s31, v111\n\t"
  "v_pk_fma_f32 v[214:215], v[216:217], v[138:139], v[214:215] op_sel:[0,1,0] op_sel_hi:[1,1,1]\n\t"
  "s_waitcnt vmcnt(3)\n\t"
  "v_lshlrev_b32_e32 v216, 16, v112\n\t"
  "v_and_b32_e32 v217, s31, v112\n\t"
  "v_pk_fma_f32 v[208:209], v[216:217], v[140:141], v[208:209] op_sel:[0,0,0] op_sel_hi:[1,0,1]\n\t"
  "v_lshlrev_b32_e32 v216, 16, v113\n\t"
  "v_and_b32_e32 v217, s31, v113\n\t"
  "v_pk_fma_f32 v[210:211], v[216:217], v[140:141], v[210:211] op_sel:[0,0,0] op_sel_hi:[1,0,1]\n\t"
  "v_lshlrev_b32_e32 v216, 16, v114\n\t"
  "v_and_b32_e32 v217, s31, v114\n\t"
  "v_pk_fma_f32 v[212:213], v[216:217], v[140:141], v[212:213] op_sel:[0,0,0] op_sel_hi:[1,0,1]\n\t"
  "v_lshlrev_b32_e32 v216, 16, v115\n\t"
  "v_and_b32_e32 v217, s31, v115\n\t"
  "v_pk_fma_f32 v[214:215], v[216:217], v[140:141], v[214:215] op_sel:[0,0,0] op_sel_hi:[1,0,1]\n\t"
  "s_waitcnt vmcnt(2)\n\t"
  "v_lshlrev_b32_e32 v216, 16, v116\n\t"
  "v_and_b32_e32 v217, s31, v116\n\t"
  "v_pk_fma_f32 v[208:209], v[216:217], v[140:141], v[208:209] op_sel:[0,1,0] op_sel_hi:[1,1,1]\n\t"
  "v_lshlrev_b32_e32 v216, 16, v117\n\t"
  "v_and_b32_e32 v217, s31, v117\n\t"
  "v_pk_fma_f32 v[210:211], v[216:217], v[140:141], v[210:211] op_sel:[0,1,0] op_sel_hi:[1,1,1]\n\t"
  "v_lshlrev_b32_e32 v216, 16, v118\n\t"
  "v_and_b32_e32 v217, s31, v118\n\t"
  "v_pk_fma_f32 v[212:213], v[216:217], v[140:141], v[212:213] op_sel:[0,1,0] op_sel_hi:[1,1,1]\n\t"
  "v_lshlrev_b32_e32 v216, 16, v119\n\t"
  "v_and_b32_e32 v217, s31, v119\n\t"
  "v_pk_fma_f32 v[214:215], v[216:217], v[140:141], v[214:215] op_sel:[0,1,0] op_sel_hi:[1,1,1]\n\t"
  "s_waitcnt vmcnt(1)\n\t"
  "v_lshlrev_b32_e32 v216, 16, v120\n\t"
  "v_and_b32_e32 v217, s31, v120\n\t"
  "v_pk_fma_f32 v[208:209], v[216:217], v[142:143], v[208:209] op_sel:[0,0,0] op_sel_hi:[1,0,1]\n\t"
  "v_lshlrev_b32_e32 v216, 16, v121\n\t"
  "v_and_b32_e32 v217, s31, v121\n\t"
  "v_pk_fma_f32 v[210:211], v[216:217], v[142:143], v[210:211] op_sel:[0,0,0] op_sel_hi:[1,0,1]\n\t"
  "v_lshlrev_b32_e32 v216, 16, v122\n\t"
  "v_and_b32_e32 v217, s31, v122\n\t"
  "v_pk_fma_f32 v[212:213], v[216:217], v[142:143], v[212:213] op_sel:[0,0,0] op_sel_hi:[1,0,1]\n\t"
  "v_lshlrev_b32_e32 v216, 16, v123\n\t"
  "v_and_b32_e32 v217, s31, v123\n\t"
  "v_pk_fma_f32 v[214:215], v[216:217], v[142:143], v[214:215] op_sel:[0,0,0] op_sel_hi:[1,0,1]\n\t"
  "s_waitcnt vmcnt(0)\n\t"
  "v_lshlrev_b32_e32 v216, 16, v124\n\t"
  "v_and_b32_e32 v217, s31, v124\n\t"
  "v_pk_fma_f32 v[208:209], v[216:217], v[142:143], v[208:209] op_sel:[0,1,0] op_sel_hi:[1,1,1]\n\t"
  "v_lshlrev_b32_e32 v216, 16, v125\n\t"
  "v_and_b32_e32 v217, s31, v125\n\t"
  "v_pk_fma_f32 v[210:211], v[216:217], v[142:143], v[210:211] op_sel:[0,1,0] op_sel_hi:[1,1,1]\n\t"
  "v_lshlrev_b32_e32 v216, 16, v126\n\t"
  "v_and_b32_e32 v217, s31, v126\n\t"
  "v_pk_fma_f32 v[212:213], v[216:217], v[142:143], v[212:213] op_sel:[0,1,0] op_sel_hi:[1,1,1]\n\t"
  "v_lshlrev_b32_e32 v216, 16, v127\n\t"
  "v_and_b32_e32 v217, s31, v127\n\t"
  "v_pk_fma_f32 v[214:215], v[216:217], v[142:143], v[214:215] op_sel:[0,1,0] op_sel_hi:[1,1,1]\n\t"
  "s_branch .LjR\n\t"
  ".LqR:\n\t"
  "s_nop 7\n\t"
  "v_lshlrev_b32_e32 v4, 4, v254\n\t"
  "v_add_u32_e32 v4, 263168, v4\n\t"
  "s_mov_b32 s96, 1048576\n\t"
  ".Larv:\n\t"
  "global_load_dwordx4 v[0:3], v4, s[98:99] sc1\n\t"
  "buffer_load_dwordx2 v[144:145], v220, s[12:15], s92 offen\n\t"
  "buffer_load_dwordx2 v[146:147], v220, s[12:15], s92 offen offset:32\n\t"
  "buffer_load_dwordx2 v[148:149], v220, s[12:15], s93 offen\n\t"
  "buffer_load_dwordx2 v[150:151], v220, s[12:15], s93 offen offset:32\n\t"
  "v_bfe_u32 v216, v254, 2, 3\n\t"
  "v_and_b32_e32 v217, 3, v254\n\t"
  "v_bfe_u32 v223, v254, 4, 1\n\t"
  "v_lshlrev_b32_e32 v223, 1, v223\n\t"
  "v_xor_b32_e32 v217, v217, v223\n\t"
  "v_lshlrev_b32_e32 v217, 4, v217\n\t"
  "v_mul_u32_u24_e32 v224, 896, v216\n\t"
  "v_add_u32_e32 v224, v224, v217\n\t"
  "v_lshl_add_u32 v224, v218, 6, v224\n\t"
  "v_add_u32_e32 v232, 0, v224\n\t"
  "v_add_u32_e32 v233, 6144, v224\n\t"
  "v_add_u32_e32 v234, 12288, v224\n\t"
  "v_add_u32_e32 v235, 18432, v224\n\t"
  "v_bfe_u32 v217, v254, 1, 1\n\t"
  "v_xor_b32_e32 v217, v217, v223\n\t"
  "v_lshlrev_b32_e32 v217, 4, v217\n\t"
  "v_and_b32_e32 v223, 1, v254\n\t"
  "v_lshlrev_b32_e32 v223, 3, v223\n\t"
  "v_lshl_add_u32 v223, v216, 6, v223\n\t"
  "v_add_u32_e32 v223, v223, v217\n\t"
  "v_mul_u32_u24_e32 v224, 3584, v218\n\t"
  "v_add_u32_e32 v224, v224, v223\n\t"
  "v_lshl_add_u32 v223, v218, 10, v223\n\t"
  "s_lshl_b32 s64, s67, 5\n\t"
  "v_xor_b32_e32 v224, s64, v224\n\t"
  "s_mul_i32 s64, s67, 1536\n\t"
  "s_lshl_b32 s65, s67, 11\n\t"
  "v_add_u32_e32 v236, s64, v224\n\t"
  "v_xor_b32_e32 v224, 32, v224\n\t"
  "v_add_u32_e32 v237, s65, v224\n\t"
  "s_lshl_b32 s64, s67, 9\n\t"
  "v_add_u32_e32 v242, s64, v223\n\t"
  "v_xor_b32_e32 v243, 32, v242\n\t"
  "v_add_u32_e32 v238, 65536, v236\n\t"
  "v_add_u32_e32 v244, 65536, v242\n\t"
  "v_add_u32_e32 v239, 65536, v237\n\t"
  "v_add_u32_e32 v245, 65536, v243\n\t"
  "v_add_u32_e32 v240, 131072, v236\n\t"
  "v_add_u32_e32 v246, 131072, v242\n\t"
  "v_add_u32_e32 v241, 131072, v237\n\t"
  "v_add_u32_e32 v247, 131072, v243\n\t"
  "s_mul_i32 s64, s66, 14336\n\t"
  "s_add_u32 s64, s64, 0\n\t"
  "v_add_u32_e32 v250, s64, v251\n\t"
  "s_mul_i32 s64, s67, 6152\n\t"
  "s_mul_i32 s65, s67, 8184\n\t"
  "s_add_u32 s65, s65, 8\n\t"
  "v_add_u32_e32 v248, s64, v250\n\t"
  "v_add_u32_e32 v249, s65, v250\n\t"
  "v_bfe_u32 v223, v254, 3, 1\n\t"
  "v_mul_u32_u24_e32 v223, 7168, v223\n\t"
  "v_and_b32_e32 v224, 7, v254\n\t"
  "v_lshl_add_u32 v223, v224, 4, v223\n\t"
  "v_lshrrev_b32_e32 v224, 4, v254\n\t"
  "v_lshl_add_u32 v223, v224, 7, v223\n\t"
  "s_lshl_b32 s64, s66, 2\n\t"
  "s_xor_b32 s64, s64, s57\n\t"
  "s_andn2_b32 s64, s64, 2\n\t"
  "v_bfe_u32 v224, v254, 3, 1\n\t"
  "v_xor_b32_e32 v224, s64, v224\n\t"
  "v_mul_u32_u24_e32 v252, 1835008, v224\n\t"
  "v_add_u32_e32 v223, v223, v252\n\t"
  "v_cmp_eq_u32_e32 vcc, 0, v224\n\t"
  "v_mov_b32_e32 v224, 0x7f000000\n\t"
  "v_add_u32_e32 v253, 3670016, v223\n\t"
  "v_cndmask_b32_e32 v252, v223, v224, vcc\n\t"
  "s_mul_i32 s64, s57, 200704\n\t"
  "s_add_u32 s12, s52, s64\n\t"
  "s_addc_u32 s13, s53, 0\n\t"
  "s_and_b32 s13, s13, 0xffff\n\t"
  "s_mov_b32 s14, 200704\n\t"
  "s_mov_b32 s15, 0x20000\n\t"
  "s_mul_i32 s29, s62, 50176\n\t"
  "s_andn2_b32 s64, s59, 15\n\t"
  "s_lshl_b32 s64, s64, 5\n\t"
  "s_and_b32 s65, s59, 15\n\t"
  "s_add_u32 s64, s64, s65\n\t"
  "s_lshl_b32 s64, s64, 2\n\t"
  "s_add_u32 s64, s64, 299008\n\t"
  "v_lshlrev_b32_e32 v6, 7, v254\n\t"
  "v_add_u32_e32 v6, s64, v6\n\t"
  "v_mov_b32_e32 v7, s69\n\t"
  "s_lshl_b32 s64, s59, 7\n\t"
  "s_add_u32 s64, s64, 266240\n\t"
  "v_and_b32_e32 v216, 15, v254\n\t"
  "v_lshlrev_b32_e32 v216, 2, v216\n\t"
  "v_add_u32_e32 v216, s64, v216\n\t"
  "s_mov_b32 s30, 1048576\n\t"
  "s_waitcnt vmcnt(0)\n\t"
  ".Larvc:\n\t"
  "v_xor_b32_e32 v0, s69, v0\n\t"
  "v_xor_b32_e32 v1, s69, v1\n\t"
  "v_xor_b32_e32 v2, s69, v2\n\t"
  "v_xor_b32_e32 v3, s69, v3\n\t"
  "v_or3_b32 v0, v0, v1, v2\n\t"
  "v_or_b32_e32 v0, v0, v3\n\t"
  "v_cmp_ne_u32_e32 vcc, 0, v0\n\t"
  "s_sub_u32 s96, s96, 1\n\t"
  "s_nop 2\n\t"
  "s_cbranch_vccz .Larvd\n\t"
  "global_load_dwordx4 v[0:3], v4, s[98:99] sc1\n\t"
  "s_waitcnt vmcnt(0)\n\t"
  "s_cmp_lg_u32 s96, 0\n\t"
  "s_cbranch_scc1 .Larvc\n\t"
  ".Larvd:\n\t"
  "s_cmp_lg_u32 s63, 0\n\t"
  "s_cbranch_scc1 .Larve\n\t"
  "v_mov_b32_e32 v0, 262144\n\t"
  "v_mov_b32_e32 v1, s69\n\t"
  "global_store_dword v0, v1, s[98:99] sc1\n\t"
  ".Larve:\n\t"
  "s_lshl_b32 s96, s68, 8\n\t"
  "s_lshl_b32 s97, s38, 8\n\t"
  "s_barrier\n\t"
  "s_mov_b64 exec, 65535\n\t"
  "global_store_dword v6, v7, s[98:99] sc1\n\t"
  "s_mov_b64 exec, -1\n\t"
  ".Lpoll266240:\n\t"
  "global_load_dword v217, v216, s[98:99] sc1\n\t"
  "s_waitcnt vmcnt(0)\n\t"
  "v_xor_b32_e32 v217, s69, v217\n\t"
  "v_cmp_ne_u32_e32 vcc, 0, v217\n\t"
  "s_sub_u32 s30, s30, 1\n\t"
  "s_nop 2\n\t"
  "s_cbranch_vccz .Lpollk266240\n\t"
  "s_sleep 1\n\t"
  "s_cmp_lg_u32 s30, 0\n\t"
  "s_cbranch_scc1 .Lpoll266240\n\t"
  ".Lpollk266240:\n\t"
  "s_barrier\n\t"
  "s_lshl_b32 s65, s37, 9\n\t"
  "buffer_load_dwordx4 v[128:131], v221, s[24:27], s65 offen sc1\n\t"
  "buffer_load_dwordx4 v[132:135], v221, s[24:27], s65 offen offset:16 sc1\n\t"
  "buffer_load_dwordx4 v[136:139], v221, s[24:27], s65 offen offset:32 sc1\n\t"
  "buffer_load_dwordx4 v[140:143], v221, s[24:27], s65 offen offset:48 sc1\n\t"
  "s_add_u32 s64, s96, 3584\n\t"
  "s_add_u32 s65, s97, 3584\n\t"
  "buffer_load_dwordx4 v[152:155], v219, s[16:19], s64 offen sc1\n\t"
  "buffer_load_dwordx4 v[156:159], v219, s[16:19], s64 offen offset:64 sc1\n\t"
  "buffer_load_dwordx4 v[160:163], v219, s[16:19], s65 offen sc1\n\t"
  "buffer_load_dwordx4 v[164:167], v219, s[16:19], s65 offen offset:64 sc1\n\t"
  "s_add_u32 s64, s96, 5376\n\t"
  "s_add_u32 s65, s97, 5376\n\t"
  "buffer_load_dwordx4 v[168:171], v219, s[16:19], s64 offen sc1\n\t"
  "buffer_load_dwordx4 v[172:175], v219, s[16:19], s64 offen offset:64 sc1\n\t"
  "buffer_load_dwordx4 v[176:179], v219, s[16:19], s65 offen sc1\n\t"
  "buffer_load_dwordx4 v[180:183], v219, s[16:19], s65 offen offset:64 sc1\n\t"
  "s_add_u32 s64, s96, 7168\n\t"
  "s_add_u32 s65, s97, 7168\n\t"
  "buffer_load_dwordx4 v[184:187], v219, s[16:19], s64 offen sc1\n\t"
  "buffer_load_dwordx4 v[188:191], v219, s[16:19], s64 offen offset:64 sc1\n\t"
  "buffer_load_dwordx4 v[192:195], v219, s[16:19], s65 offen sc1\n\t"
  "buffer_load_dwordx4 v[196:199], v219, s[16:19], s65 offen offset:64 sc1\n\t"
  "s_add_u32 s64, s96, 8960\n\t"
  "s_add_u32 s65, s97, 8960\n\t"
  "buffer_load_dwordx4 a[112:115], v219, s[16:19], s64 offen sc1\n\t"
  "buffer_load_dwordx4 a[116:119], v219, s[16:19], s64 offen offset:64 sc1\n\t"
  "buffer_load_dwordx4 a[120:123], v219, s[16:19], s65 offen sc1\n\t"
  "buffer_load_dwordx4 a[124:127], v219, s[16:19], s65 offen offset:64 sc1\n\t"
  "s_add_u32 m0, s39, 0\n\t"
  "s_nop 0\n\t"
  "buffer_load_dwordx4 v225, s[8:11], s28 offen lds\n\t"
  "buffer_load_dwordx4 v226, s[8:11], s28 offen offset:1024 lds\n\t"
  "buffer_load_dwordx4 v227, s[8:11], s28 offen offset:2048 lds\n\t"
  "buffer_load_dwordx4 v228, s[8:11], s28 offen offset:3072 lds\n\t"
  "s_add_u32 m0, s39, 4096\n\t"
  "s_nop 0\n\t"
  "buffer_load_dwordx4 v229, s[8:11], s28 offen lds\n\t"
  "buffer_load_dwordx4 v230, s[8:11], s28 offen offset:1024 lds\n\t"
  "buffer_load_dwordx4 v231, s[8:11], s28 offen offset:2048 lds\n\t"
  "s_add_u32 s28, s28, 229376\n\t"
  "s_add_u32 m0, s39, 65536\n\t"
  "s_nop 0\n\t"
  "buffer_load_dwordx4 v225, s[8:11], s28 offen lds\n\t"
  "buffer_load_dwordx4 v226, s[8:11], s28 offen offset:1024 lds\n\t"
  "buffer_load_dwordx4 v227, s[8:11], s28 offen offset:2048 lds\n\t"
  "buffer_load_dwordx4 v228, s[8:11], s28 offen offset:3072 lds\n\t"
  "s_add_u32 m0, s39, 69632\n\t"
  "s_nop 0\n\t"
  "buffer_load_dwordx4 v229, s[8:11], s28 offen lds\n\t"
  "buffer_load_dwordx4 v230, s[8:11], s28 offen offset:1024 lds\n\t"
  "buffer_load_dwordx4 v231, s[8:11], s28 offen offset:2048 lds\n\t"
  "s_add_u32 s28, s28, 229376\n\t"
  "s_add_u32 m0, s39, 98304\n\t"
  "s_nop 0\n\t"
  "buffer_load_dwordx4 v225, s[8:11], s28 offen lds\n\t"
  "buffer_load_dwordx4 v226, s[8:11], s28 offen offset:1024 lds\n\t"
  "buffer_load_dwordx4 v227, s[8:11], s28 offen offset:2048 lds\n\t"
  "buffer_load_dwordx4 v228, s[8:11], s28 offen offset:3072 lds\n\t"
  "s_add_u32 m0, s39, 102400\n\t"
  "s_nop 0\n\t"
  "buffer_load_dwordx4 v229, s[8:11], s28 offen lds\n\t"
  "buffer_load_dwordx4 v230, s[8:11], s28 offen offset:1024 lds\n\t"
  "buffer_load_dwordx4 v231, s[8:11], s28 offen offset:2048 lds\n\t"
  "s_add_u32 s28, s28, 229376\n\t"
  "s_add_u32 m0, s39, 131072\n\t"
  "s_nop 0\n\t"
  "buffer_load_dwordx4 v225, s[8:11], s28 offen lds\n\t"
  "buffer_load_dwordx4 v226, s[8:11], s28 offen offset:1024 lds\n\t"
  "buffer_load_dwordx4 v227, s[8:11], s28 offen offset:2048 lds\n\t"
  "buffer_load_dwordx4 v228, s[8:11], s28 offen offset:3072 lds\n\t"
  "s_add_u32 m0, s39, 135168\n\t"
  "s_nop 0\n\t"
  "buffer_load_dwordx4 v229, s[8:11], s28 offen lds\n\t"
  "buffer_load_dwordx4 v230, s[8:11], s28 offen offset:1024 lds\n\t"
  "buffer_load_dwordx4 v231, s[8:11], s28 offen offset:2048 lds\n\t"
  "s_add_u32 s28, s28, 229376\n\t"
  "s_add_u32 m0, s39, 32768\n\t"
  "s_nop 0\n\t"
  "buffer_load_dwordx4 v225, s[8:11], s28 offen lds\n\t"
  "buffer_load_dwordx4 v226, s[8:11], s28 offen offset:1024 lds\n\t"
  "buffer_load_dwordx4 v227, s[8:11], s28 offen offset:2048 lds\n\t"
  "buffer_load_dwordx4 v228, s[8:11], s28 offen offset:3072 lds\n\t"
  "s_add_u32 m0, s39, 36864\n\t"
  "s_nop 0\n\t"
  "buffer_load_dwordx4 v229, s[8:11], s28 offen lds\n\t"
  "buffer_load_dwordx4 v230, s[8:11], s28 offen offset:1024 lds\n\t"
  "buffer_load_dwordx4 v231, s[8:11], s28 offen offset:2048 lds\n\t"
  "s_add_u32 s28, s28, 229376\n\t"
  "v_mov_b32_e32 v224, 0xbfb8aa3b\n\t"
  "s_waitcnt vmcnt(35)\n\t"
  "v_pk_add_f32 v[184:185], v[184:185], 1.0 op_sel_hi:[1,0]\n\t"
  "v_pk_mul_f32 v[168:169], v[168:169], v[224:225] op_sel_hi:[1,0]\n\t"
  "v_pk_add_f32 v[186:187], v[186:187], 1.0 op_sel_hi:[1,0]\n\t"
  "v_pk_mul_f32 v[170:171], v[170:171], v[224:225] op_sel_hi:[1,0]\n\t"
  "v_pk_add_f32 v[188:189], v[188:189], 1.0 op_sel_hi:[1,0]\n\t"
  "v_pk_mul_f32 v[172:173], v[172:173], v[224:225] op_sel_hi:[1,0]\n\t"
  "v_pk_add_f32 v[190:191], v[190:191], 1.0 op_sel_hi:[1,0]\n\t"
  "v_pk_mul_f32 v[174:175], v[174:175], v[224:225] op_sel_hi:[1,0]\n\t"
  "v_pk_add_f32 v[192:193], v[192:193], 1.0 op_sel_hi:[1,0]\n\t"
  "v_pk_mul_f32 v[176:177], v[176:177], v[224:225] op_sel_hi:[1,0]\n\t"
  "v_pk_add_f32 v[194:195], v[194:195], 1.0 op_sel_hi:[1,0]\n\t"
  "v_pk_mul_f32 v[178:179], v[178:179], v[224:225] op_sel_hi:[1,0]\n\t"
  "v_pk_add_f32 v[196:197], v[196:197], 1.0 op_sel_hi:[1,0]\n\t"
  "v_pk_mul_f32 v[180:181], v[180:181], v[224:225] op_sel_hi:[1,0]\n\t"
  "v_pk_add_f32 v[198:199], v[198:199], 1.0 op_sel_hi:[1,0]\n\t"
  "v_pk_mul_f32 v[182:183], v[182:183], v[224:225] op_sel_hi:[1,0]\n\t"
  "s_lshl_b32 s64, s59, 7\n\t"
  "s_add_u32 s64, s64, 299008\n\t"
  "v_and_b32_e32 v216, 15, v254\n\t"
  "v_lshlrev_b32_e32 v216, 2, v216\n\t"
  "v_add_u32_e32 v216, s64, v216\n\t"
  "s_mov_b32 s30, 1048576\n\t"
  ".Lpoll299008:\n\t"
  "global_load_dword v217, v216, s[98:99] sc1\n\t"
  "s_waitcnt vmcnt(0)\n\t"
  "v_xor_b32_e32 v217, s69, v217\n\t"
  "v_cmp_ne_u32_e32 vcc, 0, v217\n\t"
  "s_sub_u32 s30, s30, 1\n\t"
  "s_nop 2\n\t"
  "s_cbranch_vccz .Lpollk299008\n\t"
  "s_sleep 1\n\t"
  "s_cmp_lg_u32 s30, 0\n\t"
  "s_cbranch_scc1 .Lpoll299008\n\t"
  ".Lpollk299008:\n\t"
  "s_barrier\n\t"
  "s_lshr_b32 s64, s38, 2\n\t"
  "s_mul_i32 s64, s64, 28672\n\t"
  "s_lshl_b32 s65, s38, 10\n\t"
  "s_add_u32 s64, s64, s65\n\t"
  "s_add_u32 s64, s64, 94208\n\t"
  "v_add_u32_e32 v224, s64, v251\n\t"
  "ds_read_b128 v[64:67], v224\n\t"
  "s_add_u32 s61, s61, 1835008\n\t"
  "buffer_load_dwordx4 v[68:71], v251, s[20:23], s61 offen sc1\n\t"
  "s_add_u32 s61, s61, 1835008\n\t"
  "buffer_load_dwordx4 v[72:75], v251, s[20:23], s61 offen sc1\n\t"
  "s_add_u32 s61, s61, 1835008\n\t"
  "buffer_load_dwordx4 v[76:79], v251, s[20:23], s61 offen sc1\n\t"
  "s_add_u32 s61, s61, 1835008\n\t"
  "buffer_load_dwordx4 v[80:83], v251, s[20:23], s61 offen sc1\n\t"
  "s_add_u32 s61, s61, 1835008\n\t"
  "buffer_load_dwordx4 v[84:87], v251, s[20:23], s61 offen sc1\n\t"
  "s_add_u32 s61, s61, 1835008\n\t"
  "buffer_load_dwordx4 v[88:91], v251, s[20:23], s61 offen sc1\n\t"
  "s_add_u32 s61, s61, 1835008\n\t"
  "buffer_load_dwordx4 v[92:95], v251, s[20:23], s61 offen sc1\n\t"
  "s_add_u32 s61, s61, 1835008\n\t"
  "buffer_load_dwordx4 v[96:99], v251, s[20:23], s61 offen sc1\n\t"
  "s_add_u32 s61, s61, 1835008\n\t"
  "buffer_load_dwordx4 v[100:103], v251, s[20:23], s61 offen sc1\n\t"
  "s_add_u32 s61, s61, 1835008\n\t"
  "buffer_load_dwordx4 v[104:107], v251, s[20:23], s61 offen sc1\n\t"
  "s_add_u32 s61, s61, 1835008\n\t"
  "buffer_load_dwordx4 v[108:111], v251, s[20:23], s61 offen sc1\n\t"
  "s_add_u32 s61, s61, 1835008\n\t"
  "buffer_load_dwordx4 v[112:115], v251, s[20:23], s61 offen sc1\n\t"
  "s_add_u32 s61, s61, 1835008\n\t"
  "buffer_load_dwordx4 v[116:119], v251, s[20:23], s61 offen sc1\n\t"
  "s_add_u32 s61, s61, 1835008\n\t"
  "buffer_load_dwordx4 v[120:123], v251, s[20:23], s61 offen sc1\n\t"
  "s_add_u32 s61, s61, 1835008\n\t"
  "buffer_load_dwordx4 v[124:127], v251, s[20:23], s61 offen sc1\n\t"
  "s_add_u32 s16, s98, 3145728\n\t"
  "s_addc_u32 s17, s99, 0\n\t"
  "s_and_b32 s17, s17, 0xffff\n\t"
  "s_mov_b32 s18, 1835008\n\t"
  "s_mov_b32 s19, 0x20000\n\t"
  "s_lshr_b32 s64, s37, 1\n\t"
  "s_mul_i32 s64, s64, 14336\n\t"
  "s_and_b32 s65, s37, 1\n\t"
  "s_lshl_b32 s65, s65, 7\n\t"
  "s_add_u32 s64, s64, s65\n\t"
  "s_lshl_b32 s65, s38, 11\n\t"
  "s_add_u32 s97, s64, s65\n\t"
  "s_lshl_b32 s65, s68, 11\n\t"
  "s_add_u32 s96, s64, s65\n\t"
  "s_lshl_b32 s64, s58, 2\n\t"
  "s_lshl_b32 s65, s66, 1\n\t"
  "s_add_u32 s64, s64, s65\n\t"
  "s_bfe_u32 s65, s57, 65537\n\t"
  "s_add_u32 s64, s64, s65\n\t"
  "s_xor_b32 s34, s64, 1\n\t"
  "s_mul_i32 s45, s64, 14336\n\t"
  "s_mul_i32 s34, s34, 14336\n\t"
  "s_lshl_b32 s65, s67, 11\n\t"
  "s_add_u32 s45, s45, s65\n\t"
  "s_add_u32 s34, s34, s65\n\t"
  "s_sub_u32 s44, 4096, s65\n\t"
  "s_lshl_b32 s64, s66, 16\n\t"
  "s_add_u32 s64, s64, 61440\n\t"
  "v_add_u32_e32 v57, s64, v251\n\t"
  "s_lshl_b32 s65, s67, 15\n\t"
  "v_add_u32_e32 v56, s65, v57\n\t"
  "s_add_u32 s20, s98, 50331648\n\t"
  "s_addc_u32 s21, s99, 0\n\t"
  "s_and_b32 s21, s21, 0xffff\n\t"
  "s_mov_b32 s22, 14680064\n\t"
  "s_mov_b32 s23, 0x20000\n\t"
  "s_lshl_b32 s64, s58, 3\n\t"
  "s_lshl_b32 s65, s66, 2\n\t"
  "s_add_u32 s64, s64, s65\n\t"
  "s_and_b32 s65, s57, 2\n\t"
  "s_add_u32 s64, s64, s65\n\t"
  "s_xor_b32 s36, s64, 2\n\t"
  "s_mul_i32 s35, s64, 7168\n\t"
  "s_mul_i32 s36, s36, 7168\n\t"
  "s_lshl_b32 s65, s67, 9\n\t"
  "s_add_u32 s35, s35, s65\n\t"
  "s_add_u32 s36, s36, s65\n\t"
  "v_and_b32_e32 v14, 7, v254\n\t"
  "v_lshlrev_b32_e32 v14, 5, v14\n\t"
  "v_add_u32_e32 v14, 28672, v14\n\t"
  "s_lshl_b32 s64, s62, 3\n\t"
  "v_add_u32_e32 v15, s64, v14\n\t"
  "v_mov_b32_e32 v12, 0xbfb8aa3b\n\t"
  "v_lshlrev_b32_e32 v16, 16, v144\n\t"
  "v_and_b32_e32 v17, s31, v144\n\t"
  "v_lshlrev_b32_e32 v18, 16, v145\n\t"
  "v_and_b32_e32 v19, s31, v145\n\t"
  "v_lshlrev_b32_e32 v20, 16, v146\n\t"
  "v_and_b32_e32 v21, s31, v146\n\t"
  "v_lshlrev_b32_e32 v22, 16, v147\n\t"
  "v_and_b32_e32 v23, s31, v147\n\t"
  "v_lshlrev_b32_e32 v24, 16, v148\n\t"
  "v_and_b32_e32 v25, s31, v148\n\t"
  "v_lshlrev_b32_e32 v26, 16, v149\n\t"
  "v_and_b32_e32 v27, s31, v149\n\t"
  "v_lshlrev_b32_e32 v28, 16, v150\n\t"
  "v_and_b32_e32 v29, s31, v150\n\t"
  "v_lshlrev_b32_e32 v30, 16, v151\n\t"
  "v_and_b32_e32 v31, s31, v151\n\t"
  "v_mov_b32_e32 v200, 0\n\t"
  "v_mov_b32_e32 v201, 0\n\t"
  "v_mov_b32_e32 v202, 0\n\t"
  "v_mov_b32_e32 v203, 0\n\t"
  "v_mov_b32_e32 v204, 0\n\t"
  "v_mov_b32_e32 v205, 0\n\t"
  "v_mov_b32_e32 v206, 0\n\t"
  "v_mov_b32_e32 v207, 0\n\t"
  "v_mov_b32_e32 v0, 0\n\t"
  "v_mov_b32_e32 v1, 0\n\t"
  "s_waitcnt lgkmcnt(0)\n\t"
  "v_lshlrev_b32_e32 v216, 16, v64\n\t"
  "v_and_b32_e32 v217, s31, v64\n\t"
  "v_pk_mul_f32 v[208:209], v[216:217], v[128:129] op_sel_hi:[1,0]\n\t"
  "v_lshlrev_b32_e32 v216, 16, v65\n\t"
  "v_and_b32_e32 v217, s31, v65\n\t"
  "v_pk_mul_f32 v[210:211], v[216:217], v[128:129] op_sel_hi:[1,0]\n\t"
  "v_lshlrev_b32_e32 v216, 16, v66\n\t"
  "v_and_b32_e32 v217, s31, v66\n\t"
  "v_pk_mul_f32 v[212:213], v[216:217], v[128:129] op_sel_hi:[1,0]\n\t"
  "v_lshlrev_b32_e32 v216, 16, v67\n\t"
  "v_and_b32_e32 v217, s31, v67\n\t"
  "v_pk_mul_f32 v[214:215], v[216:217], v[128:129] op_sel_hi:[1,0]\n\t"
  "s_waitcnt vmcnt(14)\n\t"
  "v_lshlrev_b32_e32 v216, 16, v68\n\t"
  "v_and_b32_e32 v217, s31, v68\n\t"
  "v_pk_fma_f32 v[208:209], v[216:217], v[128:129], v[208:209] op_sel:[0,1,0] op_sel_hi:[1,1,1]\n\t"
  "v_lshlrev_b32_e32 v216, 16, v69\n\t"
  "v_and_b32_e32 v217, s31, v69\n\t"
  "v_pk_fma_f32 v[210:211], v[216:217], v[128:129], v[210:211] op_sel:[0,1,0] op_sel_hi:[1,1,1]\n\t"
  "v_lshlrev_b32_e32 v216, 16, v70\n\t"
  "v_and_b32_e32 v217, s31, v70\n\t"
  "v_pk_fma_f32 v[212:213], v[216:217], v[128:129], v[212:213] op_sel:[0,1,0] op_sel_hi:[1,1,1]\n\t"
  "v_lshlrev_b32_e32 v216, 16, v71\n\t"
  "v_and_b32_e32 v217, s31, v71\n\t"
  "v_pk_fma_f32 v[214:215], v[216:217], v[128:129], v[214:215] op_sel:[0,1,0] op_sel_hi:[1,1,1]\n\t"
  "s_waitcnt vmcnt(13)\n\t"
  "v_lshlrev_b32_e32 v216, 16, v72\n\t"
  "v_and_b32_e32 v217, s31, v72\n\t"
  "v_pk_fma_f32 v[208:209], v[216:217], v[130:131], v[208:209] op_sel:[0,0,0] op_sel_hi:[1,0,1]\n\t"
  "v_lshlrev_b32_e32 v216, 16, v73\n\t"
  "v_and_b32_e32 v217, s31, v73\n\t"
  "v_pk_fma_f32 v[210:211], v[216:217], v[130:131], v[210:211] op_sel:[0,0,0] op_sel_hi:[1,0,1]\n\t"
  "v_lshlrev_b32_e32 v216, 16, v74\n\t"
  "v_and_b32_e32 v217, s31, v74\n\t"
  "v_pk_fma_f32 v[212:213], v[216:217], v[130:131], v[212:213] op_sel:[0,0,0] op_sel_hi:[1,0,1]\n\t"
  "v_lshlrev_b32_e32 v216, 16, v75\n\t"
  "v_and_b32_e32 v217, s31, v75\n\t"
  "v_pk_fma_f32 v[214:215], v[216:217], v[130:131], v[214:215] op_sel:[0,0,0] op_sel_hi:[1,0,1]\n\t"
  "s_waitcnt vmcnt(12)\n\t"
  "v_lshlrev_b32_e32 v216, 16, v76\n\t"
  "v_and_b32_e32 v217, s31, v76\n\t"
  "v_pk_fma_f32 v[208:209], v[216:217], v[130:131], v[208:209] op_sel:[0,1,0] op_sel_hi:[1,1,1]\n\t"
  "v_lshlrev_b32_e32 v216, 16, v77\n\t"
  "v_and_b32_e32 v217, s31, v77\n\t"
  "v_pk_fma_f32 v[210:211], v[216:217], v[130:131], v[210:211] op_sel:[0,1,0] op_sel_hi:[1,1,1]\n\t"
  "v_lshlrev_b32_e32 v216, 16, v78\n\t"
  "v_and_b32_e32 v217, s31, v78\n\t"
  "v_pk_fma_f32 v[212:213], v[216:217], v[130:131], v[212:213] op_sel:[0,1,0] op_sel_hi:[1,1,1]\n\t"
  "v_lshlrev_b32_e32 v216, 16, v79\n\t"
  "v_and_b32_e32 v217, s31, v79\n\t"
  "v_pk_fma_f32 v[214:215], v[216:217], v[130:131], v[214:215] op_sel:[0,1,0] op_sel_hi:[1,1,1]\n\t"
  "s_waitcnt vmcnt(11)\n\t"
  "v_lshlrev_b32_e32 v216, 16, v80\n\t"
  "v_and_b32_e32 v217, s31, v80\n\t"
  "v_pk_fma_f32 v[208:209], v[216:217], v[132:133], v[208:209] op_sel:[0,0,0] op_sel_hi:[1,0,1]\n\t"
  "v_lshlrev_b32_e32 v216, 16, v81\n\t"
  "v_and_b32_e32 v217, s31, v81\n\t"
  "v_pk_fma_f32 v[210:211], v[216:217], v[132:133], v[210:211] op_sel:[0,0,0] op_sel_hi:[1,0,1]\n\t"
  "v_lshlrev_b32_e32 v216, 16, v82\n\t"
  "v_and_b32_e32 v217, s31, v82\n\t"
  "v_pk_fma_f32 v[212:213], v[216:217], v[132:133], v[212:213] op_sel:[0,0,0] op_sel_hi:[1,0,1]\n\t"
  "v_lshlrev_b32_e32 v216, 16, v83\n\t"
  "v_and_b32_e32 v217, s31, v83\n\t"
  "v_pk_fma_f32 v[214:215], v[216:217], v[132:133], v[214:215] op_sel:[0,0,0] op_sel_hi:[1,0,1]\n\t"
  "s_waitcnt vmcnt(10)\n\t"
  "v_lshlrev_b32_e32 v216, 16, v84\n\t"
  "v_and_b32_e32 v217, s31, v84\n\t"
  "v_pk_fma_f32 v[208:209], v[216:217], v[132:133], v[208:209] op_sel:[0,1,0] op_sel_hi:[1,1,1]\n\t"
  "v_lshlrev_b32_e32 v216, 16, v85\n\t"
  "v_and_b32_e32 v217, s31, v85\n\t"
  "v_pk_fma_f32 v[210:211], v[216:217], v[132:133], v[210:211] op_sel:[0,1,0] op_sel_hi:[1,1,1]\n\t"
  "v_lshlrev_b32_e32 v216, 16, v86\n\t"
  "v_and_b32_e32 v217, s31, v86\n\t"
  "v_pk_fma_f32 v[212:213], v[216:217], v[132:133], v[212:213] op_sel:[0,1,0] op_sel_hi:[1,1,1]\n\t"
  "v_lshlrev_b32_e32 v216, 16, v87\n\t"
  "v_and_b32_e32 v217, s31, v87\n\t"
  "v_pk_fma_f32 v[214:215], v[216:217], v[132:133], v[214:215] op_sel:[0,1,0] op_sel_hi:[1,1,1]\n\t"
  "s_waitcnt vmcnt(9)\n\t"
  "v_lshlrev_b32_e32 v216, 16, v88\n\t"
  "v_and_b32_e32 v217, s31, v88\n\t"
  "v_pk_fma_f32 v[208:209], v[216:217], v[134:135], v[208:209] op_sel:[0,0,0] op_sel_hi:[1,0,1]\n\t"
  "v_lshlrev_b32_e32 v216, 16, v89\n\t"
  "v_and_b32_e32 v217, s31, v89\n\t"
  "v_pk_fma_f32 v[210:211], v[216:217], v[134:135], v[210:211] op_sel:[0,0,0] op_sel_hi:[1,0,1]\n\t"
  "v_lshlrev_b32_e32 v216, 16, v90\n\t"
  "v_and_b32_e32 v217, s31, v90\n\t"
  "v_pk_fma_f32 v[212:213], v[216:217], v[134:135], v[212:213] op_sel:[0,0,0] op_sel_hi:[1,0,1]\n\t"
  "v_lshlrev_b32_e32 v216, 16, v91\n\t"
  "v_and_b32_e32 v217, s31, v91\n\t"
  "v_pk_fma_f32 v[214:215], v[216:217], v[134:135], v[214:215] op_sel:[0,0,0] op_sel_hi:[1,0,1]\n\t"
  "s_waitcnt vmcnt(8)\n\t"
  "v_lshlrev_b32_e32 v216, 16, v92\n\t"
  "v_and_b32_e32 v217, s31, v92\n\t"
  "v_pk_fma_f32 v[208:209], v[216:217], v[134:135], v[208:209] op_sel:[0,1,0] op_sel_hi:[1,1,1]\n\t"
  "v_lshlrev_b32_e32 v216, 16, v93\n\t"
  "v_and_b32_e32 v217, s31, v93\n\t"
  "v_pk_fma_f32 v[210:211], v[216:217], v[134:135], v[210:211] op_sel:[0,1,0] op_sel_hi:[1,1,1]\n\t"
  "v_lshlrev_b32_e32 v216, 16, v94\n\t"
  "v_and_b32_e32 v217, s31, v94\n\t"
  "v_pk_fma_f32 v[212:213], v[216:217], v[134:135], v[212:213] op_sel:[0,1,0] op_sel_hi:[1,1,1]\n\t"
  "v_lshlrev_b32_e32 v216, 16, v95\n\t"
  "v_and_b32_e32 v217, s31, v95\n\t"
  "v_pk_fma_f32 v[214:215], v[216:217], v[134:135], v[214:215] op_sel:[0,1,0] op_sel_hi:[1,1,1]\n\t"
  "s_waitcnt vmcnt(7)\n\t"
  "v_lshlrev_b32_e32 v216, 16, v96\n\t"
  "v_and_b32_e32 v217, s31, v96\n\t"
  "v_pk_fma_f32 v[208:209], v[216:217], v[136:137], v[208:209] op_sel:[0,0,0] op_sel_hi:[1,0,1]\n\t"
  "v_lshlrev_b32_e32 v216, 16, v97\n\t"
  "v_and_b32_e32 v217, s31, v97\n\t"
  "v_pk_fma_f32 v[210:211], v[216:217], v[136:137], v[210:211] op_sel:[0,0,0] op_sel_hi:[1,0,1]\n\t"
  "v_lshlrev_b32_e32 v216, 16, v98\n\t"
  "v_and_b32_e32 v217, s31, v98\n\t"
  "v_pk_fma_f32 v[212:213], v[216:217], v[136:137], v[212:213] op_sel:[0,0,0] op_sel_hi:[1,0,1]\n\t"
  "v_lshlrev_b32_e32 v216, 16, v99\n\t"
  "v_and_b32_e32 v217, s31, v99\n\t"
  "v_pk_fma_f32 v[214:215], v[216:217], v[136:137], v[214:215] op_sel:[0,0,0] op_sel_hi:[1,0,1]\n\t"
  "s_waitcnt vmcnt(6)\n\t"
  "v_lshlrev_b32_e32 v216, 16, v100\n\t"
  "v_and_b32_e32 v217, s31, v100\n\t"
  "v_pk_fma_f32 v[208:209], v[216:217], v[136:137], v[208:209] op_sel:[0,1,0] op_sel_hi:[1,1,1]\n\t"
  "v_lshlrev_b32_e32 v216, 16, v101\n\t"
  "v_and_b32_e32 v217, s31, v101\n\t"
  "v_pk_fma_f32 v[210:211], v[216:217], v[136:137], v[210:211] op_sel:[0,1,0] op_sel_hi:[1,1,1]\n\t"
  "v_lshlrev_b32_e32 v216, 16, v102\n\t"
  "v_and_b32_e32 v217, s31, v102\n\t"
  "v_pk_fma_f32 v[212:213], v[216:217], v[136:137], v[212:213] op_sel:[0,1,0] op_sel_hi:[1,1,1]\n\t"
  "v_lshlrev_b32_e32 v216, 16, v103\n\t"
  "v_and_b32_e32 v217, s31, v103\n\t"
  "v_pk_fma_f32 v[214:215], v[216:217], v[136:137], v[214:215] op_sel:[0,1,0] op_sel_hi:[1,1,1]\n\t"
  "s_waitcnt vmcnt(5)\n\t"
  "v_lshlrev_b32_e32 v216, 16, v104\n\t"
  "v_and_b32_e32 v217, s31, v104\n\t"
  "v_pk_fma_f32 v[208:209], v[216:217], v[138:139], v[208:209] op_sel:[0,0,0] op_sel_hi:[1,0,1]\n\t"
  "v_lshlrev_b32_e32 v216, 16, v105\n\t"
  "v_and_b32_e32 v217, s31, v105\n\t"
  "v_pk_fma_f32 v[210:211], v[216:217], v[138:139], v[210:211] op_sel:[0,0,0] op_sel_hi:[1,0,1]\n\t"
  "v_lshlrev_b32_e32 v216, 16, v106\n\t"
  "v_and_b32_e32 v217, s31, v106\n\t"
  "v_pk_fma_f32 v[212:213], v[216:217], v[138:139], v[212:213] op_sel:[0,0,0] op_sel_hi:[1,0,1]\n\t"
  "v_lshlrev_b32_e32 v216, 16, v107\n\t"
  "v_and_b32_e32 v217, s31, v107\n\t"
  "v_pk_fma_f32 v[214:215], v[216:217], v[138:139], v[214:215] op_sel:[0,0,0] op_sel_hi:[1,0,1]\n\t"
  "s_waitcnt vmcnt(4)\n\t"
  "v_lshlrev_b32_e32 v216, 16, v108\n\t"
  "v_and_b32_e32 v217, s31, v108\n\t"
  "v_pk_fma_f32 v[208:209], v[216:217], v[138:139], v[208:209] op_sel:[0,1,0] op_sel_hi:[1,1,1]\n\t"
  "v_lshlrev_b32_e32 v216, 16, v109\n\t"
  "v_and_b32_e32 v217, s31, v109\n\t"
  "v_pk_fma_f32 v[210:211], v[216:217], v[138:139], v[210:211] op_sel:[0,1,0] op_sel_hi:[1,1,1]\n\t"
  "v_lshlrev_b32_e32 v216, 16, v110\n\t"
  "v_and_b32_e32 v217, s31, v110\n\t"
  "v_pk_fma_f32 v[212:213], v[216:217], v[138:139], v[212:213] op_sel:[0,1,0] op_sel_hi:[1,1,1]\n\t"
  "v_lshlrev_b32_e32 v216, 16, v111\n\t"
  "v_and_b32_e32 v217, s31, v111\n\t"
  "v_pk_fma_f32 v[214:215], v[216:217], v[138:139], v[214:215] op_sel:[0,1,0] op_sel_hi:[1,1,1]\n\t"
  "s_waitcnt vmcnt(3)\n\t"
  "v_lshlrev_b32_e32 v216, 16, v112\n\t"
  "v_and_b32_e32 v217, s31, v112\n\t"
  "v_pk_fma_f32 v[208:209], v[216:217], v[140:141], v[208:209] op_sel:[0,0,0] op_sel_hi:[1,0,1]\n\t"
  "v_lshlrev_b32_e32 v216, 16, v113\n\t"
  "v_and_b32_e32 v217, s31, v113\n\t"
  "v_pk_fma_f32 v[210:211], v[216:217], v[140:141], v[210:211] op_sel:[0,0,0] op_sel_hi:[1,0,1]\n\t"
  "v_lshlrev_b32_e32 v216, 16, v114\n\t"
  "v_and_b32_e32 v217, s31, v114\n\t"
  "v_pk_fma_f32 v[212:213], v[216:217], v[140:141], v[212:213] op_sel:[0,0,0] op_sel_hi:[1,0,1]\n\t"
  "v_lshlrev_b32_e32 v216, 16, v115\n\t"
  "v_and_b32_e32 v217, s31, v115\n\t"
  "v_pk_fma_f32 v[214:215], v[216:217], v[140:141], v[214:215] op_sel:[0,0,0] op_sel_hi:[1,0,1]\n\t"
  "s_waitcnt vmcnt(2)\n\t"
  "v_lshlrev_b32_e32 v216, 16, v116\n\t"
  "v_and_b32_e32 v217, s31, v116\n\t"
  "v_pk_fma_f32 v[208:209], v[216:217], v[140:141], v[208:209] op_sel:[0,1,0] op_sel_hi:[1,1,1]\n\t"
  "v_lshlrev_b32_e32 v216, 16, v117\n\t"
  "v_and_b32_e32 v217, s31, v117\n\t"
  "v_pk_fma_f32 v[210:211], v[216:217], v[140:141], v[210:211] op_sel:[0,1,0] op_sel_hi:[1,1,1]\n\t"
  "v_lshlrev_b32_e32 v216, 16, v118\n\t"
  "v_and_b32_e32 v217, s31, v118\n\t"
  "v_pk_fma_f32 v[212:213], v[216:217], v[140:141], v[212:213] op_sel:[0,1,0] op_sel_hi:[1,1,1]\n\t"
  "v_lshlrev_b32_e32 v216, 16, v119\n\t"
  "v_and_b32_e32 v217, s31, v119\n\t"
  "v_pk_fma_f32 v[214:215], v[216:217], v[140:141], v[214:215] op_sel:[0,1,0] op_sel_hi:[1,1,1]\n\t"
  "s_waitcnt vmcnt(1)\n\t"
  "v_lshlrev_b32_e32 v216, 16, v120\n\t"
  "v_and_b32_e32 v217, s31, v120\n\t"
  "v_pk_fma_f32 v[208:209], v[216:217], v[142:143], v[208:209] op_sel:[0,0,0] op_sel_hi:[1,0,1]\n\t"
  "v_lshlrev_b32_e32 v216, 16, v121\n\t"
  "v_and_b32_e32 v217, s31, v121\n\t"
  "v_pk_fma_f32 v[210:211], v[216:217], v[142:143], v[210:211] op_sel:[0,0,0] op_sel_hi:[1,0,1]\n\t"
  "v_lshlrev_b32_e32 v216, 16, v122\n\t"
  "v_and_b32_e32 v217, s31, v122\n\t"
  "v_pk_fma_f32 v[212:213], v[216:217], v[142:143], v[212:213] op_sel:[0,0,0] op_sel_hi:[1,0,1]\n\t"
  "v_lshlrev_b32_e32 v216, 16, v123\n\t"
  "v_and_b32_e32 v217, s31, v123\n\t"
  "v_pk_fma_f32 v[214:215], v[216:217], v[142:143], v[214:215] op_sel:[0,0,0] op_sel_hi:[1,0,1]\n\t"
  "s_waitcnt vmcnt(0)\n\t"
  "v_lshlrev_b32_e32 v216, 16, v124\n\t"
  "v_and_b32_e32 v217, s31, v124\n\t"
  "v_pk_fma_f32 v[208:209], v[216:217], v[142:143], v[208:209] op_sel:[0,1,0] op_sel_hi:[1,1,1]\n\t"
  "v_lshlrev_b32_e32 v216, 16, v125\n\t"
  "v_and_b32_e32 v217, s31, v125\n\t"
  "v_pk_fma_f32 v[210:211], v[216:217], v[142:143], v[210:211] op_sel:[0,1,0] op_sel_hi:[1,1,1]\n\t"
  "v_lshlrev_b32_e32 v216, 16, v126\n\t"
  "v_and_b32_e32 v217, s31, v126\n\t"
  "v_pk_fma_f32 v[212:213], v[216:217], v[142:143], v[212:213] op_sel:[0,1,0] op_sel_hi:[1,1,1]\n\t"
  "v_lshlrev_b32_e32 v216, 16, v127\n\t"
  "v_and_b32_e32 v217, s31, v127\n\t"
  "v_pk_fma_f32 v[214:215], v[216:217], v[142:143], v[214:215] op_sel:[0,1,0] op_sel_hi:[1,1,1]\n\t"
  ".LjR:\n\t"
  "s_nop 1\n\t"
  "v_pk_fma_f32 v[208:209], v[208:209], v[160:161], v[24:25]\n\t"
  "v_pk_fma_f32 v[210:211], v[210:211], v[162:163], v[26:27]\n\t"
  "v_pk_fma_f32 v[212:213], v[212:213], v[164:165], v[28:29]\n\t"
  "v_pk_fma_f32 v[214:215], v[214:215], v[166:167], v[30:31]\n\t"
  "v_pk_add_f32 v[4:5], v[208:209], v[210:211]\n\t"
  "v_pk_mul_f32 v[6:7], v[208:209], v[208:209]\n\t"
  "v_pk_add_f32 v[4:5], v[4:5], v[212:213]\n\t"
  "v_pk_fma_f32 v[6:7], v[210:211], v[210:211], v[6:7]\n\t"
  "v_pk_add_f32 v[4:5], v[4:5], v[214:215]\n\t"
  "v_pk_fma_f32 v[6:7], v[212:213], v[212:213], v[6:7]\n\t"
  "v_add_f32_e32 v4, v4, v5\n\t"
  "v_pk_fma_f32 v[6:7], v[214:215], v[214:215], v[6:7]\n\t"
  "v_add_f32_e32 v5, v6, v7\n\t"
  "v_pk_add_f32 v[0:1], v[0:1], v[4:5]\n\t"
  "s_nop 1\n\t"
  "v_add_f32_dpp v0, v0, v0 row_ror:8 row_mask:0xf bank_mask:0xf\n\t"
  "v_add_f32_dpp v1, v1, v1 row_ror:8 row_mask:0xf bank_mask:0xf\n\t"
  "s_nop 1\n\t"
  "v_permlane16_swap_b32 v0, v1\n\t"
  "v_add_f32_e32 v0, v0, v1\n\t"
  "v_mov_b32_e32 v1, v0\n\t"
  "s_nop 1\n\t"
  "v_permlane32_swap_b32 v1, v0\n\t"
  "v_add_f32_e32 v0, v0, v1\n\t"
  "v_bfe_u32 v13, v254, 4, 1\n\t"
  "v_lshl_add_u32 v15, v13, 2, v15\n\t"
  "ds_write_b32 v15, v0\n\t"
  "s_waitcnt lgkmcnt(0)\n\t"
  "s_barrier\n\t"
  "ds_read_b128 v[4:7], v14\n\t"
  "ds_read_b128 v[8:11], v14 offset:16\n\t"
  "s_waitcnt lgkmcnt(1)\n\t"
  "v_pk_add_f32 v[4:5], v[4:5], v[6:7]\n\t"
  "s_waitcnt lgkmcnt(0)\n\t"
  "v_pk_add_f32 v[8:9], v[8:9], v[10:11]\n\t"
  "v_pk_add_f32 v[8:9], v[8:9], v[4:5]\n\t"
  "v_mul_f32_e32 v8, 0x3b124925, v8\n\t"
  "v_mul_f32_e32 v9, 0x3b124925, v9\n\t"
  "v_fma_f32 v9, -v8, v8, v9\n\t"
  "v_max_f32_e32 v9, 0, v9\n\t"
  "v_add_f32_e32 v9, 0x3727c5ac, v9\n\t"
  "v_rsq_f32_e32 v9, v9\n\t"
  "s_nop 0\n\t"
  "v_mul_f32_e32 v9, v12, v9\n\t"
  "v_mul_f32_e32 v8, v8, v9\n\t"
  "v_pk_fma_f32 v[24:25], v[208:209], v[8:9], v[8:9] op_sel:[0,1,0] op_sel_hi:[1,1,0] neg_lo:[0,0,1] neg_hi:[0,0,1]\n\t"
  "v_pk_fma_f32 v[26:27], v[210:211], v[8:9], v[8:9] op_sel:[0,1,0] op_sel_hi:[1,1,0] neg_lo:[0,0,1] neg_hi:[0,0,1]\n\t"
  "v_pk_fma_f32 v[28:29], v[212:213], v[8:9], v[8:9] op_sel:[0,1,0] op_sel_hi:[1,1,0] neg_lo:[0,0,1] neg_hi:[0,0,1]\n\t"
  "v_pk_fma_f32 v[30:31], v[214:215], v[8:9], v[8:9] op_sel:[0,1,0] op_sel_hi:[1,1,0] neg_lo:[0,0,1] neg_hi:[0,0,1]\n\t"
  "v_pk_fma_f32 v[24:25], v[24:25], v[192:193], v[176:177]\n\t"
  "v_pk_fma_f32 v[26:27], v[26:27], v[194:195], v[178:179]\n\t"
  "v_pk_fma_f32 v[28:29], v[28:29], v[196:197], v[180:181]\n\t"
  "v_pk_fma_f32 v[30:31], v[30:31], v[198:199], v[182:183]\n\t"
  "v_cvt_pk_bf16_f32 v36, v24, v25\n\t"
  "v_cvt_pk_bf16_f32 v37, v26, v27\n\t"
  "v_cvt_pk_bf16_f32 v38, v28, v29\n\t"
  "v_cvt_pk_bf16_f32 v39, v30, v31\n\t"
  "buffer_store_dwordx4 v[36:39], v222, s[16:19], s97 offen sc1\n\t"
  "s_cmp_eq_u32 s62, 3\n\t"
  "s_cbranch_scc1 .Lskh\n\t"
  "v_pk_fma_f32 v[16:17], v[200:201], v[8:9], v[8:9] op_sel:[0,1,0] op_sel_hi:[1,1,0] neg_lo:[0,0,1] neg_hi:[0,0,1]\n\t"
  "v_pk_fma_f32 v[18:19], v[202:203], v[8:9], v[8:9] op_sel:[0,1,0] op_sel_hi:[1,1,0] neg_lo:[0,0,1] neg_hi:[0,0,1]\n\t"
  "v_pk_fma_f32 v[20:21], v[204:205], v[8:9], v[8:9] op_sel:[0,1,0] op_sel_hi:[1,1,0] neg_lo:[0,0,1] neg_hi:[0,0,1]\n\t"
  "v_pk_fma_f32 v[22:23], v[206:207], v[8:9], v[8:9] op_sel:[0,1,0] op_sel_hi:[1,1,0] neg_lo:[0,0,1] neg_hi:[0,0,1]\n\t"
  "v_pk_fma_f32 v[16:17], v[16:17], v[184:185], v[168:169]\n\t"
  "v_pk_fma_f32 v[18:19], v[18:19], v[186:187], v[170:171]\n\t"
  "v_pk_fma_f32 v[20:21], v[20:21], v[188:189], v[172:173]\n\t"
  "v_pk_fma_f32 v[22:23], v[22:23], v[190:191], v[174:175]\n\t"
  "v_cvt_pk_bf16_f32 v32, v16, v17\n\t"
  "v_cvt_pk_bf16_f32 v33, v18, v19\n\t"
  "v_cvt_pk_bf16_f32 v34, v20, v21\n\t"
  "v_cvt_pk_bf16_f32 v35, v22, v23\n\t"
  "buffer_store_dwordx4 v[32:35], v222, s[16:19], s96 offen sc1\n\t"
  ".Lskh:\n\t"
  "s_mov_b32 s31, s45\n\t"
  "s_andn2_b32 s64, s59, 7\n\t"
  "s_lshl_b32 s64, s64, 5\n\t"
  "s_and_b32 s65, s59, 7\n\t"
  "s_add_u32 s64, s64, s65\n\t"
  "s_lshl_b32 s64, s64, 2\n\t"
  "s_add_u32 s64, s64, 331776\n\t"
  "v_lshlrev_b32_e32 v58, 7, v254\n\t"
  "v_add_u32_e32 v58, s64, v58\n\t"
  "v_mov_b32_e32 v59, s69\n\t"
  "s_lshl_b32 s64, s59, 7\n\t"
  "s_add_u32 s64, s64, 331776\n\t"
  "v_and_b32_e32 v216, 7, v254\n\t"
  "v_lshlrev_b32_e32 v216, 2, v216\n\t"
  "v_add_u32_e32 v216, s64, v216\n\t"
  "s_mov_b32 s30, 1048576\n\t"
  "s_waitcnt vmcnt(0)\n\t"
  "s_barrier\n\t"
  "s_cmp_eq_u32 s62, 3\n\t"
  "s_cbranch_scc1 .LqB\n\t"
  "s_barrier\n\t"
  "s_branch .LjB\n\t"
  ".LqB:\n\t"
  "s_nop 7\n\t"
  "s_mov_b64 exec, 255\n\t"
  "global_store_dword v58, v59, s[98:99] sc1\n\t"
  "s_mov_b64 exec, -1\n\t"
  ".Lpoll331776:\n\t"
  "global_load_dword v217, v216, s[98:99] sc1\n\t"
  "s_waitcnt vmcnt(0)\n\t"
  "v_xor_b32_e32 v217, s69, v217\n\t"
  "v_cmp_ne_u32_e32 vcc, 0, v217\n\t"
  "s_sub_u32 s30, s30, 1\n\t"
  "s_nop 2\n\t"
  "s_cbranch_vccz .Lpollk331776\n\t"
  "s_sleep 1\n\t"
  "s_cmp_lg_u32 s30, 0\n\t"
  "s_cbranch_scc1 .Lpoll331776\n\t"
  ".Lpollk331776:\n\t"
  "s_barrier\n\t"
  ".LjB:\n\t"
  "s_nop 1\n\t"
  "buffer_load_dwordx4 a[0:3], v251, s[16:19], s31 offen sc1\n\t"
  "buffer_load_dwordx4 a[4:7], v251, s[16:19], s34 offen sc1\n\t"
  "buffer_load_dwordx4 a[8:11], v251, s[16:19], s31 offen offset:1024 sc1\n\t"
  "buffer_load_dwordx4 a[12:15], v251, s[16:19], s34 offen offset:1024 sc1\n\t"
  "s_add_u32 s31, s31, 4096\n\t"
  "s_add_u32 s34, s34, 4096\n\t"
  "buffer_load_dwordx4 a[32:35], v251, s[16:19], s31 offen sc1\n\t"
  "buffer_load_dwordx4 a[36:39], v251, s[16:19], s34 offen sc1\n\t"
  "buffer_load_dwordx4 a[40:43], v251, s[16:19], s31 offen offset:1024 sc1\n\t"
  "buffer_load_dwordx4 a[44:47], v251, s[16:19], s34 offen offset:1024 sc1\n\t"
  "s_add_u32 s31, s31, 4096\n\t"
  "s_add_u32 s34, s34, 4096\n\t"
  "s_waitcnt vmcnt(4)\n\t"
  "ds_write_b128 v56, a[0:3]\n\t"
  "ds_write_b128 v56, a[4:7] offset:1024\n\t"
  "ds_write_b128 v56, a[8:11] offset:2048\n\t"
  "ds_write_b128 v56, a[12:15] offset:3072\n\t"
  "s_waitcnt lgkmcnt(0)\n\t"
  "s_barrier\n\t"
  "ds_read_b128 a[0:3], v57\n\t"
  "ds_read_b128 a[4:7], v57 offset:1024\n\t"
  "ds_read_b128 a[8:11], v57 offset:2048\n\t"
  "ds_read_b128 a[12:15], v57 offset:3072\n\t"
  "ds_read_b64_tr_b16 v[128:129], v236\n\t"
  "ds_read_b64_tr_b16 v[130:131], v236 offset:7168\n\t"
  "ds_read_b64_tr_b16 v[132:133], v236 offset:14336\n\t"
  "ds_read_b64_tr_b16 v[134:135], v236 offset:21504\n\t"
  "ds_read_b64_tr_b16 v[136:137], v237\n\t"
  "ds_read_b64_tr_b16 v[138:139], v237 offset:7168\n\t"
  "ds_read_b64_tr_b16 v[140:141], v237 offset:14336\n\t"
  "ds_read_b64_tr_b16 v[142:143], v237 offset:21504\n\t"
  "ds_read_b128 a[16:19], v57 offset:32768\n\t"
  "ds_read_b128 a[20:23], v57 offset:33792\n\t"
  "ds_read_b128 a[24:27], v57 offset:34816\n\t"
  "s_waitcnt lgkmcnt(13)\n\t"
  "ds_read_b128 a[28:31], v57 offset:35840\n\t"
  "ds_read_b64_tr_b16 v[144:145], v236 offset:512\n\t"
  "s_waitcnt lgkmcnt(13)\n\t"
  "ds_read_b64_tr_b16 v[146:147], v236 offset:7680\n\t"
  "ds_read_b64_tr_b16 v[148:149], v236 offset:14848\n\t"
  "s_waitcnt lgkmcnt(13)\n\t"
  "ds_read_b64_tr_b16 v[150:151], v236 offset:22016\n\t"
  "ds_read_b64_tr_b16 v[152:153], v237 offset:512\n\t"
  "s_waitcnt lgkmcnt(13)\n\t"
  "ds_read_b64_tr_b16 v[154:155], v237 offset:7680\n\t"
  "s_waitcnt lgkmcnt(10)\n\t"
  "v_mfma_f32_16x16x32_bf16 v[0:3], v[128:131], a[0:3], 0\n\t"
  "ds_read_b64_tr_b16 v[156:157], v237 offset:14848\n\t"
  "v_mfma_f32_16x16x32_bf16 v[4:7], v[128:131], a[4:7], 0\n\t"
  "ds_read_b64_tr_b16 v[158:159], v237 offset:22016\n\t"
  "buffer_load_dwordx4 a[64:67], v251, s[16:19], s31 offen sc1\n\t"
  "v_mfma_f32_16x16x32_bf16 v[4:7], v[132:135], a[12:15], v[4:7]\n\t"
  "ds_read_b64_tr_b16 v[160:161], v236 offset:1024\n\t"
  "v_mfma_f32_16x16x32_bf16 v[0:3], v[132:135], a[8:11], v[0:3]\n\t"
  "ds_read_b64_tr_b16 v[162:163], v236 offset:8192\n\t"
  "buffer_load_dwordx4 a[68:71], v251, s[16:19], s34 offen sc1\n\t"
  "v_mfma_f32_16x16x32_bf16 v[8:11], v[136:139], a[0:3], 0\n\t"
  "ds_read_b64_tr_b16 v[164:165], v236 offset:15360\n\t"
  "v_mfma_f32_16x16x32_bf16 v[12:15], v[136:139], a[4:7], 0\n\t"
  "s_waitcnt lgkmcnt(13)\n\t"
  "ds_read_b64_tr_b16 v[166:167], v236 offset:22528\n\t"
  "buffer_load_dwordx4 a[72:75], v251, s[16:19], s31 offen offset:1024 sc1\n\t"
  "v_mfma_f32_16x16x32_bf16 v[12:15], v[140:143], a[12:15], v[12:15]\n\t"
  "ds_read_b64_tr_b16 v[168:169], v237 offset:1024\n\t"
  "v_mfma_f32_16x16x32_bf16 v[8:11], v[140:143], a[8:11], v[8:11]\n\t"
  "s_waitcnt lgkmcnt(13)\n\t"
  "ds_read_b64_tr_b16 v[170:171], v237 offset:8192\n\t"
  "buffer_load_dwordx4 a[76:79], v251, s[16:19], s34 offen offset:1024 sc1\n\t"
  "s_waitcnt lgkmcnt(6)\n\t"
  "v_mfma_f32_16x16x32_bf16 v[16:19], v[144:147], a[0:3], 0\n\t"
  "ds_read_b64_tr_b16 v[172:173], v237 offset:15360\n\t"
  "v_mfma_f32_16x16x32_bf16 v[20:23], v[144:147], a[4:7], 0\n\t"
  "ds_read_b64_tr_b16 v[174:175], v237 offset:22528\n\t"
  "s_add_u32 s31, s31, s44\n\t"
  "s_add_u32 s34, s34, s44\n\t"
  "buffer_load_dwordx4 a[96:99], v251, s[16:19], s31 offen sc1\n\t"
  "v_mfma_f32_16x16x32_bf16 v[20:23], v[148:151], a[12:15], v[20:23]\n\t"
  "ds_read_b64_tr_b16 v[176:177], v236 offset:1536\n\t"
  "v_mfma_f32_16x16x32_bf16 v[16:19], v[148:151], a[8:11], v[16:19]\n\t"
  "ds_read_b64_tr_b16 v[178:179], v236 offset:8704\n\t"
  "buffer_load_dwordx4 a[100:103], v251, s[16:19], s34 offen sc1\n\t"
  "v_mfma_f32_16x16x32_bf16 v[24:27], v[152:155], a[0:3], 0\n\t"
  "ds_read_b64_tr_b16 v[180:181], v236 offset:15872\n\t"
  "s_barrier\n\t"
  "v_mfma_f32_16x16x32_bf16 v[28:31], v[152:155], a[4:7], 0\n\t"
  "ds_read_b64_tr_b16 v[182:183], v236 offset:23040\n\t"
  "buffer_load_dwordx4 a[104:107], v251, s[16:19], s31 offen offset:1024 sc1\n\t"
  "v_mfma_f32_16x16x32_bf16 v[28:31], v[156:159], a[12:15], v[28:31]\n\t"
  "ds_read_b64_tr_b16 v[184:185], v238\n\t"
  "s_waitcnt vmcnt(7)\n\t"
  "ds_write_b128 v56, a[32:35]\n\t"
  "v_mfma_f32_16x16x32_bf16 v[24:27], v[156:159], a[8:11], v[24:27]\n\t"
  "ds_read_b64_tr_b16 v[186:187], v238 offset:7168\n\t"
  "buffer_load_dwordx4 a[108:111], v251, s[16:19], s34 offen offset:1024 sc1\n\t"
  "s_waitcnt lgkmcnt(7)\n\t"
  "v_mfma_f32_16x16x32_bf16 v[32:35], v[160:163], a[0:3], 0\n\t"
  "ds_read_b64_tr_b16 v[188:189], v238 offset:14336\n\t"
  "ds_write_b128 v56, a[36:39] offset:1024\n\t"
  "v_mfma_f32_16x16x32_bf16 v[36:39], v[160:163], a[4:7], 0\n\t"
  "ds_read_b64_tr_b16 v[190:191], v238 offset:21504\n\t"
  "v_mfma_f32_16x16x32_bf16 v[36:39], v[164:167], a[12:15], v[36:39]\n\t"
  "ds_read_b64_tr_b16 v[128:129], v239\n\t"
  "ds_write_b128 v56, a[40:43] offset:2048\n\t"
  "v_mfma_f32_16x16x32_bf16 v[32:35], v[164:167], a[8:11], v[32:35]\n\t"
  "ds_read_b64_tr_b16 v[130:131], v239 offset:7168\n\t"
  "v_mfma_f32_16x16x32_bf16 v[40:43], v[168:171], a[0:3], 0\n\t"
  "ds_read_b64_tr_b16 v[132:133], v239 offset:14336\n\t"
  "ds_write_b128 v56, a[44:47] offset:3072\n\t"
  "v_mfma_f32_16x16x32_bf16 v[44:47], v[168:171], a[4:7], 0\n\t"
  "s_waitcnt lgkmcnt(13)\n\t"
  "ds_read_b64_tr_b16 v[134:135], v239 offset:21504\n\t"
  "v_mfma_f32_16x16x32_bf16 v[44:47], v[172:175], a[12:15], v[44:47]\n\t"
  "ds_read_b64_tr_b16 v[136:137], v238 offset:512\n\t"
  "v_mfma_f32_16x16x32_bf16 v[40:43], v[172:175], a[8:11], v[40:43]\n\t"
  "s_waitcnt lgkmcnt(13)\n\t"
  "ds_read_b64_tr_b16 v[138:139], v238 offset:7680\n\t"
  "s_waitcnt lgkmcnt(8)\n\t"
  "v_mfma_f32_16x16x32_bf16 v[48:51], v[176:179], a[0:3], 0\n\t"
  "ds_read_b64_tr_b16 v[140:141], v238 offset:14848\n\t"
  "v_mfma_f32_16x16x32_bf16 v[52:55], v[176:179], a[4:7], 0\n\t"
  "ds_read_b64_tr_b16 v[142:143], v238 offset:22016\n\t"
  "v_mfma_f32_16x16x32_bf16 v[52:55], v[180:183], a[12:15], v[52:55]\n\t"
  "ds_read_b64_tr_b16 v[144:145], v239 offset:512\n\t"
  "v_mfma_f32_16x16x32_bf16 v[48:51], v[180:183], a[8:11], v[48:51]\n\t"
  "ds_read_b64_tr_b16 v[146:147], v239 offset:7680\n\t"
  "v_mfma_f32_16x16x32_bf16 v[0:3], v[184:187], a[16:19], v[0:3]\n\t"
  "ds_read_b64_tr_b16 v[148:149], v239 offset:14848\n\t"
  "v_mfma_f32_16x16x32_bf16 v[4:7], v[184:187], a[20:23], v[4:7]\n\t"
  "ds_read_b64_tr_b16 v[150:151], v239 offset:22016\n\t"
  "v_mfma_f32_16x16x32_bf16 v[4:7], v[188:191], a[28:31], v[4:7]\n\t"
  "ds_read_b64_tr_b16 v[152:153], v238 offset:1024\n\t"
  "v_mfma_f32_16x16x32_bf16 v[0:3], v[188:191], a[24:27], v[0:3]\n\t"
  "s_waitcnt lgkmcnt(13)\n\t"
  "ds_read_b64_tr_b16 v[154:155], v238 offset:8192\n\t"
  "s_waitcnt lgkmcnt(6)\n\t"
  "v_mfma_f32_16x16x32_bf16 v[8:11], v[128:131], a[16:19], v[8:11]\n\t"
  "ds_read_b64_tr_b16 v[156:157], v238 offset:15360\n\t"
  "v_mfma_f32_16x16x32_bf16 v[12:15], v[128:131], a[20:23], v[12:15]\n\t"
  "ds_read_b64_tr_b16 v[158:159], v238 offset:22528\n\t"
  "v_mfma_f32_16x16x32_bf16 v[12:15], v[132:135], a[28:31], v[12:15]\n\t"
  "ds_read_b64_tr_b16 v[160:161], v239 offset:1024\n\t"
  "v_mfma_f32_16x16x32_bf16 v[8:11], v[132:135], a[24:27], v[8:11]\n\t"
  "ds_read_b64_tr_b16 v[162:163], v239 offset:8192\n\t"
  "v_mfma_f32_16x16x32_bf16 v[16:19], v[136:139], a[16:19], v[16:19]\n\t"
  "ds_read_b64_tr_b16 v[164:165], v239 offset:15360\n\t"
  "v_mfma_f32_16x16x32_bf16 v[20:23], v[136:139], a[20:23], v[20:23]\n\t"
  "ds_read_b64_tr_b16 v[166:167], v239 offset:22528\n\t"
  "v_mfma_f32_16x16x32_bf16 v[20:23], v[140:143], a[28:31], v[20:23]\n\t"
  "ds_read_b64_tr_b16 v[168:169], v238 offset:1536\n\t"
  "v_mfma_f32_16x16x32_bf16 v[16:19], v[140:143], a[24:27], v[16:19]\n\t"
  "ds_read_b64_tr_b16 v[170:171], v238 offset:8704\n\t"
  "s_waitcnt lgkmcnt(6)\n\t"
  "v_mfma_f32_16x16x32_bf16 v[24:27], v[144:147], a[16:19], v[24:27]\n\t"
  "ds_read_b64_tr_b16 v[172:173], v238 offset:15872\n\t"
  "s_barrier\n\t"
  "v_mfma_f32_16x16x32_bf16 v[28:31], v[144:147], a[20:23], v[28:31]\n\t"
  "ds_read_b64_tr_b16 v[174:175], v238 offset:23040\n\t"
  "ds_read_b128 a[32:35], v57\n\t"
  "s_add_u32 m0, s39, 0\n\t"
  "v_mfma_f32_16x16x32_bf16 v[28:31], v[148:151], a[28:31], v[28:31]\n\t"
  "ds_read_b64_tr_b16 v[176:177], v238 offset:32768\n\t"
  "ds_read_b128 a[36:39], v57 offset:1024\n\t"
  "buffer_load_dwordx4 v225, s[8:11], s28 offen lds\n\t"
  "v_mfma_f32_16x16x32_bf16 v[24:27], v[148:151], a[24:27], v[24:27]\n\t"
  "ds_read_b64_tr_b16 v[178:179], v238 offset:39936\n\t"
  "v_mfma_f32_16x16x32_bf16 v[32:35], v[152:155], a[16:19], v[32:35]\n\t"
  "ds_read_b64_tr_b16 v[180:181], v238 offset:47104\n\t"
  "ds_read_b128 a[40:43], v57 offset:2048\n\t"
  "v_mfma_f32_16x16x32_bf16 v[36:39], v[152:155], a[20:23], v[36:39]\n\t"
  "ds_read_b64_tr_b16 v[182:183], v238 offset:54272\n\t"
  "s_waitcnt lgkmcnt(13)\n\t"
  "ds_read_b128 a[44:47], v57 offset:3072\n\t"
  "v_mfma_f32_16x16x32_bf16 v[36:39], v[156:159], a[28:31], v[36:39]\n\t"
  "ds_read_b64_tr_b16 v[184:185], v239 offset:32768\n\t"
  "s_waitcnt lgkmcnt(13)\n\t"
  "ds_read_b128 a[48:51], v57 offset:32768\n\t"
  "buffer_load_dwordx4 v226, s[8:11], s28 offen offset:1024 lds\n\t"
  "v_mfma_f32_16x16x32_bf16 v[32:35], v[156:159], a[24:27], v[32:35]\n\t"
  "ds_read_b64_tr_b16 v[186:187], v239 offset:39936\n\t"
  "s_waitcnt lgkmcnt(11)\n\t"
  "v_mfma_f32_16x16x32_bf16 v[40:43], v[160:163], a[16:19], v[40:43]\n\t"
  "ds_read_b64_tr_b16 v[188:189], v239 offset:47104\n\t"
  "ds_read_b128 a[52:55], v57 offset:33792\n\t"
  "v_mfma_f32_16x16x32_bf16 v[44:47], v[160:163], a[20:23], v[44:47]\n\t"
  "ds_read_b64_tr_b16 v[190:191], v239 offset:54272\n\t"
  "ds_read_b128 a[56:59], v57 offset:34816\n\t"
  "v_mfma_f32_16x16x32_bf16 v[44:47], v[164:167], a[28:31], v[44:47]\n\t"
  "s_waitcnt lgkmcnt(13)\n\t"
  "ds_read_b64_tr_b16 v[128:129], v238 offset:33280\n\t"
  "ds_read_b128 a[60:63], v57 offset:35840\n\t"
  "buffer_load_dwordx4 v227, s[8:11], s28 offen offset:2048 lds\n\t"
  "v_mfma_f32_16x16x32_bf16 v[40:43], v[164:167], a[24:27], v[40:43]\n\t"
  "s_waitcnt lgkmcnt(13)\n\t"
  "ds_read_b64_tr_b16 v[130:131], v238 offset:40448\n\t"
  "v_mfma_f32_16x16x32_bf16 v[48:51], v[168:171], a[16:19], v[48:51]\n\t"
  "ds_read_b64_tr_b16 v[132:133], v238 offset:47616\n\t"
  "v_mfma_f32_16x16x32_bf16 v[52:55], v[168:171], a[20:23], v[52:55]\n\t"
  "s_waitcnt lgkmcnt(13)\n\t"
  "ds_read_b64_tr_b16 v[134:135], v238 offset:54784\n\t"
  "v_mfma_f32_16x16x32_bf16 v[52:55], v[172:175], a[28:31], v[52:55]\n\t"
  "ds_read_b64_tr_b16 v[136:137], v239 offset:33280\n\t"
  "buffer_load_dwordx4 v228, s[8:11], s28 offen offset:3072 lds\n\t"
  "v_mfma_f32_16x16x32_bf16 v[48:51], v[172:175], a[24:27], v[48:51]\n\t"
  "s_waitcnt lgkmcnt(13)\n\t"
  "ds_read_b64_tr_b16 v[138:139], v239 offset:40448\n\t"
  "s_waitcnt lgkmcnt(8)\n\t"
  "v_mfma_f32_16x16x32_bf16 v[0:3], v[176:179], a[32:35], v[0:3]\n\t"
  "ds_read_b64_tr_b16 v[140:141], v239 offset:47616\n\t"
  "v_mfma_f32_16x16x32_bf16 v[4:7], v[176:179], a[36:39], v[4:7]\n\t"
  "ds_read_b64_tr_b16 v[142:143], v239 offset:54784\n\t"
  "s_add_u32 m0, s39, 4096\n\t"
  "v_mfma_f32_16x16x32_bf16 v[4:7], v[180:183], a[44:47], v[4:7]\n\t"
  "ds_read_b64_tr_b16 v[144:145], v238 offset:33792\n\t"
  "buffer_load_dwordx4 v229, s[8:11], s28 offen lds\n\t"
  "v_mfma_f32_16x16x32_bf16 v[0:3], v[180:183], a[40:43], v[0:3]\n\t"
  "ds_read_b64_tr_b16 v[146:147], v238 offset:40960\n\t"
  "v_mfma_f32_16x16x32_bf16 v[8:11], v[184:187], a[32:35], v[8:11]\n\t"
  "ds_read_b64_tr_b16 v[148:149], v238 offset:48128\n\t"
  "v_mfma_f32_16x16x32_bf16 v[12:15], v[184:187], a[36:39], v[12:15]\n\t"
  "ds_read_b64_tr_b16 v[150:151], v238 offset:55296\n\t"
  "v_mfma_f32_16x16x32_bf16 v[12:15], v[188:191], a[44:47], v[12:15]\n\t"
  "ds_read_b64_tr_b16 v[152:153], v239 offset:33792\n\t"
  "buffer_load_dwordx4 v230, s[8:11], s28 offen offset:1024 lds\n\t"
  "v_mfma_f32_16x16x32_bf16 v[8:11], v[188:191], a[40:43], v[8:11]\n\t"
  "s_waitcnt lgkmcnt(13)\n\t"
  "ds_read_b64_tr_b16 v[154:155], v239 offset:40960\n\t"
  "s_waitcnt lgkmcnt(6)\n\t"
  "v_mfma_f32_16x16x32_bf16 v[16:19], v[128:131], a[32:35], v[16:19]\n\t"
  "ds_read_b64_tr_b16 v[156:157], v239 offset:48128\n\t"
  "v_mfma_f32_16x16x32_bf16 v[20:23], v[128:131], a[36:39], v[20:23]\n\t"
  "ds_read_b64_tr_b16 v[158:159], v239 offset:55296\n\t"
  "v_mfma_f32_16x16x32_bf16 v[20:23], v[132:135], a[44:47], v[20:23]\n\t"
  "ds_read_b64_tr_b16 v[160:161], v238 offset:34304\n\t"
  "buffer_load_dwordx4 v231, s[8:11], s28 offen offset:2048 lds\n\t"
  "v_mfma_f32_16x16x32_bf16 v[16:19], v[132:135], a[40:43], v[16:19]\n\t"
  "ds_read_b64_tr_b16 v[162:163], v238 offset:41472\n\t"
  "s_add_u32 s28, s28, 229376\n\t"
  "v_mfma_f32_16x16x32_bf16 v[24:27], v[136:139], a[32:35], v[24:27]\n\t"
  "ds_read_b64_tr_b16 v[164:165], v238 offset:48640\n\t"
  "s_barrier\n\t"
  "v_mfma_f32_16x16x32_bf16 v[28:31], v[136:139], a[36:39], v[28:31]\n\t"
  "ds_read_b64_tr_b16 v[166:167], v238 offset:55808\n\t"
  "s_add_u32 m0, s39, 65536\n\t"
  "v_mfma_f32_16x16x32_bf16 v[28:31], v[140:143], a[44:47], v[28:31]\n\t"
  "ds_read_b64_tr_b16 v[168:169], v240\n\t"
  "s_waitcnt vmcnt(11)\n\t"
  "ds_write_b128 v56, a[64:67]\n\t"
  "buffer_load_dwordx4 v225, s[8:11], s28 offen lds\n\t"
  "v_mfma_f32_16x16x32_bf16 v[24:27], v[140:143], a[40:43], v[24:27]\n\t"
  "ds_read_b64_tr_b16 v[170:171], v240 offset:7168\n\t"
  "s_waitcnt lgkmcnt(7)\n\t"
  "v_mfma_f32_16x16x32_bf16 v[32:35], v[144:147], a[32:35], v[32:35]\n\t"
  "ds_read_b64_tr_b16 v[172:173], v240 offset:14336\n\t"
  "ds_write_b128 v56, a[68:71] offset:1024\n\t"
  "v_mfma_f32_16x16x32_bf16 v[36:39], v[144:147], a[36:39], v[36:39]\n\t"
  "ds_read_b64_tr_b16 v[174:175], v240 offset:21504\n\t"
  "v_mfma_f32_16x16x32_bf16 v[36:39], v[148:151], a[44:47], v[36:39]\n\t"
  "ds_read_b64_tr_b16 v[176:177], v241\n\t"
  "ds_write_b128 v56, a[72:75] offset:2048\n\t"
  "buffer_load_dwordx4 v226, s[8:11], s28 offen offset:1024 lds\n\t"
  "v_mfma_f32_16x16x32_bf16 v[32:35], v[148:151], a[40:43], v[32:35]\n\t"
  "ds_read_b64_tr_b16 v[178:179], v241 offset:7168\n\t"
  "v_mfma_f32_16x16x32_bf16 v[40:43], v[152:155], a[32:35], v[40:43]\n\t"
  "ds_read_b64_tr_b16 v[180:181], v241 offset:14336\n\t"
  "ds_write_b128 v56, a[76:79] offset:3072\n\t"
  "v_mfma_f32_16x16x32_bf16 v[44:47], v[152:155], a[36:39], v[44:47]\n\t"
  "s_waitcnt lgkmcnt(13)\n\t"
  "ds_read_b64_tr_b16 v[182:183], v241 offset:21504\n\t"
  "v_mfma_f32_16x16x32_bf16 v[44:47], v[156:159], a[44:47], v[44:47]\n\t"
  "ds_read_b64_tr_b16 v[184:185], v240 offset:512\n\t"
  "buffer_load_dwordx4 v227, s[8:11], s28 offen offset:2048 lds\n\t"
  "v_mfma_f32_16x16x32_bf16 v[40:43], v[156:159], a[40:43], v[40:43]\n\t"
  "s_waitcnt lgkmcnt(13)\n\t"
  "ds_read_b64_tr_b16 v[186:187], v240 offset:7680\n\t"
  "s_waitcnt lgkmcnt(8)\n\t"
  "v_mfma_f32_16x16x32_bf16 v[48:51], v[160:163], a[32:35], v[48:51]\n\t"
  "ds_read_b64_tr_b16 v[188:189], v240 offset:14848\n\t"
  "v_mfma_f32_16x16x32_bf16 v[52:55], v[160:163], a[36:39], v[52:55]\n\t"
  "ds_read_b64_tr_b16 v[190:191], v240 offset:22016\n\t"
  "v_mfma_f32_16x16x32_bf16 v[52:55], v[164:167], a[44:47], v[52:55]\n\t"
  "ds_read_b64_tr_b16 v[128:129], v241 offset:512\n\t"
  "buffer_load_dwordx4 v228, s[8:11], s28 offen offset:3072 lds\n\t"
  "v_mfma_f32_16x16x32_bf16 v[48:51], v[164:167], a[40:43], v[48:51]\n\t"
  "ds_read_b64_tr_b16 v[130:131], v241 offset:7680\n\t"
  "v_mfma_f32_16x16x32_bf16 v[0:3], v[168:171], a[48:51], v[0:3]\n\t"
  "ds_read_b64_tr_b16 v[132:133], v241 offset:14848\n\t"
  "v_mfma_f32_16x16x32_bf16 v[4:7], v[168:171], a[52:55], v[4:7]\n\t"
  "ds_read_b64_tr_b16 v[134:135], v241 offset:22016\n\t"
  "s_add_u32 m0, s39, 69632\n\t"
  "v_mfma_f32_16x16x32_bf16 v[4:7], v[172:175], a[60:63], v[4:7]\n\t"
  "ds_read_b64_tr_b16 v[136:137], v240 offset:1024\n\t"
  "buffer_load_dwordx4 v229, s[8:11], s28 offen lds\n\t"
  "v_mfma_f32_16x16x32_bf16 v[0:3], v[172:175], a[56:59], v[0:3]\n\t"
  "s_waitcnt lgkmcnt(13)\n\t"
  "ds_read_b64_tr_b16 v[138:139], v240 offset:8192\n\t"
  "s_waitcnt lgkmcnt(6)\n\t"
  "v_mfma_f32_16x16x32_bf16 v[8:11], v[176:179], a[48:51], v[8:11]\n\t"
  "ds_read_b64_tr_b16 v[140:141], v240 offset:15360\n\t"
  "v_mfma_f32_16x16x32_bf16 v[12:15], v[176:179], a[52:55], v[12:15]\n\t"
  "ds_read_b64_tr_b16 v[142:143], v240 offset:22528\n\t"
  "v_mfma_f32_16x16x32_bf16 v[12:15], v[180:183], a[60:63], v[12:15]\n\t"
  "ds_read_b64_tr_b16 v[144:145], v241 offset:1024\n\t"
  "buffer_load_dwordx4 v230, s[8:11], s28 offen offset:1024 lds\n\t"
  "v_mfma_f32_16x16x32_bf16 v[8:11], v[180:183], a[56:59], v[8:11]\n\t"
  "ds_read_b64_tr_b16 v[146:147], v241 offset:8192\n\t"
  "v_mfma_f32_16x16x32_bf16 v[16:19], v[184:187], a[48:51], v[16:19]\n\t"
  "ds_read_b64_tr_b16 v[148:149], v241 offset:15360\n\t"
  "v_mfma_f32_16x16x32_bf16 v[20:23], v[184:187], a[52:55], v[20:23]\n\t"
  "ds_read_b64_tr_b16 v[150:151], v241 offset:22528\n\t"
  "v_mfma_f32_16x16x32_bf16 v[20:23], v[188:191], a[60:63], v[20:23]\n\t"
  "ds_read_b64_tr_b16 v[152:153], v240 offset:1536\n\t"
  "buffer_load_dwordx4 v231, s[8:11], s28 offen offset:2048 lds\n\t"
  "v_mfma_f32_16x16x32_bf16 v[16:19], v[188:191], a[56:59], v[16:19]\n\t"
  "ds_read_b64_tr_b16 v[154:155], v240 offset:8704\n\t"
  "s_add_u32 s28, s28, 229376\n\t"
  "s_waitcnt lgkmcnt(6)\n\t"
  "v_mfma_f32_16x16x32_bf16 v[24:27], v[128:131], a[48:51], v[24:27]\n\t"
  "ds_read_b64_tr_b16 v[156:157], v240 offset:15872\n\t"
  "s_barrier\n\t"
  "v_mfma_f32_16x16x32_bf16 v[28:31], v[128:131], a[52:55], v[28:31]\n\t"
  "ds_read_b64_tr_b16 v[158:159], v240 offset:23040\n\t"
  "ds_read_b128 a[64:67], v57\n\t"
  "s_add_u32 m0, s39, 98304\n\t"
  "v_mfma_f32_16x16x32_bf16 v[28:31], v[132:135], a[60:63], v[28:31]\n\t"
  "ds_read_b64_tr_b16 v[160:161], v236 offset:32768\n\t"
  "ds_read_b128 a[68:71], v57 offset:1024\n\t"
  "buffer_load_dwordx4 v232, s[12:15], s29 offen lds\n\t"
  "v_mfma_f32_16x16x32_bf16 v[24:27], v[132:135], a[56:59], v[24:27]\n\t"
  "ds_read_b64_tr_b16 v[162:163], v236 offset:39936\n\t"
  "v_mfma_f32_16x16x32_bf16 v[32:35], v[136:139], a[48:51], v[32:35]\n\t"
  "ds_read_b64_tr_b16 v[164:165], v236 offset:47104\n\t"
  "ds_read_b128 a[72:75], v57 offset:2048\n\t"
  "v_mfma_f32_16x16x32_bf16 v[36:39], v[136:139], a[52:55], v[36:39]\n\t"
  "ds_read_b64_tr_b16 v[166:167], v236 offset:54272\n\t"
  "s_waitcnt lgkmcnt(13)\n\t"
  "ds_read_b128 a[76:79], v57 offset:3072\n\t"
  "v_mfma_f32_16x16x32_bf16 v[36:39], v[140:143], a[60:63], v[36:39]\n\t"
  "ds_read_b64_tr_b16 v[168:169], v237 offset:32768\n\t"
  "s_waitcnt lgkmcnt(13)\n\t"
  "ds_read_b128 a[80:83], v57 offset:32768\n\t"
  "buffer_load_dwordx4 v233, s[12:15], s29 offen offset:1024 lds\n\t"
  "v_mfma_f32_16x16x32_bf16 v[32:35], v[140:143], a[56:59], v[32:35]\n\t"
  "ds_read_b64_tr_b16 v[170:171], v237 offset:39936\n\t"
  "s_waitcnt lgkmcnt(11)\n\t"
  "v_mfma_f32_16x16x32_bf16 v[40:43], v[144:147], a[48:51], v[40:43]\n\t"
  "ds_read_b64_tr_b16 v[172:173], v237 offset:47104\n\t"
  "ds_read_b128 a[84:87], v57 offset:33792\n\t"
  "v_mfma_f32_16x16x32_bf16 v[44:47], v[144:147], a[52:55], v[44:47]\n\t"
  "ds_read_b64_tr_b16 v[174:175], v237 offset:54272\n\t"
  "ds_read_b128 a[88:91], v57 offset:34816\n\t"
  "v_mfma_f32_16x16x32_bf16 v[44:47], v[148:151], a[60:63], v[44:47]\n\t"
  "s_waitcnt lgkmcnt(13)\n\t"
  "ds_read_b64_tr_b16 v[176:177], v236 offset:33280\n\t"
  "ds_read_b128 a[92:95], v57 offset:35840\n\t"
  "buffer_load_dwordx4 v234, s[12:15], s29 offen offset:2048 lds\n\t"
  "v_mfma_f32_16x16x32_bf16 v[40:43], v[148:151], a[56:59], v[40:43]\n\t"
  "s_waitcnt lgkmcnt(13)\n\t"
  "ds_read_b64_tr_b16 v[178:179], v236 offset:40448\n\t"
  "v_mfma_f32_16x16x32_bf16 v[48:51], v[152:155], a[48:51], v[48:51]\n\t"
  "ds_read_b64_tr_b16 v[180:181], v236 offset:47616\n\t"
  "v_mfma_f32_16x16x32_bf16 v[52:55], v[152:155], a[52:55], v[52:55]\n\t"
  "s_waitcnt lgkmcnt(13)\n\t"
  "ds_read_b64_tr_b16 v[182:183], v236 offset:54784\n\t"
  "v_mfma_f32_16x16x32_bf16 v[52:55], v[156:159], a[60:63], v[52:55]\n\t"
  "ds_read_b64_tr_b16 v[184:185], v237 offset:33280\n\t"
  "buffer_load_dwordx4 v235, s[12:15], s29 offen offset:3072 lds\n\t"
  "v_mfma_f32_16x16x32_bf16 v[48:51], v[156:159], a[56:59], v[48:51]\n\t"
  "s_waitcnt lgkmcnt(13)\n\t"
  "ds_read_b64_tr_b16 v[186:187], v237 offset:40448\n\t"
  "s_add_u32 s29, s29, 28672\n\t"
  "s_waitcnt lgkmcnt(8)\n\t"
  "v_mfma_f32_16x16x32_bf16 v[0:3], v[160:163], a[64:67], v[0:3]\n\t"
  "ds_read_b64_tr_b16 v[188:189], v237 offset:47616\n\t"
  "v_mfma_f32_16x16x32_bf16 v[4:7], v[160:163], a[68:71], v[4:7]\n\t"
  "ds_read_b64_tr_b16 v[190:191], v237 offset:54784\n\t"
  "s_add_u32 m0, s39, 102400\n\t"
  "v_mfma_f32_16x16x32_bf16 v[4:7], v[164:167], a[76:79], v[4:7]\n\t"
  "ds_read_b64_tr_b16 v[128:129], v236 offset:33792\n\t"
  "buffer_load_dwordx4 v232, s[12:15], s29 offen lds\n\t"
  "v_mfma_f32_16x16x32_bf16 v[0:3], v[164:167], a[72:75], v[0:3]\n\t"
  "ds_read_b64_tr_b16 v[130:131], v236 offset:40960\n\t"
  "v_mfma_f32_16x16x32_bf16 v[8:11], v[168:171], a[64:67], v[8:11]\n\t"
  "ds_read_b64_tr_b16 v[132:133], v236 offset:48128\n\t"
  "v_mfma_f32_16x16x32_bf16 v[12:15], v[168:171], a[68:71], v[12:15]\n\t"
  "ds_read_b64_tr_b16 v[134:135], v236 offset:55296\n\t"
  "v_mfma_f32_16x16x32_bf16 v[12:15], v[172:175], a[76:79], v[12:15]\n\t"
  "ds_read_b64_tr_b16 v[136:137], v237 offset:33792\n\t"
  "buffer_load_dwordx4 v233, s[12:15], s29 offen offset:1024 lds\n\t"
  "v_mfma_f32_16x16x32_bf16 v[8:11], v[172:175], a[72:75], v[8:11]\n\t"
  "s_waitcnt lgkmcnt(13)\n\t"
  "ds_read_b64_tr_b16 v[138:139], v237 offset:40960\n\t"
  "s_waitcnt lgkmcnt(6)\n\t"
  "v_mfma_f32_16x16x32_bf16 v[16:19], v[176:179], a[64:67], v[16:19]\n\t"
  "ds_read_b64_tr_b16 v[140:141], v237 offset:48128\n\t"
  "v_mfma_f32_16x16x32_bf16 v[20:23], v[176:179], a[68:71], v[20:23]\n\t"
  "ds_read_b64_tr_b16 v[142:143], v237 offset:55296\n\t"
  "v_mfma_f32_16x16x32_bf16 v[20:23], v[180:183], a[76:79], v[20:23]\n\t"
  "ds_read_b64_tr_b16 v[144:145], v236 offset:34304\n\t"
  "buffer_load_dwordx4 v234, s[12:15], s29 offen offset:2048 lds\n\t"
  "v_mfma_f32_16x16x32_bf16 v[16:19], v[180:183], a[72:75], v[16:19]\n\t"
  "ds_read_b64_tr_b16 v[146:147], v236 offset:41472\n\t"
  "s_add_u32 s29, s29, 4294938752\n\t"
  "v_mfma_f32_16x16x32_bf16 v[24:27], v[184:187], a[64:67], v[24:27]\n\t"
  "ds_read_b64_tr_b16 v[148:149], v236 offset:48640\n\t"
  "s_waitcnt vmcnt(14)\n\t"
  "s_barrier\n\t"
  "v_mfma_f32_16x16x32_bf16 v[28:31], v[184:187], a[68:71], v[28:31]\n\t"
  "ds_read_b64_tr_b16 v[150:151], v236 offset:55808\n\t"
  "v_mfma_f32_16x16x32_bf16 v[28:31], v[188:191], a[76:79], v[28:31]\n\t"
  "ds_read_b64_tr_b16 v[152:153], v236\n\t"
  "ds_write_b128 v56, a[96:99]\n\t"
  "v_mfma_f32_16x16x32_bf16 v[24:27], v[188:191], a[72:75], v[24:27]\n\t"
  "ds_read_b64_tr_b16 v[154:155], v236 offset:7168\n\t"
  "s_waitcnt lgkmcnt(7)\n\t"
  "v_mfma_f32_16x16x32_bf16 v[32:35], v[128:131], a[64:67], v[32:35]\n\t"
  "ds_read_b64_tr_b16 v[156:157], v236 offset:14336\n\t"
  "ds_write_b128 v56, a[100:103] offset:1024\n\t"
  "v_mfma_f32_16x16x32_bf16 v[36:39], v[128:131], a[68:71], v[36:39]\n\t"
  "ds_read_b64_tr_b16 v[158:159], v236 offset:21504\n\t"
  "v_mfma_f32_16x16x32_bf16 v[36:39], v[132:135], a[76:79], v[36:39]\n\t"
  "ds_read_b64_tr_b16 v[160:161], v237\n\t"
  "ds_write_b128 v56, a[104:107] offset:2048\n\t"
  "v_mfma_f32_16x16x32_bf16 v[32:35], v[132:135], a[72:75], v[32:35]\n\t"
  "ds_read_b64_tr_b16 v[162:163], v237 offset:7168\n\t"
  "v_mfma_f32_16x16x32_bf16 v[40:43], v[136:139], a[64:67], v[40:43]\n\t"
  "ds_read_b64_tr_b16 v[164:165], v237 offset:14336\n\t"
  "ds_write_b128 v56, a[108:111] offset:3072\n\t"
  "v_mfma_f32_16x16x32_bf16 v[44:47], v[136:139], a[68:71], v[44:47]\n\t"
  "s_waitcnt lgkmcnt(13)\n\t"
  "ds_read_b64_tr_b16 v[166:167], v237 offset:21504\n\t"
  "v_mfma_f32_16x16x32_bf16 v[44:47], v[140:143], a[76:79], v[44:47]\n\t"
  "ds_read_b64_tr_b16 v[168:169], v236 offset:512\n\t"
  "v_mfma_f32_16x16x32_bf16 v[40:43], v[140:143], a[72:75], v[40:43]\n\t"
  "s_waitcnt lgkmcnt(13)\n\t"
  "ds_read_b64_tr_b16 v[170:171], v236 offset:7680\n\t"
  "s_waitcnt lgkmcnt(8)\n\t"
  "v_mfma_f32_16x16x32_bf16 v[48:51], v[144:147], a[64:67], v[48:51]\n\t"
  "ds_read_b64_tr_b16 v[172:173], v236 offset:14848\n\t"
  "v_mfma_f32_16x16x32_bf16 v[52:55], v[144:147], a[68:71], v[52:55]\n\t"
  "ds_read_b64_tr_b16 v[174:175], v236 offset:22016\n\t"
  "v_mfma_f32_16x16x32_bf16 v[52:55], v[148:151], a[76:79], v[52:55]\n\t"
  "ds_read_b64_tr_b16 v[176:177], v237 offset:512\n\t"
  "v_mfma_f32_16x16x32_bf16 v[48:51], v[148:151], a[72:75], v[48:51]\n\t"
  "ds_read_b64_tr_b16 v[178:179], v237 offset:7680\n\t"
  "v_mfma_f32_16x16x32_bf16 v[0:3], v[152:155], a[80:83], v[0:3]\n\t"
  "ds_read_b64_tr_b16 v[180:181], v237 offset:14848\n\t"
  "v_mfma_f32_16x16x32_bf16 v[4:7], v[152:155], a[84:87], v[4:7]\n\t"
  "ds_read_b64_tr_b16 v[182:183], v237 offset:22016\n\t"
  "v_mfma_f32_16x16x32_bf16 v[4:7], v[156:159], a[92:95], v[4:7]\n\t"
  "ds_read_b64_tr_b16 v[184:185], v236 offset:1024\n\t"
  "v_mfma_f32_16x16x32_bf16 v[0:3], v[156:159], a[88:91], v[0:3]\n\t"
  "s_waitcnt lgkmcnt(13)\n\t"
  "ds_read_b64_tr_b16 v[186:187], v236 offset:8192\n\t"
  "s_waitcnt lgkmcnt(6)\n\t"
  "v_mfma_f32_16x16x32_bf16 v[8:11], v[160:163], a[80:83], v[8:11]\n\t"
  "ds_read_b64_tr_b16 v[188:189], v236 offset:15360\n\t"
  "v_mfma_f32_16x16x32_bf16 v[12:15], v[160:163], a[84:87], v[12:15]\n\t"
  "ds_read_b64_tr_b16 v[190:191], v236 offset:22528\n\t"
  "v_mfma_f32_16x16x32_bf16 v[12:15], v[164:167], a[92:95], v[12:15]\n\t"
  "ds_read_b64_tr_b16 v[128:129], v237 offset:1024\n\t"
  "v_mfma_f32_16x16x32_bf16 v[8:11], v[164:167], a[88:91], v[8:11]\n\t"
  "ds_read_b64_tr_b16 v[130:131], v237 offset:8192\n\t"
  "v_mfma_f32_16x16x32_bf16 v[16:19], v[168:171], a[80:83], v[16:19]\n\t"
  "ds_read_b64_tr_b16 v[132:133], v237 offset:15360\n\t"
  "v_mfma_f32_16x16x32_bf16 v[20:23], v[168:171], a[84:87], v[20:23]\n\t"
  "ds_read_b64_tr_b16 v[134:135], v237 offset:22528\n\t"
  "v_mfma_f32_16x16x32_bf16 v[20:23], v[172:175], a[92:95], v[20:23]\n\t"
  "ds_read_b64_tr_b16 v[136:137], v236 offset:1536\n\t"
  "v_mfma_f32_16x16x32_bf16 v[16:19], v[172:175], a[88:91], v[16:19]\n\t"
  "ds_read_b64_tr_b16 v[138:139], v236 offset:8704\n\t"
  "s_waitcnt lgkmcnt(6)\n\t"
  "v_mfma_f32_16x16x32_bf16 v[24:27], v[176:179], a[80:83], v[24:27]\n\t"
  "ds_read_b64_tr_b16 v[140:141], v236 offset:15872\n\t"
  "s_waitcnt vmcnt(7)\n\t"
  "s_barrier\n\t"
  "v_mfma_f32_16x16x32_bf16 v[28:31], v[176:179], a[84:87], v[28:31]\n\t"
  "ds_read_b64_tr_b16 v[142:143], v236 offset:23040\n\t"
  "ds_read_b128 a[96:99], v57\n\t"
  "v_mfma_f32_16x16x32_bf16 v[28:31], v[180:183], a[92:95], v[28:31]\n\t"
  "ds_read_b64_tr_b16 v[144:145], v238\n\t"
  "ds_read_b128 a[100:103], v57 offset:1024\n\t"
  "v_mfma_f32_16x16x32_bf16 v[24:27], v[180:183], a[88:91], v[24:27]\n\t"
  "ds_read_b64_tr_b16 v[146:147], v238 offset:7168\n\t"
  "v_mfma_f32_16x16x32_bf16 v[32:35], v[184:187], a[80:83], v[32:35]\n\t"
  "ds_read_b64_tr_b16 v[148:149], v238 offset:14336\n\t"
  "ds_read_b128 a[104:107], v57 offset:2048\n\t"
  "v_mfma_f32_16x16x32_bf16 v[36:39], v[184:187], a[84:87], v[36:39]\n\t"
  "ds_read_b64_tr_b16 v[150:151], v238 offset:21504\n\t"
  "s_waitcnt lgkmcnt(13)\n\t"
  "ds_read_b128 a[108:111], v57 offset:3072\n\t"
  "v_mfma_f32_16x16x32_bf16 v[36:39], v[188:191], a[92:95], v[36:39]\n\t"
  "ds_read_b64_tr_b16 v[152:153], v239\n\t"
  "v_mfma_f32_16x16x32_bf16 v[32:35], v[188:191], a[88:91], v[32:35]\n\t"
  "s_waitcnt lgkmcnt(13)\n\t"
  "ds_read_b64_tr_b16 v[154:155], v239 offset:7168\n\t"
  "s_waitcnt lgkmcnt(10)\n\t"
  "v_mfma_f32_16x16x32_bf16 v[40:43], v[128:131], a[80:83], v[40:43]\n\t"
  "ds_read_b64_tr_b16 v[156:157], v239 offset:14336\n\t"
  "v_mfma_f32_16x16x32_bf16 v[44:47], v[128:131], a[84:87], v[44:47]\n\t"
  "ds_read_b64_tr_b16 v[158:159], v239 offset:21504\n\t"
  "v_mfma_f32_16x16x32_bf16 v[44:47], v[132:135], a[92:95], v[44:47]\n\t"
  "ds_read_b64_tr_b16 v[160:161], v238 offset:512\n\t"
  "v_mfma_f32_16x16x32_bf16 v[40:43], v[132:135], a[88:91], v[40:43]\n\t"
  "ds_read_b64_tr_b16 v[162:163], v238 offset:7680\n\t"
  "v_mfma_f32_16x16x32_bf16 v[48:51], v[136:139], a[80:83], v[48:51]\n\t"
  "ds_read_b64_tr_b16 v[164:165], v238 offset:14848\n\t"
  "v_mfma_f32_16x16x32_bf16 v[52:55], v[136:139], a[84:87], v[52:55]\n\t"
  "s_waitcnt lgkmcnt(13)\n\t"
  "ds_read_b64_tr_b16 v[166:167], v238 offset:22016\n\t"
  "v_mfma_f32_16x16x32_bf16 v[52:55], v[140:143], a[92:95], v[52:55]\n\t"
  "ds_read_b64_tr_b16 v[168:169], v239 offset:512\n\t"
  "v_mfma_f32_16x16x32_bf16 v[48:51], v[140:143], a[88:91], v[48:51]\n\t"
  "s_waitcnt lgkmcnt(13)\n\t"
  "ds_read_b64_tr_b16 v[170:171], v239 offset:7680\n\t"
  "s_waitcnt lgkmcnt(6)\n\t"
  "v_mfma_f32_16x16x32_bf16 v[0:3], v[144:147], a[96:99], v[0:3]\n\t"
  "ds_read_b64_tr_b16 v[172:173], v239 offset:14848\n\t"
  "v_mfma_f32_16x16x32_bf16 v[4:7], v[144:147], a[100:103], v[4:7]\n\t"
  "ds_read_b64_tr_b16 v[174:175], v239 offset:22016\n\t"
  "v_mfma_f32_16x16x32_bf16 v[4:7], v[148:151], a[108:111], v[4:7]\n\t"
  "ds_read_b64_tr_b16 v[176:177], v238 offset:1024\n\t"
  "v_mfma_f32_16x16x32_bf16 v[0:3], v[148:151], a[104:107], v[0:3]\n\t"
  "ds_read_b64_tr_b16 v[178:179], v238 offset:8192\n\t"
  "v_mfma_f32_16x16x32_bf16 v[8:11], v[152:155], a[96:99], v[8:11]\n\t"
  "ds_read_b64_tr_b16 v[180:181], v238 offset:15360\n\t"
  "v_mfma_f32_16x16x32_bf16 v[12:15], v[152:155], a[100:103], v[12:15]\n\t"
  "ds_read_b64_tr_b16 v[182:183], v238 offset:22528\n\t"
  "v_mfma_f32_16x16x32_bf16 v[12:15], v[156:159], a[108:111], v[12:15]\n\t"
  "ds_read_b64_tr_b16 v[184:185], v239 offset:1024\n\t"
  "v_mfma_f32_16x16x32_bf16 v[8:11], v[156:159], a[104:107], v[8:11]\n\t"
  "ds_read_b64_tr_b16 v[186:187], v239 offset:8192\n\t"
  "s_waitcnt lgkmcnt(6)\n\t"
  "v_mfma_f32_16x16x32_bf16 v[16:19], v[160:163], a[96:99], v[16:19]\n\t"
  "ds_read_b64_tr_b16 v[188:189], v239 offset:15360\n\t"
  "v_mfma_f32_16x16x32_bf16 v[20:23], v[160:163], a[100:103], v[20:23]\n\t"
  "ds_read_b64_tr_b16 v[190:191], v239 offset:22528\n\t"
  "v_mfma_f32_16x16x32_bf16 v[20:23], v[164:167], a[108:111], v[20:23]\n\t"
  "ds_read_b64_tr_b16 v[128:129], v238 offset:1536\n\t"
  "v_mfma_f32_16x16x32_bf16 v[16:19], v[164:167], a[104:107], v[16:19]\n\t"
  "ds_read_b64_tr_b16 v[130:131], v238 offset:8704\n\t"
  "v_mfma_f32_16x16x32_bf16 v[24:27], v[168:171], a[96:99], v[24:27]\n\t"
  "ds_read_b64_tr_b16 v[132:133], v238 offset:15872\n\t"
  "s_barrier\n\t"
  "v_mfma_f32_16x16x32_bf16 v[28:31], v[168:171], a[100:103], v[28:31]\n\t"
  "ds_read_b64_tr_b16 v[134:135], v238 offset:23040\n\t"
  "v_mfma_f32_16x16x32_bf16 v[28:31], v[172:175], a[108:111], v[28:31]\n\t"
  "v_mfma_f32_16x16x32_bf16 v[24:27], v[172:175], a[104:107], v[24:27]\n\t"
  "s_waitcnt lgkmcnt(4)\n\t"
  "v_mfma_f32_16x16x32_bf16 v[32:35], v[176:179], a[96:99], v[32:35]\n\t"
  "v_mfma_f32_16x16x32_bf16 v[36:39], v[176:179], a[100:103], v[36:39]\n\t"
  "v_mfma_f32_16x16x32_bf16 v[36:39], v[180:183], a[108:111], v[36:39]\n\t"
  "v_mfma_f32_16x16x32_bf16 v[32:35], v[180:183], a[104:107], v[32:35]\n\t"
  "v_mfma_f32_16x16x32_bf16 v[40:43], v[184:187], a[96:99], v[40:43]\n\t"
  "v_mfma_f32_16x16x32_bf16 v[44:47], v[184:187], a[100:103], v[44:47]\n\t"
  "v_mfma_f32_16x16x32_bf16 v[44:47], v[188:191], a[108:111], v[44:47]\n\t"
  "v_mfma_f32_16x16x32_bf16 v[40:43], v[188:191], a[104:107], v[40:43]\n\t"
  "s_waitcnt lgkmcnt(0)\n\t"
  "v_mfma_f32_16x16x32_bf16 v[48:51], v[128:131], a[96:99], v[48:51]\n\t"
  "v_mfma_f32_16x16x32_bf16 v[52:55], v[128:131], a[100:103], v[52:55]\n\t"
  "v_mfma_f32_16x16x32_bf16 v[52:55], v[132:135], a[108:111], v[52:55]\n\t"
  "v_mfma_f32_16x16x32_bf16 v[48:51], v[132:135], a[104:107], v[48:51]\n\t"
  "s_nop 4\n\t"
  "v_exp_f32_e32 v96, v0\n\t"
  "s_add_u32 m0, s39, 131072\n\t"
  "s_nop 0\n\t"
  "buffer_load_dwordx4 v232, s[12:15], s29 offen lds\n\t"
  "v_exp_f32_e32 v97, v1\n\t"
  "v_exp_f32_e32 v98, v2\n\t"
  "v_exp_f32_e32 v99, v3\n\t"
  "v_pk_add_f32 v[96:97], v[96:97], 1.0 op_sel_hi:[1,0]\n\t"
  "v_pk_add_f32 v[98:99], v[98:99], 1.0 op_sel_hi:[1,0]\n\t"
  "v_rcp_f32_e32 v96, v96\n\t"
  "v_rcp_f32_e32 v97, v97\n\t"
  "v_rcp_f32_e32 v98, v98\n\t"
  "v_rcp_f32_e32 v99, v99\n\t"
  "v_pk_mul_f32 v[0:1], v[0:1], v[96:97]\n\t"
  "v_pk_mul_f32 v[2:3], v[2:3], v[98:99]\n\t"
  "v_cvt_pk_bf16_f32 v0, v0, v1\n\t"
  "v_cvt_pk_bf16_f32 v1, v2, v3\n\t"
  "ds_write_b64 v248, v[0:1]\n\t"
  "buffer_load_dwordx4 v233, s[12:15], s29 offen offset:1024 lds\n\t"
  "v_exp_f32_e32 v100, v4\n\t"
  "v_exp_f32_e32 v101, v5\n\t"
  "v_exp_f32_e32 v102, v6\n\t"
  "v_exp_f32_e32 v103, v7\n\t"
  "v_pk_add_f32 v[100:101], v[100:101], 1.0 op_sel_hi:[1,0]\n\t"
  "v_pk_add_f32 v[102:103], v[102:103], 1.0 op_sel_hi:[1,0]\n\t"
  "v_rcp_f32_e32 v100, v100\n\t"
  "v_rcp_f32_e32 v101, v101\n\t"
  "v_rcp_f32_e32 v102, v102\n\t"
  "v_rcp_f32_e32 v103, v103\n\t"
  "v_pk_mul_f32 v[4:5], v[4:5], v[100:101]\n\t"
  "v_pk_mul_f32 v[6:7], v[6:7], v[102:103]\n\t"
  "v_cvt_pk_bf16_f32 v4, v4, v5\n\t"
  "v_cvt_pk_bf16_f32 v5, v6, v7\n\t"
  "buffer_load_dwordx4 v234, s[12:15], s29 offen offset:2048 lds\n\t"
  "ds_write_b64 v248, v[4:5] offset:1024\n\t"
  "v_exp_f32_e32 v96, v8\n\t"
  "v_exp_f32_e32 v97, v9\n\t"
  "v_exp_f32_e32 v98, v10\n\t"
  "v_exp_f32_e32 v99, v11\n\t"
  "v_pk_add_f32 v[96:97], v[96:97], 1.0 op_sel_hi:[1,0]\n\t"
  "v_pk_add_f32 v[98:99], v[98:99], 1.0 op_sel_hi:[1,0]\n\t"
  "v_rcp_f32_e32 v96, v96\n\t"
  "v_rcp_f32_e32 v97, v97\n\t"
  "v_rcp_f32_e32 v98, v98\n\t"
  "v_rcp_f32_e32 v99, v99\n\t"
  "v_pk_mul_f32 v[8:9], v[8:9], v[96:97]\n\t"
  "v_pk_mul_f32 v[10:11], v[10:11], v[98:99]\n\t"
  "v_cvt_pk_bf16_f32 v8, v8, v9\n\t"
  "buffer_load_dwordx4 v235, s[12:15], s29 offen offset:3072 lds\n\t"
  "s_add_u32 s29, s29, 28672\n\t"
  "v_cvt_pk_bf16_f32 v9, v10, v11\n\t"
  "ds_write_b64 v249, v[8:9]\n\t"
  "v_exp_f32_e32 v100, v12\n\t"
  "v_exp_f32_e32 v101, v13\n\t"
  "v_exp_f32_e32 v102, v14\n\t"
  "v_exp_f32_e32 v103, v15\n\t"
  "v_pk_add_f32 v[100:101], v[100:101], 1.0 op_sel_hi:[1,0]\n\t"
  "v_pk_add_f32 v[102:103], v[102:103], 1.0 op_sel_hi:[1,0]\n\t"
  "v_rcp_f32_e32 v100, v100\n\t"
  "v_rcp_f32_e32 v101, v101\n\t"
  "v_rcp_f32_e32 v102, v102\n\t"
  "v_rcp_f32_e32 v103, v103\n\t"
  "v_pk_mul_f32 v[12:13], v[12:13], v[100:101]\n\t"
  "v_pk_mul_f32 v[14:15], v[14:15], v[102:103]\n\t"
  "s_add_u32 m0, s39, 135168\n\t"
  "s_nop 0\n\t"
  "buffer_load_dwordx4 v232, s[12:15], s29 offen lds\n\t"
  "v_cvt_pk_bf16_f32 v12, v12, v13\n\t"
  "v_cvt_pk_bf16_f32 v13, v14, v15\n\t"
  "ds_write_b64 v249, v[12:13] offset:1024\n\t"
  "v_exp_f32_e32 v96, v16\n\t"
  "v_exp_f32_e32 v97, v17\n\t"
  "v_exp_f32_e32 v98, v18\n\t"
  "v_exp_f32_e32 v99, v19\n\t"
  "v_pk_add_f32 v[96:97], v[96:97], 1.0 op_sel_hi:[1,0]\n\t"
  "v_pk_add_f32 v[98:99], v[98:99], 1.0 op_sel_hi:[1,0]\n\t"
  "v_rcp_f32_e32 v96, v96\n\t"
  "v_rcp_f32_e32 v97, v97\n\t"
  "v_rcp_f32_e32 v98, v98\n\t"
  "v_rcp_f32_e32 v99, v99\n\t"
  "v_pk_mul_f32 v[16:17], v[16:17], v[96:97]\n\t"
  "buffer_load_dwordx4 v233, s[12:15], s29 offen offset:1024 lds\n\t"
  "v_pk_mul_f32 v[18:19], v[18:19], v[98:99]\n\t"
  "v_cvt_pk_bf16_f32 v16, v16, v17\n\t"
  "v_cvt_pk_bf16_f32 v17, v18, v19\n\t"
  "ds_write_b64 v248, v[16:17] offset:2048\n\t"
  "v_exp_f32_e32 v100, v20\n\t"
  "v_exp_f32_e32 v101, v21\n\t"
  "v_exp_f32_e32 v102, v22\n\t"
  "v_exp_f32_e32 v103, v23\n\t"
  "v_pk_add_f32 v[100:101], v[100:101], 1.0 op_sel_hi:[1,0]\n\t"
  "v_pk_add_f32 v[102:103], v[102:103], 1.0 op_sel_hi:[1,0]\n\t"
  "v_rcp_f32_e32 v100, v100\n\t"
  "v_rcp_f32_e32 v101, v101\n\t"
  "v_rcp_f32_e32 v102, v102\n\t"
  "v_rcp_f32_e32 v103, v103\n\t"
  "buffer_load_dwordx4 v234, s[12:15], s29 offen offset:2048 lds\n\t"
  "s_add_u32 s29, s29, 4294938752\n\t"
  "v_pk_mul_f32 v[20:21], v[20:21], v[100:101]\n\t"
  "v_pk_mul_f32 v[22:23], v[22:23], v[102:103]\n\t"
  "v_cvt_pk_bf16_f32 v20, v20, v21\n\t"
  "v_cvt_pk_bf16_f32 v21, v22, v23\n\t"
  "ds_write_b64 v248, v[20:21] offset:3072\n\t"
  "v_exp_f32_e32 v96, v24\n\t"
  "v_exp_f32_e32 v97, v25\n\t"
  "v_exp_f32_e32 v98, v26\n\t"
  "v_exp_f32_e32 v99, v27\n\t"
  "v_pk_add_f32 v[96:97], v[96:97], 1.0 op_sel_hi:[1,0]\n\t"
  "v_pk_add_f32 v[98:99], v[98:99], 1.0 op_sel_hi:[1,0]\n\t"
  "v_rcp_f32_e32 v96, v96\n\t"
  "v_rcp_f32_e32 v97, v97\n\t"
  "v_rcp_f32_e32 v98, v98\n\t"
  "s_add_u32 m0, s39, 32768\n\t"
  "s_nop 0\n\t"
  "buffer_load_dwordx4 v232, s[12:15], s29 offen lds\n\t"
  "v_rcp_f32_e32 v99, v99\n\t"
  "v_pk_mul_f32 v[24:25], v[24:25], v[96:97]\n\t"
  "v_pk_mul_f32 v[26:27], v[26:27], v[98:99]\n\t"
  "v_cvt_pk_bf16_f32 v24, v24, v25\n\t"
  "v_cvt_pk_bf16_f32 v25, v26, v27\n\t"
  "ds_write_b64 v249, v[24:25] offset:2048\n\t"
  "v_exp_f32_e32 v100, v28\n\t"
  "v_exp_f32_e32 v101, v29\n\t"
  "v_exp_f32_e32 v102, v30\n\t"
  "v_exp_f32_e32 v103, v31\n\t"
  "v_pk_add_f32 v[100:101], v[100:101], 1.0 op_sel_hi:[1,0]\n\t"
  "v_pk_add_f32 v[102:103], v[102:103], 1.0 op_sel_hi:[1,0]\n\t"
  "v_rcp_f32_e32 v100, v100\n\t"
  "v_rcp_f32_e32 v101, v101\n\t"
  "buffer_load_dwordx4 v233, s[12:15], s29 offen offset:1024 lds\n\t"
  "v_rcp_f32_e32 v102, v102\n\t"
  "v_rcp_f32_e32 v103, v103\n\t"
  "v_pk_mul_f32 v[28:29], v[28:29], v[100:101]\n\t"
  "v_pk_mul_f32 v[30:31], v[30:31], v[102:103]\n\t"
  "v_cvt_pk_bf16_f32 v28, v28, v29\n\t"
  "v_cvt_pk_bf16_f32 v29, v30, v31\n\t"
  "ds_write_b64 v249, v[28:29] offset:3072\n\t"
  "v_exp_f32_e32 v96, v32\n\t"
  "v_exp_f32_e32 v97, v33\n\t"
  "v_exp_f32_e32 v98, v34\n\t"
  "v_exp_f32_e32 v99, v35\n\t"
  "v_pk_add_f32 v[96:97], v[96:97], 1.0 op_sel_hi:[1,0]\n\t"
  "v_pk_add_f32 v[98:99], v[98:99], 1.0 op_sel_hi:[1,0]\n\t"
  "v_rcp_f32_e32 v96, v96\n\t"
  "buffer_load_dwordx4 v234, s[12:15], s29 offen offset:2048 lds\n\t"
  "v_rcp_f32_e32 v97, v97\n\t"
  "v_rcp_f32_e32 v98, v98\n\t"
  "v_rcp_f32_e32 v99, v99\n\t"
  "v_pk_mul_f32 v[32:33], v[32:33], v[96:97]\n\t"
  "v_pk_mul_f32 v[34:35], v[34:35], v[98:99]\n\t"
  "v_cvt_pk_bf16_f32 v32, v32, v33\n\t"
  "v_cvt_pk_bf16_f32 v33, v34, v35\n\t"
  "ds_write_b64 v248, v[32:33] offset:4096\n\t"
  "v_exp_f32_e32 v100, v36\n\t"
  "v_exp_f32_e32 v101, v37\n\t"
  "v_exp_f32_e32 v102, v38\n\t"
  "v_exp_f32_e32 v103, v39\n\t"
  "v_pk_add_f32 v[100:101], v[100:101], 1.0 op_sel_hi:[1,0]\n\t"
  "v_pk_add_f32 v[102:103], v[102:103], 1.0 op_sel_hi:[1,0]\n\t"
  "buffer_load_dwordx4 v235, s[12:15], s29 offen offset:3072 lds\n\t"
  "s_add_u32 s29, s29, 28672\n\t"
  "v_rcp_f32_e32 v100, v100\n\t"
  "v_rcp_f32_e32 v101, v101\n\t"
  "v_rcp_f32_e32 v102, v102\n\t"
  "v_rcp_f32_e32 v103, v103\n\t"
  "v_pk_mul_f32 v[36:37], v[36:37], v[100:101]\n\t"
  "v_pk_mul_f32 v[38:39], v[38:39], v[102:103]\n\t"
  "v_cvt_pk_bf16_f32 v36, v36, v37\n\t"
  "v_cvt_pk_bf16_f32 v37, v38, v39\n\t"
  "ds_write_b64 v248, v[36:37] offset:5120\n\t"
  "v_exp_f32_e32 v96, v40\n\t"
  "v_exp_f32_e32 v97, v41\n\t"
  "v_exp_f32_e32 v98, v42\n\t"
  "v_exp_f32_e32 v99, v43\n\t"
  "v_pk_add_f32 v[96:97], v[96:97], 1.0 op_sel_hi:[1,0]\n\t"
  "s_add_u32 m0, s39, 36864\n\t"
  "s_nop 0\n\t"
  "buffer_load_dwordx4 v232, s[12:15], s29 offen lds\n\t"
  "v_pk_add_f32 v[98:99], v[98:99], 1.0 op_sel_hi:[1,0]\n\t"
  "v_rcp_f32_e32 v96, v96\n\t"
  "v_rcp_f32_e32 v97, v97\n\t"
  "v_rcp_f32_e32 v98, v98\n\t"
  "v_rcp_f32_e32 v99, v99\n\t"
  "v_pk_mul_f32 v[40:41], v[40:41], v[96:97]\n\t"
  "v_pk_mul_f32 v[42:43], v[42:43], v[98:99]\n\t"
  "v_cvt_pk_bf16_f32 v40, v40, v41\n\t"
  "v_cvt_pk_bf16_f32 v41, v42, v43\n\t"
  "ds_write_b64 v249, v[40:41] offset:4096\n\t"
  "v_exp_f32_e32 v100, v44\n\t"
  "v_exp_f32_e32 v101, v45\n\t"
  "v_exp_f32_e32 v102, v46\n\t"
  "v_exp_f32_e32 v103, v47\n\t"
  "buffer_load_dwordx4 v233, s[12:15], s29 offen offset:1024 lds\n\t"
  "v_pk_add_f32 v[100:101], v[100:101], 1.0 op_sel_hi:[1,0]\n\t"
  "v_pk_add_f32 v[102:103], v[102:103], 1.0 op_sel_hi:[1,0]\n\t"
  "v_rcp_f32_e32 v100, v100\n\t"
  "v_rcp_f32_e32 v101, v101\n\t"
  "v_rcp_f32_e32 v102, v102\n\t"
  "v_rcp_f32_e32 v103, v103\n\t"
  "v_pk_mul_f32 v[44:45], v[44:45], v[100:101]\n\t"
  "v_pk_mul_f32 v[46:47], v[46:47], v[102:103]\n\t"
  "v_cvt_pk_bf16_f32 v44, v44, v45\n\t"
  "v_cvt_pk_bf16_f32 v45, v46, v47\n\t"
  "ds_write_b64 v249, v[44:45] offset:5120\n\t"
  "v_exp_f32_e32 v96, v48\n\t"
  "v_exp_f32_e32 v97, v49\n\t"
  "v_exp_f32_e32 v98, v50\n\t"
  "buffer_load_dwordx4 v234, s[12:15], s29 offen offset:2048 lds\n\t"
  "s_add_u32 s29, s29, 4294938752\n\t"
  "v_exp_f32_e32 v99, v51\n\t"
  "v_pk_add_f32 v[96:97], v[96:97], 1.0 op_sel_hi:[1,0]\n\t"
  "v_pk_add_f32 v[98:99], v[98:99], 1.0 op_sel_hi:[1,0]\n\t"
  "v_rcp_f32_e32 v96, v96\n\t"
  "v_rcp_f32_e32 v97, v97\n\t"
  "v_rcp_f32_e32 v98, v98\n\t"
  "v_rcp_f32_e32 v99, v99\n\t"
  "v_pk_mul_f32 v[48:49], v[48:49], v[96:97]\n\t"
  "v_pk_mul_f32 v[50:51], v[50:51], v[98:99]\n\t"
  "v_cvt_pk_bf16_f32 v48, v48, v49\n\t"
  "v_cvt_pk_bf16_f32 v49, v50, v51\n\t"
  "ds_write_b64 v248, v[48:49] offset:6144\n\t"
  "v_exp_f32_e32 v100, v52\n\t"
  "v_exp_f32_e32 v101, v53\n\t"
  "v_exp_f32_e32 v102, v54\n\t"
  "v_exp_f32_e32 v103, v55\n\t"
  "v_pk_add_f32 v[100:101], v[100:101], 1.0 op_sel_hi:[1,0]\n\t"
  "v_pk_add_f32 v[102:103], v[102:103], 1.0 op_sel_hi:[1,0]\n\t"
  "v_rcp_f32_e32 v100, v100\n\t"
  "v_rcp_f32_e32 v101, v101\n\t"
  "v_rcp_f32_e32 v102, v102\n\t"
  "v_rcp_f32_e32 v103, v103\n\t"
  "v_pk_mul_f32 v[52:53], v[52:53], v[100:101]\n\t"
  "v_pk_mul_f32 v[54:55], v[54:55], v[102:103]\n\t"
  "v_cvt_pk_bf16_f32 v52, v52, v53\n\t"
  "v_cvt_pk_bf16_f32 v53, v54, v55\n\t"
  "ds_write_b64 v248, v[52:53] offset:7168\n\t"
  "s_waitcnt vmcnt(14) lgkmcnt(0)\n\t"
  "s_barrier\n\t"
  "ds_read_b128 v[0:3], v250\n\t"
  "ds_read_b128 v[4:7], v250 offset:1024\n\t"
  "ds_read_b128 v[8:11], v250 offset:2048\n\t"
  "ds_read_b128 v[12:15], v250 offset:3072\n\t"
  "ds_read_b64_tr_b16 v[128:129], v244 offset:32768\n\t"
  "ds_read_b64_tr_b16 v[130:131], v244 offset:34816\n\t"
  "ds_read_b64_tr_b16 v[132:133], v244 offset:36864\n\t"
  "ds_read_b64_tr_b16 v[134:135], v244 offset:38912\n\t"
  "ds_read_b64_tr_b16 v[136:137], v244 offset:40960\n\t"
  "ds_read_b64_tr_b16 v[138:139], v244 offset:43008\n\t"
  "ds_read_b64_tr_b16 v[140:141], v244 offset:45056\n\t"
  "ds_read_b64_tr_b16 v[142:143], v244 offset:47104\n\t"
  "ds_read_b64_tr_b16 v[144:145], v244 offset:49152\n\t"
  "ds_read_b64_tr_b16 v[146:147], v244 offset:51200\n\t"
  "ds_read_b64_tr_b16 v[148:149], v244 offset:53248\n\t"
  "s_waitcnt lgkmcnt(13)\n\t"
  "ds_read_b64_tr_b16 v[150:151], v244 offset:55296\n\t"
  "ds_read_b64_tr_b16 v[152:153], v244 offset:57344\n\t"
  "s_waitcnt lgkmcnt(13)\n\t"
  "ds_read_b64_tr_b16 v[154:155], v244 offset:59392\n\t"
  "s_waitcnt lgkmcnt(6)\n\t"
  "v_mfma_f32_16x16x32_bf16 v[64:67], v[128:131], v[0:3], 0\n\t"
  "ds_read_b64_tr_b16 v[156:157], v245 offset:32768\n\t"
  "ds_read_b128 v[16:19], v250 offset:4096\n\t"
  "s_add_u32 m0, s39, 65536\n\t"
  "v_mfma_f32_16x16x32_bf16 v[68:71], v[128:131], v[4:7], 0\n\t"
  "ds_read_b64_tr_b16 v[158:159], v245 offset:34816\n\t"
  "ds_read_b128 v[20:23], v250 offset:5120\n\t"
  "buffer_load_dwordx4 v232, s[12:15], s29 offen lds\n\t"
  "v_mfma_f32_16x16x32_bf16 v[68:71], v[132:135], v[12:15], v[68:71]\n\t"
  "ds_read_b64_tr_b16 v[160:161], v245 offset:36864\n\t"
  "ds_read_b128 v[24:27], v250 offset:6144\n\t"
  "v_mfma_f32_16x16x32_bf16 v[64:67], v[132:135], v[8:11], v[64:67]\n\t"
  "ds_read_b64_tr_b16 v[162:163], v245 offset:38912\n\t"
  "ds_read_b128 v[28:31], v250 offset:7168\n\t"
  "s_waitcnt lgkmcnt(6)\n\t"
  "v_mfma_f32_16x16x32_bf16 v[64:67], v[136:139], v[16:19], v[64:67]\n\t"
  "ds_read_b64_tr_b16 v[164:165], v245 offset:40960\n\t"
  "ds_read_b128 v[32:35], v250 offset:8192\n\t"
  "s_waitcnt lgkmcnt(6)\n\t"
  "v_mfma_f32_16x16x32_bf16 v[68:71], v[136:139], v[20:23], v[68:71]\n\t"
  "ds_read_b64_tr_b16 v[166:167], v245 offset:43008\n\t"
  "ds_read_b128 v[36:39], v250 offset:9216\n\t"
  "buffer_load_dwordx4 v233, s[12:15], s29 offen offset:1024 lds\n\t"
  "s_waitcnt lgkmcnt(4)\n\t"
  "v_mfma_f32_16x16x32_bf16 v[68:71], v[140:143], v[28:31], v[68:71]\n\t"
  "ds_read_b64_tr_b16 v[168:169], v245 offset:45056\n\t"
  "ds_read_b128 v[40:43], v250 offset:10240\n\t"
  "v_mfma_f32_16x16x32_bf16 v[64:67], v[140:143], v[24:27], v[64:67]\n\t"
  "ds_read_b64_tr_b16 v[170:171], v245 offset:47104\n\t"
  "ds_read_b128 v[44:47], v250 offset:11264\n\t"
  "s_waitcnt lgkmcnt(6)\n\t"
  "v_mfma_f32_16x16x32_bf16 v[64:67], v[144:147], v[32:35], v[64:67]\n\t"
  "ds_read_b64_tr_b16 v[172:173], v245 offset:49152\n\t"
  "ds_read_b128 v[48:51], v250 offset:12288\n\t"
  "s_waitcnt lgkmcnt(6)\n\t"
  "v_mfma_f32_16x16x32_bf16 v[68:71], v[144:147], v[36:39], v[68:71]\n\t"
  "ds_read_b64_tr_b16 v[174:175], v245 offset:51200\n\t"
  "ds_read_b128 v[52:55], v250 offset:13312\n\t"
  "buffer_load_dwordx4 v234, s[12:15], s29 offen offset:2048 lds\n\t"
  "s_waitcnt lgkmcnt(4)\n\t"
  "v_mfma_f32_16x16x32_bf16 v[68:71], v[148:151], v[44:47], v[68:71]\n\t"
  "ds_read_b64_tr_b16 v[176:177], v245 offset:53248\n\t"
  "v_mfma_f32_16x16x32_bf16 v[64:67], v[148:151], v[40:43], v[64:67]\n\t"
  "ds_read_b64_tr_b16 v[178:179], v245 offset:55296\n\t"
  "s_waitcnt lgkmcnt(4)\n\t"
  "v_mfma_f32_16x16x32_bf16 v[64:67], v[152:155], v[48:51], v[64:67]\n\t"
  "ds_read_b64_tr_b16 v[180:181], v245 offset:57344\n\t"
  "s_waitcnt vmcnt(10) lgkmcnt(2)\n\t"
  "s_barrier\n\t"
  "v_mfma_f32_16x16x32_bf16 v[68:71], v[152:155], v[52:55], v[68:71]\n\t"
  "ds_read_b64_tr_b16 v[182:183], v245 offset:59392\n\t"
  "buffer_load_dwordx4 v235, s[12:15], s29 offen offset:3072 lds\n\t"
  "v_mfma_f32_16x16x32_bf16 v[72:75], v[156:159], v[0:3], 0\n\t"
  "ds_read_b64_tr_b16 v[184:185], v246\n\t"
  "s_add_u32 s29, s29, 28672\n\t"
  "v_mfma_f32_16x16x32_bf16 v[76:79], v[156:159], v[4:7], 0\n\t"
  "ds_read_b64_tr_b16 v[186:187], v246 offset:2048\n\t"
  "v_mfma_f32_16x16x32_bf16 v[76:79], v[160:163], v[12:15], v[76:79]\n\t"
  "ds_read_b64_tr_b16 v[188:189], v246 offset:4096\n\t"
  "s_add_u32 m0, s39, 69632\n\t"
  "v_mfma_f32_16x16x32_bf16 v[72:75], v[160:163], v[8:11], v[72:75]\n\t"
  "ds_read_b64_tr_b16 v[190:191], v246 offset:6144\n\t"
  "buffer_load_dwordx4 v232, s[12:15], s29 offen lds\n\t"
  "v_mfma_f32_16x16x32_bf16 v[72:75], v[164:167], v[16:19], v[72:75]\n\t"
  "ds_read_b64_tr_b16 v[128:129], v246 offset:8192\n\t"
  "v_mfma_f32_16x16x32_bf16 v[76:79], v[164:167], v[20:23], v[76:79]\n\t"
  "ds_read_b64_tr_b16 v[130:131], v246 offset:10240\n\t"
  "v_mfma_f32_16x16x32_bf16 v[76:79], v[168:171], v[28:31], v[76:79]\n\t"
  "ds_read_b64_tr_b16 v[132:133], v246 offset:12288\n\t"
  "v_mfma_f32_16x16x32_bf16 v[72:75], v[168:171], v[24:27], v[72:75]\n\t"
  "ds_read_b64_tr_b16 v[134:135], v246 offset:14336\n\t"
  "buffer_load_dwordx4 v233, s[12:15], s29 offen offset:1024 lds\n\t"
  "v_mfma_f32_16x16x32_bf16 v[72:75], v[172:175], v[32:35], v[72:75]\n\t"
  "ds_read_b64_tr_b16 v[136:137], v246 offset:16384\n\t"
  "v_mfma_f32_16x16x32_bf16 v[76:79], v[172:175], v[36:39], v[76:79]\n\t"
  "ds_read_b64_tr_b16 v[138:139], v246 offset:18432\n\t"
  "s_waitcnt lgkmcnt(6)\n\t"
  "v_mfma_f32_16x16x32_bf16 v[76:79], v[176:179], v[44:47], v[76:79]\n\t"
  "ds_read_b64_tr_b16 v[140:141], v246 offset:20480\n\t"
  "v_mfma_f32_16x16x32_bf16 v[72:75], v[176:179], v[40:43], v[72:75]\n\t"
  "ds_read_b64_tr_b16 v[142:143], v246 offset:22528\n\t"
  "buffer_load_dwordx4 v234, s[12:15], s29 offen offset:2048 lds\n\t"
  "v_mfma_f32_16x16x32_bf16 v[72:75], v[180:183], v[48:51], v[72:75]\n\t"
  "ds_read_b64_tr_b16 v[144:145], v246 offset:24576\n\t"
  "s_add_u32 s29, s29, 4294938752\n\t"
  "v_mfma_f32_16x16x32_bf16 v[76:79], v[180:183], v[52:55], v[76:79]\n\t"
  "ds_read_b64_tr_b16 v[146:147], v246 offset:26624\n\t"
  "v_mfma_f32_16x16x32_bf16 v[80:83], v[184:187], v[0:3], 0\n\t"
  "ds_read_b64_tr_b16 v[148:149], v247\n\t"
  "s_add_u32 m0, s39, 0\n\t"
  "v_mfma_f32_16x16x32_bf16 v[84:87], v[184:187], v[4:7], 0\n\t"
  "ds_read_b64_tr_b16 v[150:151], v247 offset:2048\n\t"
  "buffer_load_dwordx4 v232, s[12:15], s29 offen lds\n\t"
  "v_mfma_f32_16x16x32_bf16 v[84:87], v[188:191], v[12:15], v[84:87]\n\t"
  "ds_read_b64_tr_b16 v[152:153], v247 offset:4096\n\t"
  "v_mfma_f32_16x16x32_bf16 v[80:83], v[188:191], v[8:11], v[80:83]\n\t"
  "ds_read_b64_tr_b16 v[154:155], v247 offset:6144\n\t"
  "s_waitcnt lgkmcnt(6)\n\t"
  "v_mfma_f32_16x16x32_bf16 v[80:83], v[128:131], v[16:19], v[80:83]\n\t"
  "ds_read_b64_tr_b16 v[156:157], v247 offset:8192\n\t"
  "v_cvt_pk_bf16_f32 v96, v64, v65\n\t"
  "v_mfma_f32_16x16x32_bf16 v[84:87], v[128:131], v[20:23], v[84:87]\n\t"
  "ds_read_b64_tr_b16 v[158:159], v247 offset:10240\n\t"
  "buffer_load_dwordx4 v233, s[12:15], s29 offen offset:1024 lds\n\t"
  "v_cvt_pk_bf16_f32 v97, v66, v67\n\t"
  "v_mfma_f32_16x16x32_bf16 v[84:87], v[132:135], v[28:31], v[84:87]\n\t"
  "ds_read_b64_tr_b16 v[160:161], v247 offset:12288\n\t"
  "v_cvt_pk_bf16_f32 v98, v72, v73\n\t"
  "v_mfma_f32_16x16x32_bf16 v[80:83], v[132:135], v[24:27], v[80:83]\n\t"
  "ds_read_b64_tr_b16 v[162:163], v247 offset:14336\n\t"
  "v_cvt_pk_bf16_f32 v99, v74, v75\n\t"
  "v_mfma_f32_16x16x32_bf16 v[80:83], v[136:139], v[32:35], v[80:83]\n\t"
  "ds_read_b64_tr_b16 v[164:165], v247 offset:16384\n\t"
  "v_mfma_f32_16x16x32_bf16 v[84:87], v[136:139], v[36:39], v[84:87]\n\t"
  "ds_read_b64_tr_b16 v[166:167], v247 offset:18432\n\t"
  "buffer_load_dwordx4 v234, s[12:15], s29 offen offset:2048 lds\n\t"
  "v_mfma_f32_16x16x32_bf16 v[84:87], v[140:143], v[44:47], v[84:87]\n\t"
  "ds_read_b64_tr_b16 v[168:169], v247 offset:20480\n\t"
  "v_mfma_f32_16x16x32_bf16 v[80:83], v[140:143], v[40:43], v[80:83]\n\t"
  "ds_read_b64_tr_b16 v[170:171], v247 offset:22528\n\t"
  "buffer_store_dwordx4 v[96:99], v252, s[20:23], s35 offen sc1\n\t"
  "s_waitcnt lgkmcnt(6)\n\t"
  "v_mfma_f32_16x16x32_bf16 v[80:83], v[144:147], v[48:51], v[80:83]\n\t"
  "ds_read_b64_tr_b16 v[172:173], v247 offset:24576\n\t"
  "s_add_u32 s35, s35, 1024\n\t"
  "v_cvt_pk_bf16_f32 v56, v68, v69\n\t"
  "s_waitcnt vmcnt(11)\n\t"
  "s_barrier\n\t"
  "v_mfma_f32_16x16x32_bf16 v[84:87], v[144:147], v[52:55], v[84:87]\n\t"
  "ds_read_b64_tr_b16 v[174:175], v247 offset:26624\n\t"
  "buffer_load_dwordx4 v235, s[12:15], s29 offen offset:3072 lds\n\t"
  "v_cvt_pk_bf16_f32 v57, v70, v71\n\t"
  "v_mfma_f32_16x16x32_bf16 v[88:91], v[148:151], v[0:3], 0\n\t"
  "ds_read_b64_tr_b16 v[176:177], v242 offset:32768\n\t"
  "s_add_u32 s29, s29, 28672\n\t"
  "v_cvt_pk_bf16_f32 v58, v76, v77\n\t"
  "v_mfma_f32_16x16x32_bf16 v[92:95], v[148:151], v[4:7], 0\n\t"
  "ds_read_b64_tr_b16 v[178:179], v242 offset:34816\n\t"
  "v_cvt_pk_bf16_f32 v59, v78, v79\n\t"
  "v_mfma_f32_16x16x32_bf16 v[92:95], v[152:155], v[12:15], v[92:95]\n\t"
  "ds_read_b64_tr_b16 v[180:181], v242 offset:36864\n\t"
  "s_add_u32 m0, s39, 4096\n\t"
  "v_mfma_f32_16x16x32_bf16 v[88:91], v[152:155], v[8:11], v[88:91]\n\t"
  "ds_read_b64_tr_b16 v[182:183], v242 offset:38912\n\t"
  "buffer_load_dwordx4 v232, s[12:15], s29 offen lds\n\t"
  "v_mfma_f32_16x16x32_bf16 v[88:91], v[156:159], v[16:19], v[88:91]\n\t"
  "ds_read_b64_tr_b16 v[184:185], v242 offset:40960\n\t"
  "v_mfma_f32_16x16x32_bf16 v[92:95], v[156:159], v[20:23], v[92:95]\n\t"
  "ds_read_b64_tr_b16 v[186:187], v242 offset:43008\n\t"
  "buffer_store_dwordx4 v[56:59], v253, s[20:23], s36 offen sc1\n\t"
  "s_waitcnt lgkmcnt(6)\n\t"
  "v_mfma_f32_16x16x32_bf16 v[92:95], v[160:163], v[28:31], v[92:95]\n\t"
  "ds_read_b64_tr_b16 v[188:189], v242 offset:45056\n\t"
  "s_add_u32 s36, s36, 1024\n\t"
  "v_mfma_f32_16x16x32_bf16 v[88:91], v[160:163], v[24:27], v[88:91]\n\t"
  "ds_read_b64_tr_b16 v[190:191], v242 offset:47104\n\t"
  "buffer_load_dwordx4 v233, s[12:15], s29 offen offset:1024 lds\n\t"
  "v_mfma_f32_16x16x32_bf16 v[88:91], v[164:167], v[32:35], v[88:91]\n\t"
  "ds_read_b64_tr_b16 v[128:129], v242 offset:49152\n\t"
  "v_mfma_f32_16x16x32_bf16 v[92:95], v[164:167], v[36:39], v[92:95]\n\t"
  "ds_read_b64_tr_b16 v[130:131], v242 offset:51200\n\t"
  "v_mfma_f32_16x16x32_bf16 v[92:95], v[168:171], v[44:47], v[92:95]\n\t"
  "ds_read_b64_tr_b16 v[132:133], v242 offset:53248\n\t"
  "v_mfma_f32_16x16x32_bf16 v[88:91], v[168:171], v[40:43], v[88:91]\n\t"
  "ds_read_b64_tr_b16 v[134:135], v242 offset:55296\n\t"
  "buffer_load_dwordx4 v234, s[12:15], s29 offen offset:2048 lds\n\t"
  "v_mfma_f32_16x16x32_bf16 v[88:91], v[172:175], v[48:51], v[88:91]\n\t"
  "ds_read_b64_tr_b16 v[136:137], v242 offset:57344\n\t"
  "s_add_u32 s29, s29, 4294938752\n\t"
  "v_mfma_f32_16x16x32_bf16 v[92:95], v[172:175], v[52:55], v[92:95]\n\t"
  "ds_read_b64_tr_b16 v[138:139], v242 offset:59392\n\t"
  "s_waitcnt lgkmcnt(6)\n\t"
  "v_mfma_f32_16x16x32_bf16 v[64:67], v[176:179], v[0:3], 0\n\t"
  "ds_read_b64_tr_b16 v[140:141], v243 offset:32768\n\t"
  "s_add_u32 m0, s39, 98304\n\t"
  "v_mfma_f32_16x16x32_bf16 v[68:71], v[176:179], v[4:7], 0\n\t"
  "ds_read_b64_tr_b16 v[142:143], v243 offset:34816\n\t"
  "buffer_load_dwordx4 v232, s[12:15], s29 offen lds\n\t"
  "v_mfma_f32_16x16x32_bf16 v[68:71], v[180:183], v[12:15], v[68:71]\n\t"
  "ds_read_b64_tr_b16 v[144:145], v243 offset:36864\n\t"
  "v_mfma_f32_16x16x32_bf16 v[64:67], v[180:183], v[8:11], v[64:67]\n\t"
  "ds_read_b64_tr_b16 v[146:147], v243 offset:38912\n\t"
  "v_mfma_f32_16x16x32_bf16 v[64:67], v[184:187], v[16:19], v[64:67]\n\t"
  "ds_read_b64_tr_b16 v[148:149], v243 offset:40960\n\t"
  "v_cvt_pk_bf16_f32 v100, v80, v81\n\t"
  "v_mfma_f32_16x16x32_bf16 v[68:71], v[184:187], v[20:23], v[68:71]\n\t"
  "ds_read_b64_tr_b16 v[150:151], v243 offset:43008\n\t"
  "buffer_load_dwordx4 v233, s[12:15], s29 offen offset:1024 lds\n\t"
  "v_cvt_pk_bf16_f32 v101, v82, v83\n\t"
  "v_mfma_f32_16x16x32_bf16 v[68:71], v[188:191], v[28:31], v[68:71]\n\t"
  "ds_read_b64_tr_b16 v[152:153], v243 offset:45056\n\t"
  "v_cvt_pk_bf16_f32 v102, v88, v89\n\t"
  "v_mfma_f32_16x16x32_bf16 v[64:67], v[188:191], v[24:27], v[64:67]\n\t"
  "ds_read_b64_tr_b16 v[154:155], v243 offset:47104\n\t"
  "v_cvt_pk_bf16_f32 v103, v90, v91\n\t"
  "s_waitcnt lgkmcnt(6)\n\t"
  "v_mfma_f32_16x16x32_bf16 v[64:67], v[128:131], v[32:35], v[64:67]\n\t"
  "ds_read_b64_tr_b16 v[156:157], v243 offset:49152\n\t"
  "v_mfma_f32_16x16x32_bf16 v[68:71], v[128:131], v[36:39], v[68:71]\n\t"
  "ds_read_b64_tr_b16 v[158:159], v243 offset:51200\n\t"
  "buffer_load_dwordx4 v234, s[12:15], s29 offen offset:2048 lds\n\t"
  "v_mfma_f32_16x16x32_bf16 v[68:71], v[132:135], v[44:47], v[68:71]\n\t"
  "ds_read_b64_tr_b16 v[160:161], v243 offset:53248\n\t"
  "v_mfma_f32_16x16x32_bf16 v[64:67], v[132:135], v[40:43], v[64:67]\n\t"
  "ds_read_b64_tr_b16 v[162:163], v243 offset:55296\n\t"
  "buffer_store_dwordx4 v[100:103], v252, s[20:23], s35 offen sc1\n\t"
  "v_mfma_f32_16x16x32_bf16 v[64:67], v[136:139], v[48:51], v[64:67]\n\t"
  "ds_read_b64_tr_b16 v[164:165], v243 offset:57344\n\t"
  "s_add_u32 s35, s35, 1024\n\t"
  "v_cvt_pk_bf16_f32 v60, v84, v85\n\t"
  "s_waitcnt vmcnt(13)\n\t"
  "s_barrier\n\t"
  "v_mfma_f32_16x16x32_bf16 v[68:71], v[136:139], v[52:55], v[68:71]\n\t"
  "ds_read_b64_tr_b16 v[166:167], v243 offset:59392\n\t"
  "buffer_load_dwordx4 v235, s[12:15], s29 offen offset:3072 lds\n\t"
  "v_cvt_pk_bf16_f32 v61, v86, v87\n\t"
  "v_mfma_f32_16x16x32_bf16 v[72:75], v[140:143], v[0:3], 0\n\t"
  "ds_read_b64_tr_b16 v[168:169], v244\n\t"
  "s_add_u32 s29, s29, 28672\n\t"
  "v_cvt_pk_bf16_f32 v62, v92, v93\n\t"
  "v_mfma_f32_16x16x32_bf16 v[76:79], v[140:143], v[4:7], 0\n\t"
  "ds_read_b64_tr_b16 v[170:171], v244 offset:2048\n\t"
  "v_cvt_pk_bf16_f32 v63, v94, v95\n\t"
  "s_waitcnt lgkmcnt(6)\n\t"
  "v_mfma_f32_16x16x32_bf16 v[76:79], v[144:147], v[12:15], v[76:79]\n\t"
  "ds_read_b64_tr_b16 v[172:173], v244 offset:4096\n\t"
  "s_add_u32 m0, s39, 102400\n\t"
  "v_mfma_f32_16x16x32_bf16 v[72:75], v[144:147], v[8:11], v[72:75]\n\t"
  "ds_read_b64_tr_b16 v[174:175], v244 offset:6144\n\t"
  "buffer_load_dwordx4 v232, s[12:15], s29 offen lds\n\t"
  "v_mfma_f32_16x16x32_bf16 v[72:75], v[148:151], v[16:19], v[72:75]\n\t"
  "ds_read_b64_tr_b16 v[176:177], v244 offset:8192\n\t"
  "v_mfma_f32_16x16x32_bf16 v[76:79], v[148:151], v[20:23], v[76:79]\n\t"
  "ds_read_b64_tr_b16 v[178:179], v244 offset:10240\n\t"
  "buffer_store_dwordx4 v[60:63], v253, s[20:23], s36 offen sc1\n\t"
  "v_mfma_f32_16x16x32_bf16 v[76:79], v[152:155], v[28:31], v[76:79]\n\t"
  "ds_read_b64_tr_b16 v[180:181], v244 offset:12288\n\t"
  "s_add_u32 s36, s36, 1024\n\t"
  "v_mfma_f32_16x16x32_bf16 v[72:75], v[152:155], v[24:27], v[72:75]\n\t"
  "ds_read_b64_tr_b16 v[182:183], v244 offset:14336\n\t"
  "buffer_load_dwordx4 v233, s[12:15], s29 offen offset:1024 lds\n\t"
  "v_mfma_f32_16x16x32_bf16 v[72:75], v[156:159], v[32:35], v[72:75]\n\t"
  "ds_read_b64_tr_b16 v[184:185], v244 offset:16384\n\t"
  "v_mfma_f32_16x16x32_bf16 v[76:79], v[156:159], v[36:39], v[76:79]\n\t"
  "ds_read_b64_tr_b16 v[186:187], v244 offset:18432\n\t"
  "s_waitcnt lgkmcnt(6)\n\t"
  "v_mfma_f32_16x16x32_bf16 v[76:79], v[160:163], v[44:47], v[76:79]\n\t"
  "ds_read_b64_tr_b16 v[188:189], v244 offset:20480\n\t"
  "v_mfma_f32_16x16x32_bf16 v[72:75], v[160:163], v[40:43], v[72:75]\n\t"
  "ds_read_b64_tr_b16 v[190:191], v244 offset:22528\n\t"
  "buffer_load_dwordx4 v234, s[12:15], s29 offen offset:2048 lds\n\t"
  "v_mfma_f32_16x16x32_bf16 v[72:75], v[164:167], v[48:51], v[72:75]\n\t"
  "ds_read_b64_tr_b16 v[128:129], v244 offset:24576\n\t"
  "s_add_u32 s29, s29, 4294938752\n\t"
  "v_mfma_f32_16x16x32_bf16 v[76:79], v[164:167], v[52:55], v[76:79]\n\t"
  "ds_read_b64_tr_b16 v[130:131], v244 offset:26624\n\t"
  "v_mfma_f32_16x16x32_bf16 v[80:83], v[168:171], v[0:3], 0\n\t"
  "ds_read_b64_tr_b16 v[132:133], v245\n\t"
  "s_add_u32 m0, s39, 131072\n\t"
  "v_mfma_f32_16x16x32_bf16 v[84:87], v[168:171], v[4:7], 0\n\t"
  "ds_read_b64_tr_b16 v[134:135], v245 offset:2048\n\t"
  "buffer_load_dwordx4 v232, s[12:15], s29 offen lds\n\t"
  "v_mfma_f32_16x16x32_bf16 v[84:87], v[172:175], v[12:15], v[84:87]\n\t"
  "ds_read_b64_tr_b16 v[136:137], v245 offset:4096\n\t"
  "v_mfma_f32_16x16x32_bf16 v[80:83], v[172:175], v[8:11], v[80:83]\n\t"
  "ds_read_b64_tr_b16 v[138:139], v245 offset:6144\n\t"
  "s_waitcnt lgkmcnt(6)\n\t"
  "v_mfma_f32_16x16x32_bf16 v[80:83], v[176:179], v[16:19], v[80:83]\n\t"
  "ds_read_b64_tr_b16 v[140:141], v245 offset:8192\n\t"
  "v_cvt_pk_bf16_f32 v104, v64, v65\n\t"
  "v_mfma_f32_16x16x32_bf16 v[84:87], v[176:179], v[20:23], v[84:87]\n\t"
  "ds_read_b64_tr_b16 v[142:143], v245 offset:10240\n\t"
  "buffer_load_dwordx4 v233, s[12:15], s29 offen offset:1024 lds\n\t"
  "v_cvt_pk_bf16_f32 v105, v66, v67\n\t"
  "v_mfma_f32_16x16x32_bf16 v[84:87], v[180:183], v[28:31], v[84:87]\n\t"
  "ds_read_b64_tr_b16 v[144:145], v245 offset:12288\n\t"
  "v_cvt_pk_bf16_f32 v106, v72, v73\n\t"
  "v_mfma_f32_16x16x32_bf16 v[80:83], v[180:183], v[24:27], v[80:83]\n\t"
  "ds_read_b64_tr_b16 v[146:147], v245 offset:14336\n\t"
  "v_cvt_pk_bf16_f32 v107, v74, v75\n\t"
  "v_mfma_f32_16x16x32_bf16 v[80:83], v[184:187], v[32:35], v[80:83]\n\t"
  "ds_read_b64_tr_b16 v[148:149], v245 offset:16384\n\t"
  "v_mfma_f32_16x16x32_bf16 v[84:87], v[184:187], v[36:39], v[84:87]\n\t"
  "ds_read_b64_tr_b16 v[150:151], v245 offset:18432\n\t"
  "buffer_load_dwordx4 v234, s[12:15], s29 offen offset:2048 lds\n\t"
  "v_mfma_f32_16x16x32_bf16 v[84:87], v[188:191], v[44:47], v[84:87]\n\t"
  "ds_read_b64_tr_b16 v[152:153], v245 offset:20480\n\t"
  "v_mfma_f32_16x16x32_bf16 v[80:83], v[188:191], v[40:43], v[80:83]\n\t"
  "ds_read_b64_tr_b16 v[154:155], v245 offset:22528\n\t"
  "buffer_store_dwordx4 v[104:107], v252, s[20:23], s35 offen sc1\n\t"
  "s_waitcnt lgkmcnt(6)\n\t"
  "v_mfma_f32_16x16x32_bf16 v[80:83], v[128:131], v[48:51], v[80:83]\n\t"
  "ds_read_b64_tr_b16 v[156:157], v245 offset:24576\n\t"
  "s_add_u32 s35, s35, 1024\n\t"
  "v_cvt_pk_bf16_f32 v192, v68, v69\n\t"
  "s_waitcnt vmcnt(13)\n\t"
  "s_barrier\n\t"
  "v_mfma_f32_16x16x32_bf16 v[84:87], v[128:131], v[52:55], v[84:87]\n\t"
  "ds_read_b64_tr_b16 v[158:159], v245 offset:26624\n\t"
  "buffer_load_dwordx4 v235, s[12:15], s29 offen offset:3072 lds\n\t"
  "v_cvt_pk_bf16_f32 v193, v70, v71\n\t"
  "v_mfma_f32_16x16x32_bf16 v[88:91], v[132:135], v[0:3], 0\n\t"
  "ds_read_b64_tr_b16 v[160:161], v242\n\t"
  "s_add_u32 s29, s29, 28672\n\t"
  "v_cvt_pk_bf16_f32 v194, v76, v77\n\t"
  "v_mfma_f32_16x16x32_bf16 v[92:95], v[132:135], v[4:7], 0\n\t"
  "ds_read_b64_tr_b16 v[162:163], v242 offset:2048\n\t"
  "v_cvt_pk_bf16_f32 v195, v78, v79\n\t"
  "v_mfma_f32_16x16x32_bf16 v[92:95], v[136:139], v[12:15], v[92:95]\n\t"
  "ds_read_b64_tr_b16 v[164:165], v242 offset:4096\n\t"
  "s_add_u32 m0, s39, 135168\n\t"
  "v_mfma_f32_16x16x32_bf16 v[88:91], v[136:139], v[8:11], v[88:91]\n\t"
  "ds_read_b64_tr_b16 v[166:167], v242 offset:6144\n\t"
  "buffer_load_dwordx4 v232, s[12:15], s29 offen lds\n\t"
  "v_mfma_f32_16x16x32_bf16 v[88:91], v[140:143], v[16:19], v[88:91]\n\t"
  "ds_read_b64_tr_b16 v[168:169], v242 offset:8192\n\t"
  "v_mfma_f32_16x16x32_bf16 v[92:95], v[140:143], v[20:23], v[92:95]\n\t"
  "ds_read_b64_tr_b16 v[170:171], v242 offset:10240\n\t"
  "buffer_store_dwordx4 v[192:195], v253, s[20:23], s36 offen sc1\n\t"
  "s_waitcnt lgkmcnt(6)\n\t"
  "v_mfma_f32_16x16x32_bf16 v[92:95], v[144:147], v[28:31], v[92:95]\n\t"
  "ds_read_b64_tr_b16 v[172:173], v242 offset:12288\n\t"
  "s_add_u32 s36, s36, 1024\n\t"
  "v_mfma_f32_16x16x32_bf16 v[88:91], v[144:147], v[24:27], v[88:91]\n\t"
  "ds_read_b64_tr_b16 v[174:175], v242 offset:14336\n\t"
  "buffer_load_dwordx4 v233, s[12:15], s29 offen offset:1024 lds\n\t"
  "v_mfma_f32_16x16x32_bf16 v[88:91], v[148:151], v[32:35], v[88:91]\n\t"
  "ds_read_b64_tr_b16 v[176:177], v242 offset:16384\n\t"
  "v_mfma_f32_16x16x32_bf16 v[92:95], v[148:151], v[36:39], v[92:95]\n\t"
  "ds_read_b64_tr_b16 v[178:179], v242 offset:18432\n\t"
  "v_mfma_f32_16x16x32_bf16 v[92:95], v[152:155], v[44:47], v[92:95]\n\t"
  "ds_read_b64_tr_b16 v[180:181], v242 offset:20480\n\t"
  "v_mfma_f32_16x16x32_bf16 v[88:91], v[152:155], v[40:43], v[88:91]\n\t"
  "ds_read_b64_tr_b16 v[182:183], v242 offset:22528\n\t"
  "buffer_load_dwordx4 v234, s[12:15], s29 offen offset:2048 lds\n\t"
  "v_mfma_f32_16x16x32_bf16 v[88:91], v[156:159], v[48:51], v[88:91]\n\t"
  "ds_read_b64_tr_b16 v[184:185], v242 offset:24576\n\t"
  "s_add_u32 s29, s29, 4294938752\n\t"
  "v_mfma_f32_16x16x32_bf16 v[92:95], v[156:159], v[52:55], v[92:95]\n\t"
  "ds_read_b64_tr_b16 v[186:187], v242 offset:26624\n\t"
  "s_waitcnt lgkmcnt(6)\n\t"
  "v_mfma_f32_16x16x32_bf16 v[64:67], v[160:163], v[0:3], 0\n\t"
  "ds_read_b64_tr_b16 v[188:189], v243\n\t"
  "v_mfma_f32_16x16x32_bf16 v[68:71], v[160:163], v[4:7], 0\n\t"
  "ds_read_b64_tr_b16 v[190:191], v243 offset:2048\n\t"
  "v_mfma_f32_16x16x32_bf16 v[68:71], v[164:167], v[12:15], v[68:71]\n\t"
  "ds_read_b64_tr_b16 v[128:129], v243 offset:4096\n\t"
  "v_mfma_f32_16x16x32_bf16 v[64:67], v[164:167], v[8:11], v[64:67]\n\t"
  "ds_read_b64_tr_b16 v[130:131], v243 offset:6144\n\t"
  "v_mfma_f32_16x16x32_bf16 v[64:67], v[168:171], v[16:19], v[64:67]\n\t"
  "ds_read_b64_tr_b16 v[132:133], v243 offset:8192\n\t"
  "v_cvt_pk_bf16_f32 v108, v80, v81\n\t"
  "v_mfma_f32_16x16x32_bf16 v[68:71], v[168:171], v[20:23], v[68:71]\n\t"
  "ds_read_b64_tr_b16 v[134:135], v243 offset:10240\n\t"
  "v_cvt_pk_bf16_f32 v109, v82, v83\n\t"
  "v_mfma_f32_16x16x32_bf16 v[68:71], v[172:175], v[28:31], v[68:71]\n\t"
  "ds_read_b64_tr_b16 v[136:137], v243 offset:12288\n\t"
  "v_cvt_pk_bf16_f32 v110, v88, v89\n\t"
  "v_mfma_f32_16x16x32_bf16 v[64:67], v[172:175], v[24:27], v[64:67]\n\t"
  "ds_read_b64_tr_b16 v[138:139], v243 offset:14336\n\t"
  "v_cvt_pk_bf16_f32 v111, v90, v91\n\t"
  "s_waitcnt lgkmcnt(6)\n\t"
  "v_mfma_f32_16x16x32_bf16 v[64:67], v[176:179], v[32:35], v[64:67]\n\t"
  "ds_read_b64_tr_b16 v[140:141], v243 offset:16384\n\t"
  "v_mfma_f32_16x16x32_bf16 v[68:71], v[176:179], v[36:39], v[68:71]\n\t"
  "ds_read_b64_tr_b16 v[142:143], v243 offset:18432\n\t"
  "buffer_store_dwordx4 v[108:111], v252, s[20:23], s35 offen sc1\n\t"
  "v_mfma_f32_16x16x32_bf16 v[68:71], v[180:183], v[44:47], v[68:71]\n\t"
  "ds_read_b64_tr_b16 v[144:145], v243 offset:20480\n\t"
  "s_add_u32 s35, s35, 1024\n\t"
  "v_mfma_f32_16x16x32_bf16 v[64:67], v[180:183], v[40:43], v[64:67]\n\t"
  "ds_read_b64_tr_b16 v[146:147], v243 offset:22528\n\t"
  "v_mfma_f32_16x16x32_bf16 v[64:67], v[184:187], v[48:51], v[64:67]\n\t"
  "ds_read_b64_tr_b16 v[148:149], v243 offset:24576\n\t"
  "v_cvt_pk_bf16_f32 v196, v84, v85\n\t"
  "s_waitcnt vmcnt(10)\n\t"
  "s_barrier\n\t"
  "v_mfma_f32_16x16x32_bf16 v[68:71], v[184:187], v[52:55], v[68:71]\n\t"
  "ds_read_b64_tr_b16 v[150:151], v243 offset:26624\n\t"
  "v_cvt_pk_bf16_f32 v197, v86, v87\n\t"
  "v_mfma_f32_16x16x32_bf16 v[72:75], v[188:191], v[0:3], 0\n\t"
  "ds_read_b64_tr_b16 v[152:153], v244 offset:32768\n\t"
  "v_cvt_pk_bf16_f32 v198, v92, v93\n\t"
  "v_mfma_f32_16x16x32_bf16 v[76:79], v[188:191], v[4:7], 0\n\t"
  "ds_read_b64_tr_b16 v[154:155], v244 offset:34816\n\t"
  "v_cvt_pk_bf16_f32 v199, v94, v95\n\t"
  "s_waitcnt lgkmcnt(6)\n\t"
  "v_mfma_f32_16x16x32_bf16 v[76:79], v[128:131], v[12:15], v[76:79]\n\t"
  "ds_read_b64_tr_b16 v[156:157], v244 offset:36864\n\t"
  "v_mfma_f32_16x16x32_bf16 v[72:75], v[128:131], v[8:11], v[72:75]\n\t"
  "ds_read_b64_tr_b16 v[158:159], v244 offset:38912\n\t"
  "buffer_store_dwordx4 v[196:199], v253, s[20:23], s36 offen sc1\n\t"
  "v_mfma_f32_16x16x32_bf16 v[72:75], v[132:135], v[16:19], v[72:75]\n\t"
  "ds_read_b64_tr_b16 v[160:161], v244 offset:40960\n\t"
  "s_add_u32 s36, s36, 1024\n\t"
  "v_mfma_f32_16x16x32_bf16 v[76:79], v[132:135], v[20:23], v[76:79]\n\t"
  "ds_read_b64_tr_b16 v[162:163], v244 offset:43008\n\t"
  "v_mfma_f32_16x16x32_bf16 v[76:79], v[136:139], v[28:31], v[76:79]\n\t"
  "ds_read_b64_tr_b16 v[164:165], v244 offset:45056\n\t"
  "v_mfma_f32_16x16x32_bf16 v[72:75], v[136:139], v[24:27], v[72:75]\n\t"
  "ds_read_b64_tr_b16 v[166:167], v244 offset:47104\n\t"
  "v_mfma_f32_16x16x32_bf16 v[72:75], v[140:143], v[32:35], v[72:75]\n\t"
  "ds_read_b64_tr_b16 v[168:169], v244 offset:49152\n\t"
  "v_mfma_f32_16x16x32_bf16 v[76:79], v[140:143], v[36:39], v[76:79]\n\t"
  "ds_read_b64_tr_b16 v[170:171], v244 offset:51200\n\t"
  "s_waitcnt lgkmcnt(6)\n\t"
  "v_mfma_f32_16x16x32_bf16 v[76:79], v[144:147], v[44:47], v[76:79]\n\t"
  "ds_read_b64_tr_b16 v[172:173], v244 offset:53248\n\t"
  "v_mfma_f32_16x16x32_bf16 v[72:75], v[144:147], v[40:43], v[72:75]\n\t"
  "ds_read_b64_tr_b16 v[174:175], v244 offset:55296\n\t"
  "v_mfma_f32_16x16x32_bf16 v[72:75], v[148:151], v[48:51], v[72:75]\n\t"
  "ds_read_b64_tr_b16 v[176:177], v244 offset:57344\n\t"
  "v_mfma_f32_16x16x32_bf16 v[76:79], v[148:151], v[52:55], v[76:79]\n\t"
  "ds_read_b64_tr_b16 v[178:179], v244 offset:59392\n\t"
  "v_mfma_f32_16x16x32_bf16 v[80:83], v[152:155], v[0:3], 0\n\t"
  "ds_read_b64_tr_b16 v[180:181], v245 offset:32768\n\t"
  "v_mfma_f32_16x16x32_bf16 v[84:87], v[152:155], v[4:7], 0\n\t"
  "ds_read_b64_tr_b16 v[182:183], v245 offset:34816\n\t"
  "v_mfma_f32_16x16x32_bf16 v[84:87], v[156:159], v[12:15], v[84:87]\n\t"
  "ds_read_b64_tr_b16 v[184:185], v245 offset:36864\n\t"
  "v_mfma_f32_16x16x32_bf16 v[80:83], v[156:159], v[8:11], v[80:83]\n\t"
  "ds_read_b64_tr_b16 v[186:187], v245 offset:38912\n\t"
  "s_waitcnt lgkmcnt(6)\n\t"
  "v_mfma_f32_16x16x32_bf16 v[80:83], v[160:163], v[16:19], v[80:83]\n\t"
  "ds_read_b64_tr_b16 v[188:189], v245 offset:40960\n\t"
  "v_cvt_pk_bf16_f32 v112, v64, v65\n\t"
  "v_mfma_f32_16x16x32_bf16 v[84:87], v[160:163], v[20:23], v[84:87]\n\t"
  "ds_read_b64_tr_b16 v[190:191], v245 offset:43008\n\t"
  "v_cvt_pk_bf16_f32 v113, v66, v67\n\t"
  "v_mfma_f32_16x16x32_bf16 v[84:87], v[164:167], v[28:31], v[84:87]\n\t"
  "ds_read_b64_tr_b16 v[128:129], v245 offset:45056\n\t"
  "v_cvt_pk_bf16_f32 v114, v72, v73\n\t"
  "v_mfma_f32_16x16x32_bf16 v[80:83], v[164:167], v[24:27], v[80:83]\n\t"
  "ds_read_b64_tr_b16 v[130:131], v245 offset:47104\n\t"
  "v_cvt_pk_bf16_f32 v115, v74, v75\n\t"
  "v_mfma_f32_16x16x32_bf16 v[80:83], v[168:171], v[32:35], v[80:83]\n\t"
  "ds_read_b64_tr_b16 v[132:133], v245 offset:49152\n\t"
  "v_mfma_f32_16x16x32_bf16 v[84:87], v[168:171], v[36:39], v[84:87]\n\t"
  "ds_read_b64_tr_b16 v[134:135], v245 offset:51200\n\t"
  "buffer_store_dwordx4 v[112:115], v252, s[20:23], s35 offen sc1\n\t"
  "v_mfma_f32_16x16x32_bf16 v[84:87], v[172:175], v[44:47], v[84:87]\n\t"
  "ds_read_b64_tr_b16 v[136:137], v245 offset:53248\n\t"
  "s_add_u32 s35, s35, 1024\n\t"
  "v_mfma_f32_16x16x32_bf16 v[80:83], v[172:175], v[40:43], v[80:83]\n\t"
  "ds_read_b64_tr_b16 v[138:139], v245 offset:55296\n\t"
  "s_waitcnt lgkmcnt(6)\n\t"
  "v_mfma_f32_16x16x32_bf16 v[80:83], v[176:179], v[48:51], v[80:83]\n\t"
  "ds_read_b64_tr_b16 v[140:141], v245 offset:57344\n\t"
  "v_cvt_pk_bf16_f32 v124, v68, v69\n\t"
  "s_waitcnt vmcnt(3)\n\t"
  "s_barrier\n\t"
  "v_mfma_f32_16x16x32_bf16 v[84:87], v[176:179], v[52:55], v[84:87]\n\t"
  "ds_read_b64_tr_b16 v[142:143], v245 offset:59392\n\t"
  "v_cvt_pk_bf16_f32 v125, v70, v71\n\t"
  "v_mfma_f32_16x16x32_bf16 v[88:91], v[180:183], v[0:3], 0\n\t"
  "ds_read_b64_tr_b16 v[144:145], v246\n\t"
  "v_cvt_pk_bf16_f32 v126, v76, v77\n\t"
  "v_mfma_f32_16x16x32_bf16 v[92:95], v[180:183], v[4:7], 0\n\t"
  "ds_read_b64_tr_b16 v[146:147], v246 offset:2048\n\t"
  "v_cvt_pk_bf16_f32 v127, v78, v79\n\t"
  "v_mfma_f32_16x16x32_bf16 v[92:95], v[184:187], v[12:15], v[92:95]\n\t"
  "ds_read_b64_tr_b16 v[148:149], v246 offset:4096\n\t"
  "v_mfma_f32_16x16x32_bf16 v[88:91], v[184:187], v[8:11], v[88:91]\n\t"
  "ds_read_b64_tr_b16 v[150:151], v246 offset:6144\n\t"
  "buffer_store_dwordx4 v[124:127], v253, s[20:23], s36 offen sc1\n\t"
  "v_mfma_f32_16x16x32_bf16 v[88:91], v[188:191], v[16:19], v[88:91]\n\t"
  "ds_read_b64_tr_b16 v[152:153], v246 offset:8192\n\t"
  "s_add_u32 s36, s36, 1024\n\t"
  "v_mfma_f32_16x16x32_bf16 v[92:95], v[188:191], v[20:23], v[92:95]\n\t"
  "ds_read_b64_tr_b16 v[154:155], v246 offset:10240\n\t"
  "s_waitcnt lgkmcnt(6)\n\t"
  "v_mfma_f32_16x16x32_bf16 v[92:95], v[128:131], v[28:31], v[92:95]\n\t"
  "ds_read_b64_tr_b16 v[156:157], v246 offset:12288\n\t"
  "v_mfma_f32_16x16x32_bf16 v[88:91], v[128:131], v[24:27], v[88:91]\n\t"
  "ds_read_b64_tr_b16 v[158:159], v246 offset:14336\n\t"
  "v_mfma_f32_16x16x32_bf16 v[88:91], v[132:135], v[32:35], v[88:91]\n\t"
  "ds_read_b64_tr_b16 v[160:161], v246 offset:16384\n\t"
  "v_mfma_f32_16x16x32_bf16 v[92:95], v[132:135], v[36:39], v[92:95]\n\t"
  "ds_read_b64_tr_b16 v[162:163], v246 offset:18432\n\t"
  "v_mfma_f32_16x16x32_bf16 v[92:95], v[136:139], v[44:47], v[92:95]\n\t"
  "ds_read_b64_tr_b16 v[164:165], v246 offset:20480\n\t"
  "v_mfma_f32_16x16x32_bf16 v[88:91], v[136:139], v[40:43], v[88:91]\n\t"
  "ds_read_b64_tr_b16 v[166:167], v246 offset:22528\n\t"
  "v_mfma_f32_16x16x32_bf16 v[88:91], v[140:143], v[48:51], v[88:91]\n\t"
  "ds_read_b64_tr_b16 v[168:169], v246 offset:24576\n\t"
  "v_mfma_f32_16x16x32_bf16 v[92:95], v[140:143], v[52:55], v[92:95]\n\t"
  "ds_read_b64_tr_b16 v[170:171], v246 offset:26624\n\t"
  "s_waitcnt lgkmcnt(6)\n\t"
  "v_mfma_f32_16x16x32_bf16 v[64:67], v[144:147], v[0:3], 0\n\t"
  "ds_read_b64_tr_b16 v[172:173], v247\n\t"
  "v_mfma_f32_16x16x32_bf16 v[68:71], v[144:147], v[4:7], 0\n\t"
  "ds_read_b64_tr_b16 v[174:175], v247 offset:2048\n\t"
  "v_mfma_f32_16x16x32_bf16 v[68:71], v[148:151], v[12:15], v[68:71]\n\t"
  "ds_read_b64_tr_b16 v[176:177], v247 offset:4096\n\t"
  "v_mfma_f32_16x16x32_bf16 v[64:67], v[148:151], v[8:11], v[64:67]\n\t"
  "ds_read_b64_tr_b16 v[178:179], v247 offset:6144\n\t"
  "v_mfma_f32_16x16x32_bf16 v[64:67], v[152:155], v[16:19], v[64:67]\n\t"
  "ds_read_b64_tr_b16 v[180:181], v247 offset:8192\n\t"
  "v_cvt_pk_bf16_f32 v116, v80, v81\n\t"
  "v_mfma_f32_16x16x32_bf16 v[68:71], v[152:155], v[20:23], v[68:71]\n\t"
  "ds_read_b64_tr_b16 v[182:183], v247 offset:10240\n\t"
  "v_cvt_pk_bf16_f32 v117, v82, v83\n\t"
  "v_mfma_f32_16x16x32_bf16 v[68:71], v[156:159], v[28:31], v[68:71]\n\t"
  "ds_read_b64_tr_b16 v[184:185], v247 offset:12288\n\t"
  "v_cvt_pk_bf16_f32 v118, v88, v89\n\t"
  "v_mfma_f32_16x16x32_bf16 v[64:67], v[156:159], v[24:27], v[64:67]\n\t"
  "ds_read_b64_tr_b16 v[186:187], v247 offset:14336\n\t"
  "v_cvt_pk_bf16_f32 v119, v90, v91\n\t"
  "s_waitcnt lgkmcnt(6)\n\t"
  "v_mfma_f32_16x16x32_bf16 v[64:67], v[160:163], v[32:35], v[64:67]\n\t"
  "ds_read_b64_tr_b16 v[188:189], v247 offset:16384\n\t"
  "v_mfma_f32_16x16x32_bf16 v[68:71], v[160:163], v[36:39], v[68:71]\n\t"
  "ds_read_b64_tr_b16 v[190:191], v247 offset:18432\n\t"
  "buffer_store_dwordx4 v[116:119], v252, s[20:23], s35 offen sc1\n\t"
  "v_mfma_f32_16x16x32_bf16 v[68:71], v[164:167], v[44:47], v[68:71]\n\t"
  "ds_read_b64_tr_b16 v[128:129], v247 offset:20480\n\t"
  "s_add_u32 s35, s35, 1024\n\t"
  "v_mfma_f32_16x16x32_bf16 v[64:67], v[164:167], v[40:43], v[64:67]\n\t"
  "ds_read_b64_tr_b16 v[130:131], v247 offset:22528\n\t"
  "v_mfma_f32_16x16x32_bf16 v[64:67], v[168:171], v[48:51], v[64:67]\n\t"
  "ds_read_b64_tr_b16 v[132:133], v247 offset:24576\n\t"
  "v_cvt_pk_bf16_f32 v56, v84, v85\n\t"
  "v_mfma_f32_16x16x32_bf16 v[68:71], v[168:171], v[52:55], v[68:71]\n\t"
  "ds_read_b64_tr_b16 v[134:135], v247 offset:26624\n\t"
  "v_cvt_pk_bf16_f32 v57, v86, v87\n\t"
  "v_mfma_f32_16x16x32_bf16 v[72:75], v[172:175], v[0:3], 0\n\t"
  "v_cvt_pk_bf16_f32 v58, v92, v93\n\t"
  "v_mfma_f32_16x16x32_bf16 v[76:79], v[172:175], v[4:7], 0\n\t"
  "v_cvt_pk_bf16_f32 v59, v94, v95\n\t"
  "s_waitcnt lgkmcnt(4)\n\t"
  "v_mfma_f32_16x16x32_bf16 v[76:79], v[176:179], v[12:15], v[76:79]\n\t"
  "v_mfma_f32_16x16x32_bf16 v[72:75], v[176:179], v[8:11], v[72:75]\n\t"
  "buffer_store_dwordx4 v[56:59], v253, s[20:23], s36 offen sc1\n\t"
  "v_mfma_f32_16x16x32_bf16 v[72:75], v[180:183], v[16:19], v[72:75]\n\t"
  "s_add_u32 s36, s36, 1024\n\t"
  "v_mfma_f32_16x16x32_bf16 v[76:79], v[180:183], v[20:23], v[76:79]\n\t"
  "v_mfma_f32_16x16x32_bf16 v[76:79], v[184:187], v[28:31], v[76:79]\n\t"
  "v_mfma_f32_16x16x32_bf16 v[72:75], v[184:187], v[24:27], v[72:75]\n\t"
  "v_mfma_f32_16x16x32_bf16 v[72:75], v[188:191], v[32:35], v[72:75]\n\t"
  "v_mfma_f32_16x16x32_bf16 v[76:79], v[188:191], v[36:39], v[76:79]\n\t"
  "s_waitcnt lgkmcnt(0)\n\t"
  "v_mfma_f32_16x16x32_bf16 v[76:79], v[128:131], v[44:47], v[76:79]\n\t"
  "v_mfma_f32_16x16x32_bf16 v[72:75], v[128:131], v[40:43], v[72:75]\n\t"
  "v_mfma_f32_16x16x32_bf16 v[72:75], v[132:135], v[48:51], v[72:75]\n\t"
  "v_mfma_f32_16x16x32_bf16 v[76:79], v[132:135], v[52:55], v[76:79]\n\t"
  "s_nop 7\n\t"
  "v_cvt_pk_bf16_f32 v120, v64, v65\n\t"
  "v_cvt_pk_bf16_f32 v121, v66, v67\n\t"
  "v_cvt_pk_bf16_f32 v122, v72, v73\n\t"
  "v_cvt_pk_bf16_f32 v123, v74, v75\n\t"
  "buffer_store_dwordx4 v[120:123], v252, s[20:23], s35 offen sc1\n\t"
  "s_add_u32 s35, s35, 1024\n\t"
  "v_cvt_pk_bf16_f32 v60, v68, v69\n\t"
  "v_cvt_pk_bf16_f32 v61, v70, v71\n\t"
  "v_cvt_pk_bf16_f32 v62, v76, v77\n\t"
  "v_cvt_pk_bf16_f32 v63, v78, v79\n\t"
  "buffer_store_dwordx4 v[60:63], v253, s[20:23], s36 offen sc1\n\t"
  "s_add_u32 s36, s36, 1024\n\t"
  "s_lshr_b32 s64, s57, 2\n\t"
  "s_cmp_eq_u32 s64, s66\n\t"
  "s_cselect_b32 s46, 0x00ff00ff, 0\n\t"
  "s_and_b32 s65, s57, 1\n\t"
  "s_lshl_b32 s65, s65, 3\n\t"
  "s_lshl_b32 s46, s46, s65\n\t"
  "s_mov_b32 s47, s46\n\t"
  "v_lshrrev_b32_e32 v223, 4, v254\n\t"
  "v_lshlrev_b32_e32 v223, 7, v223\n\t"
  "v_and_b32_e32 v224, 7, v254\n\t"
  "v_lshl_add_u32 v223, v224, 4, v223\n\t"
  "v_add_u32_e32 v223, 94208, v223\n\t"
  "s_lshl_b32 s65, s67, 9\n\t"
  "v_add_u32_e32 v223, s65, v223\n\t"
  "s_mov_b64 exec, s[46:47]\n\t"
  "s_cbranch_execz .Lkw7\n\t"
  "ds_write_b128 v223, v[96:99]\n\t"
  "ds_write_b128 v223, v[100:103] offset:1024\n\t"
  "ds_write_b128 v223, v[104:107] offset:2048\n\t"
  "ds_write_b128 v223, v[108:111] offset:3072\n\t"
  "ds_write_b128 v223, v[112:115] offset:32768\n\t"
  "ds_write_b128 v223, v[116:119] offset:33792\n\t"
  "ds_write_b128 v223, v[120:123] offset:34816\n\t"
  ".Lkw7:\n\t"
  "s_mov_b64 exec, -1\n\t"
  "s_waitcnt lgkmcnt(0)\n\t"
  "s_mov_b32 s31, 0xffff0000\n\t"
  "s_mul_i32 s60, s37, 7168\n\t"
  "s_lshl_b32 s65, s38, 10\n\t"
  "s_add_u32 s61, s60, s65\n\t"
  "s_lshl_b32 s65, s68, 10\n\t"
  "s_add_u32 s60, s60, s65\n\t"
  "s_add_u32 s16, s98, 50331648\n\t"
  "s_addc_u32 s17, s99, 0\n\t"
  "s_and_b32 s17, s17, 0xffff\n\t"
  "s_mov_b32 s18, 14680064\n\t"
  "s_mov_b32 s19, 0x20000\n\t"
  "s_add_u32 s12, s54, 0\n\t"
  "s_addc_u32 s13, s55, 0\n\t"
  "s_and_b32 s13, s13, 0xffff\n\t"
  "s_mov_b32 s14, 1835008\n\t"
  "s_mov_b32 s15, 0x20000\n\t"
  "s_cmp_eq_u32 s62, 3\n\t"
  "s_cbranch_scc1 .LqF\n\t"
  "v_accvgpr_read_b32 v128, a112\n\t"
  "v_accvgpr_read_b32 v129, a113\n\t"
  "v_accvgpr_read_b32 v130, a114\n\t"
  "v_accvgpr_read_b32 v131, a115\n\t"
  "v_accvgpr_read_b32 v132, a116\n\t"
  "v_accvgpr_read_b32 v133, a117\n\t"
  "v_accvgpr_read_b32 v134, a118\n\t"
  "v_accvgpr_read_b32 v135, a119\n\t"
  "v_accvgpr_read_b32 v136, a120\n\t"
  "v_accvgpr_read_b32 v137, a121\n\t"
  "v_accvgpr_read_b32 v138, a122\n\t"
  "v_accvgpr_read_b32 v139, a123\n\t"
  "v_accvgpr_read_b32 v140, a124\n\t"
  "v_accvgpr_read_b32 v141, a125\n\t"
  "v_accvgpr_read_b32 v142, a126\n\t"
  "v_accvgpr_read_b32 v143, a127\n\t"
  "v_mul_f32_e32 v128, 0xbf317218, v128\n\t"
  "v_mul_f32_e32 v129, 0xbf317218, v129\n\t"
  "v_mul_f32_e32 v130, 0xbf317218, v130\n\t"
  "v_mul_f32_e32 v131, 0xbf317218, v131\n\t"
  "v_mul_f32_e32 v132, 0xbf317218, v132\n\t"
  "v_mul_f32_e32 v133, 0xbf317218, v133\n\t"
  "v_mul_f32_e32 v134, 0xbf317218, v134\n\t"
  "v_mul_f32_e32 v135, 0xbf317218, v135\n\t"
  "v_mul_f32_e32 v136, 0xbf317218, v136\n\t"
  "v_mul_f32_e32 v137, 0xbf317218, v137\n\t"
  "v_mul_f32_e32 v138, 0xbf317218, v138\n\t"
  "v_mul_f32_e32 v139, 0xbf317218, v139\n\t"
  "v_mul_f32_e32 v140, 0xbf317218, v140\n\t"
  "v_mul_f32_e32 v141, 0xbf317218, v141\n\t"
  "v_mul_f32_e32 v142, 0xbf317218, v142\n\t"
  "v_mul_f32_e32 v143, 0xbf317218, v143\n\t"
  "s_waitcnt vmcnt(0)\n\t"
  "s_barrier\n\t"
  "s_barrier\n\t"
  "s_lshr_b32 s64, s68, 2\n\t"
  "s_mul_i32 s64, s64, 28672\n\t"
  "s_lshl_b32 s65, s68, 10\n\t"
  "s_add_u32 s64, s64, s65\n\t"
  "s_add_u32 s64, s64, 94208\n\t"
  "v_add_u32_e32 v224, s64, v251\n\t"
  "ds_read_b128 v[0:3], v224\n\t"
  "s_add_u32 s60, s60, 1835008\n\t"
  "buffer_load_dwordx4 v[4:7], v251, s[16:19], s60 offen sc1\n\t"
  "s_add_u32 s60, s60, 1835008\n\t"
  "buffer_load_dwordx4 v[8:11], v251, s[16:19], s60 offen sc1\n\t"
  "s_add_u32 s60, s60, 1835008\n\t"
  "buffer_load_dwordx4 v[12:15], v251, s[16:19], s60 offen sc1\n\t"
  "s_add_u32 s60, s60, 1835008\n\t"
  "buffer_load_dwordx4 v[16:19], v251, s[16:19], s60 offen sc1\n\t"
  "s_add_u32 s60, s60, 1835008\n\t"
  "buffer_load_dwordx4 v[20:23], v251, s[16:19], s60 offen sc1\n\t"
  "s_add_u32 s60, s60, 1835008\n\t"
  "buffer_load_dwordx4 v[24:27], v251, s[16:19], s60 offen sc1\n\t"
  "s_add_u32 s60, s60, 1835008\n\t"
  "buffer_load_dwordx4 v[28:31], v251, s[16:19], s60 offen sc1\n\t"
  "s_lshr_b32 s64, s38, 2\n\t"
  "s_mul_i32 s64, s64, 28672\n\t"
  "s_lshl_b32 s65, s38, 10\n\t"
  "s_add_u32 s64, s64, s65\n\t"
  "s_add_u32 s64, s64, 94208\n\t"
  "v_add_u32_e32 v224, s64, v251\n\t"
  "ds_read_b128 v[32:35], v224\n\t"
  "s_add_u32 s61, s61, 1835008\n\t"
  "buffer_load_dwordx4 v[36:39], v251, s[16:19], s61 offen sc1\n\t"
  "s_add_u32 s61, s61, 1835008\n\t"
  "buffer_load_dwordx4 v[40:43], v251, s[16:19], s61 offen sc1\n\t"
  "s_add_u32 s61, s61, 1835008\n\t"
  "buffer_load_dwordx4 v[44:47], v251, s[16:19], s61 offen sc1\n\t"
  "s_add_u32 s61, s61, 1835008\n\t"
  "buffer_load_dwordx4 v[48:51], v251, s[16:19], s61 offen sc1\n\t"
  "s_add_u32 s61, s61, 1835008\n\t"
  "buffer_load_dwordx4 v[52:55], v251, s[16:19], s61 offen sc1\n\t"
  "s_add_u32 s61, s61, 1835008\n\t"
  "buffer_load_dwordx4 v[56:59], v251, s[16:19], s61 offen sc1\n\t"
  "s_add_u32 s61, s61, 1835008\n\t"
  "buffer_load_dwordx4 v[60:63], v251, s[16:19], s61 offen sc1\n\t"
  "s_waitcnt lgkmcnt(1)\n\t"
  "v_lshlrev_b32_e32 v64, 16, v0\n\t"
  "v_and_b32_e32 v65, s31, v0\n\t"
  "v_lshlrev_b32_e32 v66, 16, v1\n\t"
  "v_and_b32_e32 v67, s31, v1\n\t"
  "v_lshlrev_b32_e32 v68, 16, v2\n\t"
  "v_and_b32_e32 v69, s31, v2\n\t"
  "v_lshlrev_b32_e32 v70, 16, v3\n\t"
  "v_and_b32_e32 v71, s31, v3\n\t"
  "s_waitcnt vmcnt(13)\n\t"
  "v_lshlrev_b32_e32 v216, 16, v4\n\t"
  "v_and_b32_e32 v217, s31, v4\n\t"
  "v_pk_add_f32 v[64:65], v[64:65], v[216:217]\n\t"
  "v_lshlrev_b32_e32 v216, 16, v5\n\t"
  "v_and_b32_e32 v217, s31, v5\n\t"
  "v_pk_add_f32 v[66:67], v[66:67], v[216:217]\n\t"
  "v_lshlrev_b32_e32 v216, 16, v6\n\t"
  "v_and_b32_e32 v217, s31, v6\n\t"
  "v_pk_add_f32 v[68:69], v[68:69], v[216:217]\n\t"
  "v_lshlrev_b32_e32 v216, 16, v7\n\t"
  "v_and_b32_e32 v217, s31, v7\n\t"
  "v_pk_add_f32 v[70:71], v[70:71], v[216:217]\n\t"
  "s_waitcnt vmcnt(12)\n\t"
  "v_lshlrev_b32_e32 v216, 16, v8\n\t"
  "v_and_b32_e32 v217, s31, v8\n\t"
  "v_pk_add_f32 v[64:65], v[64:65], v[216:217]\n\t"
  "v_lshlrev_b32_e32 v216, 16, v9\n\t"
  "v_and_b32_e32 v217, s31, v9\n\t"
  "v_pk_add_f32 v[66:67], v[66:67], v[216:217]\n\t"
  "v_lshlrev_b32_e32 v216, 16, v10\n\t"
  "v_and_b32_e32 v217, s31, v10\n\t"
  "v_pk_add_f32 v[68:69], v[68:69], v[216:217]\n\t"
  "v_lshlrev_b32_e32 v216, 16, v11\n\t"
  "v_and_b32_e32 v217, s31, v11\n\t"
  "v_pk_add_f32 v[70:71], v[70:71], v[216:217]\n\t"
  "s_waitcnt vmcnt(11)\n\t"
  "v_lshlrev_b32_e32 v216, 16, v12\n\t"
  "v_and_b32_e32 v217, s31, v12\n\t"
  "v_pk_add_f32 v[64:65], v[64:65], v[216:217]\n\t"
  "v_lshlrev_b32_e32 v216, 16, v13\n\t"
  "v_and_b32_e32 v217, s31, v13\n\t"
  "v_pk_add_f32 v[66:67], v[66:67], v[216:217]\n\t"
  "v_lshlrev_b32_e32 v216, 16, v14\n\t"
  "v_and_b32_e32 v217, s31, v14\n\t"
  "v_pk_add_f32 v[68:69], v[68:69], v[216:217]\n\t"
  "v_lshlrev_b32_e32 v216, 16, v15\n\t"
  "v_and_b32_e32 v217, s31, v15\n\t"
  "v_pk_add_f32 v[70:71], v[70:71], v[216:217]\n\t"
  "s_waitcnt vmcnt(10)\n\t"
  "v_lshlrev_b32_e32 v216, 16, v16\n\t"
  "v_and_b32_e32 v217, s31, v16\n\t"
  "v_pk_add_f32 v[64:65], v[64:65], v[216:217]\n\t"
  "v_lshlrev_b32_e32 v216, 16, v17\n\t"
  "v_and_b32_e32 v217, s31, v17\n\t"
  "v_pk_add_f32 v[66:67], v[66:67], v[216:217]\n\t"
  "v_lshlrev_b32_e32 v216, 16, v18\n\t"
  "v_and_b32_e32 v217, s31, v18\n\t"
  "v_pk_add_f32 v[68:69], v[68:69], v[216:217]\n\t"
  "v_lshlrev_b32_e32 v216, 16, v19\n\t"
  "v_and_b32_e32 v217, s31, v19\n\t"
  "v_pk_add_f32 v[70:71], v[70:71], v[216:217]\n\t"
  "s_waitcnt vmcnt(9)\n\t"
  "v_lshlrev_b32_e32 v216, 16, v20\n\t"
  "v_and_b32_e32 v217, s31, v20\n\t"
  "v_pk_add_f32 v[64:65], v[64:65], v[216:217]\n\t"
  "v_lshlrev_b32_e32 v216, 16, v21\n\t"
  "v_and_b32_e32 v217, s31, v21\n\t"
  "v_pk_add_f32 v[66:67], v[66:67], v[216:217]\n\t"
  "v_lshlrev_b32_e32 v216, 16, v22\n\t"
  "v_and_b32_e32 v217, s31, v22\n\t"
  "v_pk_add_f32 v[68:69], v[68:69], v[216:217]\n\t"
  "v_lshlrev_b32_e32 v216, 16, v23\n\t"
  "v_and_b32_e32 v217, s31, v23\n\t"
  "v_pk_add_f32 v[70:71], v[70:71], v[216:217]\n\t"
  "s_waitcnt vmcnt(8)\n\t"
  "v_lshlrev_b32_e32 v216, 16, v24\n\t"
  "v_and_b32_e32 v217, s31, v24\n\t"
  "v_pk_add_f32 v[64:65], v[64:65], v[216:217]\n\t"
  "v_lshlrev_b32_e32 v216, 16, v25\n\t"
  "v_and_b32_e32 v217, s31, v25\n\t"
  "v_pk_add_f32 v[66:67], v[66:67], v[216:217]\n\t"
  "v_lshlrev_b32_e32 v216, 16, v26\n\t"
  "v_and_b32_e32 v217, s31, v26\n\t"
  "v_pk_add_f32 v[68:69], v[68:69], v[216:217]\n\t"
  "v_lshlrev_b32_e32 v216, 16, v27\n\t"
  "v_and_b32_e32 v217, s31, v27\n\t"
  "v_pk_add_f32 v[70:71], v[70:71], v[216:217]\n\t"
  "s_waitcnt vmcnt(7)\n\t"
  "v_lshlrev_b32_e32 v216, 16, v28\n\t"
  "v_and_b32_e32 v217, s31, v28\n\t"
  "v_pk_add_f32 v[64:65], v[64:65], v[216:217]\n\t"
  "v_lshlrev_b32_e32 v216, 16, v29\n\t"
  "v_and_b32_e32 v217, s31, v29\n\t"
  "v_pk_add_f32 v[66:67], v[66:67], v[216:217]\n\t"
  "v_lshlrev_b32_e32 v216, 16, v30\n\t"
  "v_and_b32_e32 v217, s31, v30\n\t"
  "v_pk_add_f32 v[68:69], v[68:69], v[216:217]\n\t"
  "v_lshlrev_b32_e32 v216, 16, v31\n\t"
  "v_and_b32_e32 v217, s31, v31\n\t"
  "v_pk_add_f32 v[70:71], v[70:71], v[216:217]\n\t"
  "v_fmac_f32_e32 v200, v64, v128\n\t"
  "v_fmac_f32_e32 v201, v65, v129\n\t"
  "v_fmac_f32_e32 v202, v66, v130\n\t"
  "v_fmac_f32_e32 v203, v67, v131\n\t"
  "v_fmac_f32_e32 v204, v68, v132\n\t"
  "v_fmac_f32_e32 v205, v69, v133\n\t"
  "v_fmac_f32_e32 v206, v70, v134\n\t"
  "v_fmac_f32_e32 v207, v71, v135\n\t"
  "v_cvt_pk_bf16_f32 v80, v200, v201\n\t"
  "v_cvt_pk_bf16_f32 v81, v202, v203\n\t"
  "v_cvt_pk_bf16_f32 v82, v204, v205\n\t"
  "v_cvt_pk_bf16_f32 v83, v206, v207\n\t"
  "buffer_store_dwordx2 v[80:81], v220, s[12:15], s92 offen\n\t"
  "buffer_store_dwordx2 v[82:83], v220, s[12:15], s92 offen offset:32\n\t"
  "s_waitcnt lgkmcnt(0)\n\t"
  "v_lshlrev_b32_e32 v72, 16, v32\n\t"
  "v_and_b32_e32 v73, s31, v32\n\t"
  "v_lshlrev_b32_e32 v74, 16, v33\n\t"
  "v_and_b32_e32 v75, s31, v33\n\t"
  "v_lshlrev_b32_e32 v76, 16, v34\n\t"
  "v_and_b32_e32 v77, s31, v34\n\t"
  "v_lshlrev_b32_e32 v78, 16, v35\n\t"
  "v_and_b32_e32 v79, s31, v35\n\t"
  "s_waitcnt vmcnt(6)\n\t"
  "v_lshlrev_b32_e32 v216, 16, v36\n\t"
  "v_and_b32_e32 v217, s31, v36\n\t"
  "v_pk_add_f32 v[72:73], v[72:73], v[216:217]\n\t"
  "v_lshlrev_b32_e32 v216, 16, v37\n\t"
  "v_and_b32_e32 v217, s31, v37\n\t"
  "v_pk_add_f32 v[74:75], v[74:75], v[216:217]\n\t"
  "v_lshlrev_b32_e32 v216, 16, v38\n\t"
  "v_and_b32_e32 v217, s31, v38\n\t"
  "v_pk_add_f32 v[76:77], v[76:77], v[216:217]\n\t"
  "v_lshlrev_b32_e32 v216, 16, v39\n\t"
  "v_and_b32_e32 v217, s31, v39\n\t"
  "v_pk_add_f32 v[78:79], v[78:79], v[216:217]\n\t"
  "s_waitcnt vmcnt(5)\n\t"
  "v_lshlrev_b32_e32 v216, 16, v40\n\t"
  "v_and_b32_e32 v217, s31, v40\n\t"
  "v_pk_add_f32 v[72:73], v[72:73], v[216:217]\n\t"
  "v_lshlrev_b32_e32 v216, 16, v41\n\t"
  "v_and_b32_e32 v217, s31, v41\n\t"
  "v_pk_add_f32 v[74:75], v[74:75], v[216:217]\n\t"
  "v_lshlrev_b32_e32 v216, 16, v42\n\t"
  "v_and_b32_e32 v217, s31, v42\n\t"
  "v_pk_add_f32 v[76:77], v[76:77], v[216:217]\n\t"
  "v_lshlrev_b32_e32 v216, 16, v43\n\t"
  "v_and_b32_e32 v217, s31, v43\n\t"
  "v_pk_add_f32 v[78:79], v[78:79], v[216:217]\n\t"
  "s_waitcnt vmcnt(4)\n\t"
  "v_lshlrev_b32_e32 v216, 16, v44\n\t"
  "v_and_b32_e32 v217, s31, v44\n\t"
  "v_pk_add_f32 v[72:73], v[72:73], v[216:217]\n\t"
  "v_lshlrev_b32_e32 v216, 16, v45\n\t"
  "v_and_b32_e32 v217, s31, v45\n\t"
  "v_pk_add_f32 v[74:75], v[74:75], v[216:217]\n\t"
  "v_lshlrev_b32_e32 v216, 16, v46\n\t"
  "v_and_b32_e32 v217, s31, v46\n\t"
  "v_pk_add_f32 v[76:77], v[76:77], v[216:217]\n\t"
  "v_lshlrev_b32_e32 v216, 16, v47\n\t"
  "v_and_b32_e32 v217, s31, v47\n\t"
  "v_pk_add_f32 v[78:79], v[78:79], v[216:217]\n\t"
  "s_waitcnt vmcnt(3)\n\t"
  "v_lshlrev_b32_e32 v216, 16, v48\n\t"
  "v_and_b32_e32 v217, s31, v48\n\t"
  "v_pk_add_f32 v[72:73], v[72:73], v[216:217]\n\t"
  "v_lshlrev_b32_e32 v216, 16, v49\n\t"
  "v_and_b32_e32 v217, s31, v49\n\t"
  "v_pk_add_f32 v[74:75], v[74:75], v[216:217]\n\t"
  "v_lshlrev_b32_e32 v216, 16, v50\n\t"
  "v_and_b32_e32 v217, s31, v50\n\t"
  "v_pk_add_f32 v[76:77], v[76:77], v[216:217]\n\t"
  "v_lshlrev_b32_e32 v216, 16, v51\n\t"
  "v_and_b32_e32 v217, s31, v51\n\t"
  "v_pk_add_f32 v[78:79], v[78:79], v[216:217]\n\t"
  "s_waitcnt vmcnt(2)\n\t"
  "v_lshlrev_b32_e32 v216, 16, v52\n\t"
  "v_and_b32_e32 v217, s31, v52\n\t"
  "v_pk_add_f32 v[72:73], v[72:73], v[216:217]\n\t"
  "v_lshlrev_b32_e32 v216, 16, v53\n\t"
  "v_and_b32_e32 v217, s31, v53\n\t"
  "v_pk_add_f32 v[74:75], v[74:75], v[216:217]\n\t"
  "v_lshlrev_b32_e32 v216, 16, v54\n\t"
  "v_and_b32_e32 v217, s31, v54\n\t"
  "v_pk_add_f32 v[76:77], v[76:77], v[216:217]\n\t"
  "v_lshlrev_b32_e32 v216, 16, v55\n\t"
  "v_and_b32_e32 v217, s31, v55\n\t"
  "v_pk_add_f32 v[78:79], v[78:79], v[216:217]\n\t"
  "s_waitcnt vmcnt(1)\n\t"
  "v_lshlrev_b32_e32 v216, 16, v56\n\t"
  "v_and_b32_e32 v217, s31, v56\n\t"
  "v_pk_add_f32 v[72:73], v[72:73], v[216:217]\n\t"
  "v_lshlrev_b32_e32 v216, 16, v57\n\t"
  "v_and_b32_e32 v217, s31, v57\n\t"
  "v_pk_add_f32 v[74:75], v[74:75], v[216:217]\n\t"
  "v_lshlrev_b32_e32 v216, 16, v58\n\t"
  "v_and_b32_e32 v217, s31, v58\n\t"
  "v_pk_add_f32 v[76:77], v[76:77], v[216:217]\n\t"
  "v_lshlrev_b32_e32 v216, 16, v59\n\t"
  "v_and_b32_e32 v217, s31, v59\n\t"
  "v_pk_add_f32 v[78:79], v[78:79], v[216:217]\n\t"
  "s_waitcnt vmcnt(0)\n\t"
  "v_lshlrev_b32_e32 v216, 16, v60\n\t"
  "v_and_b32_e32 v217, s31, v60\n\t"
  "v_pk_add_f32 v[72:73], v[72:73], v[216:217]\n\t"
  "v_lshlrev_b32_e32 v216, 16, v61\n\t"
  "v_and_b32_e32 v217, s31, v61\n\t"
  "v_pk_add_f32 v[74:75], v[74:75], v[216:217]\n\t"
  "v_lshlrev_b32_e32 v216, 16, v62\n\t"
  "v_and_b32_e32 v217, s31, v62\n\t"
  "v_pk_add_f32 v[76:77], v[76:77], v[216:217]\n\t"
  "v_lshlrev_b32_e32 v216, 16, v63\n\t"
  "v_and_b32_e32 v217, s31, v63\n\t"
  "v_pk_add_f32 v[78:79], v[78:79], v[216:217]\n\t"
  "v_fmac_f32_e32 v208, v72, v136\n\t"
  "v_fmac_f32_e32 v209, v73, v137\n\t"
  "v_fmac_f32_e32 v210, v74, v138\n\t"
  "v_fmac_f32_e32 v211, v75, v139\n\t"
  "v_fmac_f32_e32 v212, v76, v140\n\t"
  "v_fmac_f32_e32 v213, v77, v141\n\t"
  "v_fmac_f32_e32 v214, v78, v142\n\t"
  "v_fmac_f32_e32 v215, v79, v143\n\t"
  "v_cvt_pk_bf16_f32 v84, v208, v209\n\t"
  "v_cvt_pk_bf16_f32 v85, v210, v211\n\t"
  "v_cvt_pk_bf16_f32 v86, v212, v213\n\t"
  "v_cvt_pk_bf16_f32 v87, v214, v215\n\t"
  "buffer_store_dwordx2 v[84:85], v220, s[12:15], s93 offen\n\t"
  "buffer_store_dwordx2 v[86:87], v220, s[12:15], s93 offen offset:32\n\t"
  "s_branch .LjF\n\t"
  ".LqF:\n\t"
  "s_nop 7\n\t"
  "v_accvgpr_read_b32 v128, a112\n\t"
  "v_accvgpr_read_b32 v129, a113\n\t"
  "v_accvgpr_read_b32 v130, a114\n\t"
  "v_accvgpr_read_b32 v131, a115\n\t"
  "v_accvgpr_read_b32 v132, a116\n\t"
  "v_accvgpr_read_b32 v133, a117\n\t"
  "v_accvgpr_read_b32 v134, a118\n\t"
  "v_accvgpr_read_b32 v135, a119\n\t"
  "v_accvgpr_read_b32 v136, a120\n\t"
  "v_accvgpr_read_b32 v137, a121\n\t"
  "v_accvgpr_read_b32 v138, a122\n\t"
  "v_accvgpr_read_b32 v139, a123\n\t"
  "v_accvgpr_read_b32 v140, a124\n\t"
  "v_accvgpr_read_b32 v141, a125\n\t"
  "v_accvgpr_read_b32 v142, a126\n\t"
  "v_accvgpr_read_b32 v143, a127\n\t"
  "v_mul_f32_e32 v128, 0xbf317218, v128\n\t"
  "v_mul_f32_e32 v129, 0xbf317218, v129\n\t"
  "v_mul_f32_e32 v130, 0xbf317218, v130\n\t"
  "v_mul_f32_e32 v131, 0xbf317218, v131\n\t"
  "v_mul_f32_e32 v132, 0xbf317218, v132\n\t"
  "v_mul_f32_e32 v133, 0xbf317218, v133\n\t"
  "v_mul_f32_e32 v134, 0xbf317218, v134\n\t"
  "v_mul_f32_e32 v135, 0xbf317218, v135\n\t"
  "v_mul_f32_e32 v136, 0xbf317218, v136\n\t"
  "v_mul_f32_e32 v137, 0xbf317218, v137\n\t"
  "v_mul_f32_e32 v138, 0xbf317218, v138\n\t"
  "v_mul_f32_e32 v139, 0xbf317218, v139\n\t"
  "v_mul_f32_e32 v140, 0xbf317218, v140\n\t"
  "v_mul_f32_e32 v141, 0xbf317218, v141\n\t"
  "v_mul_f32_e32 v142, 0xbf317218, v142\n\t"
  "v_mul_f32_e32 v143, 0xbf317218, v143\n\t"
  "s_andn2_b32 s64, s59, 7\n\t"
  "s_lshl_b32 s64, s64, 5\n\t"
  "s_and_b32 s65, s59, 7\n\t"
  "s_add_u32 s64, s64, s65\n\t"
  "s_lshl_b32 s64, s64, 2\n\t"
  "s_add_u32 s64, s64, 397312\n\t"
  "v_lshlrev_b32_e32 v222, 7, v254\n\t"
  "v_add_u32_e32 v222, s64, v222\n\t"
  "v_mov_b32_e32 v223, s69\n\t"
  "s_lshl_b32 s64, s59, 7\n\t"
  "s_add_u32 s64, s64, 397312\n\t"
  "v_and_b32_e32 v216, 7, v254\n\t"
  "v_lshlrev_b32_e32 v216, 2, v216\n\t"
  "v_add_u32_e32 v216, s64, v216\n\t"
  "s_mov_b32 s30, 1048576\n\t"
  "s_waitcnt vmcnt(0)\n\t"
  "s_barrier\n\t"
  "s_mov_b64 exec, 255\n\t"
  "global_store_dword v222, v223, s[98:99] sc1\n\t"
  "s_mov_b64 exec, -1\n\t"
  ".Lpoll397312:\n\t"
  "global_load_dword v217, v216, s[98:99] sc1\n\t"
  "s_waitcnt vmcnt(0)\n\t"
  "v_xor_b32_e32 v217, s69, v217\n\t"
  "v_cmp_ne_u32_e32 vcc, 0, v217\n\t"
  "s_sub_u32 s30, s30, 1\n\t"
  "s_nop 2\n\t"
  "s_cbranch_vccz .Lpollk397312\n\t"
  "s_sleep 1\n\t"
  "s_cmp_lg_u32 s30, 0\n\t"
  "s_cbranch_scc1 .Lpoll397312\n\t"
  ".Lpollk397312:\n\t"
  "s_barrier\n\t"
  "s_lshr_b32 s64, s38, 2\n\t"
  "s_mul_i32 s64, s64, 28672\n\t"
  "s_lshl_b32 s65, s38, 10\n\t"
  "s_add_u32 s64, s64, s65\n\t"
  "s_add_u32 s64, s64, 94208\n\t"
  "v_add_u32_e32 v224, s64, v251\n\t"
  "ds_read_b128 v[32:35], v224\n\t"
  "s_add_u32 s61, s61, 1835008\n\t"
  "buffer_load_dwordx4 v[36:39], v251, s[16:19], s61 offen sc1\n\t"
  "s_add_u32 s61, s61, 1835008\n\t"
  "buffer_load_dwordx4 v[40:43], v251, s[16:19], s61 offen sc1\n\t"
  "s_add_u32 s61, s61, 1835008\n\t"
  "buffer_load_dwordx4 v[44:47], v251, s[16:19], s61 offen sc1\n\t"
  "s_add_u32 s61, s61, 1835008\n\t"
  "buffer_load_dwordx4 v[48:51], v251, s[16:19], s61 offen sc1\n\t"
  "s_add_u32 s61, s61, 1835008\n\t"
  "buffer_load_dwordx4 v[52:55], v251, s[16:19], s61 offen sc1\n\t"
  "s_add_u32 s61, s61, 1835008\n\t"
  "buffer_load_dwordx4 v[56:59], v251, s[16:19], s61 offen sc1\n\t"
  "s_add_u32 s61, s61, 1835008\n\t"
  "buffer_load_dwordx4 v[60:63], v251, s[16:19], s61 offen sc1\n\t"
  "s_waitcnt lgkmcnt(0)\n\t"
  "v_lshlrev_b32_e32 v72, 16, v32\n\t"
  "v_and_b32_e32 v73, s31, v32\n\t"
  "v_lshlrev_b32_e32 v74, 16, v33\n\t"
  "v_and_b32_e32 v75, s31, v33\n\t"
  "v_lshlrev_b32_e32 v76, 16, v34\n\t"
  "v_and_b32_e32 v77, s31, v34\n\t"
  "v_lshlrev_b32_e32 v78, 16, v35\n\t"
  "v_and_b32_e32 v79, s31, v35\n\t"
  "s_waitcnt vmcnt(6)\n\t"
  "v_lshlrev_b32_e32 v216, 16, v36\n\t"
  "v_and_b32_e32 v217, s31, v36\n\t"
  "v_pk_add_f32 v[72:73], v[72:73], v[216:217]\n\t"
  "v_lshlrev_b32_e32 v216, 16, v37\n\t"
  "v_and_b32_e32 v217, s31, v37\n\t"
  "v_pk_add_f32 v[74:75], v[74:75], v[216:217]\n\t"
  "v_lshlrev_b32_e32 v216, 16, v38\n\t"
  "v_and_b32_e32 v217, s31, v38\n\t"
  "v_pk_add_f32 v[76:77], v[76:77], v[216:217]\n\t"
  "v_lshlrev_b32_e32 v216, 16, v39\n\t"
  "v_and_b32_e32 v217, s31, v39\n\t"
  "v_pk_add_f32 v[78:79], v[78:79], v[216:217]\n\t"
  "s_waitcnt vmcnt(5)\n\t"
  "v_lshlrev_b32_e32 v216, 16, v40\n\t"
  "v_and_b32_e32 v217, s31, v40\n\t"
  "v_pk_add_f32 v[72:73], v[72:73], v[216:217]\n\t"
  "v_lshlrev_b32_e32 v216, 16, v41\n\t"
  "v_and_b32_e32 v217, s31, v41\n\t"
  "v_pk_add_f32 v[74:75], v[74:75], v[216:217]\n\t"
  "v_lshlrev_b32_e32 v216, 16, v42\n\t"
  "v_and_b32_e32 v217, s31, v42\n\t"
  "v_pk_add_f32 v[76:77], v[76:77], v[216:217]\n\t"
  "v_lshlrev_b32_e32 v216, 16, v43\n\t"
  "v_and_b32_e32 v217, s31, v43\n\t"
  "v_pk_add_f32 v[78:79], v[78:79], v[216:217]\n\t"
  "s_waitcnt vmcnt(4)\n\t"
  "v_lshlrev_b32_e32 v216, 16, v44\n\t"
  "v_and_b32_e32 v217, s31, v44\n\t"
  "v_pk_add_f32 v[72:73], v[72:73], v[216:217]\n\t"
  "v_lshlrev_b32_e32 v216, 16, v45\n\t"
  "v_and_b32_e32 v217, s31, v45\n\t"
  "v_pk_add_f32 v[74:75], v[74:75], v[216:217]\n\t"
  "v_lshlrev_b32_e32 v216, 16, v46\n\t"
  "v_and_b32_e32 v217, s31, v46\n\t"
  "v_pk_add_f32 v[76:77], v[76:77], v[216:217]\n\t"
  "v_lshlrev_b32_e32 v216, 16, v47\n\t"
  "v_and_b32_e32 v217, s31, v47\n\t"
  "v_pk_add_f32 v[78:79], v[78:79], v[216:217]\n\t"
  "s_waitcnt vmcnt(3)\n\t"
  "v_lshlrev_b32_e32 v216, 16, v48\n\t"
  "v_and_b32_e32 v217, s31, v48\n\t"
  "v_pk_add_f32 v[72:73], v[72:73], v[216:217]\n\t"
  "v_lshlrev_b32_e32 v216, 16, v49\n\t"
  "v_and_b32_e32 v217, s31, v49\n\t"
  "v_pk_add_f32 v[74:75], v[74:75], v[216:217]\n\t"
  "v_lshlrev_b32_e32 v216, 16, v50\n\t"
  "v_and_b32_e32 v217, s31, v50\n\t"
  "v_pk_add_f32 v[76:77], v[76:77], v[216:217]\n\t"
  "v_lshlrev_b32_e32 v216, 16, v51\n\t"
  "v_and_b32_e32 v217, s31, v51\n\t"
  "v_pk_add_f32 v[78:79], v[78:79], v[216:217]\n\t"
  "s_waitcnt vmcnt(2)\n\t"
  "v_lshlrev_b32_e32 v216, 16, v52\n\t"
  "v_and_b32_e32 v217, s31, v52\n\t"
  "v_pk_add_f32 v[72:73], v[72:73], v[216:217]\n\t"
  "v_lshlrev_b32_e32 v216, 16, v53\n\t"
  "v_and_b32_e32 v217, s31, v53\n\t"
  "v_pk_add_f32 v[74:75], v[74:75], v[216:217]\n\t"
  "v_lshlrev_b32_e32 v216, 16, v54\n\t"
  "v_and_b32_e32 v217, s31, v54\n\t"
  "v_pk_add_f32 v[76:77], v[76:77], v[216:217]\n\t"
  "v_lshlrev_b32_e32 v216, 16, v55\n\t"
  "v_and_b32_e32 v217, s31, v55\n\t"
  "v_pk_add_f32 v[78:79], v[78:79], v[216:217]\n\t"
  "s_waitcnt vmcnt(1)\n\t"
  "v_lshlrev_b32_e32 v216, 16, v56\n\t"
  "v_and_b32_e32 v217, s31, v56\n\t"
  "v_pk_add_f32 v[72:73], v[72:73], v[216:217]\n\t"
  "v_lshlrev_b32_e32 v216, 16, v57\n\t"
  "v_and_b32_e32 v217, s31, v57\n\t"
  "v_pk_add_f32 v[74:75], v[74:75], v[216:217]\n\t"
  "v_lshlrev_b32_e32 v216, 16, v58\n\t"
  "v_and_b32_e32 v217, s31, v58\n\t"
  "v_pk_add_f32 v[76:77], v[76:77], v[216:217]\n\t"
  "v_lshlrev_b32_e32 v216, 16, v59\n\t"
  "v_and_b32_e32 v217, s31, v59\n\t"
  "v_pk_add_f32 v[78:79], v[78:79], v[216:217]\n\t"
  "s_waitcnt vmcnt(0)\n\t"
  "v_lshlrev_b32_e32 v216, 16, v60\n\t"
  "v_and_b32_e32 v217, s31, v60\n\t"
  "v_pk_add_f32 v[72:73], v[72:73], v[216:217]\n\t"
  "v_lshlrev_b32_e32 v216, 16, v61\n\t"
  "v_and_b32_e32 v217, s31, v61\n\t"
  "v_pk_add_f32 v[74:75], v[74:75], v[216:217]\n\t"
  "v_lshlrev_b32_e32 v216, 16, v62\n\t"
  "v_and_b32_e32 v217, s31, v62\n\t"
  "v_pk_add_f32 v[76:77], v[76:77], v[216:217]\n\t"
  "v_lshlrev_b32_e32 v216, 16, v63\n\t"
  "v_and_b32_e32 v217, s31, v63\n\t"
  "v_pk_add_f32 v[78:79], v[78:79], v[216:217]\n\t"
  "v_fmac_f32_e32 v208, v72, v136\n\t"
  "v_fmac_f32_e32 v209, v73, v137\n\t"
  "v_fmac_f32_e32 v210, v74, v138\n\t"
  "v_fmac_f32_e32 v211, v75, v139\n\t"
  "v_fmac_f32_e32 v212, v76, v140\n\t"
  "v_fmac_f32_e32 v213, v77, v141\n\t"
  "v_fmac_f32_e32 v214, v78, v142\n\t"
  "v_fmac_f32_e32 v215, v79, v143\n\t"
  "v_cvt_pk_bf16_f32 v84, v208, v209\n\t"
  "v_cvt_pk_bf16_f32 v85, v210, v211\n\t"
  "v_cvt_pk_bf16_f32 v86, v212, v213\n\t"
  "v_cvt_pk_bf16_f32 v87, v214, v215\n\t"
  "buffer_store_dwordx2 v[84:85], v220, s[12:15], s93 offen\n\t"
  "buffer_store_dwordx2 v[86:87], v220, s[12:15], s93 offen offset:32\n\t"
  ".LjF:\n\t"
  "s_nop 1\n\t"
  "s_endpgm\n\t"
  ".Lfallback:\n\t"
  "s_mov_b32 s68, -1\n\t"
  "s_waitcnt vmcnt(0) lgkmcnt(0)\n\t"
  "s_barrier\n\t"
  "v_bfe_u32 v246, v254, 2, 3\n\t"
  "v_mul_u32_u24_e32 v246, 384, v246\n\t"
  "v_sub_u32_e32 v246, v235, v246\n\t"
  "v_add_u32_e32 v235, 3072, v246\n\t"
  "v_add_u32_e32 v236, 2176, v246\n\t"
  "v_add_u32_e32 v237, 1280, v246\n\t"
  "v_add_u32_e32 v238, 384, v246\n\t"
  "s_mov_b32 s28, s30\n\t"
  "s_mov_b32 s29, s30\n\t"
  "s_add_u32 m0, s30, 0\n\t"
  "s_nop 0\n\t"
  "buffer_load_dwordx4 v235, s[8:11], s28 offen lds\n\t"
  "buffer_load_dwordx4 v236, s[8:11], s28 offen offset:1024 lds\n\t"
  "buffer_load_dwordx4 v237, s[8:11], s28 offen offset:2048 lds\n\t"
  "buffer_load_dwordx4 v238, s[8:11], s28 offen offset:3072 lds\n\t"
  "s_add_u32 s28, s28, 4096\n\t"
  "s_add_u32 m0, s30, 4096\n\t"
  "s_nop 0\n\t"
  "buffer_load_dwordx4 v235, s[8:11], s28 offen lds\n\t"
  "buffer_load_dwordx4 v236, s[8:11], s28 offen offset:1024 lds\n\t"
  "buffer_load_dwordx4 v237, s[8:11], s28 offen offset:2048 lds\n\t"
  "buffer_load_dwordx4 v238, s[8:11], s28 offen offset:3072 lds\n\t"
  "s_add_u32 s28, s28, 28672\n\t"
  "s_add_u32 m0, s30, 32768\n\t"
  "s_nop 0\n\t"
  "buffer_load_dwordx4 v235, s[8:11], s28 offen lds\n\t"
  "buffer_load_dwordx4 v236, s[8:11], s28 offen offset:1024 lds\n\t"
  "buffer_load_dwordx4 v237, s[8:11], s28 offen offset:2048 lds\n\t"
  "buffer_load_dwordx4 v238, s[8:11], s28 offen offset:3072 lds\n\t"
  "s_add_u32 s28, s28, 4096\n\t"
  "s_add_u32 m0, s30, 36864\n\t"
  "s_nop 0\n\t"
  "buffer_load_dwordx4 v235, s[8:11], s28 offen lds\n\t"
  "buffer_load_dwordx4 v236, s[8:11], s28 offen offset:1024 lds\n\t"
  "buffer_load_dwordx4 v237, s[8:11], s28 offen offset:2048 lds\n\t"
  "buffer_load_dwordx4 v238, s[8:11], s28 offen offset:3072 lds\n\t"
  "s_add_u32 s28, s28, 28672\n\t"
  "s_waitcnt vmcnt(8)\n\t"
  "s_barrier\n\t"
  "ds_read_b128 v[208:211], v232\n\t"
  "ds_read_b128 v[212:215], v232 offset:512\n\t"
  "ds_read_b128 v[216:219], v232 offset:1024\n\t"
  "ds_read_b128 v[220:223], v232 offset:1536\n\t"
  "s_waitcnt lgkmcnt(2)\n\t"
  "v_mfma_f32_16x16x32_bf16 v[128:131], v[208:211], a[128:131], 0\n\t"
  "ds_read_b128 v[224:227], v232 offset:2048\n\t"
  "v_mfma_f32_16x16x32_bf16 v[136:139], v[208:211], a[132:135], 0\n\t"
  "v_mfma_f32_16x16x32_bf16 v[144:147], v[208:211], a[136:139], 0\n\t"
  "v_mfma_f32_16x16x32_bf16 v[152:155], v[208:211], a[140:143], 0\n\t"
  "v_mfma_f32_16x16x32_bf16 v[152:155], v[212:215], a[156:159], v[152:155]\n\t"
  "ds_read_b128 v[208:211], v232 offset:2560\n\t"
  "v_mfma_f32_16x16x32_bf16 v[144:147], v[212:215], a[152:155], v[144:147]\n\t"
  "v_mfma_f32_16x16x32_bf16 v[136:139], v[212:215], a[148:151], v[136:139]\n\t"
  "v_mfma_f32_16x16x32_bf16 v[128:131], v[212:215], a[144:147], v[128:131]\n\t"
  "s_waitcnt lgkmcnt(2)\n\t"
  "v_mfma_f32_16x16x32_bf16 v[128:131], v[216:219], a[160:163], v[128:131]\n\t"
  "ds_read_b128 v[212:215], v232 offset:3072\n\t"
  "v_mfma_f32_16x16x32_bf16 v[136:139], v[216:219], a[164:167], v[136:139]\n\t"
  "v_mfma_f32_16x16x32_bf16 v[144:147], v[216:219], a[168:171], v[144:147]\n\t"
  "v_mfma_f32_16x16x32_bf16 v[152:155], v[216:219], a[172:175], v[152:155]\n\t"
  "v_mfma_f32_16x16x32_bf16 v[152:155], v[220:223], a[188:191], v[152:155]\n\t"
  "ds_read_b128 v[216:219], v232 offset:3584\n\t"
  "v_mfma_f32_16x16x32_bf16 v[144:147], v[220:223], a[184:187], v[144:147]\n\t"
  "v_mfma_f32_16x16x32_bf16 v[136:139], v[220:223], a[180:183], v[136:139]\n\t"
  "v_mfma_f32_16x16x32_bf16 v[128:131], v[220:223], a[176:179], v[128:131]\n\t"
  "s_waitcnt lgkmcnt(2)\n\t"
  "v_mfma_f32_16x16x32_bf16 v[128:131], v[224:227], a[192:195], v[128:131]\n\t"
  "ds_read_b128 v[220:223], v232 offset:8192\n\t"
  "v_mfma_f32_16x16x32_bf16 v[136:139], v[224:227], a[196:199], v[136:139]\n\t"
  "v_mfma_f32_16x16x32_bf16 v[144:147], v[224:227], a[200:203], v[144:147]\n\t"
  "v_mfma_f32_16x16x32_bf16 v[152:155], v[224:227], a[204:207], v[152:155]\n\t"
  "v_mfma_f32_16x16x32_bf16 v[152:155], v[208:211], a[220:223], v[152:155]\n\t"
  "ds_read_b128 v[224:227], v232 offset:8704\n\t"
  "v_mfma_f32_16x16x32_bf16 v[144:147], v[208:211], a[216:219], v[144:147]\n\t"
  "v_mfma_f32_16x16x32_bf16 v[136:139], v[208:211], a[212:215], v[136:139]\n\t"
  "v_mfma_f32_16x16x32_bf16 v[128:131], v[208:211], a[208:211], v[128:131]\n\t"
  "s_waitcnt lgkmcnt(2)\n\t"
  "v_mfma_f32_16x16x32_bf16 v[128:131], v[212:215], a[224:227], v[128:131]\n\t"
  "ds_read_b128 v[208:211], v232 offset:9216\n\t"
  "v_mfma_f32_16x16x32_bf16 v[136:139], v[212:215], a[228:231], v[136:139]\n\t"
  "v_mfma_f32_16x16x32_bf16 v[144:147], v[212:215], a[232:235], v[144:147]\n\t"
  "v_mfma_f32_16x16x32_bf16 v[152:155], v[212:215], a[236:239], v[152:155]\n\t"
  "v_mfma_f32_16x16x32_bf16 v[152:155], v[216:219], a[252:255], v[152:155]\n\t"
  "ds_read_b128 v[212:215], v232 offset:9728\n\t"
  "v_mfma_f32_16x16x32_bf16 v[144:147], v[216:219], a[248:251], v[144:147]\n\t"
  "v_mfma_f32_16x16x32_bf16 v[136:139], v[216:219], a[244:247], v[136:139]\n\t"
  "v_mfma_f32_16x16x32_bf16 v[128:131], v[216:219], a[240:243], v[128:131]\n\t"
  "s_waitcnt lgkmcnt(2)\n\t"
  "v_mfma_f32_16x16x32_bf16 v[132:135], v[220:223], a[128:131], 0\n\t"
  "ds_read_b128 v[216:219], v232 offset:10240\n\t"
  "v_mfma_f32_16x16x32_bf16 v[140:143], v[220:223], a[132:135], 0\n\t"
  "v_mfma_f32_16x16x32_bf16 v[148:151], v[220:223], a[136:139], 0\n\t"
  "v_mfma_f32_16x16x32_bf16 v[156:159], v[220:223], a[140:143], 0\n\t"
  "v_mfma_f32_16x16x32_bf16 v[156:159], v[224:227], a[156:159], v[156:159]\n\t"
  "ds_read_b128 v[220:223], v232 offset:10752\n\t"
  "v_mfma_f32_16x16x32_bf16 v[148:151], v[224:227], a[152:155], v[148:151]\n\t"
  "v_mfma_f32_16x16x32_bf16 v[140:143], v[224:227], a[148:151], v[140:143]\n\t"
  "v_mfma_f32_16x16x32_bf16 v[132:135], v[224:227], a[144:147], v[132:135]\n\t"
  "s_waitcnt lgkmcnt(2)\n\t"
  "v_mfma_f32_16x16x32_bf16 v[132:135], v[208:211], a[160:163], v[132:135]\n\t"
  "ds_read_b128 v[224:227], v232 offset:11264\n\t"
  "v_mfma_f32_16x16x32_bf16 v[140:143], v[208:211], a[164:167], v[140:143]\n\t"
  "v_mfma_f32_16x16x32_bf16 v[148:151], v[208:211], a[168:171], v[148:151]\n\t"
  "v_mfma_f32_16x16x32_bf16 v[156:159], v[208:211], a[172:175], v[156:159]\n\t"
  "v_mfma_f32_16x16x32_bf16 v[156:159], v[212:215], a[188:191], v[156:159]\n\t"
  "ds_read_b128 v[208:211], v232 offset:11776\n\t"
  "v_mfma_f32_16x16x32_bf16 v[148:151], v[212:215], a[184:187], v[148:151]\n\t"
  "v_mfma_f32_16x16x32_bf16 v[140:143], v[212:215], a[180:183], v[140:143]\n\t"
  "v_mfma_f32_16x16x32_bf16 v[132:135], v[212:215], a[176:179], v[132:135]\n\t"
  "s_waitcnt lgkmcnt(2)\n\t"
  "v_mfma_f32_16x16x32_bf16 v[132:135], v[216:219], a[192:195], v[132:135]\n\t"
  "v_mfma_f32_16x16x32_bf16 v[140:143], v[216:219], a[196:199], v[140:143]\n\t"
  "v_mfma_f32_16x16x32_bf16 v[148:151], v[216:219], a[200:203], v[148:151]\n\t"
  "v_mfma_f32_16x16x32_bf16 v[156:159], v[216:219], a[204:207], v[156:159]\n\t"
  "v_mfma_f32_16x16x32_bf16 v[156:159], v[220:223], a[220:223], v[156:159]\n\t"
  "v_mfma_f32_16x16x32_bf16 v[148:151], v[220:223], a[216:219], v[148:151]\n\t"
  "v_mfma_f32_16x16x32_bf16 v[140:143], v[220:223], a[212:215], v[140:143]\n\t"
  "s_waitcnt vmcnt(0) lgkmcnt(0)\n\t"
  "s_barrier\n\t"
  "v_mfma_f32_16x16x32_bf16 v[132:135], v[220:223], a[208:211], v[132:135]\n\t"
  "v_mfma_f32_16x16x32_bf16 v[132:135], v[224:227], a[224:227], v[132:135]\n\t"
  "v_mfma_f32_16x16x32_bf16 v[140:143], v[224:227], a[228:231], v[140:143]\n\t"
  "s_add_u32 m0, s30, 65536\n\t"
  "v_mfma_f32_16x16x32_bf16 v[148:151], v[224:227], a[232:235], v[148:151]\n\t"
  "buffer_load_dwordx4 v235, s[12:15], s29 offen lds\n\t"
  "v_mfma_f32_16x16x32_bf16 v[156:159], v[224:227], a[236:239], v[156:159]\n\t"
  "v_mfma_f32_16x16x32_bf16 v[156:159], v[208:211], a[252:255], v[156:159]\n\t"
  "v_mfma_f32_16x16x32_bf16 v[148:151], v[208:211], a[248:251], v[148:151]\n\t"
  "v_mfma_f32_16x16x32_bf16 v[140:143], v[208:211], a[244:247], v[140:143]\n\t"
  "buffer_load_dwordx4 v236, s[12:15], s29 offen offset:1024 lds\n\t"
  "v_mfma_f32_16x16x32_bf16 v[132:135], v[208:211], a[240:243], v[132:135]\n\t"
  "buffer_load_dwordx4 v237, s[12:15], s29 offen offset:2048 lds\n\t"
  "s_nop 7\n\t"
  "v_max3_f32 v250, v128, v129, v130\n\t"
  "v_max3_f32 v250, v250, v131, v132\n\t"
  "v_max3_f32 v250, v250, v133, v134\n\t"
  "v_max_f32_e32 v250, v250, v135\n\t"
  "v_mov_b32_e32 v246, v250\n\t"
  "s_nop 1\n\t"
  "v_permlane16_swap_b32 v246, v250\n\t"
  "s_nop 1\n\t"
  "v_max_f32_e32 v250, v250, v246\n\t"
  "v_mov_b32_e32 v246, v250\n\t"
  "s_nop 1\n\t"
  "v_permlane32_swap_b32 v246, v250\n\t"
  "s_nop 1\n\t"
  "v_max_f32_e32 v250, v250, v246\n\t"
  "v_max3_f32 v251, v136, v137, v138\n\t"
  "v_max3_f32 v251, v251, v139, v140\n\t"
  "v_max3_f32 v251, v251, v141, v142\n\t"
  "v_max_f32_e32 v251, v251, v143\n\t"
  "v_mov_b32_e32 v246, v251\n\t"
  "s_nop 1\n\t"
  "v_permlane16_swap_b32 v246, v251\n\t"
  "s_nop 1\n\t"
  "v_max_f32_e32 v251, v251, v246\n\t"
  "v_mov_b32_e32 v246, v251\n\t"
  "s_nop 1\n\t"
  "v_permlane32_swap_b32 v246, v251\n\t"
  "s_nop 1\n\t"
  "v_max_f32_e32 v251, v251, v246\n\t"
  "v_max3_f32 v252, v144, v145, v146\n\t"
  "v_max3_f32 v252, v252, v147, v148\n\t"
  "v_max3_f32 v252, v252, v149, v150\n\t"
  "v_max_f32_e32 v252, v252, v151\n\t"
  "v_mov_b32_e32 v246, v252\n\t"
  "s_nop 1\n\t"
  "v_permlane16_swap_b32 v246, v252\n\t"
  "s_nop 1\n\t"
  "v_max_f32_e32 v252, v252, v246\n\t"
  "v_mov_b32_e32 v246, v252\n\t"
  "s_nop 1\n\t"
  "v_permlane32_swap_b32 v246, v252\n\t"
  "s_nop 1\n\t"
  "v_max_f32_e32 v252, v252, v246\n\t"
  "v_max3_f32 v253, v152, v153, v154\n\t"
  "v_max3_f32 v253, v253, v155, v156\n\t"
  "v_max3_f32 v253, v253, v157, v158\n\t"
  "v_max_f32_e32 v253, v253, v159\n\t"
  "v_mov_b32_e32 v246, v253\n\t"
  "s_nop 1\n\t"
  "v_permlane16_swap_b32 v246, v253\n\t"
  "s_nop 1\n\t"
  "v_max_f32_e32 v253, v253, v246\n\t"
  "v_mov_b32_e32 v246, v253\n\t"
  "s_nop 1\n\t"
  "v_permlane32_swap_b32 v246, v253\n\t"
  "s_nop 1\n\t"
  "v_max_f32_e32 v253, v253, v246\n\t"
  "s_lshl_b32 s64, s62, 10\n\t"
  "s_add_u32 s64, s64, 131072\n\t"
  "s_xor_b32 s65, s64, 1024\n\t"
  "v_lshlrev_b32_e32 v247, 4, v254\n\t"
  "v_add_u32_e32 v248, s64, v247\n\t"
  "v_add_u32_e32 v249, s65, v247\n\t"
  "ds_write_b128 v248, v[250:253]\n\t"
  "s_waitcnt lgkmcnt(0)\n\t"
  "s_barrier\n\t"
  "ds_read_b128 v[192:195], v249\n\t"
  "s_waitcnt lgkmcnt(0)\n\t"
  "v_max_f32_e32 v250, v250, v194\n\t"
  "v_max_f32_e32 v251, v251, v195\n\t"
  "v_max_f32_e32 v252, v252, v192\n\t"
  "v_max_f32_e32 v253, v253, v193\n\t"
  "v_sub_f32_e32 v128, v128, v250\n\t"
  "v_sub_f32_e32 v129, v129, v250\n\t"
  "v_sub_f32_e32 v130, v130, v250\n\t"
  "v_sub_f32_e32 v131, v131, v250\n\t"
  "v_sub_f32_e32 v132, v132, v250\n\t"
  "v_sub_f32_e32 v133, v133, v250\n\t"
  "v_sub_f32_e32 v134, v134, v250\n\t"
  "v_sub_f32_e32 v135, v135, v250\n\t"
  "v_sub_f32_e32 v192, 0, v250\n\t"
  "v_sub_f32_e32 v193, 0, v250\n\t"
  "v_sub_f32_e32 v194, 0, v250\n\t"
  "v_sub_f32_e32 v195, 0, v250\n\t"
  "v_mov_b32_e32 v239, 0\n\t"
  "v_sub_f32_e32 v136, v136, v251\n\t"
  "v_sub_f32_e32 v137, v137, v251\n\t"
  "v_sub_f32_e32 v138, v138, v251\n\t"
  "v_sub_f32_e32 v139, v139, v251\n\t"
  "v_sub_f32_e32 v140, v140, v251\n\t"
  "v_sub_f32_e32 v141, v141, v251\n\t"
  "v_sub_f32_e32 v142, v142, v251\n\t"
  "v_sub_f32_e32 v143, v143, v251\n\t"
  "v_sub_f32_e32 v196, 0, v251\n\t"
  "v_sub_f32_e32 v197, 0, v251\n\t"
  "v_sub_f32_e32 v198, 0, v251\n\t"
  "v_sub_f32_e32 v199, 0, v251\n\t"
  "v_mov_b32_e32 v240, 0\n\t"
  "v_sub_f32_e32 v144, v144, v252\n\t"
  "v_sub_f32_e32 v145, v145, v252\n\t"
  "v_sub_f32_e32 v146, v146, v252\n\t"
  "v_sub_f32_e32 v147, v147, v252\n\t"
  "v_sub_f32_e32 v148, v148, v252\n\t"
  "v_sub_f32_e32 v149, v149, v252\n\t"
  "v_sub_f32_e32 v150, v150, v252\n\t"
  "v_sub_f32_e32 v151, v151, v252\n\t"
  "v_sub_f32_e32 v200, 0, v252\n\t"
  "v_sub_f32_e32 v201, 0, v252\n\t"
  "v_sub_f32_e32 v202, 0, v252\n\t"
  "v_sub_f32_e32 v203, 0, v252\n\t"
  "v_mov_b32_e32 v241, 0\n\t"
  "v_sub_f32_e32 v152, v152, v253\n\t"
  "v_sub_f32_e32 v153, v153, v253\n\t"
  "v_sub_f32_e32 v154, v154, v253\n\t"
  "v_sub_f32_e32 v155, v155, v253\n\t"
  "v_sub_f32_e32 v156, v156, v253\n\t"
  "v_sub_f32_e32 v157, v157, v253\n\t"
  "v_sub_f32_e32 v158, v158, v253\n\t"
  "v_sub_f32_e32 v159, v159, v253\n\t"
  "v_sub_f32_e32 v204, 0, v253\n\t"
  "v_sub_f32_e32 v205, 0, v253\n\t"
  "v_sub_f32_e32 v206, 0, v253\n\t"
  "v_sub_f32_e32 v207, 0, v253\n\t"
  "v_mov_b32_e32 v242, 0\n\t"
  "v_exp_f32_e32 v128, v128\n\t"
  "v_exp_f32_e32 v129, v129\n\t"
  "v_exp_f32_e32 v130, v130\n\t"
  "v_exp_f32_e32 v131, v131\n\t"
  "v_exp_f32_e32 v132, v132\n\t"
  "v_exp_f32_e32 v133, v133\n\t"
  "v_exp_f32_e32 v134, v134\n\t"
  "v_add_f32_e32 v239, v239, v128\n\t"
  "v_add_f32_e32 v239, v239, v129\n\t"
  "v_cvt_pk_bf16_f32 v128, v128, v129\n\t"
  "v_add_f32_e32 v239, v239, v130\n\t"
  "v_add_f32_e32 v239, v239, v131\n\t"
  "v_cvt_pk_bf16_f32 v129, v130, v131\n\t"
  "v_add_f32_e32 v239, v239, v132\n\t"
  "v_add_f32_e32 v239, v239, v133\n\t"
  "v_cvt_pk_bf16_f32 v130, v132, v133\n\t"
  "v_exp_f32_e32 v135, v135\n\t"
  "v_add_f32_e32 v239, v239, v134\n\t"
  "v_exp_f32_e32 v136, v136\n\t"
  "v_exp_f32_e32 v137, v137\n\t"
  "v_exp_f32_e32 v138, v138\n\t"
  "v_exp_f32_e32 v139, v139\n\t"
  "v_exp_f32_e32 v140, v140\n\t"
  "buffer_load_dwordx4 v238, s[12:15], s29 offen offset:3072 lds\n\t"
  "s_add_u32 s29, s29, 4096\n\t"
  "v_add_f32_e32 v239, v239, v135\n\t"
  "v_cvt_pk_bf16_f32 v131, v134, v135\n\t"
  "v_add_f32_e32 v240, v240, v136\n\t"
  "v_add_f32_e32 v240, v240, v137\n\t"
  "v_cvt_pk_bf16_f32 v136, v136, v137\n\t"
  "v_add_f32_e32 v240, v240, v138\n\t"
  "v_add_f32_e32 v240, v240, v139\n\t"
  "v_cvt_pk_bf16_f32 v137, v138, v139\n\t"
  "v_exp_f32_e32 v141, v141\n\t"
  "v_add_f32_e32 v240, v240, v140\n\t"
  "s_add_u32 m0, s30, 69632\n\t"
  "s_nop 0\n\t"
  "buffer_load_dwordx4 v235, s[12:15], s29 offen lds\n\t"
  "buffer_load_dwordx4 v236, s[12:15], s29 offen offset:1024 lds\n\t"
  "buffer_load_dwordx4 v237, s[12:15], s29 offen offset:2048 lds\n\t"
  "buffer_load_dwordx4 v238, s[12:15], s29 offen offset:3072 lds\n\t"
  "s_add_u32 s29, s29, 28672\n\t"
  "s_add_u32 m0, s30, 0\n\t"
  "s_nop 0\n\t"
  "buffer_load_dwordx4 v235, s[8:11], s28 offen lds\n\t"
  "buffer_load_dwordx4 v236, s[8:11], s28 offen offset:1024 lds\n\t"
  "buffer_load_dwordx4 v237, s[8:11], s28 offen offset:2048 lds\n\t"
  "buffer_load_dwordx4 v238, s[8:11], s28 offen offset:3072 lds\n\t"
  "s_add_u32 s28, s28, 4096\n\t"
  "s_add_u32 m0, s30, 4096\n\t"
  "s_nop 0\n\t"
  "buffer_load_dwordx4 v235, s[8:11], s28 offen lds\n\t"
  "buffer_load_dwordx4 v236, s[8:11], s28 offen offset:1024 lds\n\t"
  "ds_read_b128 v[208:211], v232 offset:32768\n\t"
  "buffer_load_dwordx4 v237, s[8:11], s28 offen offset:2048 lds\n\t"
  "ds_read_b128 v[212:215], v232 offset:33280\n\t"
  "buffer_load_dwordx4 v238, s[8:11], s28 offen offset:3072 lds\n\t"
  "s_add_u32 s28, s28, 28672\n\t"
  "ds_read_b128 v[216:219], v232 offset:33792\n\t"
  "ds_read_b128 v[220:223], v232 offset:34304\n\t"
  "s_waitcnt lgkmcnt(2)\n\t"
  "v_mfma_f32_16x16x32_bf16 v[160:163], v[208:211], a[128:131], v[192:195]\n\t"
  "ds_read_b128 v[224:227], v232 offset:34816\n\t"
  "v_exp_f32_e32 v142, v142\n\t"
  "v_mfma_f32_16x16x32_bf16 v[168:171], v[208:211], a[132:135], v[196:199]\n\t"
  "v_add_f32_e32 v240, v240, v141\n\t"
  "v_cvt_pk_bf16_f32 v138, v140, v141\n\t"
  "v_mfma_f32_16x16x32_bf16 v[176:179], v[208:211], a[136:139], v[200:203]\n\t"
  "v_exp_f32_e32 v143, v143\n\t"
  "v_add_f32_e32 v240, v240, v142\n\t"
  "v_mfma_f32_16x16x32_bf16 v[184:187], v[208:211], a[140:143], v[204:207]\n\t"
  "v_exp_f32_e32 v144, v144\n\t"
  "v_mfma_f32_16x16x32_bf16 v[184:187], v[212:215], a[156:159], v[184:187]\n\t"
  "ds_read_b128 v[208:211], v232 offset:35328\n\t"
  "v_add_f32_e32 v240, v240, v143\n\t"
  "v_cvt_pk_bf16_f32 v139, v142, v143\n\t"
  "v_mfma_f32_16x16x32_bf16 v[176:179], v[212:215], a[152:155], v[176:179]\n\t"
  "v_exp_f32_e32 v145, v145\n\t"
  "v_add_f32_e32 v241, v241, v144\n\t"
  "v_mfma_f32_16x16x32_bf16 v[168:171], v[212:215], a[148:151], v[168:171]\n\t"
  "v_exp_f32_e32 v146, v146\n\t"
  "v_mfma_f32_16x16x32_bf16 v[160:163], v[212:215], a[144:147], v[160:163]\n\t"
  "v_add_f32_e32 v241, v241, v145\n\t"
  "v_cvt_pk_bf16_f32 v144, v144, v145\n\t"
  "s_waitcnt lgkmcnt(2)\n\t"
  "v_mfma_f32_16x16x32_bf16 v[160:163], v[216:219], a[160:163], v[160:163]\n\t"
  "ds_read_b128 v[212:215], v232 offset:35840\n\t"
  "v_exp_f32_e32 v147, v147\n\t"
  "v_mfma_f32_16x16x32_bf16 v[168:171], v[216:219], a[164:167], v[168:171]\n\t"
  "v_add_f32_e32 v241, v241, v146\n\t"
  "v_exp_f32_e32 v148, v148\n\t"
  "v_mfma_f32_16x16x32_bf16 v[176:179], v[216:219], a[168:171], v[176:179]\n\t"
  "v_add_f32_e32 v241, v241, v147\n\t"
  "v_cvt_pk_bf16_f32 v145, v146, v147\n\t"
  "v_mfma_f32_16x16x32_bf16 v[184:187], v[216:219], a[172:175], v[184:187]\n\t"
  "v_exp_f32_e32 v149, v149\n\t"
  "v_add_f32_e32 v241, v241, v148\n\t"
  "v_mfma_f32_16x16x32_bf16 v[184:187], v[220:223], a[188:191], v[184:187]\n\t"
  "ds_read_b128 v[216:219], v232 offset:36352\n\t"
  "v_exp_f32_e32 v150, v150\n\t"
  "v_mfma_f32_16x16x32_bf16 v[176:179], v[220:223], a[184:187], v[176:179]\n\t"
  "v_add_f32_e32 v241, v241, v149\n\t"
  "v_cvt_pk_bf16_f32 v146, v148, v149\n\t"
  "v_mfma_f32_16x16x32_bf16 v[168:171], v[220:223], a[180:183], v[168:171]\n\t"
  "v_exp_f32_e32 v151, v151\n\t"
  "v_add_f32_e32 v241, v241, v150\n\t"
  "v_mfma_f32_16x16x32_bf16 v[160:163], v[220:223], a[176:179], v[160:163]\n\t"
  "v_exp_f32_e32 v152, v152\n\t"
  "s_waitcnt lgkmcnt(2)\n\t"
  "v_mfma_f32_16x16x32_bf16 v[160:163], v[224:227], a[192:195], v[160:163]\n\t"
  "ds_read_b128 v[220:223], v232 offset:40960\n\t"
  "v_add_f32_e32 v241, v241, v151\n\t"
  "v_cvt_pk_bf16_f32 v147, v150, v151\n\t"
  "v_mfma_f32_16x16x32_bf16 v[168:171], v[224:227], a[196:199], v[168:171]\n\t"
  "v_exp_f32_e32 v153, v153\n\t"
  "v_add_f32_e32 v242, v242, v152\n\t"
  "v_mfma_f32_16x16x32_bf16 v[176:179], v[224:227], a[200:203], v[176:179]\n\t"
  "v_exp_f32_e32 v154, v154\n\t"
  "v_mfma_f32_16x16x32_bf16 v[184:187], v[224:227], a[204:207], v[184:187]\n\t"
  "v_add_f32_e32 v242, v242, v153\n\t"
  "v_cvt_pk_bf16_f32 v152, v152, v153\n\t"
  "v_mfma_f32_16x16x32_bf16 v[184:187], v[208:211], a[220:223], v[184:187]\n\t"
  "ds_read_b128 v[224:227], v232 offset:41472\n\t"
  "v_exp_f32_e32 v155, v155\n\t"
  "v_mfma_f32_16x16x32_bf16 v[176:179], v[208:211], a[216:219], v[176:179]\n\t"
  "v_add_f32_e32 v242, v242, v154\n\t"
  "v_exp_f32_e32 v156, v156\n\t"
  "v_mfma_f32_16x16x32_bf16 v[168:171], v[208:211], a[212:215], v[168:171]\n\t"
  "v_add_f32_e32 v242, v242, v155\n\t"
  "v_cvt_pk_bf16_f32 v153, v154, v155\n\t"
  "v_mfma_f32_16x16x32_bf16 v[160:163], v[208:211], a[208:211], v[160:163]\n\t"
  "v_exp_f32_e32 v157, v157\n\t"
  "s_waitcnt lgkmcnt(2)\n\t"
  "v_mfma_f32_16x16x32_bf16 v[160:163], v[212:215], a[224:227], v[160:163]\n\t"
  "ds_read_b128 v[208:211], v232 offset:41984\n\t"
  "v_add_f32_e32 v242, v242, v156\n\t"
  "v_mfma_f32_16x16x32_bf16 v[168:171], v[212:215], a[228:231], v[168:171]\n\t"
  "v_exp_f32_e32 v158, v158\n\t"
  "v_add_f32_e32 v242, v242, v157\n\t"
  "v_mfma_f32_16x16x32_bf16 v[176:179], v[212:215], a[232:235], v[176:179]\n\t"
  "v_cvt_pk_bf16_f32 v154, v156, v157\n\t"
  "v_exp_f32_e32 v159, v159\n\t"
  "v_mfma_f32_16x16x32_bf16 v[184:187], v[212:215], a[236:239], v[184:187]\n\t"
  "v_add_f32_e32 v242, v242, v158\n\t"
  "v_mfma_f32_16x16x32_bf16 v[184:187], v[216:219], a[252:255], v[184:187]\n\t"
  "ds_read_b128 v[212:215], v232 offset:42496\n\t"
  "v_add_f32_e32 v242, v242, v159\n\t"
  "v_cvt_pk_bf16_f32 v155, v158, v159\n\t"
  "v_mfma_f32_16x16x32_bf16 v[176:179], v[216:219], a[248:251], v[176:179]\n\t"
  "v_mfma_f32_16x16x32_bf16 v[168:171], v[216:219], a[244:247], v[168:171]\n\t"
  "v_mfma_f32_16x16x32_bf16 v[160:163], v[216:219], a[240:243], v[160:163]\n\t"
  "s_waitcnt lgkmcnt(2)\n\t"
  "v_mfma_f32_16x16x32_bf16 v[164:167], v[220:223], a[128:131], v[192:195]\n\t"
  "ds_read_b128 v[216:219], v232 offset:43008\n\t"
  "v_mfma_f32_16x16x32_bf16 v[172:175], v[220:223], a[132:135], v[196:199]\n\t"
  "v_mfma_f32_16x16x32_bf16 v[180:183], v[220:223], a[136:139], v[200:203]\n\t"
  "v_mfma_f32_16x16x32_bf16 v[188:191], v[220:223], a[140:143], v[204:207]\n\t"
  "v_mfma_f32_16x16x32_bf16 v[188:191], v[224:227], a[156:159], v[188:191]\n\t"
  "ds_read_b128 v[220:223], v232 offset:43520\n\t"
  "v_max3_f32 v243, v160, v161, v162\n\t"
  "v_max3_f32 v243, v243, v163, v168\n\t"
  "v_mfma_f32_16x16x32_bf16 v[180:183], v[224:227], a[152:155], v[180:183]\n\t"
  "v_max3_f32 v243, v243, v169, v170\n\t"
  "v_max3_f32 v243, v243, v171, v176\n\t"
  "v_max3_f32 v243, v243, v177, v178\n\t"
  "v_mfma_f32_16x16x32_bf16 v[172:175], v[224:227], a[148:151], v[172:175]\n\t"
  "v_max3_f32 v243, v243, v179, v184\n\t"
  "v_max3_f32 v243, v243, v185, v186\n\t"
  "v_mfma_f32_16x16x32_bf16 v[164:167], v[224:227], a[144:147], v[164:167]\n\t"
  "s_waitcnt lgkmcnt(2)\n\t"
  "v_mfma_f32_16x16x32_bf16 v[164:167], v[208:211], a[160:163], v[164:167]\n\t"
  "ds_read_b128 v[224:227], v232 offset:44032\n\t"
  "v_mfma_f32_16x16x32_bf16 v[172:175], v[208:211], a[164:167], v[172:175]\n\t"
  "v_mfma_f32_16x16x32_bf16 v[180:183], v[208:211], a[168:171], v[180:183]\n\t"
  "v_mfma_f32_16x16x32_bf16 v[188:191], v[208:211], a[172:175], v[188:191]\n\t"
  "v_mfma_f32_16x16x32_bf16 v[188:191], v[212:215], a[188:191], v[188:191]\n\t"
  "ds_read_b128 v[208:211], v232 offset:44544\n\t"
  "v_mfma_f32_16x16x32_bf16 v[180:183], v[212:215], a[184:187], v[180:183]\n\t"
  "v_mfma_f32_16x16x32_bf16 v[172:175], v[212:215], a[180:183], v[172:175]\n\t"
  "v_mfma_f32_16x16x32_bf16 v[164:167], v[212:215], a[176:179], v[164:167]\n\t"
  "s_waitcnt lgkmcnt(2)\n\t"
  "v_mfma_f32_16x16x32_bf16 v[164:167], v[216:219], a[192:195], v[164:167]\n\t"
  "v_mfma_f32_16x16x32_bf16 v[172:175], v[216:219], a[196:199], v[172:175]\n\t"
  "v_mfma_f32_16x16x32_bf16 v[180:183], v[216:219], a[200:203], v[180:183]\n\t"
  "v_mfma_f32_16x16x32_bf16 v[188:191], v[216:219], a[204:207], v[188:191]\n\t"
  "v_mfma_f32_16x16x32_bf16 v[188:191], v[220:223], a[220:223], v[188:191]\n\t"
  "v_mfma_f32_16x16x32_bf16 v[180:183], v[220:223], a[216:219], v[180:183]\n\t"
  "v_mfma_f32_16x16x32_bf16 v[172:175], v[220:223], a[212:215], v[172:175]\n\t"
  "s_waitcnt vmcnt(0) lgkmcnt(0)\n\t"
  "s_barrier\n\t"
  "v_mfma_f32_16x16x32_bf16 v[164:167], v[220:223], a[208:211], v[164:167]\n\t"
  "ds_read_b64_tr_b16 v[212:213], v233\n\t"
  "v_mfma_f32_16x16x32_bf16 v[164:167], v[224:227], a[224:227], v[164:167]\n\t"
  "ds_read_b64_tr_b16 v[214:215], v233 offset:8192\n\t"
  "v_mfma_f32_16x16x32_bf16 v[172:175], v[224:227], a[228:231], v[172:175]\n\t"
  "ds_read_b64_tr_b16 v[216:217], v234\n\t"
  "s_add_u32 m0, s30, 98304\n\t"
  "v_mfma_f32_16x16x32_bf16 v[180:183], v[224:227], a[232:235], v[180:183]\n\t"
  "ds_read_b64_tr_b16 v[218:219], v234 offset:8192\n\t"
  "buffer_load_dwordx4 v235, s[12:15], s29 offen lds\n\t"
  "v_mfma_f32_16x16x32_bf16 v[188:191], v[224:227], a[236:239], v[188:191]\n\t"
  "ds_read_b64_tr_b16 v[220:221], v233 offset:512\n\t"
  "v_mfma_f32_16x16x32_bf16 v[188:191], v[208:211], a[252:255], v[188:191]\n\t"
  "ds_read_b64_tr_b16 v[222:223], v233 offset:8704\n\t"
  "v_mfma_f32_16x16x32_bf16 v[180:183], v[208:211], a[248:251], v[180:183]\n\t"
  "ds_read_b64_tr_b16 v[224:225], v234 offset:512\n\t"
  "v_mfma_f32_16x16x32_bf16 v[172:175], v[208:211], a[244:247], v[172:175]\n\t"
  "ds_read_b64_tr_b16 v[226:227], v234 offset:8704\n\t"
  "buffer_load_dwordx4 v236, s[12:15], s29 offen offset:1024 lds\n\t"
  "v_mfma_f32_16x16x32_bf16 v[164:167], v[208:211], a[240:243], v[164:167]\n\t"
  "s_waitcnt lgkmcnt(4)\n\t"
  "v_mfma_f32_16x16x32_bf16 a[0:3], v[212:215], v[128:131], 0\n\t"
  "ds_read_b64_tr_b16 v[208:209], v233 offset:1024\n\t"
  "v_mfma_f32_16x16x32_bf16 a[4:7], v[212:215], v[136:139], 0\n\t"
  "ds_read_b64_tr_b16 v[210:211], v233 offset:9216\n\t"
  "v_mfma_f32_16x16x32_bf16 v[0:3], v[212:215], v[144:147], 0\n\t"
  "buffer_load_dwordx4 v237, s[12:15], s29 offen offset:2048 lds\n\t"
  "v_mfma_f32_16x16x32_bf16 v[8:11], v[212:215], v[152:155], 0\n\t"
  "v_mfma_f32_16x16x32_bf16 v[12:15], v[216:219], v[152:155], 0\n\t"
  "ds_read_b64_tr_b16 v[212:213], v234 offset:1024\n\t"
  "v_max3_f32 v243, v243, v187, v164\n\t"
  "v_max3_f32 v243, v243, v165, v166\n\t"
  "v_mfma_f32_16x16x32_bf16 v[4:7], v[216:219], v[144:147], 0\n\t"
  "ds_read_b64_tr_b16 v[214:215], v234 offset:9216\n\t"
  "v_max3_f32 v243, v243, v167, v172\n\t"
  "v_max3_f32 v243, v243, v173, v174\n\t"
  "v_mfma_f32_16x16x32_bf16 a[12:15], v[216:219], v[136:139], 0\n\t"
  "v_max3_f32 v243, v243, v175, v180\n\t"
  "buffer_load_dwordx4 v238, s[12:15], s29 offen offset:3072 lds\n\t"
  "v_mfma_f32_16x16x32_bf16 a[8:11], v[216:219], v[128:131], 0\n\t"
  "v_max3_f32 v243, v243, v181, v182\n\t"
  "s_add_u32 s29, s29, 4096\n\t"
  "s_waitcnt lgkmcnt(4)\n\t"
  "v_mfma_f32_16x16x32_bf16 a[16:19], v[220:223], v[128:131], 0\n\t"
  "ds_read_b64_tr_b16 v[216:217], v233 offset:1536\n\t"
  "v_max3_f32 v243, v243, v183, v188\n\t"
  "v_max3_f32 v243, v243, v189, v190\n\t"
  "v_mfma_f32_16x16x32_bf16 a[20:23], v[220:223], v[136:139], 0\n\t"
  "ds_read_b64_tr_b16 v[218:219], v233 offset:9728\n\t"
  "v_max_f32_e32 v243, v243, v191\n\t"
  "s_add_u32 m0, s30, 102400\n\t"
  "v_mfma_f32_16x16x32_bf16 v[16:19], v[220:223], v[144:147], 0\n\t"
  "v_cmp_lt_f32_e32 vcc, 0x41000000, v243\n\t"
  "buffer_load_dwordx4 v235, s[12:15], s29 offen lds\n\t"
  "v_mfma_f32_16x16x32_bf16 v[24:27], v[220:223], v[152:155], 0\n\t"
  "s_cbranch_vccnz .Lrare_ags\n\t"
  ".Lret_ags:\n\t"
  "v_exp_f32_e32 v160, v160\n\t"
  "v_mfma_f32_16x16x32_bf16 v[28:31], v[224:227], v[152:155], 0\n\t"
  "ds_read_b64_tr_b16 v[220:221], v234 offset:1536\n\t"
  "v_exp_f32_e32 v161, v161\n\t"
  "v_mfma_f32_16x16x32_bf16 v[20:23], v[224:227], v[144:147], 0\n\t"
  "ds_read_b64_tr_b16 v[222:223], v234 offset:9728\n\t"
  "v_add_f32_e32 v239, v239, v160\n\t"
  "v_mfma_f32_16x16x32_bf16 a[28:31], v[224:227], v[136:139], 0\n\t"
  "v_add_f32_e32 v239, v239, v161\n\t"
  "buffer_load_dwordx4 v236, s[12:15], s29 offen offset:1024 lds\n\t"
  "v_mfma_f32_16x16x32_bf16 a[24:27], v[224:227], v[128:131], 0\n\t"
  "v_exp_f32_e32 v162, v162\n\t"
  "s_waitcnt lgkmcnt(4)\n\t"
  "v_mfma_f32_16x16x32_bf16 a[32:35], v[208:211], v[128:131], 0\n\t"
  "ds_read_b64_tr_b16 v[224:225], v233 offset:2048\n\t"
  "v_cvt_pk_bf16_f32 v160, v160, v161\n\t"
  "v_mfma_f32_16x16x32_bf16 a[36:39], v[208:211], v[136:139], 0\n\t"
  "ds_read_b64_tr_b16 v[226:227], v233 offset:10240\n\t"
  "v_exp_f32_e32 v163, v163\n\t"
  "v_mfma_f32_16x16x32_bf16 v[32:35], v[208:211], v[144:147], 0\n\t"
  "v_add_f32_e32 v239, v239, v162\n\t"
  "buffer_load_dwordx4 v237, s[12:15], s29 offen offset:2048 lds\n\t"
  "v_mfma_f32_16x16x32_bf16 v[40:43], v[208:211], v[152:155], 0\n\t"
  "v_mfma_f32_16x16x32_bf16 v[44:47], v[212:215], v[152:155], 0\n\t"
  "ds_read_b64_tr_b16 v[228:229], v234 offset:2048\n\t"
  "v_add_f32_e32 v239, v239, v163\n\t"
  "v_mfma_f32_16x16x32_bf16 v[36:39], v[212:215], v[144:147], 0\n\t"
  "ds_read_b64_tr_b16 v[230:231], v234 offset:10240\n\t"
  "v_cvt_pk_bf16_f32 v161, v162, v163\n\t"
  "v_mfma_f32_16x16x32_bf16 a[44:47], v[212:215], v[136:139], 0\n\t"
  "buffer_load_dwordx4 v238, s[12:15], s29 offen offset:3072 lds\n\t"
  "v_mfma_f32_16x16x32_bf16 a[40:43], v[212:215], v[128:131], 0\n\t"
  "s_add_u32 s29, s29, 28672\n\t"
  "s_waitcnt lgkmcnt(4)\n\t"
  "v_mfma_f32_16x16x32_bf16 a[48:51], v[216:219], v[128:131], 0\n\t"
  "ds_read_b64_tr_b16 v[208:209], v233 offset:2560\n\t"
  "v_exp_f32_e32 v164, v164\n\t"
  "v_mfma_f32_16x16x32_bf16 a[52:55], v[216:219], v[136:139], 0\n\t"
  "ds_read_b64_tr_b16 v[210:211], v233 offset:10752\n\t"
  "s_add_u32 m0, s30, 32768\n\t"
  "v_mfma_f32_16x16x32_bf16 v[48:51], v[216:219], v[144:147], 0\n\t"
  "v_add_f32_e32 v239, v239, v164\n\t"
  "buffer_load_dwordx4 v235, s[8:11], s28 offen lds\n\t"
  "v_mfma_f32_16x16x32_bf16 v[56:59], v[216:219], v[152:155], 0\n\t"
  "v_exp_f32_e32 v165, v165\n\t"
  "v_mfma_f32_16x16x32_bf16 v[60:63], v[220:223], v[152:155], 0\n\t"
  "ds_read_b64_tr_b16 v[212:213], v234 offset:2560\n\t"
  "v_mfma_f32_16x16x32_bf16 v[52:55], v[220:223], v[144:147], 0\n\t"
  "ds_read_b64_tr_b16 v[214:215], v234 offset:10752\n\t"
  "v_add_f32_e32 v239, v239, v165\n\t"
  "v_mfma_f32_16x16x32_bf16 a[60:63], v[220:223], v[136:139], 0\n\t"
  "v_cvt_pk_bf16_f32 v162, v164, v165\n\t"
  "buffer_load_dwordx4 v236, s[8:11], s28 offen offset:1024 lds\n\t"
  "v_mfma_f32_16x16x32_bf16 a[56:59], v[220:223], v[128:131], 0\n\t"
  "s_waitcnt lgkmcnt(4)\n\t"
  "v_mfma_f32_16x16x32_bf16 a[64:67], v[224:227], v[128:131], 0\n\t"
  "ds_read_b64_tr_b16 v[216:217], v233 offset:3072\n\t"
  "v_exp_f32_e32 v166, v166\n\t"
  "v_mfma_f32_16x16x32_bf16 a[68:71], v[224:227], v[136:139], 0\n\t"
  "ds_read_b64_tr_b16 v[218:219], v233 offset:11264\n\t"
  "v_mfma_f32_16x16x32_bf16 v[64:67], v[224:227], v[144:147], 0\n\t"
  "v_add_f32_e32 v239, v239, v166\n\t"
  "buffer_load_dwordx4 v237, s[8:11], s28 offen offset:2048 lds\n\t"
  "v_mfma_f32_16x16x32_bf16 v[72:75], v[224:227], v[152:155], 0\n\t"
  "v_mfma_f32_16x16x32_bf16 v[76:79], v[228:231], v[152:155], 0\n\t"
  "ds_read_b64_tr_b16 v[220:221], v234 offset:3072\n\t"
  "v_exp_f32_e32 v167, v167\n\t"
  "v_mfma_f32_16x16x32_bf16 v[68:71], v[228:231], v[144:147], 0\n\t"
  "ds_read_b64_tr_b16 v[222:223], v234 offset:11264\n\t"
  "v_mfma_f32_16x16x32_bf16 a[76:79], v[228:231], v[136:139], 0\n\t"
  "v_add_f32_e32 v239, v239, v167\n\t"
  "buffer_load_dwordx4 v238, s[8:11], s28 offen offset:3072 lds\n\t"
  "v_mfma_f32_16x16x32_bf16 a[72:75], v[228:231], v[128:131], 0\n\t"
  "v_cvt_pk_bf16_f32 v163, v166, v167\n\t"
  "s_add_u32 s28, s28, 4096\n\t"
  "s_waitcnt lgkmcnt(4)\n\t"
  "v_mfma_f32_16x16x32_bf16 a[80:83], v[208:211], v[128:131], 0\n\t"
  "ds_read_b64_tr_b16 v[224:225], v233 offset:3584\n\t"
  "v_mfma_f32_16x16x32_bf16 a[84:87], v[208:211], v[136:139], 0\n\t"
  "ds_read_b64_tr_b16 v[226:227], v233 offset:11776\n\t"
  "s_add_u32 m0, s30, 36864\n\t"
  "v_mfma_f32_16x16x32_bf16 v[80:83], v[208:211], v[144:147], 0\n\t"
  "buffer_load_dwordx4 v235, s[8:11], s28 offen lds\n\t"
  "v_mfma_f32_16x16x32_bf16 v[88:91], v[208:211], v[152:155], 0\n\t"
  "v_exp_f32_e32 v168, v168\n\t"
  "v_mfma_f32_16x16x32_bf16 v[92:95], v[212:215], v[152:155], 0\n\t"
  "ds_read_b64_tr_b16 v[228:229], v234 offset:3584\n\t"
  "v_exp_f32_e32 v169, v169\n\t"
  "v_mfma_f32_16x16x32_bf16 v[84:87], v[212:215], v[144:147], 0\n\t"
  "ds_read_b64_tr_b16 v[230:231], v234 offset:11776\n\t"
  "v_add_f32_e32 v240, v240, v168\n\t"
  "v_mfma_f32_16x16x32_bf16 a[92:95], v[212:215], v[136:139], 0\n\t"
  "v_add_f32_e32 v240, v240, v169\n\t"
  "buffer_load_dwordx4 v236, s[8:11], s28 offen offset:1024 lds\n\t"
  "v_mfma_f32_16x16x32_bf16 a[88:91], v[212:215], v[128:131], 0\n\t"
  "v_cvt_pk_bf16_f32 v168, v168, v169\n\t"
  "s_waitcnt lgkmcnt(4)\n\t"
  "v_mfma_f32_16x16x32_bf16 a[96:99], v[216:219], v[128:131], 0\n\t"
  "ds_read_b128 v[208:211], v232\n\t"
  "v_mfma_f32_16x16x32_bf16 a[100:103], v[216:219], v[136:139], 0\n\t"
  "v_mfma_f32_16x16x32_bf16 v[96:99], v[216:219], v[144:147], 0\n\t"
  "buffer_load_dwordx4 v237, s[8:11], s28 offen offset:2048 lds\n\t"
  "v_mfma_f32_16x16x32_bf16 v[104:107], v[216:219], v[152:155], 0\n\t"
  "v_exp_f32_e32 v170, v170\n\t"
  "v_mfma_f32_16x16x32_bf16 v[108:111], v[220:223], v[152:155], 0\n\t"
  "ds_read_b128 v[212:215], v232 offset:512\n\t"
  "v_exp_f32_e32 v171, v171\n\t"
  "v_mfma_f32_16x16x32_bf16 v[100:103], v[220:223], v[144:147], 0\n\t"
  "v_add_f32_e32 v240, v240, v170\n\t"
  "v_mfma_f32_16x16x32_bf16 a[108:111], v[220:223], v[136:139], 0\n\t"
  "buffer_load_dwordx4 v238, s[8:11], s28 offen offset:3072 lds\n\t"
  "v_mfma_f32_16x16x32_bf16 a[104:107], v[220:223], v[128:131], 0\n\t"
  "v_add_f32_e32 v240, v240, v171\n\t"
  "s_add_u32 s28, s28, 28672\n\t"
  "s_waitcnt lgkmcnt(2)\n\t"
  "v_mfma_f32_16x16x32_bf16 a[112:115], v[224:227], v[128:131], 0\n\t"
  "ds_read_b128 v[216:219], v232 offset:1024\n\t"
  "v_cvt_pk_bf16_f32 v169, v170, v171\n\t"
  "v_mfma_f32_16x16x32_bf16 a[116:119], v[224:227], v[136:139], 0\n\t"
  "v_mfma_f32_16x16x32_bf16 v[112:115], v[224:227], v[144:147], 0\n\t"
  "v_exp_f32_e32 v172, v172\n\t"
  "v_mfma_f32_16x16x32_bf16 v[120:123], v[224:227], v[152:155], 0\n\t"
  "v_mfma_f32_16x16x32_bf16 v[124:127], v[228:231], v[152:155], 0\n\t"
  "ds_read_b128 v[220:223], v232 offset:1536\n\t"
  "v_add_f32_e32 v240, v240, v172\n\t"
  "v_mfma_f32_16x16x32_bf16 v[116:119], v[228:231], v[144:147], 0\n\t"
  "v_mfma_f32_16x16x32_bf16 a[124:127], v[228:231], v[136:139], 0\n\t"
  "v_exp_f32_e32 v173, v173\n\t"
  "v_mfma_f32_16x16x32_bf16 a[120:123], v[228:231], v[128:131], 0\n\t"
  "s_cmp_lg_u32 s34, 0\n\t"
  "s_cbranch_scc1 .Lrare_bgs\n\t"
  ".Lret_bgs:\n\t"
  "s_mov_b32 s31, 14\n\t"
  ".Lloops:\n\t"
  "s_waitcnt lgkmcnt(2)\n\t"
  "v_mfma_f32_16x16x32_bf16 v[128:131], v[208:211], a[128:131], v[192:195]\n\t"
  "ds_read_b128 v[224:227], v232 offset:2048\n\t"
  "v_exp_f32_e32 v174, v174\n\t"
  "v_mfma_f32_16x16x32_bf16 v[136:139], v[208:211], a[132:135], v[196:199]\n\t"
  "v_add_f32_e32 v240, v240, v173\n\t"
  "v_cvt_pk_bf16_f32 v170, v172, v173\n\t"
  "v_mfma_f32_16x16x32_bf16 v[144:147], v[208:211], a[136:139], v[200:203]\n\t"
  "v_exp_f32_e32 v175, v175\n\t"
  "v_add_f32_e32 v240, v240, v174\n\t"
  "v_mfma_f32_16x16x32_bf16 v[152:155], v[208:211], a[140:143], v[204:207]\n\t"
  "v_exp_f32_e32 v176, v176\n\t"
  "v_mfma_f32_16x16x32_bf16 v[152:155], v[212:215], a[156:159], v[152:155]\n\t"
  "ds_read_b128 v[208:211], v232 offset:2560\n\t"
  "v_add_f32_e32 v240, v240, v175\n\t"
  "v_cvt_pk_bf16_f32 v171, v174, v175\n\t"
  "v_mfma_f32_16x16x32_bf16 v[144:147], v[212:215], a[152:155], v[144:147]\n\t"
  "v_exp_f32_e32 v177, v177\n\t"
  "v_add_f32_e32 v241, v241, v176\n\t"
  "v_mfma_f32_16x16x32_bf16 v[136:139], v[212:215], a[148:151], v[136:139]\n\t"
  "v_exp_f32_e32 v178, v178\n\t"
  "v_mfma_f32_16x16x32_bf16 v[128:131], v[212:215], a[144:147], v[128:131]\n\t"
  "v_add_f32_e32 v241, v241, v177\n\t"
  "v_cvt_pk_bf16_f32 v176, v176, v177\n\t"
  "s_waitcnt lgkmcnt(2)\n\t"
  "v_mfma_f32_16x16x32_bf16 v[128:131], v[216:219], a[160:163], v[128:131]\n\t"
  "ds_read_b128 v[212:215], v232 offset:3072\n\t"
  "v_exp_f32_e32 v179, v179\n\t"
  "v_mfma_f32_16x16x32_bf16 v[136:139], v[216:219], a[164:167], v[136:139]\n\t"
  "v_add_f32_e32 v241, v241, v178\n\t"
  "v_exp_f32_e32 v180, v180\n\t"
  "v_mfma_f32_16x16x32_bf16 v[144:147], v[216:219], a[168:171], v[144:147]\n\t"
  "v_add_f32_e32 v241, v241, v179\n\t"
  "v_cvt_pk_bf16_f32 v177, v178, v179\n\t"
  "v_mfma_f32_16x16x32_bf16 v[152:155], v[216:219], a[172:175], v[152:155]\n\t"
  "v_exp_f32_e32 v181, v181\n\t"
  "v_add_f32_e32 v241, v241, v180\n\t"
  "v_mfma_f32_16x16x32_bf16 v[152:155], v[220:223], a[188:191], v[152:155]\n\t"
  "ds_read_b128 v[216:219], v232 offset:3584\n\t"
  "v_exp_f32_e32 v182, v182\n\t"
  "v_mfma_f32_16x16x32_bf16 v[144:147], v[220:223], a[184:187], v[144:147]\n\t"
  "v_add_f32_e32 v241, v241, v181\n\t"
  "v_cvt_pk_bf16_f32 v178, v180, v181\n\t"
  "v_mfma_f32_16x16x32_bf16 v[136:139], v[220:223], a[180:183], v[136:139]\n\t"
  "v_exp_f32_e32 v183, v183\n\t"
  "v_add_f32_e32 v241, v241, v182\n\t"
  "v_mfma_f32_16x16x32_bf16 v[128:131], v[220:223], a[176:179], v[128:131]\n\t"
  "v_exp_f32_e32 v184, v184\n\t"
  "s_waitcnt lgkmcnt(2)\n\t"
  "v_mfma_f32_16x16x32_bf16 v[128:131], v[224:227], a[192:195], v[128:131]\n\t"
  "ds_read_b128 v[220:223], v232 offset:8192\n\t"
  "v_add_f32_e32 v241, v241, v183\n\t"
  "v_cvt_pk_bf16_f32 v179, v182, v183\n\t"
  "v_mfma_f32_16x16x32_bf16 v[136:139], v[224:227], a[196:199], v[136:139]\n\t"
  "v_exp_f32_e32 v185, v185\n\t"
  "v_add_f32_e32 v242, v242, v184\n\t"
  "v_mfma_f32_16x16x32_bf16 v[144:147], v[224:227], a[200:203], v[144:147]\n\t"
  "v_exp_f32_e32 v186, v186\n\t"
  "v_mfma_f32_16x16x32_bf16 v[152:155], v[224:227], a[204:207], v[152:155]\n\t"
  "v_add_f32_e32 v242, v242, v185\n\t"
  "v_cvt_pk_bf16_f32 v184, v184, v185\n\t"
  "v_mfma_f32_16x16x32_bf16 v[152:155], v[208:211], a[220:223], v[152:155]\n\t"
  "ds_read_b128 v[224:227], v232 offset:8704\n\t"
  "v_exp_f32_e32 v187, v187\n\t"
  "v_mfma_f32_16x16x32_bf16 v[144:147], v[208:211], a[216:219], v[144:147]\n\t"
  "v_add_f32_e32 v242, v242, v186\n\t"
  "v_exp_f32_e32 v188, v188\n\t"
  "v_mfma_f32_16x16x32_bf16 v[136:139], v[208:211], a[212:215], v[136:139]\n\t"
  "v_add_f32_e32 v242, v242, v187\n\t"
  "v_cvt_pk_bf16_f32 v185, v186, v187\n\t"
  "v_mfma_f32_16x16x32_bf16 v[128:131], v[208:211], a[208:211], v[128:131]\n\t"
  "v_exp_f32_e32 v189, v189\n\t"
  "s_waitcnt lgkmcnt(2)\n\t"
  "v_mfma_f32_16x16x32_bf16 v[128:131], v[212:215], a[224:227], v[128:131]\n\t"
  "ds_read_b128 v[208:211], v232 offset:9216\n\t"
  "v_add_f32_e32 v242, v242, v188\n\t"
  "v_mfma_f32_16x16x32_bf16 v[136:139], v[212:215], a[228:231], v[136:139]\n\t"
  "v_exp_f32_e32 v190, v190\n\t"
  "v_add_f32_e32 v242, v242, v189\n\t"
  "v_mfma_f32_16x16x32_bf16 v[144:147], v[212:215], a[232:235], v[144:147]\n\t"
  "v_cvt_pk_bf16_f32 v186, v188, v189\n\t"
  "v_exp_f32_e32 v191, v191\n\t"
  "v_mfma_f32_16x16x32_bf16 v[152:155], v[212:215], a[236:239], v[152:155]\n\t"
  "v_add_f32_e32 v242, v242, v190\n\t"
  "v_mfma_f32_16x16x32_bf16 v[152:155], v[216:219], a[252:255], v[152:155]\n\t"
  "ds_read_b128 v[212:215], v232 offset:9728\n\t"
  "v_add_f32_e32 v242, v242, v191\n\t"
  "v_cvt_pk_bf16_f32 v187, v190, v191\n\t"
  "v_mfma_f32_16x16x32_bf16 v[144:147], v[216:219], a[248:251], v[144:147]\n\t"
  "v_mfma_f32_16x16x32_bf16 v[136:139], v[216:219], a[244:247], v[136:139]\n\t"
  "v_mfma_f32_16x16x32_bf16 v[128:131], v[216:219], a[240:243], v[128:131]\n\t"
  "s_waitcnt lgkmcnt(2)\n\t"
  "v_mfma_f32_16x16x32_bf16 v[132:135], v[220:223], a[128:131], v[192:195]\n\t"
  "ds_read_b128 v[216:219], v232 offset:10240\n\t"
  "v_mfma_f32_16x16x32_bf16 v[140:143], v[220:223], a[132:135], v[196:199]\n\t"
  "v_mfma_f32_16x16x32_bf16 v[148:151], v[220:223], a[136:139], v[200:203]\n\t"
  "v_mfma_f32_16x16x32_bf16 v[156:159], v[220:223], a[140:143], v[204:207]\n\t"
  "v_mfma_f32_16x16x32_bf16 v[156:159], v[224:227], a[156:159], v[156:159]\n\t"
  "ds_read_b128 v[220:223], v232 offset:10752\n\t"
  "v_max3_f32 v243, v128, v129, v130\n\t"
  "v_max3_f32 v243, v243, v131, v136\n\t"
  "v_mfma_f32_16x16x32_bf16 v[148:151], v[224:227], a[152:155], v[148:151]\n\t"
  "v_max3_f32 v243, v243, v137, v138\n\t"
  "v_max3_f32 v243, v243, v139, v144\n\t"
  "v_max3_f32 v243, v243, v145, v146\n\t"
  "v_mfma_f32_16x16x32_bf16 v[140:143], v[224:227], a[148:151], v[140:143]\n\t"
  "v_max3_f32 v243, v243, v147, v152\n\t"
  "v_max3_f32 v243, v243, v153, v154\n\t"
  "v_mfma_f32_16x16x32_bf16 v[132:135], v[224:227], a[144:147], v[132:135]\n\t"
  "s_waitcnt lgkmcnt(2)\n\t"
  "v_mfma_f32_16x16x32_bf16 v[132:135], v[208:211], a[160:163], v[132:135]\n\t"
  "ds_read_b128 v[224:227], v232 offset:11264\n\t"
  "v_mfma_f32_16x16x32_bf16 v[140:143], v[208:211], a[164:167], v[140:143]\n\t"
  "v_mfma_f32_16x16x32_bf16 v[148:151], v[208:211], a[168:171], v[148:151]\n\t"
  "v_mfma_f32_16x16x32_bf16 v[156:159], v[208:211], a[172:175], v[156:159]\n\t"
  "v_mfma_f32_16x16x32_bf16 v[156:159], v[212:215], a[188:191], v[156:159]\n\t"
  "ds_read_b128 v[208:211], v232 offset:11776\n\t"
  "v_mfma_f32_16x16x32_bf16 v[148:151], v[212:215], a[184:187], v[148:151]\n\t"
  "v_mfma_f32_16x16x32_bf16 v[140:143], v[212:215], a[180:183], v[140:143]\n\t"
  "v_mfma_f32_16x16x32_bf16 v[132:135], v[212:215], a[176:179], v[132:135]\n\t"
  "s_waitcnt lgkmcnt(2)\n\t"
  "v_mfma_f32_16x16x32_bf16 v[132:135], v[216:219], a[192:195], v[132:135]\n\t"
  "v_mfma_f32_16x16x32_bf16 v[140:143], v[216:219], a[196:199], v[140:143]\n\t"
  "v_mfma_f32_16x16x32_bf16 v[148:151], v[216:219], a[200:203], v[148:151]\n\t"
  "v_mfma_f32_16x16x32_bf16 v[156:159], v[216:219], a[204:207], v[156:159]\n\t"
  "v_mfma_f32_16x16x32_bf16 v[156:159], v[220:223], a[220:223], v[156:159]\n\t"
  "v_mfma_f32_16x16x32_bf16 v[148:151], v[220:223], a[216:219], v[148:151]\n\t"
  "v_mfma_f32_16x16x32_bf16 v[140:143], v[220:223], a[212:215], v[140:143]\n\t"
  "s_waitcnt vmcnt(0) lgkmcnt(0)\n\t"
  "s_barrier\n\t"
  "v_mfma_f32_16x16x32_bf16 v[132:135], v[220:223], a[208:211], v[132:135]\n\t"
  "ds_read_b64_tr_b16 v[212:213], v233 offset:32768\n\t"
  "v_mfma_f32_16x16x32_bf16 v[132:135], v[224:227], a[224:227], v[132:135]\n\t"
  "ds_read_b64_tr_b16 v[214:215], v233 offset:40960\n\t"
  "v_mfma_f32_16x16x32_bf16 v[140:143], v[224:227], a[228:231], v[140:143]\n\t"
  "ds_read_b64_tr_b16 v[216:217], v234 offset:32768\n\t"
  "s_add_u32 m0, s30, 65536\n\t"
  "v_mfma_f32_16x16x32_bf16 v[148:151], v[224:227], a[232:235], v[148:151]\n\t"
  "ds_read_b64_tr_b16 v[218:219], v234 offset:40960\n\t"
  "buffer_load_dwordx4 v235, s[12:15], s29 offen lds\n\t"
  "v_mfma_f32_16x16x32_bf16 v[156:159], v[224:227], a[236:239], v[156:159]\n\t"
  "ds_read_b64_tr_b16 v[220:221], v233 offset:33280\n\t"
  "v_mfma_f32_16x16x32_bf16 v[156:159], v[208:211], a[252:255], v[156:159]\n\t"
  "ds_read_b64_tr_b16 v[222:223], v233 offset:41472\n\t"
  "v_mfma_f32_16x16x32_bf16 v[148:151], v[208:211], a[248:251], v[148:151]\n\t"
  "ds_read_b64_tr_b16 v[224:225], v234 offset:33280\n\t"
  "v_mfma_f32_16x16x32_bf16 v[140:143], v[208:211], a[244:247], v[140:143]\n\t"
  "ds_read_b64_tr_b16 v[226:227], v234 offset:41472\n\t"
  "buffer_load_dwordx4 v236, s[12:15], s29 offen offset:1024 lds\n\t"
  "v_mfma_f32_16x16x32_bf16 v[132:135], v[208:211], a[240:243], v[132:135]\n\t"
  "s_waitcnt lgkmcnt(4)\n\t"
  "v_mfma_f32_16x16x32_bf16 a[0:3], v[212:215], v[160:163], a[0:3]\n\t"
  "ds_read_b64_tr_b16 v[208:209], v233 offset:33792\n\t"
  "v_mfma_f32_16x16x32_bf16 a[4:7], v[212:215], v[168:171], a[4:7]\n\t"
  "ds_read_b64_tr_b16 v[210:211], v233 offset:41984\n\t"
  "v_mfma_f32_16x16x32_bf16 v[0:3], v[212:215], v[176:179], v[0:3]\n\t"
  "buffer_load_dwordx4 v237, s[12:15], s29 offen offset:2048 lds\n\t"
  "v_mfma_f32_16x16x32_bf16 v[8:11], v[212:215], v[184:187], v[8:11]\n\t"
  "v_mfma_f32_16x16x32_bf16 v[12:15], v[216:219], v[184:187], v[12:15]\n\t"
  "ds_read_b64_tr_b16 v[212:213], v234 offset:33792\n\t"
  "v_max3_f32 v243, v243, v155, v132\n\t"
  "v_max3_f32 v243, v243, v133, v134\n\t"
  "v_mfma_f32_16x16x32_bf16 v[4:7], v[216:219], v[176:179], v[4:7]\n\t"
  "ds_read_b64_tr_b16 v[214:215], v234 offset:41984\n\t"
  "v_max3_f32 v243, v243, v135, v140\n\t"
  "v_max3_f32 v243, v243, v141, v142\n\t"
  "v_mfma_f32_16x16x32_bf16 a[12:15], v[216:219], v[168:171], a[12:15]\n\t"
  "v_max3_f32 v243, v243, v143, v148\n\t"
  "buffer_load_dwordx4 v238, s[12:15], s29 offen offset:3072 lds\n\t"
  "v_mfma_f32_16x16x32_bf16 a[8:11], v[216:219], v[160:163], a[8:11]\n\t"
  "v_max3_f32 v243, v243, v149, v150\n\t"
  "s_add_u32 s29, s29, 4096\n\t"
  "s_waitcnt lgkmcnt(4)\n\t"
  "v_mfma_f32_16x16x32_bf16 a[16:19], v[220:223], v[160:163], a[16:19]\n\t"
  "ds_read_b64_tr_b16 v[216:217], v233 offset:34304\n\t"
  "v_max3_f32 v243, v243, v151, v156\n\t"
  "v_max3_f32 v243, v243, v157, v158\n\t"
  "v_mfma_f32_16x16x32_bf16 a[20:23], v[220:223], v[168:171], a[20:23]\n\t"
  "ds_read_b64_tr_b16 v[218:219], v233 offset:42496\n\t"
  "v_max_f32_e32 v243, v243, v159\n\t"
  "s_add_u32 m0, s30, 69632\n\t"
  "v_mfma_f32_16x16x32_bf16 v[16:19], v[220:223], v[176:179], v[16:19]\n\t"
  "v_cmp_lt_f32_e32 vcc, 0x41000000, v243\n\t"
  "buffer_load_dwordx4 v235, s[12:15], s29 offen lds\n\t"
  "v_mfma_f32_16x16x32_bf16 v[24:27], v[220:223], v[184:187], v[24:27]\n\t"
  "s_cbranch_vccnz .Lrare_aes\n\t"
  ".Lret_aes:\n\t"
  "v_exp_f32_e32 v128, v128\n\t"
  "v_mfma_f32_16x16x32_bf16 v[28:31], v[224:227], v[184:187], v[28:31]\n\t"
  "ds_read_b64_tr_b16 v[220:221], v234 offset:34304\n\t"
  "v_exp_f32_e32 v129, v129\n\t"
  "v_mfma_f32_16x16x32_bf16 v[20:23], v[224:227], v[176:179], v[20:23]\n\t"
  "ds_read_b64_tr_b16 v[222:223], v234 offset:42496\n\t"
  "v_add_f32_e32 v239, v239, v128\n\t"
  "v_mfma_f32_16x16x32_bf16 a[28:31], v[224:227], v[168:171], a[28:31]\n\t"
  "v_add_f32_e32 v239, v239, v129\n\t"
  "buffer_load_dwordx4 v236, s[12:15], s29 offen offset:1024 lds\n\t"
  "v_mfma_f32_16x16x32_bf16 a[24:27], v[224:227], v[160:163], a[24:27]\n\t"
  "v_exp_f32_e32 v130, v130\n\t"
  "s_waitcnt lgkmcnt(4)\n\t"
  "v_mfma_f32_16x16x32_bf16 a[32:35], v[208:211], v[160:163], a[32:35]\n\t"
  "ds_read_b64_tr_b16 v[224:225], v233 offset:34816\n\t"
  "v_cvt_pk_bf16_f32 v128, v128, v129\n\t"
  "v_mfma_f32_16x16x32_bf16 a[36:39], v[208:211], v[168:171], a[36:39]\n\t"
  "ds_read_b64_tr_b16 v[226:227], v233 offset:43008\n\t"
  "v_exp_f32_e32 v131, v131\n\t"
  "v_mfma_f32_16x16x32_bf16 v[32:35], v[208:211], v[176:179], v[32:35]\n\t"
  "v_add_f32_e32 v239, v239, v130\n\t"
  "buffer_load_dwordx4 v237, s[12:15], s29 offen offset:2048 lds\n\t"
  "v_mfma_f32_16x16x32_bf16 v[40:43], v[208:211], v[184:187], v[40:43]\n\t"
  "v_mfma_f32_16x16x32_bf16 v[44:47], v[212:215], v[184:187], v[44:47]\n\t"
  "ds_read_b64_tr_b16 v[228:229], v234 offset:34816\n\t"
  "v_add_f32_e32 v239, v239, v131\n\t"
  "v_mfma_f32_16x16x32_bf16 v[36:39], v[212:215], v[176:179], v[36:39]\n\t"
  "ds_read_b64_tr_b16 v[230:231], v234 offset:43008\n\t"
  "v_cvt_pk_bf16_f32 v129, v130, v131\n\t"
  "v_mfma_f32_16x16x32_bf16 a[44:47], v[212:215], v[168:171], a[44:47]\n\t"
  "buffer_load_dwordx4 v238, s[12:15], s29 offen offset:3072 lds\n\t"
  "v_mfma_f32_16x16x32_bf16 a[40:43], v[212:215], v[160:163], a[40:43]\n\t"
  "s_add_u32 s29, s29, 28672\n\t"
  "s_waitcnt lgkmcnt(4)\n\t"
  "v_mfma_f32_16x16x32_bf16 a[48:51], v[216:219], v[160:163], a[48:51]\n\t"
  "ds_read_b64_tr_b16 v[208:209], v233 offset:35328\n\t"
  "v_exp_f32_e32 v132, v132\n\t"
  "v_mfma_f32_16x16x32_bf16 a[52:55], v[216:219], v[168:171], a[52:55]\n\t"
  "ds_read_b64_tr_b16 v[210:211], v233 offset:43520\n\t"
  "s_add_u32 m0, s30, 0\n\t"
  "v_mfma_f32_16x16x32_bf16 v[48:51], v[216:219], v[176:179], v[48:51]\n\t"
  "v_add_f32_e32 v239, v239, v132\n\t"
  "buffer_load_dwordx4 v235, s[8:11], s28 offen lds\n\t"
  "v_mfma_f32_16x16x32_bf16 v[56:59], v[216:219], v[184:187], v[56:59]\n\t"
  "v_exp_f32_e32 v133, v133\n\t"
  "v_mfma_f32_16x16x32_bf16 v[60:63], v[220:223], v[184:187], v[60:63]\n\t"
  "ds_read_b64_tr_b16 v[212:213], v234 offset:35328\n\t"
  "v_mfma_f32_16x16x32_bf16 v[52:55], v[220:223], v[176:179], v[52:55]\n\t"
  "ds_read_b64_tr_b16 v[214:215], v234 offset:43520\n\t"
  "v_add_f32_e32 v239, v239, v133\n\t"
  "v_mfma_f32_16x16x32_bf16 a[60:63], v[220:223], v[168:171], a[60:63]\n\t"
  "v_cvt_pk_bf16_f32 v130, v132, v133\n\t"
  "buffer_load_dwordx4 v236, s[8:11], s28 offen offset:1024 lds\n\t"
  "v_mfma_f32_16x16x32_bf16 a[56:59], v[220:223], v[160:163], a[56:59]\n\t"
  "s_waitcnt lgkmcnt(4)\n\t"
  "v_mfma_f32_16x16x32_bf16 a[64:67], v[224:227], v[160:163], a[64:67]\n\t"
  "ds_read_b64_tr_b16 v[216:217], v233 offset:35840\n\t"
  "v_exp_f32_e32 v134, v134\n\t"
  "v_mfma_f32_16x16x32_bf16 a[68:71], v[224:227], v[168:171], a[68:71]\n\t"
  "ds_read_b64_tr_b16 v[218:219], v233 offset:44032\n\t"
  "v_mfma_f32_16x16x32_bf16 v[64:67], v[224:227], v[176:179], v[64:67]\n\t"
  "v_add_f32_e32 v239, v239, v134\n\t"
  "buffer_load_dwordx4 v237, s[8:11], s28 offen offset:2048 lds\n\t"
  "v_mfma_f32_16x16x32_bf16 v[72:75], v[224:227], v[184:187], v[72:75]\n\t"
  "v_mfma_f32_16x16x32_bf16 v[76:79], v[228:231], v[184:187], v[76:79]\n\t"
  "ds_read_b64_tr_b16 v[220:221], v234 offset:35840\n\t"
  "v_exp_f32_e32 v135, v135\n\t"
  "v_mfma_f32_16x16x32_bf16 v[68:71], v[228:231], v[176:179], v[68:71]\n\t"
  "ds_read_b64_tr_b16 v[222:223], v234 offset:44032\n\t"
  "v_mfma_f32_16x16x32_bf16 a[76:79], v[228:231], v[168:171], a[76:79]\n\t"
  "v_add_f32_e32 v239, v239, v135\n\t"
  "buffer_load_dwordx4 v238, s[8:11], s28 offen offset:3072 lds\n\t"
  "v_mfma_f32_16x16x32_bf16 a[72:75], v[228:231], v[160:163], a[72:75]\n\t"
  "v_cvt_pk_bf16_f32 v131, v134, v135\n\t"
  "s_add_u32 s28, s28, 4096\n\t"
  "s_waitcnt lgkmcnt(4)\n\t"
  "v_mfma_f32_16x16x32_bf16 a[80:83], v[208:211], v[160:163], a[80:83]\n\t"
  "ds_read_b64_tr_b16 v[224:225], v233 offset:36352\n\t"
  "v_mfma_f32_16x16x32_bf16 a[84:87], v[208:211], v[168:171], a[84:87]\n\t"
  "ds_read_b64_tr_b16 v[226:227], v233 offset:44544\n\t"
  "s_add_u32 m0, s30, 4096\n\t"
  "v_mfma_f32_16x16x32_bf16 v[80:83], v[208:211], v[176:179], v[80:83]\n\t"
  "buffer_load_dwordx4 v235, s[8:11], s28 offen lds\n\t"
  "v_mfma_f32_16x16x32_bf16 v[88:91], v[208:211], v[184:187], v[88:91]\n\t"
  "v_exp_f32_e32 v136, v136\n\t"
  "v_mfma_f32_16x16x32_bf16 v[92:95], v[212:215], v[184:187], v[92:95]\n\t"
  "ds_read_b64_tr_b16 v[228:229], v234 offset:36352\n\t"
  "v_exp_f32_e32 v137, v137\n\t"
  "v_mfma_f32_16x16x32_bf16 v[84:87], v[212:215], v[176:179], v[84:87]\n\t"
  "ds_read_b64_tr_b16 v[230:231], v234 offset:44544\n\t"
  "v_add_f32_e32 v240, v240, v136\n\t"
  "v_mfma_f32_16x16x32_bf16 a[92:95], v[212:215], v[168:171], a[92:95]\n\t"
  "v_add_f32_e32 v240, v240, v137\n\t"
  "buffer_load_dwordx4 v236, s[8:11], s28 offen offset:1024 lds\n\t"
  "v_mfma_f32_16x16x32_bf16 a[88:91], v[212:215], v[160:163], a[88:91]\n\t"
  "v_cvt_pk_bf16_f32 v136, v136, v137\n\t"
  "s_waitcnt lgkmcnt(4)\n\t"
  "v_mfma_f32_16x16x32_bf16 a[96:99], v[216:219], v[160:163], a[96:99]\n\t"
  "ds_read_b128 v[208:211], v232 offset:32768\n\t"
  "v_mfma_f32_16x16x32_bf16 a[100:103], v[216:219], v[168:171], a[100:103]\n\t"
  "v_mfma_f32_16x16x32_bf16 v[96:99], v[216:219], v[176:179], v[96:99]\n\t"
  "buffer_load_dwordx4 v237, s[8:11], s28 offen offset:2048 lds\n\t"
  "v_mfma_f32_16x16x32_bf16 v[104:107], v[216:219], v[184:187], v[104:107]\n\t"
  "v_exp_f32_e32 v138, v138\n\t"
  "v_mfma_f32_16x16x32_bf16 v[108:111], v[220:223], v[184:187], v[108:111]\n\t"
  "ds_read_b128 v[212:215], v232 offset:33280\n\t"
  "v_exp_f32_e32 v139, v139\n\t"
  "v_mfma_f32_16x16x32_bf16 v[100:103], v[220:223], v[176:179], v[100:103]\n\t"
  "v_add_f32_e32 v240, v240, v138\n\t"
  "v_mfma_f32_16x16x32_bf16 a[108:111], v[220:223], v[168:171], a[108:111]\n\t"
  "buffer_load_dwordx4 v238, s[8:11], s28 offen offset:3072 lds\n\t"
  "v_mfma_f32_16x16x32_bf16 a[104:107], v[220:223], v[160:163], a[104:107]\n\t"
  "v_add_f32_e32 v240, v240, v139\n\t"
  "s_add_u32 s28, s28, 28672\n\t"
  "s_waitcnt lgkmcnt(2)\n\t"
  "v_mfma_f32_16x16x32_bf16 a[112:115], v[224:227], v[160:163], a[112:115]\n\t"
  "ds_read_b128 v[216:219], v232 offset:33792\n\t"
  "v_cvt_pk_bf16_f32 v137, v138, v139\n\t"
  "v_mfma_f32_16x16x32_bf16 a[116:119], v[224:227], v[168:171], a[116:119]\n\t"
  "v_mfma_f32_16x16x32_bf16 v[112:115], v[224:227], v[176:179], v[112:115]\n\t"
  "v_exp_f32_e32 v140, v140\n\t"
  "v_mfma_f32_16x16x32_bf16 v[120:123], v[224:227], v[184:187], v[120:123]\n\t"
  "v_mfma_f32_16x16x32_bf16 v[124:127], v[228:231], v[184:187], v[124:127]\n\t"
  "ds_read_b128 v[220:223], v232 offset:34304\n\t"
  "v_add_f32_e32 v240, v240, v140\n\t"
  "v_mfma_f32_16x16x32_bf16 v[116:119], v[228:231], v[176:179], v[116:119]\n\t"
  "v_mfma_f32_16x16x32_bf16 a[124:127], v[228:231], v[168:171], a[124:127]\n\t"
  "v_exp_f32_e32 v141, v141\n\t"
  "v_mfma_f32_16x16x32_bf16 a[120:123], v[228:231], v[160:163], a[120:123]\n\t"
  "s_cmp_lg_u32 s34, 0\n\t"
  "s_cbranch_scc1 .Lrare_bes\n\t"
  ".Lret_bes:\n\t"
  "s_waitcnt lgkmcnt(2)\n\t"
  "v_mfma_f32_16x16x32_bf16 v[160:163], v[208:211], a[128:131], v[192:195]\n\t"
  "ds_read_b128 v[224:227], v232 offset:34816\n\t"
  "v_exp_f32_e32 v142, v142\n\t"
  "v_mfma_f32_16x16x32_bf16 v[168:171], v[208:211], a[132:135], v[196:199]\n\t"
  "v_add_f32_e32 v240, v240, v141\n\t"
  "v_cvt_pk_bf16_f32 v138, v140, v141\n\t"
  "v_mfma_f32_16x16x32_bf16 v[176:179], v[208:211], a[136:139], v[200:203]\n\t"
  "v_exp_f32_e32 v143, v143\n\t"
  "v_add_f32_e32 v240, v240, v142\n\t"
  "v_mfma_f32_16x16x32_bf16 v[184:187], v[208:211], a[140:143], v[204:207]\n\t"
  "v_exp_f32_e32 v144, v144\n\t"
  "v_mfma_f32_16x16x32_bf16 v[184:187], v[212:215], a[156:159], v[184:187]\n\t"
  "ds_read_b128 v[208:211], v232 offset:35328\n\t"
  "v_add_f32_e32 v240, v240, v143\n\t"
  "v_cvt_pk_bf16_f32 v139, v142, v143\n\t"
  "v_mfma_f32_16x16x32_bf16 v[176:179], v[212:215], a[152:155], v[176:179]\n\t"
  "v_exp_f32_e32 v145, v145\n\t"
  "v_add_f32_e32 v241, v241, v144\n\t"
  "v_mfma_f32_16x16x32_bf16 v[168:171], v[212:215], a[148:151], v[168:171]\n\t"
  "v_exp_f32_e32 v146, v146\n\t"
  "v_mfma_f32_16x16x32_bf16 v[160:163], v[212:215], a[144:147], v[160:163]\n\t"
  "v_add_f32_e32 v241, v241, v145\n\t"
  "v_cvt_pk_bf16_f32 v144, v144, v145\n\t"
  "s_waitcnt lgkmcnt(2)\n\t"
  "v_mfma_f32_16x16x32_bf16 v[160:163], v[216:219], a[160:163], v[160:163]\n\t"
  "ds_read_b128 v[212:215], v232 offset:35840\n\t"
  "v_exp_f32_e32 v147, v147\n\t"
  "v_mfma_f32_16x16x32_bf16 v[168:171], v[216:219], a[164:167], v[168:171]\n\t"
  "v_add_f32_e32 v241, v241, v146\n\t"
  "v_exp_f32_e32 v148, v148\n\t"
  "v_mfma_f32_16x16x32_bf16 v[176:179], v[216:219], a[168:171], v[176:179]\n\t"
  "v_add_f32_e32 v241, v241, v147\n\t"
  "v_cvt_pk_bf16_f32 v145, v146, v147\n\t"
  "v_mfma_f32_16x16x32_bf16 v[184:187], v[216:219], a[172:175], v[184:187]\n\t"
  "v_exp_f32_e32 v149, v149\n\t"
  "v_add_f32_e32 v241, v241, v148\n\t"
  "v_mfma_f32_16x16x32_bf16 v[184:187], v[220:223], a[188:191], v[184:187]\n\t"
  "ds_read_b128 v[216:219], v232 offset:36352\n\t"
  "v_exp_f32_e32 v150, v150\n\t"
  "v_mfma_f32_16x16x32_bf16 v[176:179], v[220:223], a[184:187], v[176:179]\n\t"
  "v_add_f32_e32 v241, v241, v149\n\t"
  "v_cvt_pk_bf16_f32 v146, v148, v149\n\t"
  "v_mfma_f32_16x16x32_bf16 v[168:171], v[220:223], a[180:183], v[168:171]\n\t"
  "v_exp_f32_e32 v151, v151\n\t"
  "v_add_f32_e32 v241, v241, v150\n\t"
  "v_mfma_f32_16x16x32_bf16 v[160:163], v[220:223], a[176:179], v[160:163]\n\t"
  "v_exp_f32_e32 v152, v152\n\t"
  "s_waitcnt lgkmcnt(2)\n\t"
  "v_mfma_f32_16x16x32_bf16 v[160:163], v[224:227], a[192:195], v[160:163]\n\t"
  "ds_read_b128 v[220:223], v232 offset:40960\n\t"
  "v_add_f32_e32 v241, v241, v151\n\t"
  "v_cvt_pk_bf16_f32 v147, v150, v151\n\t"
  "v_mfma_f32_16x16x32_bf16 v[168:171], v[224:227], a[196:199], v[168:171]\n\t"
  "v_exp_f32_e32 v153, v153\n\t"
  "v_add_f32_e32 v242, v242, v152\n\t"
  "v_mfma_f32_16x16x32_bf16 v[176:179], v[224:227], a[200:203], v[176:179]\n\t"
  "v_exp_f32_e32 v154, v154\n\t"
  "v_mfma_f32_16x16x32_bf16 v[184:187], v[224:227], a[204:207], v[184:187]\n\t"
  "v_add_f32_e32 v242, v242, v153\n\t"
  "v_cvt_pk_bf16_f32 v152, v152, v153\n\t"
  "v_mfma_f32_16x16x32_bf16 v[184:187], v[208:211], a[220:223], v[184:187]\n\t"
  "ds_read_b128 v[224:227], v232 offset:41472\n\t"
  "v_exp_f32_e32 v155, v155\n\t"
  "v_mfma_f32_16x16x32_bf16 v[176:179], v[208:211], a[216:219], v[176:179]\n\t"
  "v_add_f32_e32 v242, v242, v154\n\t"
  "v_exp_f32_e32 v156, v156\n\t"
  "v_mfma_f32_16x16x32_bf16 v[168:171], v[208:211], a[212:215], v[168:171]\n\t"
  "v_add_f32_e32 v242, v242, v155\n\t"
  "v_cvt_pk_bf16_f32 v153, v154, v155\n\t"
  "v_mfma_f32_16x16x32_bf16 v[160:163], v[208:211], a[208:211], v[160:163]\n\t"
  "v_exp_f32_e32 v157, v157\n\t"
  "s_waitcnt lgkmcnt(2)\n\t"
  "v_mfma_f32_16x16x32_bf16 v[160:163], v[212:215], a[224:227], v[160:163]\n\t"
  "ds_read_b128 v[208:211], v232 offset:41984\n\t"
  "v_add_f32_e32 v242, v242, v156\n\t"
  "v_mfma_f32_16x16x32_bf16 v[168:171], v[212:215], a[228:231], v[168:171]\n\t"
  "v_exp_f32_e32 v158, v158\n\t"
  "v_add_f32_e32 v242, v242, v157\n\t"
  "v_mfma_f32_16x16x32_bf16 v[176:179], v[212:215], a[232:235], v[176:179]\n\t"
  "v_cvt_pk_bf16_f32 v154, v156, v157\n\t"
  "v_exp_f32_e32 v159, v159\n\t"
  "v_mfma_f32_16x16x32_bf16 v[184:187], v[212:215], a[236:239], v[184:187]\n\t"
  "v_add_f32_e32 v242, v242, v158\n\t"
  "v_mfma_f32_16x16x32_bf16 v[184:187], v[216:219], a[252:255], v[184:187]\n\t"
  "ds_read_b128 v[212:215], v232 offset:42496\n\t"
  "v_add_f32_e32 v242, v242, v159\n\t"
  "v_cvt_pk_bf16_f32 v155, v158, v159\n\t"
  "v_mfma_f32_16x16x32_bf16 v[176:179], v[216:219], a[248:251], v[176:179]\n\t"
  "v_mfma_f32_16x16x32_bf16 v[168:171], v[216:219], a[244:247], v[168:171]\n\t"
  "v_mfma_f32_16x16x32_bf16 v[160:163], v[216:219], a[240:243], v[160:163]\n\t"
  "s_waitcnt lgkmcnt(2)\n\t"
  "v_mfma_f32_16x16x32_bf16 v[164:167], v[220:223], a[128:131], v[192:195]\n\t"
  "ds_read_b128 v[216:219], v232 offset:43008\n\t"
  "v_mfma_f32_16x16x32_bf16 v[172:175], v[220:223], a[132:135], v[196:199]\n\t"
  "v_mfma_f32_16x16x32_bf16 v[180:183], v[220:223], a[136:139], v[200:203]\n\t"
  "v_mfma_f32_16x16x32_bf16 v[188:191], v[220:223], a[140:143], v[204:207]\n\t"
  "v_mfma_f32_16x16x32_bf16 v[188:191], v[224:227], a[156:159], v[188:191]\n\t"
  "ds_read_b128 v[220:223], v232 offset:43520\n\t"
  "v_max3_f32 v243, v160, v161, v162\n\t"
  "v_max3_f32 v243, v243, v163, v168\n\t"
  "v_mfma_f32_16x16x32_bf16 v[180:183], v[224:227], a[152:155], v[180:183]\n\t"
  "v_max3_f32 v243, v243, v169, v170\n\t"
  "v_max3_f32 v243, v243, v171, v176\n\t"
  "v_max3_f32 v243, v243, v177, v178\n\t"
  "v_mfma_f32_16x16x32_bf16 v[172:175], v[224:227], a[148:151], v[172:175]\n\t"
  "v_max3_f32 v243, v243, v179, v184\n\t"
  "v_max3_f32 v243, v243, v185, v186\n\t"
  "v_mfma_f32_16x16x32_bf16 v[164:167], v[224:227], a[144:147], v[164:167]\n\t"
  "s_waitcnt lgkmcnt(2)\n\t"
  "v_mfma_f32_16x16x32_bf16 v[164:167], v[208:211], a[160:163], v[164:167]\n\t"
  "ds_read_b128 v[224:227], v232 offset:44032\n\t"
  "v_mfma_f32_16x16x32_bf16 v[172:175], v[208:211], a[164:167], v[172:175]\n\t"
  "v_mfma_f32_16x16x32_bf16 v[180:183], v[208:211], a[168:171], v[180:183]\n\t"
  "v_mfma_f32_16x16x32_bf16 v[188:191], v[208:211], a[172:175], v[188:191]\n\t"
  "v_mfma_f32_16x16x32_bf16 v[188:191], v[212:215], a[188:191], v[188:191]\n\t"
  "ds_read_b128 v[208:211], v232 offset:44544\n\t"
  "v_mfma_f32_16x16x32_bf16 v[180:183], v[212:215], a[184:187], v[180:183]\n\t"
  "v_mfma_f32_16x16x32_bf16 v[172:175], v[212:215], a[180:183], v[172:175]\n\t"
  "v_mfma_f32_16x16x32_bf16 v[164:167], v[212:215], a[176:179], v[164:167]\n\t"
  "s_waitcnt lgkmcnt(2)\n\t"
  "v_mfma_f32_16x16x32_bf16 v[164:167], v[216:219], a[192:195], v[164:167]\n\t"
  "v_mfma_f32_16x16x32_bf16 v[172:175], v[216:219], a[196:199], v[172:175]\n\t"
  "v_mfma_f32_16x16x32_bf16 v[180:183], v[216:219], a[200:203], v[180:183]\n\t"
  "v_mfma_f32_16x16x32_bf16 v[188:191], v[216:219], a[204:207], v[188:191]\n\t"
  "v_mfma_f32_16x16x32_bf16 v[188:191], v[220:223], a[220:223], v[188:191]\n\t"
  "v_mfma_f32_16x16x32_bf16 v[180:183], v[220:223], a[216:219], v[180:183]\n\t"
  "v_mfma_f32_16x16x32_bf16 v[172:175], v[220:223], a[212:215], v[172:175]\n\t"
  "s_waitcnt vmcnt(0) lgkmcnt(0)\n\t"
  "s_barrier\n\t"
  "v_mfma_f32_16x16x32_bf16 v[164:167], v[220:223], a[208:211], v[164:167]\n\t"
  "ds_read_b64_tr_b16 v[212:213], v233\n\t"
  "v_mfma_f32_16x16x32_bf16 v[164:167], v[224:227], a[224:227], v[164:167]\n\t"
  "ds_read_b64_tr_b16 v[214:215], v233 offset:8192\n\t"
  "v_mfma_f32_16x16x32_bf16 v[172:175], v[224:227], a[228:231], v[172:175]\n\t"
  "ds_read_b64_tr_b16 v[216:217], v234\n\t"
  "s_add_u32 m0, s30, 98304\n\t"
  "v_mfma_f32_16x16x32_bf16 v[180:183], v[224:227], a[232:235], v[180:183]\n\t"
  "ds_read_b64_tr_b16 v[218:219], v234 offset:8192\n\t"
  "buffer_load_dwordx4 v235, s[12:15], s29 offen lds\n\t"
  "v_mfma_f32_16x16x32_bf16 v[188:191], v[224:227], a[236:239], v[188:191]\n\t"
  "ds_read_b64_tr_b16 v[220:221], v233 offset:512\n\t"
  "v_mfma_f32_16x16x32_bf16 v[188:191], v[208:211], a[252:255], v[188:191]\n\t"
  "ds_read_b64_tr_b16 v[222:223], v233 offset:8704\n\t"
  "v_mfma_f32_16x16x32_bf16 v[180:183], v[208:211], a[248:251], v[180:183]\n\t"
  "ds_read_b64_tr_b16 v[224:225], v234 offset:512\n\t"
  "v_mfma_f32_16x16x32_bf16 v[172:175], v[208:211], a[244:247], v[172:175]\n\t"
  "ds_read_b64_tr_b16 v[226:227], v234 offset:8704\n\t"
  "buffer_load_dwordx4 v236, s[12:15], s29 offen offset:1024 lds\n\t"
  "v_mfma_f32_16x16x32_bf16 v[164:167], v[208:211], a[240:243], v[164:167]\n\t"
  "s_waitcnt lgkmcnt(4)\n\t"
  "v_mfma_f32_16x16x32_bf16 a[0:3], v[212:215], v[128:131], a[0:3]\n\t"
  "ds_read_b64_tr_b16 v[208:209], v233 offset:1024\n\t"
  "v_mfma_f32_16x16x32_bf16 a[4:7], v[212:215], v[136:139], a[4:7]\n\t"
  "ds_read_b64_tr_b16 v[210:211], v233 offset:9216\n\t"
  "v_mfma_f32_16x16x32_bf16 v[0:3], v[212:215], v[144:147], v[0:3]\n\t"
  "buffer_load_dwordx4 v237, s[12:15], s29 offen offset:2048 lds\n\t"
  "v_mfma_f32_16x16x32_bf16 v[8:11], v[212:215], v[152:155], v[8:11]\n\t"
  "v_mfma_f32_16x16x32_bf16 v[12:15], v[216:219], v[152:155], v[12:15]\n\t"
  "ds_read_b64_tr_b16 v[212:213], v234 offset:1024\n\t"
  "v_max3_f32 v243, v243, v187, v164\n\t"
  "v_max3_f32 v243, v243, v165, v166\n\t"
  "v_mfma_f32_16x16x32_bf16 v[4:7], v[216:219], v[144:147], v[4:7]\n\t"
  "ds_read_b64_tr_b16 v[214:215], v234 offset:9216\n\t"
  "v_max3_f32 v243, v243, v167, v172\n\t"
  "v_max3_f32 v243, v243, v173, v174\n\t"
  "v_mfma_f32_16x16x32_bf16 a[12:15], v[216:219], v[136:139], a[12:15]\n\t"
  "v_max3_f32 v243, v243, v175, v180\n\t"
  "buffer_load_dwordx4 v238, s[12:15], s29 offen offset:3072 lds\n\t"
  "v_mfma_f32_16x16x32_bf16 a[8:11], v[216:219], v[128:131], a[8:11]\n\t"
  "v_max3_f32 v243, v243, v181, v182\n\t"
  "s_add_u32 s29, s29, 4096\n\t"
  "s_waitcnt lgkmcnt(4)\n\t"
  "v_mfma_f32_16x16x32_bf16 a[16:19], v[220:223], v[128:131], a[16:19]\n\t"
  "ds_read_b64_tr_b16 v[216:217], v233 offset:1536\n\t"
  "v_max3_f32 v243, v243, v183, v188\n\t"
  "v_max3_f32 v243, v243, v189, v190\n\t"
  "v_mfma_f32_16x16x32_bf16 a[20:23], v[220:223], v[136:139], a[20:23]\n\t"
  "ds_read_b64_tr_b16 v[218:219], v233 offset:9728\n\t"
  "v_max_f32_e32 v243, v243, v191\n\t"
  "s_add_u32 m0, s30, 102400\n\t"
  "v_mfma_f32_16x16x32_bf16 v[16:19], v[220:223], v[144:147], v[16:19]\n\t"
  "v_cmp_lt_f32_e32 vcc, 0x41000000, v243\n\t"
  "buffer_load_dwordx4 v235, s[12:15], s29 offen lds\n\t"
  "v_mfma_f32_16x16x32_bf16 v[24:27], v[220:223], v[152:155], v[24:27]\n\t"
  "s_cbranch_vccnz .Lrare_aos\n\t"
  ".Lret_aos:\n\t"
  "v_exp_f32_e32 v160, v160\n\t"
  "v_mfma_f32_16x16x32_bf16 v[28:31], v[224:227], v[152:155], v[28:31]\n\t"
  "ds_read_b64_tr_b16 v[220:221], v234 offset:1536\n\t"
  "v_exp_f32_e32 v161, v161\n\t"
  "v_mfma_f32_16x16x32_bf16 v[20:23], v[224:227], v[144:147], v[20:23]\n\t"
  "ds_read_b64_tr_b16 v[222:223], v234 offset:9728\n\t"
  "v_add_f32_e32 v239, v239, v160\n\t"
  "v_mfma_f32_16x16x32_bf16 a[28:31], v[224:227], v[136:139], a[28:31]\n\t"
  "v_add_f32_e32 v239, v239, v161\n\t"
  "buffer_load_dwordx4 v236, s[12:15], s29 offen offset:1024 lds\n\t"
  "v_mfma_f32_16x16x32_bf16 a[24:27], v[224:227], v[128:131], a[24:27]\n\t"
  "v_exp_f32_e32 v162, v162\n\t"
  "s_waitcnt lgkmcnt(4)\n\t"
  "v_mfma_f32_16x16x32_bf16 a[32:35], v[208:211], v[128:131], a[32:35]\n\t"
  "ds_read_b64_tr_b16 v[224:225], v233 offset:2048\n\t"
  "v_cvt_pk_bf16_f32 v160, v160, v161\n\t"
  "v_mfma_f32_16x16x32_bf16 a[36:39], v[208:211], v[136:139], a[36:39]\n\t"
  "ds_read_b64_tr_b16 v[226:227], v233 offset:10240\n\t"
  "v_exp_f32_e32 v163, v163\n\t"
  "v_mfma_f32_16x16x32_bf16 v[32:35], v[208:211], v[144:147], v[32:35]\n\t"
  "v_add_f32_e32 v239, v239, v162\n\t"
  "buffer_load_dwordx4 v237, s[12:15], s29 offen offset:2048 lds\n\t"
  "v_mfma_f32_16x16x32_bf16 v[40:43], v[208:211], v[152:155], v[40:43]\n\t"
  "v_mfma_f32_16x16x32_bf16 v[44:47], v[212:215], v[152:155], v[44:47]\n\t"
  "ds_read_b64_tr_b16 v[228:229], v234 offset:2048\n\t"
  "v_add_f32_e32 v239, v239, v163\n\t"
  "v_mfma_f32_16x16x32_bf16 v[36:39], v[212:215], v[144:147], v[36:39]\n\t"
  "ds_read_b64_tr_b16 v[230:231], v234 offset:10240\n\t"
  "v_cvt_pk_bf16_f32 v161, v162, v163\n\t"
  "v_mfma_f32_16x16x32_bf16 a[44:47], v[212:215], v[136:139], a[44:47]\n\t"
  "buffer_load_dwordx4 v238, s[12:15], s29 offen offset:3072 lds\n\t"
  "v_mfma_f32_16x16x32_bf16 a[40:43], v[212:215], v[128:131], a[40:43]\n\t"
  "s_add_u32 s29, s29, 28672\n\t"
  "s_waitcnt lgkmcnt(4)\n\t"
  "v_mfma_f32_16x16x32_bf16 a[48:51], v[216:219], v[128:131], a[48:51]\n\t"
  "ds_read_b64_tr_b16 v[208:209], v233 offset:2560\n\t"
  "v_exp_f32_e32 v164, v164\n\t"
  "v_mfma_f32_16x16x32_bf16 a[52:55], v[216:219], v[136:139], a[52:55]\n\t"
  "ds_read_b64_tr_b16 v[210:211], v233 offset:10752\n\t"
  "s_add_u32 m0, s30, 32768\n\t"
  "v_mfma_f32_16x16x32_bf16 v[48:51], v[216:219], v[144:147], v[48:51]\n\t"
  "v_add_f32_e32 v239, v239, v164\n\t"
  "buffer_load_dwordx4 v235, s[8:11], s28 offen lds\n\t"
  "v_mfma_f32_16x16x32_bf16 v[56:59], v[216:219], v[152:155], v[56:59]\n\t"
  "v_exp_f32_e32 v165, v165\n\t"
  "v_mfma_f32_16x16x32_bf16 v[60:63], v[220:223], v[152:155], v[60:63]\n\t"
  "ds_read_b64_tr_b16 v[212:213], v234 offset:2560\n\t"
  "v_mfma_f32_16x16x32_bf16 v[52:55], v[220:223], v[144:147], v[52:55]\n\t"
  "ds_read_b64_tr_b16 v[214:215], v234 offset:10752\n\t"
  "v_add_f32_e32 v239, v239, v165\n\t"
  "v_mfma_f32_16x16x32_bf16 a[60:63], v[220:223], v[136:139], a[60:63]\n\t"
  "v_cvt_pk_bf16_f32 v162, v164, v165\n\t"
  "buffer_load_dwordx4 v236, s[8:11], s28 offen offset:1024 lds\n\t"
  "v_mfma_f32_16x16x32_bf16 a[56:59], v[220:223], v[128:131], a[56:59]\n\t"
  "s_waitcnt lgkmcnt(4)\n\t"
  "v_mfma_f32_16x16x32_bf16 a[64:67], v[224:227], v[128:131], a[64:67]\n\t"
  "ds_read_b64_tr_b16 v[216:217], v233 offset:3072\n\t"
  "v_exp_f32_e32 v166, v166\n\t"
  "v_mfma_f32_16x16x32_bf16 a[68:71], v[224:227], v[136:139], a[68:71]\n\t"
  "ds_read_b64_tr_b16 v[218:219], v233 offset:11264\n\t"
  "v_mfma_f32_16x16x32_bf16 v[64:67], v[224:227], v[144:147], v[64:67]\n\t"
  "v_add_f32_e32 v239, v239, v166\n\t"
  "buffer_load_dwordx4 v237, s[8:11], s28 offen offset:2048 lds\n\t"
  "v_mfma_f32_16x16x32_bf16 v[72:75], v[224:227], v[152:155], v[72:75]\n\t"
  "v_mfma_f32_16x16x32_bf16 v[76:79], v[228:231], v[152:155], v[76:79]\n\t"
  "ds_read_b64_tr_b16 v[220:221], v234 offset:3072\n\t"
  "v_exp_f32_e32 v167, v167\n\t"
  "v_mfma_f32_16x16x32_bf16 v[68:71], v[228:231], v[144:147], v[68:71]\n\t"
  "ds_read_b64_tr_b16 v[222:223], v234 offset:11264\n\t"
  "v_mfma_f32_16x16x32_bf16 a[76:79], v[228:231], v[136:139], a[76:79]\n\t"
  "v_add_f32_e32 v239, v239, v167\n\t"
  "buffer_load_dwordx4 v238, s[8:11], s28 offen offset:3072 lds\n\t"
  "v_mfma_f32_16x16x32_bf16 a[72:75], v[228:231], v[128:131], a[72:75]\n\t"
  "v_cvt_pk_bf16_f32 v163, v166, v167\n\t"
  "s_add_u32 s28, s28, 4096\n\t"
  "s_waitcnt lgkmcnt(4)\n\t"
  "v_mfma_f32_16x16x32_bf16 a[80:83], v[208:211], v[128:131], a[80:83]\n\t"
  "ds_read_b64_tr_b16 v[224:225], v233 offset:3584\n\t"
  "v_mfma_f32_16x16x32_bf16 a[84:87], v[208:211], v[136:139], a[84:87]\n\t"
  "ds_read_b64_tr_b16 v[226:227], v233 offset:11776\n\t"
  "s_add_u32 m0, s30, 36864\n\t"
  "v_mfma_f32_16x16x32_bf16 v[80:83], v[208:211], v[144:147], v[80:83]\n\t"
  "buffer_load_dwordx4 v235, s[8:11], s28 offen lds\n\t"
  "v_mfma_f32_16x16x32_bf16 v[88:91], v[208:211], v[152:155], v[88:91]\n\t"
  "v_exp_f32_e32 v168, v168\n\t"
  "v_mfma_f32_16x16x32_bf16 v[92:95], v[212:215], v[152:155], v[92:95]\n\t"
  "ds_read_b64_tr_b16 v[228:229], v234 offset:3584\n\t"
  "v_exp_f32_e32 v169, v169\n\t"
  "v_mfma_f32_16x16x32_bf16 v[84:87], v[212:215], v[144:147], v[84:87]\n\t"
  "ds_read_b64_tr_b16 v[230:231], v234 offset:11776\n\t"
  "v_add_f32_e32 v240, v240, v168\n\t"
  "v_mfma_f32_16x16x32_bf16 a[92:95], v[212:215], v[136:139], a[92:95]\n\t"
  "v_add_f32_e32 v240, v240, v169\n\t"
  "buffer_load_dwordx4 v236, s[8:11], s28 offen offset:1024 lds\n\t"
  "v_mfma_f32_16x16x32_bf16 a[88:91], v[212:215], v[128:131], a[88:91]\n\t"
  "v_cvt_pk_bf16_f32 v168, v168, v169\n\t"
  "s_waitcnt lgkmcnt(4)\n\t"
  "v_mfma_f32_16x16x32_bf16 a[96:99], v[216:219], v[128:131], a[96:99]\n\t"
  "ds_read_b128 v[208:211], v232\n\t"
  "v_mfma_f32_16x16x32_bf16 a[100:103], v[216:219], v[136:139], a[100:103]\n\t"
  "v_mfma_f32_16x16x32_bf16 v[96:99], v[216:219], v[144:147], v[96:99]\n\t"
  "buffer_load_dwordx4 v237, s[8:11], s28 offen offset:2048 lds\n\t"
  "v_mfma_f32_16x16x32_bf16 v[104:107], v[216:219], v[152:155], v[104:107]\n\t"
  "v_exp_f32_e32 v170, v170\n\t"
  "v_mfma_f32_16x16x32_bf16 v[108:111], v[220:223], v[152:155], v[108:111]\n\t"
  "ds_read_b128 v[212:215], v232 offset:512\n\t"
  "v_exp_f32_e32 v171, v171\n\t"
  "v_mfma_f32_16x16x32_bf16 v[100:103], v[220:223], v[144:147], v[100:103]\n\t"
  "v_add_f32_e32 v240, v240, v170\n\t"
  "v_mfma_f32_16x16x32_bf16 a[108:111], v[220:223], v[136:139], a[108:111]\n\t"
  "buffer_load_dwordx4 v238, s[8:11], s28 offen offset:3072 lds\n\t"
  "v_mfma_f32_16x16x32_bf16 a[104:107], v[220:223], v[128:131], a[104:107]\n\t"
  "v_add_f32_e32 v240, v240, v171\n\t"
  "s_add_u32 s28, s28, 28672\n\t"
  "s_waitcnt lgkmcnt(2)\n\t"
  "v_mfma_f32_16x16x32_bf16 a[112:115], v[224:227], v[128:131], a[112:115]\n\t"
  "ds_read_b128 v[216:219], v232 offset:1024\n\t"
  "v_cvt_pk_bf16_f32 v169, v170, v171\n\t"
  "v_mfma_f32_16x16x32_bf16 a[116:119], v[224:227], v[136:139], a[116:119]\n\t"
  "v_mfma_f32_16x16x32_bf16 v[112:115], v[224:227], v[144:147], v[112:115]\n\t"
  "v_exp_f32_e32 v172, v172\n\t"
  "v_mfma_f32_16x16x32_bf16 v[120:123], v[224:227], v[152:155], v[120:123]\n\t"
  "v_mfma_f32_16x16x32_bf16 v[124:127], v[228:231], v[152:155], v[124:127]\n\t"
  "ds_read_b128 v[220:223], v232 offset:1536\n\t"
  "v_add_f32_e32 v240, v240, v172\n\t"
  "v_mfma_f32_16x16x32_bf16 v[116:119], v[228:231], v[144:147], v[116:119]\n\t"
  "v_mfma_f32_16x16x32_bf16 a[124:127], v[228:231], v[136:139], a[124:127]\n\t"
  "v_exp_f32_e32 v173, v173\n\t"
  "v_mfma_f32_16x16x32_bf16 a[120:123], v[228:231], v[128:131], a[120:123]\n\t"
  "s_cmp_lg_u32 s34, 0\n\t"
  "s_cbranch_scc1 .Lrare_bos\n\t"
  ".Lret_bos:\n\t"
  "s_sub_u32 s31, s31, 1\n\t"
  "s_cmp_lg_u32 s31, 0\n\t"
  "s_cbranch_scc1 .Lloops\n\t"
  "s_waitcnt lgkmcnt(2)\n\t"
  "v_mfma_f32_16x16x32_bf16 v[128:131], v[208:211], a[128:131], v[192:195]\n\t"
  "ds_read_b128 v[224:227], v232 offset:2048\n\t"
  "v_exp_f32_e32 v174, v174\n\t"
  "v_mfma_f32_16x16x32_bf16 v[136:139], v[208:211], a[132:135], v[196:199]\n\t"
  "v_add_f32_e32 v240, v240, v173\n\t"
  "v_cvt_pk_bf16_f32 v170, v172, v173\n\t"
  "s_add_u32 s24, s98, 65536\n\t"
  "s_addc_u32 s25, s99, 0\n\t"
  "v_mfma_f32_16x16x32_bf16 v[144:147], v[208:211], a[136:139], v[200:203]\n\t"
  "v_exp_f32_e32 v175, v175\n\t"
  "v_add_f32_e32 v240, v240, v174\n\t"
  "v_mfma_f32_16x16x32_bf16 v[152:155], v[208:211], a[140:143], v[204:207]\n\t"
  "v_exp_f32_e32 v176, v176\n\t"
  "s_and_b32 s25, s25, 0xffff\n\t"
  "v_mfma_f32_16x16x32_bf16 v[152:155], v[212:215], a[156:159], v[152:155]\n\t"
  "ds_read_b128 v[208:211], v232 offset:2560\n\t"
  "v_add_f32_e32 v240, v240, v175\n\t"
  "v_cvt_pk_bf16_f32 v171, v174, v175\n\t"
  "v_mfma_f32_16x16x32_bf16 v[144:147], v[212:215], a[152:155], v[144:147]\n\t"
  "v_exp_f32_e32 v177, v177\n\t"
  "v_add_f32_e32 v241, v241, v176\n\t"
  "v_mfma_f32_16x16x32_bf16 v[136:139], v[212:215], a[148:151], v[136:139]\n\t"
  "v_exp_f32_e32 v178, v178\n\t"
  "s_mov_b32 s26, 131072\n\t"
  "v_mfma_f32_16x16x32_bf16 v[128:131], v[212:215], a[144:147], v[128:131]\n\t"
  "v_add_f32_e32 v241, v241, v177\n\t"
  "v_cvt_pk_bf16_f32 v176, v176, v177\n\t"
  "s_waitcnt lgkmcnt(2)\n\t"
  "v_mfma_f32_16x16x32_bf16 v[128:131], v[216:219], a[160:163], v[128:131]\n\t"
  "ds_read_b128 v[212:215], v232 offset:3072\n\t"
  "v_exp_f32_e32 v179, v179\n\t"
  "v_mfma_f32_16x16x32_bf16 v[136:139], v[216:219], a[164:167], v[136:139]\n\t"
  "v_add_f32_e32 v241, v241, v178\n\t"
  "v_exp_f32_e32 v180, v180\n\t"
  "v_mfma_f32_16x16x32_bf16 v[144:147], v[216:219], a[168:171], v[144:147]\n\t"
  "v_add_f32_e32 v241, v241, v179\n\t"
  "v_cvt_pk_bf16_f32 v177, v178, v179\n\t"
  "v_mfma_f32_16x16x32_bf16 v[152:155], v[216:219], a[172:175], v[152:155]\n\t"
  "v_exp_f32_e32 v181, v181\n\t"
  "v_add_f32_e32 v241, v241, v180\n\t"
  "v_mfma_f32_16x16x32_bf16 v[152:155], v[220:223], a[188:191], v[152:155]\n\t"
  "ds_read_b128 v[216:219], v232 offset:3584\n\t"
  "v_exp_f32_e32 v182, v182\n\t"
  "v_mfma_f32_16x16x32_bf16 v[144:147], v[220:223], a[184:187], v[144:147]\n\t"
  "v_add_f32_e32 v241, v241, v181\n\t"
  "v_cvt_pk_bf16_f32 v178, v180, v181\n\t"
  "v_mfma_f32_16x16x32_bf16 v[136:139], v[220:223], a[180:183], v[136:139]\n\t"
  "v_exp_f32_e32 v183, v183\n\t"
  "v_add_f32_e32 v241, v241, v182\n\t"
  "v_mfma_f32_16x16x32_bf16 v[128:131], v[220:223], a[176:179], v[128:131]\n\t"
  "v_exp_f32_e32 v184, v184\n\t"
  "s_waitcnt lgkmcnt(2)\n\t"
  "v_mfma_f32_16x16x32_bf16 v[128:131], v[224:227], a[192:195], v[128:131]\n\t"
  "ds_read_b128 v[220:223], v232 offset:8192\n\t"
  "v_add_f32_e32 v241, v241, v183\n\t"
  "v_cvt_pk_bf16_f32 v179, v182, v183\n\t"
  "v_mfma_f32_16x16x32_bf16 v[136:139], v[224:227], a[196:199], v[136:139]\n\t"
  "v_exp_f32_e32 v185, v185\n\t"
  "v_add_f32_e32 v242, v242, v184\n\t"
  "v_mfma_f32_16x16x32_bf16 v[144:147], v[224:227], a[200:203], v[144:147]\n\t"
  "v_exp_f32_e32 v186, v186\n\t"
  "v_mfma_f32_16x16x32_bf16 v[152:155], v[224:227], a[204:207], v[152:155]\n\t"
  "v_add_f32_e32 v242, v242, v185\n\t"
  "v_cvt_pk_bf16_f32 v184, v184, v185\n\t"
  "v_mfma_f32_16x16x32_bf16 v[152:155], v[208:211], a[220:223], v[152:155]\n\t"
  "ds_read_b128 v[224:227], v232 offset:8704\n\t"
  "v_exp_f32_e32 v187, v187\n\t"
  "v_mfma_f32_16x16x32_bf16 v[144:147], v[208:211], a[216:219], v[144:147]\n\t"
  "v_add_f32_e32 v242, v242, v186\n\t"
  "v_exp_f32_e32 v188, v188\n\t"
  "v_mfma_f32_16x16x32_bf16 v[136:139], v[208:211], a[212:215], v[136:139]\n\t"
  "v_add_f32_e32 v242, v242, v187\n\t"
  "v_cvt_pk_bf16_f32 v185, v186, v187\n\t"
  "v_mfma_f32_16x16x32_bf16 v[128:131], v[208:211], a[208:211], v[128:131]\n\t"
  "v_exp_f32_e32 v189, v189\n\t"
  "s_waitcnt lgkmcnt(2)\n\t"
  "v_mfma_f32_16x16x32_bf16 v[128:131], v[212:215], a[224:227], v[128:131]\n\t"
  "ds_read_b128 v[208:211], v232 offset:9216\n\t"
  "v_add_f32_e32 v242, v242, v188\n\t"
  "v_mfma_f32_16x16x32_bf16 v[136:139], v[212:215], a[228:231], v[136:139]\n\t"
  "v_exp_f32_e32 v190, v190\n\t"
  "v_add_f32_e32 v242, v242, v189\n\t"
  "v_mfma_f32_16x16x32_bf16 v[144:147], v[212:215], a[232:235], v[144:147]\n\t"
  "v_cvt_pk_bf16_f32 v186, v188, v189\n\t"
  "v_exp_f32_e32 v191, v191\n\t"
  "v_mfma_f32_16x16x32_bf16 v[152:155], v[212:215], a[236:239], v[152:155]\n\t"
  "v_add_f32_e32 v242, v242, v190\n\t"
  "v_mfma_f32_16x16x32_bf16 v[152:155], v[216:219], a[252:255], v[152:155]\n\t"
  "ds_read_b128 v[212:215], v232 offset:9728\n\t"
  "v_add_f32_e32 v242, v242, v191\n\t"
  "v_cvt_pk_bf16_f32 v187, v190, v191\n\t"
  "v_mfma_f32_16x16x32_bf16 v[144:147], v[216:219], a[248:251], v[144:147]\n\t"
  "v_mfma_f32_16x16x32_bf16 v[136:139], v[216:219], a[244:247], v[136:139]\n\t"
  "v_mfma_f32_16x16x32_bf16 v[128:131], v[216:219], a[240:243], v[128:131]\n\t"
  "s_waitcnt lgkmcnt(2)\n\t"
  "v_mfma_f32_16x16x32_bf16 v[132:135], v[220:223], a[128:131], v[192:195]\n\t"
  "ds_read_b128 v[216:219], v232 offset:10240\n\t"
  "v_mfma_f32_16x16x32_bf16 v[140:143], v[220:223], a[132:135], v[196:199]\n\t"
  "v_mfma_f32_16x16x32_bf16 v[148:151], v[220:223], a[136:139], v[200:203]\n\t"
  "v_mfma_f32_16x16x32_bf16 v[156:159], v[220:223], a[140:143], v[204:207]\n\t"
  "v_mfma_f32_16x16x32_bf16 v[156:159], v[224:227], a[156:159], v[156:159]\n\t"
  "ds_read_b128 v[220:223], v232 offset:10752\n\t"
  "v_max3_f32 v243, v128, v129, v130\n\t"
  "v_max3_f32 v243, v243, v131, v136\n\t"
  "v_mfma_f32_16x16x32_bf16 v[148:151], v[224:227], a[152:155], v[148:151]\n\t"
  "v_max3_f32 v243, v243, v137, v138\n\t"
  "v_max3_f32 v243, v243, v139, v144\n\t"
  "v_max3_f32 v243, v243, v145, v146\n\t"
  "v_mfma_f32_16x16x32_bf16 v[140:143], v[224:227], a[148:151], v[140:143]\n\t"
  "v_max3_f32 v243, v243, v147, v152\n\t"
  "v_max3_f32 v243, v243, v153, v154\n\t"
  "v_mfma_f32_16x16x32_bf16 v[132:135], v[224:227], a[144:147], v[132:135]\n\t"
  "s_waitcnt lgkmcnt(2)\n\t"
  "v_mfma_f32_16x16x32_bf16 v[132:135], v[208:211], a[160:163], v[132:135]\n\t"
  "ds_read_b128 v[224:227], v232 offset:11264\n\t"
  "v_mfma_f32_16x16x32_bf16 v[140:143], v[208:211], a[164:167], v[140:143]\n\t"
  "v_mfma_f32_16x16x32_bf16 v[148:151], v[208:211], a[168:171], v[148:151]\n\t"
  "v_mfma_f32_16x16x32_bf16 v[156:159], v[208:211], a[172:175], v[156:159]\n\t"
  "v_mfma_f32_16x16x32_bf16 v[156:159], v[212:215], a[188:191], v[156:159]\n\t"
  "ds_read_b128 v[208:211], v232 offset:11776\n\t"
  "v_mfma_f32_16x16x32_bf16 v[148:151], v[212:215], a[184:187], v[148:151]\n\t"
  "v_mfma_f32_16x16x32_bf16 v[140:143], v[212:215], a[180:183], v[140:143]\n\t"
  "v_mfma_f32_16x16x32_bf16 v[132:135], v[212:215], a[176:179], v[132:135]\n\t"
  "s_waitcnt lgkmcnt(2)\n\t"
  "v_mfma_f32_16x16x32_bf16 v[132:135], v[216:219], a[192:195], v[132:135]\n\t"
  "v_mfma_f32_16x16x32_bf16 v[140:143], v[216:219], a[196:199], v[140:143]\n\t"
  "v_mfma_f32_16x16x32_bf16 v[148:151], v[216:219], a[200:203], v[148:151]\n\t"
  "v_mfma_f32_16x16x32_bf16 v[156:159], v[216:219], a[204:207], v[156:159]\n\t"
  "v_mfma_f32_16x16x32_bf16 v[156:159], v[220:223], a[220:223], v[156:159]\n\t"
  "v_mfma_f32_16x16x32_bf16 v[148:151], v[220:223], a[216:219], v[148:151]\n\t"
  "v_mfma_f32_16x16x32_bf16 v[140:143], v[220:223], a[212:215], v[140:143]\n\t"
  "s_waitcnt vmcnt(0) lgkmcnt(0)\n\t"
  "s_barrier\n\t"
  "v_mfma_f32_16x16x32_bf16 v[132:135], v[220:223], a[208:211], v[132:135]\n\t"
  "ds_read_b64_tr_b16 v[212:213], v233 offset:32768\n\t"
  "v_mfma_f32_16x16x32_bf16 v[132:135], v[224:227], a[224:227], v[132:135]\n\t"
  "ds_read_b64_tr_b16 v[214:215], v233 offset:40960\n\t"
  "v_mfma_f32_16x16x32_bf16 v[140:143], v[224:227], a[228:231], v[140:143]\n\t"
  "ds_read_b64_tr_b16 v[216:217], v234 offset:32768\n\t"
  "s_add_u32 m0, s30, 65536\n\t"
  "v_mfma_f32_16x16x32_bf16 v[148:151], v[224:227], a[232:235], v[148:151]\n\t"
  "ds_read_b64_tr_b16 v[218:219], v234 offset:40960\n\t"
  "buffer_load_dwordx4 v235, s[12:15], s29 offen lds\n\t"
  "v_mfma_f32_16x16x32_bf16 v[156:159], v[224:227], a[236:239], v[156:159]\n\t"
  "ds_read_b64_tr_b16 v[220:221], v233 offset:33280\n\t"
  "v_mfma_f32_16x16x32_bf16 v[156:159], v[208:211], a[252:255], v[156:159]\n\t"
  "ds_read_b64_tr_b16 v[222:223], v233 offset:41472\n\t"
  "v_mfma_f32_16x16x32_bf16 v[148:151], v[208:211], a[248:251], v[148:151]\n\t"
  "ds_read_b64_tr_b16 v[224:225], v234 offset:33280\n\t"
  "v_mfma_f32_16x16x32_bf16 v[140:143], v[208:211], a[244:247], v[140:143]\n\t"
  "ds_read_b64_tr_b16 v[226:227], v234 offset:41472\n\t"
  "buffer_load_dwordx4 v236, s[12:15], s29 offen offset:1024 lds\n\t"
  "v_mfma_f32_16x16x32_bf16 v[132:135], v[208:211], a[240:243], v[132:135]\n\t"
  "s_waitcnt lgkmcnt(4)\n\t"
  "v_mfma_f32_16x16x32_bf16 a[0:3], v[212:215], v[160:163], a[0:3]\n\t"
  "ds_read_b64_tr_b16 v[208:209], v233 offset:33792\n\t"
  "v_mfma_f32_16x16x32_bf16 a[4:7], v[212:215], v[168:171], a[4:7]\n\t"
  "ds_read_b64_tr_b16 v[210:211], v233 offset:41984\n\t"
  "v_mfma_f32_16x16x32_bf16 v[0:3], v[212:215], v[176:179], v[0:3]\n\t"
  "buffer_load_dwordx4 v237, s[12:15], s29 offen offset:2048 lds\n\t"
  "v_mfma_f32_16x16x32_bf16 v[8:11], v[212:215], v[184:187], v[8:11]\n\t"
  "v_mfma_f32_16x16x32_bf16 v[12:15], v[216:219], v[184:187], v[12:15]\n\t"
  "ds_read_b64_tr_b16 v[212:213], v234 offset:33792\n\t"
  "v_max3_f32 v243, v243, v155, v132\n\t"
  "v_max3_f32 v243, v243, v133, v134\n\t"
  "v_mfma_f32_16x16x32_bf16 v[4:7], v[216:219], v[176:179], v[4:7]\n\t"
  "ds_read_b64_tr_b16 v[214:215], v234 offset:41984\n\t"
  "v_max3_f32 v243, v243, v135, v140\n\t"
  "v_max3_f32 v243, v243, v141, v142\n\t"
  "v_mfma_f32_16x16x32_bf16 a[12:15], v[216:219], v[168:171], a[12:15]\n\t"
  "v_max3_f32 v243, v243, v143, v148\n\t"
  "buffer_load_dwordx4 v238, s[12:15], s29 offen offset:3072 lds\n\t"
  "v_mfma_f32_16x16x32_bf16 a[8:11], v[216:219], v[160:163], a[8:11]\n\t"
  "v_max3_f32 v243, v243, v149, v150\n\t"
  "s_add_u32 s29, s29, 4096\n\t"
  "s_waitcnt lgkmcnt(4)\n\t"
  "v_mfma_f32_16x16x32_bf16 a[16:19], v[220:223], v[160:163], a[16:19]\n\t"
  "ds_read_b64_tr_b16 v[216:217], v233 offset:34304\n\t"
  "v_max3_f32 v243, v243, v151, v156\n\t"
  "v_max3_f32 v243, v243, v157, v158\n\t"
  "v_mfma_f32_16x16x32_bf16 a[20:23], v[220:223], v[168:171], a[20:23]\n\t"
  "ds_read_b64_tr_b16 v[218:219], v233 offset:42496\n\t"
  "v_max_f32_e32 v243, v243, v159\n\t"
  "s_add_u32 m0, s30, 69632\n\t"
  "v_mfma_f32_16x16x32_bf16 v[16:19], v[220:223], v[176:179], v[16:19]\n\t"
  "v_cmp_lt_f32_e32 vcc, 0x41000000, v243\n\t"
  "buffer_load_dwordx4 v235, s[12:15], s29 offen lds\n\t"
  "v_mfma_f32_16x16x32_bf16 v[24:27], v[220:223], v[184:187], v[24:27]\n\t"
  "s_cbranch_vccnz .Lrare_ays\n\t"
  ".Lret_ays:\n\t"
  "v_exp_f32_e32 v128, v128\n\t"
  "v_mfma_f32_16x16x32_bf16 v[28:31], v[224:227], v[184:187], v[28:31]\n\t"
  "ds_read_b64_tr_b16 v[220:221], v234 offset:34304\n\t"
  "v_exp_f32_e32 v129, v129\n\t"
  "v_mfma_f32_16x16x32_bf16 v[20:23], v[224:227], v[176:179], v[20:23]\n\t"
  "ds_read_b64_tr_b16 v[222:223], v234 offset:42496\n\t"
  "v_add_f32_e32 v239, v239, v128\n\t"
  "v_mfma_f32_16x16x32_bf16 a[28:31], v[224:227], v[168:171], a[28:31]\n\t"
  "v_add_f32_e32 v239, v239, v129\n\t"
  "buffer_load_dwordx4 v236, s[12:15], s29 offen offset:1024 lds\n\t"
  "v_mfma_f32_16x16x32_bf16 a[24:27], v[224:227], v[160:163], a[24:27]\n\t"
  "v_exp_f32_e32 v130, v130\n\t"
  "s_waitcnt lgkmcnt(4)\n\t"
  "v_mfma_f32_16x16x32_bf16 a[32:35], v[208:211], v[160:163], a[32:35]\n\t"
  "ds_read_b64_tr_b16 v[224:225], v233 offset:34816\n\t"
  "v_cvt_pk_bf16_f32 v128, v128, v129\n\t"
  "v_mfma_f32_16x16x32_bf16 a[36:39], v[208:211], v[168:171], a[36:39]\n\t"
  "ds_read_b64_tr_b16 v[226:227], v233 offset:43008\n\t"
  "v_exp_f32_e32 v131, v131\n\t"
  "v_mfma_f32_16x16x32_bf16 v[32:35], v[208:211], v[176:179], v[32:35]\n\t"
  "v_add_f32_e32 v239, v239, v130\n\t"
  "buffer_load_dwordx4 v237, s[12:15], s29 offen offset:2048 lds\n\t"
  "v_mfma_f32_16x16x32_bf16 v[40:43], v[208:211], v[184:187], v[40:43]\n\t"
  "v_mfma_f32_16x16x32_bf16 v[44:47], v[212:215], v[184:187], v[44:47]\n\t"
  "ds_read_b64_tr_b16 v[228:229], v234 offset:34816\n\t"
  "v_add_f32_e32 v239, v239, v131\n\t"
  "v_mfma_f32_16x16x32_bf16 v[36:39], v[212:215], v[176:179], v[36:39]\n\t"
  "ds_read_b64_tr_b16 v[230:231], v234 offset:43008\n\t"
  "v_cvt_pk_bf16_f32 v129, v130, v131\n\t"
  "v_mfma_f32_16x16x32_bf16 a[44:47], v[212:215], v[168:171], a[44:47]\n\t"
  "buffer_load_dwordx4 v238, s[12:15], s29 offen offset:3072 lds\n\t"
  "v_mfma_f32_16x16x32_bf16 a[40:43], v[212:215], v[160:163], a[40:43]\n\t"
  "s_add_u32 s29, s29, 28672\n\t"
  "s_waitcnt lgkmcnt(4)\n\t"
  "v_mfma_f32_16x16x32_bf16 a[48:51], v[216:219], v[160:163], a[48:51]\n\t"
  "ds_read_b64_tr_b16 v[208:209], v233 offset:35328\n\t"
  "v_exp_f32_e32 v132, v132\n\t"
  "v_mfma_f32_16x16x32_bf16 a[52:55], v[216:219], v[168:171], a[52:55]\n\t"
  "ds_read_b64_tr_b16 v[210:211], v233 offset:43520\n\t"
  "s_add_u32 m0, s30, 0\n\t"
  "v_mfma_f32_16x16x32_bf16 v[48:51], v[216:219], v[176:179], v[48:51]\n\t"
  "v_add_f32_e32 v239, v239, v132\n\t"
  "buffer_load_dwordx4 v235, s[12:15], s29 offen lds\n\t"
  "v_mfma_f32_16x16x32_bf16 v[56:59], v[216:219], v[184:187], v[56:59]\n\t"
  "v_exp_f32_e32 v133, v133\n\t"
  "v_mfma_f32_16x16x32_bf16 v[60:63], v[220:223], v[184:187], v[60:63]\n\t"
  "ds_read_b64_tr_b16 v[212:213], v234 offset:35328\n\t"
  "v_mfma_f32_16x16x32_bf16 v[52:55], v[220:223], v[176:179], v[52:55]\n\t"
  "ds_read_b64_tr_b16 v[214:215], v234 offset:43520\n\t"
  "v_add_f32_e32 v239, v239, v133\n\t"
  "v_mfma_f32_16x16x32_bf16 a[60:63], v[220:223], v[168:171], a[60:63]\n\t"
  "v_cvt_pk_bf16_f32 v130, v132, v133\n\t"
  "buffer_load_dwordx4 v236, s[12:15], s29 offen offset:1024 lds\n\t"
  "v_mfma_f32_16x16x32_bf16 a[56:59], v[220:223], v[160:163], a[56:59]\n\t"
  "s_waitcnt lgkmcnt(4)\n\t"
  "v_mfma_f32_16x16x32_bf16 a[64:67], v[224:227], v[160:163], a[64:67]\n\t"
  "ds_read_b64_tr_b16 v[216:217], v233 offset:35840\n\t"
  "v_exp_f32_e32 v134, v134\n\t"
  "v_mfma_f32_16x16x32_bf16 a[68:71], v[224:227], v[168:171], a[68:71]\n\t"
  "ds_read_b64_tr_b16 v[218:219], v233 offset:44032\n\t"
  "v_mfma_f32_16x16x32_bf16 v[64:67], v[224:227], v[176:179], v[64:67]\n\t"
  "v_add_f32_e32 v239, v239, v134\n\t"
  "buffer_load_dwordx4 v237, s[12:15], s29 offen offset:2048 lds\n\t"
  "v_mfma_f32_16x16x32_bf16 v[72:75], v[224:227], v[184:187], v[72:75]\n\t"
  "v_mfma_f32_16x16x32_bf16 v[76:79], v[228:231], v[184:187], v[76:79]\n\t"
  "ds_read_b64_tr_b16 v[220:221], v234 offset:35840\n\t"
  "v_exp_f32_e32 v135, v135\n\t"
  "v_mfma_f32_16x16x32_bf16 v[68:71], v[228:231], v[176:179], v[68:71]\n\t"
  "ds_read_b64_tr_b16 v[222:223], v234 offset:44032\n\t"
  "v_mfma_f32_16x16x32_bf16 a[76:79], v[228:231], v[168:171], a[76:79]\n\t"
  "v_add_f32_e32 v239, v239, v135\n\t"
  "buffer_load_dwordx4 v238, s[12:15], s29 offen offset:3072 lds\n\t"
  "v_mfma_f32_16x16x32_bf16 a[72:75], v[228:231], v[160:163], a[72:75]\n\t"
  "v_cvt_pk_bf16_f32 v131, v134, v135\n\t"
  "s_add_u32 s29, s29, 4096\n\t"
  "s_waitcnt lgkmcnt(4)\n\t"
  "v_mfma_f32_16x16x32_bf16 a[80:83], v[208:211], v[160:163], a[80:83]\n\t"
  "ds_read_b64_tr_b16 v[224:225], v233 offset:36352\n\t"
  "v_mfma_f32_16x16x32_bf16 a[84:87], v[208:211], v[168:171], a[84:87]\n\t"
  "ds_read_b64_tr_b16 v[226:227], v233 offset:44544\n\t"
  "s_add_u32 m0, s30, 4096\n\t"
  "v_mfma_f32_16x16x32_bf16 v[80:83], v[208:211], v[176:179], v[80:83]\n\t"
  "buffer_load_dwordx4 v235, s[12:15], s29 offen lds\n\t"
  "v_mfma_f32_16x16x32_bf16 v[88:91], v[208:211], v[184:187], v[88:91]\n\t"
  "v_exp_f32_e32 v136, v136\n\t"
  "v_mfma_f32_16x16x32_bf16 v[92:95], v[212:215], v[184:187], v[92:95]\n\t"
  "ds_read_b64_tr_b16 v[228:229], v234 offset:36352\n\t"
  "v_exp_f32_e32 v137, v137\n\t"
  "v_mfma_f32_16x16x32_bf16 v[84:87], v[212:215], v[176:179], v[84:87]\n\t"
  "ds_read_b64_tr_b16 v[230:231], v234 offset:44544\n\t"
  "v_add_f32_e32 v240, v240, v136\n\t"
  "v_mfma_f32_16x16x32_bf16 a[92:95], v[212:215], v[168:171], a[92:95]\n\t"
  "v_add_f32_e32 v240, v240, v137\n\t"
  "buffer_load_dwordx4 v236, s[12:15], s29 offen offset:1024 lds\n\t"
  "v_mfma_f32_16x16x32_bf16 a[88:91], v[212:215], v[160:163], a[88:91]\n\t"
  "v_cvt_pk_bf16_f32 v136, v136, v137\n\t"
  "s_waitcnt lgkmcnt(4)\n\t"
  "v_mfma_f32_16x16x32_bf16 a[96:99], v[216:219], v[160:163], a[96:99]\n\t"
  "ds_read_b128 v[208:211], v232 offset:32768\n\t"
  "v_mfma_f32_16x16x32_bf16 a[100:103], v[216:219], v[168:171], a[100:103]\n\t"
  "v_mfma_f32_16x16x32_bf16 v[96:99], v[216:219], v[176:179], v[96:99]\n\t"
  "buffer_load_dwordx4 v237, s[12:15], s29 offen offset:2048 lds\n\t"
  "v_mfma_f32_16x16x32_bf16 v[104:107], v[216:219], v[184:187], v[104:107]\n\t"
  "v_exp_f32_e32 v138, v138\n\t"
  "v_mfma_f32_16x16x32_bf16 v[108:111], v[220:223], v[184:187], v[108:111]\n\t"
  "ds_read_b128 v[212:215], v232 offset:33280\n\t"
  "v_exp_f32_e32 v139, v139\n\t"
  "v_mfma_f32_16x16x32_bf16 v[100:103], v[220:223], v[176:179], v[100:103]\n\t"
  "v_add_f32_e32 v240, v240, v138\n\t"
  "v_mfma_f32_16x16x32_bf16 a[108:111], v[220:223], v[168:171], a[108:111]\n\t"
  "buffer_load_dwordx4 v238, s[12:15], s29 offen offset:3072 lds\n\t"
  "v_mfma_f32_16x16x32_bf16 a[104:107], v[220:223], v[160:163], a[104:107]\n\t"
  "v_add_f32_e32 v240, v240, v139\n\t"
  "s_add_u32 s29, s29, 28672\n\t"
  "s_waitcnt lgkmcnt(2)\n\t"
  "v_mfma_f32_16x16x32_bf16 a[112:115], v[224:227], v[160:163], a[112:115]\n\t"
  "ds_read_b128 v[216:219], v232 offset:33792\n\t"
  "v_cvt_pk_bf16_f32 v137, v138, v139\n\t"
  "v_mfma_f32_16x16x32_bf16 a[116:119], v[224:227], v[168:171], a[116:119]\n\t"
  "v_mfma_f32_16x16x32_bf16 v[112:115], v[224:227], v[176:179], v[112:115]\n\t"
  "v_exp_f32_e32 v140, v140\n\t"
  "v_mfma_f32_16x16x32_bf16 v[120:123], v[224:227], v[184:187], v[120:123]\n\t"
  "v_mfma_f32_16x16x32_bf16 v[124:127], v[228:231], v[184:187], v[124:127]\n\t"
  "ds_read_b128 v[220:223], v232 offset:34304\n\t"
  "v_add_f32_e32 v240, v240, v140\n\t"
  "v_mfma_f32_16x16x32_bf16 v[116:119], v[228:231], v[176:179], v[116:119]\n\t"
  "v_mfma_f32_16x16x32_bf16 a[124:127], v[228:231], v[168:171], a[124:127]\n\t"
  "v_exp_f32_e32 v141, v141\n\t"
  "v_mfma_f32_16x16x32_bf16 a[120:123], v[228:231], v[160:163], a[120:123]\n\t"
  "s_cmp_lg_u32 s34, 0\n\t"
  "s_cbranch_scc1 .Lrare_bys\n\t"
  ".Lret_bys:\n\t"
  "s_waitcnt lgkmcnt(2)\n\t"
  "v_mfma_f32_16x16x32_bf16 v[160:163], v[208:211], a[128:131], v[192:195]\n\t"
  "ds_read_b128 v[224:227], v232 offset:34816\n\t"
  "s_mul_i32 s64, s60, 229376\n\t"
  "s_add_u32 s16, s48, s64\n\t"
  "s_addc_u32 s17, s49, 0\n\t"
  "v_mfma_f32_16x16x32_bf16 v[168:171], v[208:211], a[132:135], v[196:199]\n\t"
  "v_exp_f32_e32 v142, v142\n\t"
  "s_and_b32 s17, s17, 0xffff\n\t"
  "v_mfma_f32_16x16x32_bf16 v[176:179], v[208:211], a[136:139], v[200:203]\n\t"
  "v_add_f32_e32 v240, v240, v141\n\t"
  "v_cvt_pk_bf16_f32 v138, v140, v141\n\t"
  "s_mov_b32 s18, 229376\n\t"
  "v_mfma_f32_16x16x32_bf16 v[184:187], v[208:211], a[140:143], v[204:207]\n\t"
  "v_exp_f32_e32 v143, v143\n\t"
  "s_mul_i32 s36, s62, 57344\n\t"
  "v_mfma_f32_16x16x32_bf16 v[184:187], v[212:215], a[156:159], v[184:187]\n\t"
  "ds_read_b128 v[208:211], v232 offset:35328\n\t"
  "v_add_f32_e32 v240, v240, v142\n\t"
  "v_bfe_u32 v246, v254, 2, 3\n\t"
  "v_mfma_f32_16x16x32_bf16 v[176:179], v[212:215], a[152:155], v[176:179]\n\t"
  "v_exp_f32_e32 v144, v144\n\t"
  "v_mul_u32_u24_e32 v246, 384, v246\n\t"
  "v_mfma_f32_16x16x32_bf16 v[168:171], v[212:215], a[148:151], v[168:171]\n\t"
  "v_add_f32_e32 v240, v240, v143\n\t"
  "v_cvt_pk_bf16_f32 v139, v142, v143\n\t"
  "v_add_u32_e32 v246, v246, v235\n\t"
  "v_mfma_f32_16x16x32_bf16 v[160:163], v[212:215], a[144:147], v[160:163]\n\t"
  "v_add_f32_e32 v241, v241, v144\n\t"
  "v_subrev_u32_e32 v246, 3072, v246\n\t"
  "s_waitcnt lgkmcnt(2)\n\t"
  "v_mfma_f32_16x16x32_bf16 v[160:163], v[216:219], a[160:163], v[160:163]\n\t"
  "ds_read_b128 v[212:215], v232 offset:35840\n\t"
  "v_add_u32_e32 v235, 0, v246\n\t"
  "v_mfma_f32_16x16x32_bf16 v[168:171], v[216:219], a[164:167], v[168:171]\n\t"
  "v_exp_f32_e32 v145, v145\n\t"
  "v_add_u32_e32 v236, 6144, v246\n\t"
  "v_mfma_f32_16x16x32_bf16 v[176:179], v[216:219], a[168:171], v[176:179]\n\t"
  "v_exp_f32_e32 v146, v146\n\t"
  "v_add_u32_e32 v237, 12288, v246\n\t"
  "v_mfma_f32_16x16x32_bf16 v[184:187], v[216:219], a[172:175], v[184:187]\n\t"
  "v_add_f32_e32 v241, v241, v145\n\t"
  "v_cvt_pk_bf16_f32 v144, v144, v145\n\t"
  "v_add_u32_e32 v238, 18432, v246\n\t"
  "v_mfma_f32_16x16x32_bf16 v[184:187], v[220:223], a[188:191], v[184:187]\n\t"
  "ds_read_b128 v[216:219], v232 offset:36352\n\t"
  "v_exp_f32_e32 v147, v147\n\t"
  "v_mfma_f32_16x16x32_bf16 v[176:179], v[220:223], a[184:187], v[176:179]\n\t"
  "v_add_f32_e32 v241, v241, v146\n\t"
  "v_exp_f32_e32 v148, v148\n\t"
  "v_mfma_f32_16x16x32_bf16 v[168:171], v[220:223], a[180:183], v[168:171]\n\t"
  "v_add_f32_e32 v241, v241, v147\n\t"
  "v_cvt_pk_bf16_f32 v145, v146, v147\n\t"
  "v_mfma_f32_16x16x32_bf16 v[160:163], v[220:223], a[176:179], v[160:163]\n\t"
  "v_exp_f32_e32 v149, v149\n\t"
  "s_waitcnt lgkmcnt(2)\n\t"
  "v_mfma_f32_16x16x32_bf16 v[160:163], v[224:227], a[192:195], v[160:163]\n\t"
  "ds_read_b128 v[220:223], v232 offset:40960\n\t"
  "v_add_f32_e32 v241, v241, v148\n\t"
  "v_mfma_f32_16x16x32_bf16 v[168:171], v[224:227], a[196:199], v[168:171]\n\t"
  "v_exp_f32_e32 v150, v150\n\t"
  "v_add_f32_e32 v241, v241, v149\n\t"
  "v_mfma_f32_16x16x32_bf16 v[176:179], v[224:227], a[200:203], v[176:179]\n\t"
  "v_cvt_pk_bf16_f32 v146, v148, v149\n\t"
  "v_exp_f32_e32 v151, v151\n\t"
  "v_mfma_f32_16x16x32_bf16 v[184:187], v[224:227], a[204:207], v[184:187]\n\t"
  "v_add_f32_e32 v241, v241, v150\n\t"
  "v_exp_f32_e32 v152, v152\n\t"
  "v_mfma_f32_16x16x32_bf16 v[184:187], v[208:211], a[220:223], v[184:187]\n\t"
  "ds_read_b128 v[224:227], v232 offset:41472\n\t"
  "v_add_f32_e32 v241, v241, v151\n\t"
  "v_cvt_pk_bf16_f32 v147, v150, v151\n\t"
  "v_mfma_f32_16x16x32_bf16 v[176:179], v[208:211], a[216:219], v[176:179]\n\t"
  "v_exp_f32_e32 v153, v153\n\t"
  "v_add_f32_e32 v242, v242, v152\n\t"
  "v_mfma_f32_16x16x32_bf16 v[168:171], v[208:211], a[212:215], v[168:171]\n\t"
  "v_exp_f32_e32 v154, v154\n\t"
  "v_mfma_f32_16x16x32_bf16 v[160:163], v[208:211], a[208:211], v[160:163]\n\t"
  "v_add_f32_e32 v242, v242, v153\n\t"
  "v_cvt_pk_bf16_f32 v152, v152, v153\n\t"
  "s_waitcnt lgkmcnt(2)\n\t"
  "v_mfma_f32_16x16x32_bf16 v[160:163], v[212:215], a[224:227], v[160:163]\n\t"
  "ds_read_b128 v[208:211], v232 offset:41984\n\t"
  "v_exp_f32_e32 v155, v155\n\t"
  "v_mfma_f32_16x16x32_bf16 v[168:171], v[212:215], a[228:231], v[168:171]\n\t"
  "v_add_f32_e32 v242, v242, v154\n\t"
  "v_exp_f32_e32 v156, v156\n\t"
  "v_mfma_f32_16x16x32_bf16 v[176:179], v[212:215], a[232:235], v[176:179]\n\t"
  "v_add_f32_e32 v242, v242, v155\n\t"
  "v_cvt_pk_bf16_f32 v153, v154, v155\n\t"
  "v_mfma_f32_16x16x32_bf16 v[184:187], v[212:215], a[236:239], v[184:187]\n\t"
  "v_exp_f32_e32 v157, v157\n\t"
  "v_add_f32_e32 v242, v242, v156\n\t"
  "v_mfma_f32_16x16x32_bf16 v[184:187], v[216:219], a[252:255], v[184:187]\n\t"
  "ds_read_b128 v[212:215], v232 offset:42496\n\t"
  "v_exp_f32_e32 v158, v158\n\t"
  "v_mfma_f32_16x16x32_bf16 v[176:179], v[216:219], a[248:251], v[176:179]\n\t"
  "v_add_f32_e32 v242, v242, v157\n\t"
  "v_cvt_pk_bf16_f32 v154, v156, v157\n\t"
  "v_mfma_f32_16x16x32_bf16 v[168:171], v[216:219], a[244:247], v[168:171]\n\t"
  "v_exp_f32_e32 v159, v159\n\t"
  "v_add_f32_e32 v242, v242, v158\n\t"
  "v_mfma_f32_16x16x32_bf16 v[160:163], v[216:219], a[240:243], v[160:163]\n\t"
  "s_waitcnt lgkmcnt(2)\n\t"
  "v_mfma_f32_16x16x32_bf16 v[164:167], v[220:223], a[128:131], v[192:195]\n\t"
  "ds_read_b128 v[216:219], v232 offset:43008\n\t"
  "v_add_f32_e32 v242, v242, v159\n\t"
  "v_cvt_pk_bf16_f32 v155, v158, v159\n\t"
  "v_mfma_f32_16x16x32_bf16 v[172:175], v[220:223], a[132:135], v[196:199]\n\t"
  "v_mfma_f32_16x16x32_bf16 v[180:183], v[220:223], a[136:139], v[200:203]\n\t"
  "v_mfma_f32_16x16x32_bf16 v[188:191], v[220:223], a[140:143], v[204:207]\n\t"
  "v_mfma_f32_16x16x32_bf16 v[188:191], v[224:227], a[156:159], v[188:191]\n\t"
  "ds_read_b128 v[220:223], v232 offset:43520\n\t"
  "v_max3_f32 v243, v160, v161, v162\n\t"
  "v_max3_f32 v243, v243, v163, v168\n\t"
  "v_mfma_f32_16x16x32_bf16 v[180:183], v[224:227], a[152:155], v[180:183]\n\t"
  "v_max3_f32 v243, v243, v169, v170\n\t"
  "v_max3_f32 v243, v243, v171, v176\n\t"
  "v_max3_f32 v243, v243, v177, v178\n\t"
  "v_mfma_f32_16x16x32_bf16 v[172:175], v[224:227], a[148:151], v[172:175]\n\t"
  "v_max3_f32 v243, v243, v179, v184\n\t"
  "v_max3_f32 v243, v243, v185, v186\n\t"
  "v_mfma_f32_16x16x32_bf16 v[164:167], v[224:227], a[144:147], v[164:167]\n\t"
  "s_waitcnt lgkmcnt(2)\n\t"
  "v_mfma_f32_16x16x32_bf16 v[164:167], v[208:211], a[160:163], v[164:167]\n\t"
  "ds_read_b128 v[224:227], v232 offset:44032\n\t"
  "v_mfma_f32_16x16x32_bf16 v[172:175], v[208:211], a[164:167], v[172:175]\n\t"
  "v_mfma_f32_16x16x32_bf16 v[180:183], v[208:211], a[168:171], v[180:183]\n\t"
  "v_mfma_f32_16x16x32_bf16 v[188:191], v[208:211], a[172:175], v[188:191]\n\t"
  "v_mfma_f32_16x16x32_bf16 v[188:191], v[212:215], a[188:191], v[188:191]\n\t"
  "ds_read_b128 v[208:211], v232 offset:44544\n\t"
  "v_mfma_f32_16x16x32_bf16 v[180:183], v[212:215], a[184:187], v[180:183]\n\t"
  "v_mfma_f32_16x16x32_bf16 v[172:175], v[212:215], a[180:183], v[172:175]\n\t"
  "v_mfma_f32_16x16x32_bf16 v[164:167], v[212:215], a[176:179], v[164:167]\n\t"
  "s_waitcnt lgkmcnt(2)\n\t"
  "v_mfma_f32_16x16x32_bf16 v[164:167], v[216:219], a[192:195], v[164:167]\n\t"
  "v_mfma_f32_16x16x32_bf16 v[172:175], v[216:219], a[196:199], v[172:175]\n\t"
  "v_mfma_f32_16x16x32_bf16 v[180:183], v[216:219], a[200:203], v[180:183]\n\t"
  "v_mfma_f32_16x16x32_bf16 v[188:191], v[216:219], a[204:207], v[188:191]\n\t"
  "v_mfma_f32_16x16x32_bf16 v[188:191], v[220:223], a[220:223], v[188:191]\n\t"
  "v_mfma_f32_16x16x32_bf16 v[180:183], v[220:223], a[216:219], v[180:183]\n\t"
  "v_mfma_f32_16x16x32_bf16 v[172:175], v[220:223], a[212:215], v[172:175]\n\t"
  "s_waitcnt vmcnt(0) lgkmcnt(0)\n\t"
  "s_barrier\n\t"
  "v_mfma_f32_16x16x32_bf16 v[164:167], v[220:223], a[208:211], v[164:167]\n\t"
  "ds_read_b64_tr_b16 v[212:213], v233\n\t"
  "v_mfma_f32_16x16x32_bf16 v[164:167], v[224:227], a[224:227], v[164:167]\n\t"
  "ds_read_b64_tr_b16 v[214:215], v233 offset:8192\n\t"
  "v_mfma_f32_16x16x32_bf16 v[172:175], v[224:227], a[228:231], v[172:175]\n\t"
  "ds_read_b64_tr_b16 v[216:217], v234\n\t"
  "s_add_u32 m0, s30, 131072\n\t"
  "v_mfma_f32_16x16x32_bf16 v[180:183], v[224:227], a[232:235], v[180:183]\n\t"
  "ds_read_b64_tr_b16 v[218:219], v234 offset:8192\n\t"
  "buffer_load_dwordx4 v235, s[16:19], s36 offen lds\n\t"
  "v_mfma_f32_16x16x32_bf16 v[188:191], v[224:227], a[236:239], v[188:191]\n\t"
  "ds_read_b64_tr_b16 v[220:221], v233 offset:512\n\t"
  "v_mfma_f32_16x16x32_bf16 v[188:191], v[208:211], a[252:255], v[188:191]\n\t"
  "ds_read_b64_tr_b16 v[222:223], v233 offset:8704\n\t"
  "v_mfma_f32_16x16x32_bf16 v[180:183], v[208:211], a[248:251], v[180:183]\n\t"
  "ds_read_b64_tr_b16 v[224:225], v234 offset:512\n\t"
  "v_mfma_f32_16x16x32_bf16 v[172:175], v[208:211], a[244:247], v[172:175]\n\t"
  "ds_read_b64_tr_b16 v[226:227], v234 offset:8704\n\t"
  "buffer_load_dwordx4 v236, s[16:19], s36 offen offset:1024 lds\n\t"
  "v_mfma_f32_16x16x32_bf16 v[164:167], v[208:211], a[240:243], v[164:167]\n\t"
  "s_waitcnt lgkmcnt(4)\n\t"
  "v_mfma_f32_16x16x32_bf16 a[0:3], v[212:215], v[128:131], a[0:3]\n\t"
  "ds_read_b64_tr_b16 v[208:209], v233 offset:1024\n\t"
  "v_mfma_f32_16x16x32_bf16 a[4:7], v[212:215], v[136:139], a[4:7]\n\t"
  "ds_read_b64_tr_b16 v[210:211], v233 offset:9216\n\t"
  "v_mfma_f32_16x16x32_bf16 v[0:3], v[212:215], v[144:147], v[0:3]\n\t"
  "buffer_load_dwordx4 v237, s[16:19], s36 offen offset:2048 lds\n\t"
  "v_mfma_f32_16x16x32_bf16 v[8:11], v[212:215], v[152:155], v[8:11]\n\t"
  "v_mfma_f32_16x16x32_bf16 v[12:15], v[216:219], v[152:155], v[12:15]\n\t"
  "ds_read_b64_tr_b16 v[212:213], v234 offset:1024\n\t"
  "v_max3_f32 v243, v243, v187, v164\n\t"
  "v_max3_f32 v243, v243, v165, v166\n\t"
  "v_mfma_f32_16x16x32_bf16 v[4:7], v[216:219], v[144:147], v[4:7]\n\t"
  "ds_read_b64_tr_b16 v[214:215], v234 offset:9216\n\t"
  "v_max3_f32 v243, v243, v167, v172\n\t"
  "v_max3_f32 v243, v243, v173, v174\n\t"
  "v_mfma_f32_16x16x32_bf16 a[12:15], v[216:219], v[136:139], a[12:15]\n\t"
  "v_max3_f32 v243, v243, v175, v180\n\t"
  "buffer_load_dwordx4 v238, s[16:19], s36 offen offset:3072 lds\n\t"
  "v_mfma_f32_16x16x32_bf16 a[8:11], v[216:219], v[128:131], a[8:11]\n\t"
  "v_max3_f32 v243, v243, v181, v182\n\t"
  "s_add_u32 s36, s36, 28672\n\t"
  "s_waitcnt lgkmcnt(4)\n\t"
  "v_mfma_f32_16x16x32_bf16 a[16:19], v[220:223], v[128:131], a[16:19]\n\t"
  "ds_read_b64_tr_b16 v[216:217], v233 offset:1536\n\t"
  "v_max3_f32 v243, v243, v183, v188\n\t"
  "v_max3_f32 v243, v243, v189, v190\n\t"
  "v_mfma_f32_16x16x32_bf16 a[20:23], v[220:223], v[136:139], a[20:23]\n\t"
  "ds_read_b64_tr_b16 v[218:219], v233 offset:9728\n\t"
  "v_max_f32_e32 v243, v243, v191\n\t"
  "s_add_u32 m0, s30, 135168\n\t"
  "v_mfma_f32_16x16x32_bf16 v[16:19], v[220:223], v[144:147], v[16:19]\n\t"
  "v_cmp_lt_f32_e32 vcc, 0x41000000, v243\n\t"
  "buffer_load_dwordx4 v235, s[16:19], s36 offen lds\n\t"
  "v_mfma_f32_16x16x32_bf16 v[24:27], v[220:223], v[152:155], v[24:27]\n\t"
  "s_cbranch_vccnz .Lrare_azs\n\t"
  ".Lret_azs:\n\t"
  "s_nop 1\n\t"
  "s_lshl_b32 s64, s62, 10\n\t"
  "s_add_u32 s64, s64, 106496\n\t"
  "s_xor_b32 s65, s64, 1024\n\t"
  "v_lshlrev_b32_e32 v247, 4, v254\n\t"
  "v_add_u32_e32 v248, s64, v247\n\t"
  "v_add_u32_e32 v249, s65, v247\n\t"
  "ds_write_b32 v248, v192\n\t"
  "ds_write_b32 v248, v196 offset:4\n\t"
  "ds_write_b32 v248, v200 offset:8\n\t"
  "ds_write_b32 v248, v204 offset:12\n\t"
  "v_mov_b32_e32 v243, v249\n\t"
  "s_waitcnt lgkmcnt(0)\n\t"
  "s_barrier\n\t"
  "ds_read_b128 v[246:249], v243\n\t"
  "s_waitcnt lgkmcnt(0)\n\t"
  "s_cmp_lg_u32 s34, 0\n\t"
  "s_cbranch_scc1 .Leq_pend\n\t"
  "v_mov_b32_e32 v250, 1.0\n\t"
  "v_mov_b32_e32 v251, 1.0\n\t"
  "v_mov_b32_e32 v252, 1.0\n\t"
  "v_mov_b32_e32 v253, 1.0\n\t"
  ".Leq_pend:\n\t"
  "v_min_f32_e32 v243, v192, v248\n\t"
  "v_sub_f32_e32 v255, v243, v192\n\t"
  "v_sub_f32_e32 v243, v192, v243\n\t"
  "v_exp_f32_e32 v255, v255\n\t"
  "v_sub_f32_e32 v160, v160, v243\n\t"
  "v_sub_f32_e32 v161, v161, v243\n\t"
  "v_sub_f32_e32 v162, v162, v243\n\t"
  "v_sub_f32_e32 v163, v163, v243\n\t"
  "v_sub_f32_e32 v164, v164, v243\n\t"
  "v_sub_f32_e32 v165, v165, v243\n\t"
  "v_sub_f32_e32 v166, v166, v243\n\t"
  "v_sub_f32_e32 v167, v167, v243\n\t"
  "v_sub_f32_e32 v192, v192, v243\n\t"
  "v_sub_f32_e32 v193, v193, v243\n\t"
  "v_sub_f32_e32 v194, v194, v243\n\t"
  "v_sub_f32_e32 v195, v195, v243\n\t"
  "v_mul_f32_e32 v239, v239, v255\n\t"
  "v_mul_f32_e32 v250, v250, v255\n\t"
  "v_min_f32_e32 v243, v196, v249\n\t"
  "v_sub_f32_e32 v255, v243, v196\n\t"
  "v_sub_f32_e32 v243, v196, v243\n\t"
  "v_exp_f32_e32 v255, v255\n\t"
  "v_sub_f32_e32 v168, v168, v243\n\t"
  "v_sub_f32_e32 v169, v169, v243\n\t"
  "v_sub_f32_e32 v170, v170, v243\n\t"
  "v_sub_f32_e32 v171, v171, v243\n\t"
  "v_sub_f32_e32 v172, v172, v243\n\t"
  "v_sub_f32_e32 v173, v173, v243\n\t"
  "v_sub_f32_e32 v174, v174, v243\n\t"
  "v_sub_f32_e32 v175, v175, v243\n\t"
  "v_sub_f32_e32 v196, v196, v243\n\t"
  "v_sub_f32_e32 v197, v197, v243\n\t"
  "v_sub_f32_e32 v198, v198, v243\n\t"
  "v_sub_f32_e32 v199, v199, v243\n\t"
  "v_mul_f32_e32 v240, v240, v255\n\t"
  "v_mul_f32_e32 v251, v251, v255\n\t"
  "v_min_f32_e32 v243, v200, v246\n\t"
  "v_sub_f32_e32 v255, v243, v200\n\t"
  "v_sub_f32_e32 v243, v200, v243\n\t"
  "v_exp_f32_e32 v255, v255\n\t"
  "v_sub_f32_e32 v176, v176, v243\n\t"
  "v_sub_f32_e32 v177, v177, v243\n\t"
  "v_sub_f32_e32 v178, v178, v243\n\t"
  "v_sub_f32_e32 v179, v179, v243\n\t"
  "v_sub_f32_e32 v180, v180, v243\n\t"
  "v_sub_f32_e32 v181, v181, v243\n\t"
  "v_sub_f32_e32 v182, v182, v243\n\t"
  "v_sub_f32_e32 v183, v183, v243\n\t"
  "v_sub_f32_e32 v200, v200, v243\n\t"
  "v_sub_f32_e32 v201, v201, v243\n\t"
  "v_sub_f32_e32 v202, v202, v243\n\t"
  "v_sub_f32_e32 v203, v203, v243\n\t"
  "v_mul_f32_e32 v241, v241, v255\n\t"
  "v_mul_f32_e32 v252, v252, v255\n\t"
  "v_min_f32_e32 v243, v204, v247\n\t"
  "v_sub_f32_e32 v255, v243, v204\n\t"
  "v_sub_f32_e32 v243, v204, v243\n\t"
  "v_exp_f32_e32 v255, v255\n\t"
  "v_sub_f32_e32 v184, v184, v243\n\t"
  "v_sub_f32_e32 v185, v185, v243\n\t"
  "v_sub_f32_e32 v186, v186, v243\n\t"
  "v_sub_f32_e32 v187, v187, v243\n\t"
  "v_sub_f32_e32 v188, v188, v243\n\t"
  "v_sub_f32_e32 v189, v189, v243\n\t"
  "v_sub_f32_e32 v190, v190, v243\n\t"
  "v_sub_f32_e32 v191, v191, v243\n\t"
  "v_sub_f32_e32 v204, v204, v243\n\t"
  "v_sub_f32_e32 v205, v205, v243\n\t"
  "v_sub_f32_e32 v206, v206, v243\n\t"
  "v_sub_f32_e32 v207, v207, v243\n\t"
  "v_mul_f32_e32 v242, v242, v255\n\t"
  "v_mul_f32_e32 v253, v253, v255\n\t"
  "s_mov_b32 s34, 1\n\t"
  "s_nop 1\n\t"
  "v_exp_f32_e32 v160, v160\n\t"
  "v_mfma_f32_16x16x32_bf16 v[28:31], v[224:227], v[152:155], v[28:31]\n\t"
  "ds_read_b64_tr_b16 v[220:221], v234 offset:1536\n\t"
  "v_exp_f32_e32 v161, v161\n\t"
  "v_mfma_f32_16x16x32_bf16 v[20:23], v[224:227], v[144:147], v[20:23]\n\t"
  "ds_read_b64_tr_b16 v[222:223], v234 offset:9728\n\t"
  "v_add_f32_e32 v239, v239, v160\n\t"
  "v_mfma_f32_16x16x32_bf16 a[28:31], v[224:227], v[136:139], a[28:31]\n\t"
  "v_add_f32_e32 v239, v239, v161\n\t"
  "buffer_load_dwordx4 v236, s[16:19], s36 offen offset:1024 lds\n\t"
  "v_mfma_f32_16x16x32_bf16 a[24:27], v[224:227], v[128:131], a[24:27]\n\t"
  "v_exp_f32_e32 v162, v162\n\t"
  "v_mfma_f32_16x16x32_bf16 a[32:35], v[208:211], v[128:131], a[32:35]\n\t"
  "ds_read_b64_tr_b16 v[224:225], v233 offset:2048\n\t"
  "v_cvt_pk_bf16_f32 v160, v160, v161\n\t"
  "v_mfma_f32_16x16x32_bf16 a[36:39], v[208:211], v[136:139], a[36:39]\n\t"
  "ds_read_b64_tr_b16 v[226:227], v233 offset:10240\n\t"
  "v_exp_f32_e32 v163, v163\n\t"
  "v_mfma_f32_16x16x32_bf16 v[32:35], v[208:211], v[144:147], v[32:35]\n\t"
  "v_add_f32_e32 v239, v239, v162\n\t"
  "buffer_load_dwordx4 v237, s[16:19], s36 offen offset:2048 lds\n\t"
  "v_mfma_f32_16x16x32_bf16 v[40:43], v[208:211], v[152:155], v[40:43]\n\t"
  "v_mfma_f32_16x16x32_bf16 v[44:47], v[212:215], v[152:155], v[44:47]\n\t"
  "ds_read_b64_tr_b16 v[228:229], v234 offset:2048\n\t"
  "v_add_f32_e32 v239, v239, v163\n\t"
  "v_mfma_f32_16x16x32_bf16 v[36:39], v[212:215], v[144:147], v[36:39]\n\t"
  "ds_read_b64_tr_b16 v[230:231], v234 offset:10240\n\t"
  "v_cvt_pk_bf16_f32 v161, v162, v163\n\t"
  "v_mfma_f32_16x16x32_bf16 a[44:47], v[212:215], v[136:139], a[44:47]\n\t"
  "buffer_load_dwordx4 v238, s[16:19], s36 offen offset:3072 lds\n\t"
  "v_mfma_f32_16x16x32_bf16 a[40:43], v[212:215], v[128:131], a[40:43]\n\t"
  "s_add_u32 s36, s36, 4294938752\n\t"
  "s_waitcnt lgkmcnt(4)\n\t"
  "v_mfma_f32_16x16x32_bf16 a[48:51], v[216:219], v[128:131], a[48:51]\n\t"
  "ds_read_b64_tr_b16 v[208:209], v233 offset:2560\n\t"
  "v_exp_f32_e32 v164, v164\n\t"
  "v_mfma_f32_16x16x32_bf16 a[52:55], v[216:219], v[136:139], a[52:55]\n\t"
  "ds_read_b64_tr_b16 v[210:211], v233 offset:10752\n\t"
  "s_add_u32 m0, s30, 32768\n\t"
  "v_mfma_f32_16x16x32_bf16 v[48:51], v[216:219], v[144:147], v[48:51]\n\t"
  "v_add_f32_e32 v239, v239, v164\n\t"
  "buffer_load_dwordx4 v235, s[16:19], s36 offen lds\n\t"
  "v_mfma_f32_16x16x32_bf16 v[56:59], v[216:219], v[152:155], v[56:59]\n\t"
  "v_exp_f32_e32 v165, v165\n\t"
  "v_mfma_f32_16x16x32_bf16 v[60:63], v[220:223], v[152:155], v[60:63]\n\t"
  "ds_read_b64_tr_b16 v[212:213], v234 offset:2560\n\t"
  "v_mfma_f32_16x16x32_bf16 v[52:55], v[220:223], v[144:147], v[52:55]\n\t"
  "ds_read_b64_tr_b16 v[214:215], v234 offset:10752\n\t"
  "v_add_f32_e32 v239, v239, v165\n\t"
  "v_mfma_f32_16x16x32_bf16 a[60:63], v[220:223], v[136:139], a[60:63]\n\t"
  "v_cvt_pk_bf16_f32 v162, v164, v165\n\t"
  "buffer_load_dwordx4 v236, s[16:19], s36 offen offset:1024 lds\n\t"
  "v_mfma_f32_16x16x32_bf16 a[56:59], v[220:223], v[128:131], a[56:59]\n\t"
  "s_waitcnt lgkmcnt(4)\n\t"
  "v_mfma_f32_16x16x32_bf16 a[64:67], v[224:227], v[128:131], a[64:67]\n\t"
  "ds_read_b64_tr_b16 v[216:217], v233 offset:3072\n\t"
  "v_exp_f32_e32 v166, v166\n\t"
  "v_mfma_f32_16x16x32_bf16 a[68:71], v[224:227], v[136:139], a[68:71]\n\t"
  "ds_read_b64_tr_b16 v[218:219], v233 offset:11264\n\t"
  "v_mfma_f32_16x16x32_bf16 v[64:67], v[224:227], v[144:147], v[64:67]\n\t"
  "v_add_f32_e32 v239, v239, v166\n\t"
  "buffer_load_dwordx4 v237, s[16:19], s36 offen offset:2048 lds\n\t"
  "v_mfma_f32_16x16x32_bf16 v[72:75], v[224:227], v[152:155], v[72:75]\n\t"
  "v_mfma_f32_16x16x32_bf16 v[76:79], v[228:231], v[152:155], v[76:79]\n\t"
  "ds_read_b64_tr_b16 v[220:221], v234 offset:3072\n\t"
  "v_exp_f32_e32 v167, v167\n\t"
  "v_mfma_f32_16x16x32_bf16 v[68:71], v[228:231], v[144:147], v[68:71]\n\t"
  "ds_read_b64_tr_b16 v[222:223], v234 offset:11264\n\t"
  "v_mfma_f32_16x16x32_bf16 a[76:79], v[228:231], v[136:139], a[76:79]\n\t"
  "v_add_f32_e32 v239, v239, v167\n\t"
  "buffer_load_dwordx4 v238, s[16:19], s36 offen offset:3072 lds\n\t"
  "v_mfma_f32_16x16x32_bf16 a[72:75], v[228:231], v[128:131], a[72:75]\n\t"
  "v_cvt_pk_bf16_f32 v163, v166, v167\n\t"
  "s_add_u32 s36, s36, 28672\n\t"
  "s_waitcnt lgkmcnt(4)\n\t"
  "v_mfma_f32_16x16x32_bf16 a[80:83], v[208:211], v[128:131], a[80:83]\n\t"
  "ds_read_b64_tr_b16 v[224:225], v233 offset:3584\n\t"
  "v_mfma_f32_16x16x32_bf16 a[84:87], v[208:211], v[136:139], a[84:87]\n\t"
  "ds_read_b64_tr_b16 v[226:227], v233 offset:11776\n\t"
  "s_add_u32 m0, s30, 36864\n\t"
  "v_mfma_f32_16x16x32_bf16 v[80:83], v[208:211], v[144:147], v[80:83]\n\t"
  "buffer_load_dwordx4 v235, s[16:19], s36 offen lds\n\t"
  "v_mfma_f32_16x16x32_bf16 v[88:91], v[208:211], v[152:155], v[88:91]\n\t"
  "v_exp_f32_e32 v168, v168\n\t"
  "v_mfma_f32_16x16x32_bf16 v[92:95], v[212:215], v[152:155], v[92:95]\n\t"
  "ds_read_b64_tr_b16 v[228:229], v234 offset:3584\n\t"
  "v_exp_f32_e32 v169, v169\n\t"
  "v_mfma_f32_16x16x32_bf16 v[84:87], v[212:215], v[144:147], v[84:87]\n\t"
  "ds_read_b64_tr_b16 v[230:231], v234 offset:11776\n\t"
  "v_add_f32_e32 v240, v240, v168\n\t"
  "v_mfma_f32_16x16x32_bf16 a[92:95], v[212:215], v[136:139], a[92:95]\n\t"
  "v_add_f32_e32 v240, v240, v169\n\t"
  "buffer_load_dwordx4 v236, s[16:19], s36 offen offset:1024 lds\n\t"
  "v_mfma_f32_16x16x32_bf16 a[88:91], v[212:215], v[128:131], a[88:91]\n\t"
  "v_cvt_pk_bf16_f32 v168, v168, v169\n\t"
  "s_waitcnt lgkmcnt(4)\n\t"
  "v_mfma_f32_16x16x32_bf16 a[96:99], v[216:219], v[128:131], a[96:99]\n\t"
  "v_mfma_f32_16x16x32_bf16 a[100:103], v[216:219], v[136:139], a[100:103]\n\t"
  "v_mfma_f32_16x16x32_bf16 v[96:99], v[216:219], v[144:147], v[96:99]\n\t"
  "buffer_load_dwordx4 v237, s[16:19], s36 offen offset:2048 lds\n\t"
  "v_mfma_f32_16x16x32_bf16 v[104:107], v[216:219], v[152:155], v[104:107]\n\t"
  "v_exp_f32_e32 v170, v170\n\t"
  "v_mfma_f32_16x16x32_bf16 v[108:111], v[220:223], v[152:155], v[108:111]\n\t"
  "v_exp_f32_e32 v171, v171\n\t"
  "v_mfma_f32_16x16x32_bf16 v[100:103], v[220:223], v[144:147], v[100:103]\n\t"
  "v_add_f32_e32 v240, v240, v170\n\t"
  "v_mfma_f32_16x16x32_bf16 a[108:111], v[220:223], v[136:139], a[108:111]\n\t"
  "buffer_load_dwordx4 v238, s[16:19], s36 offen offset:3072 lds\n\t"
  "v_mfma_f32_16x16x32_bf16 a[104:107], v[220:223], v[128:131], a[104:107]\n\t"
  "v_add_f32_e32 v240, v240, v171\n\t"
  "s_add_u32 s36, s36, 4294938752\n\t"
  "s_waitcnt lgkmcnt(0)\n\t"
  "v_mfma_f32_16x16x32_bf16 a[112:115], v[224:227], v[128:131], a[112:115]\n\t"
  "v_cvt_pk_bf16_f32 v169, v170, v171\n\t"
  "v_mfma_f32_16x16x32_bf16 a[116:119], v[224:227], v[136:139], a[116:119]\n\t"
  "v_mfma_f32_16x16x32_bf16 v[112:115], v[224:227], v[144:147], v[112:115]\n\t"
  "v_exp_f32_e32 v172, v172\n\t"
  "v_mfma_f32_16x16x32_bf16 v[120:123], v[224:227], v[152:155], v[120:123]\n\t"
  "v_mfma_f32_16x16x32_bf16 v[124:127], v[228:231], v[152:155], v[124:127]\n\t"
  "v_add_f32_e32 v240, v240, v172\n\t"
  "v_mfma_f32_16x16x32_bf16 v[116:119], v[228:231], v[144:147], v[116:119]\n\t"
  "v_mfma_f32_16x16x32_bf16 a[124:127], v[228:231], v[136:139], a[124:127]\n\t"
  "v_exp_f32_e32 v173, v173\n\t"
  "v_mfma_f32_16x16x32_bf16 a[120:123], v[228:231], v[128:131], a[120:123]\n\t"
  "s_cmp_lg_u32 s34, 0\n\t"
  "s_cbranch_scc1 .Lrare_bzs\n\t"
  ".Lret_bzs:\n\t"
  "v_exp_f32_e32 v174, v174\n\t"
  "v_add_f32_e32 v240, v240, v173\n\t"
  "v_cvt_pk_bf16_f32 v170, v172, v173\n\t"
  "v_exp_f32_e32 v175, v175\n\t"
  "v_add_f32_e32 v240, v240, v174\n\t"
  "v_exp_f32_e32 v176, v176\n\t"
  "v_add_f32_e32 v240, v240, v175\n\t"
  "v_cvt_pk_bf16_f32 v171, v174, v175\n\t"
  "v_exp_f32_e32 v177, v177\n\t"
  "v_add_f32_e32 v241, v241, v176\n\t"
  "v_exp_f32_e32 v178, v178\n\t"
  "v_add_f32_e32 v241, v241, v177\n\t"
  "v_cvt_pk_bf16_f32 v176, v176, v177\n\t"
  "v_exp_f32_e32 v179, v179\n\t"
  "v_add_f32_e32 v241, v241, v178\n\t"
  "v_exp_f32_e32 v180, v180\n\t"
  "v_add_f32_e32 v241, v241, v179\n\t"
  "v_cvt_pk_bf16_f32 v177, v178, v179\n\t"
  "v_exp_f32_e32 v181, v181\n\t"
  "v_add_f32_e32 v241, v241, v180\n\t"
  "v_exp_f32_e32 v182, v182\n\t"
  "v_add_f32_e32 v241, v241, v181\n\t"
  "v_cvt_pk_bf16_f32 v178, v180, v181\n\t"
  "v_exp_f32_e32 v183, v183\n\t"
  "v_add_f32_e32 v241, v241, v182\n\t"
  "v_exp_f32_e32 v184, v184\n\t"
  "v_add_f32_e32 v241, v241, v183\n\t"
  "v_cvt_pk_bf16_f32 v179, v182, v183\n\t"
  "v_exp_f32_e32 v185, v185\n\t"
  "v_add_f32_e32 v242, v242, v184\n\t"
  "v_exp_f32_e32 v186, v186\n\t"
  "v_add_f32_e32 v242, v242, v185\n\t"
  "v_cvt_pk_bf16_f32 v184, v184, v185\n\t"
  "v_exp_f32_e32 v187, v187\n\t"
  "v_add_f32_e32 v242, v242, v186\n\t"
  "v_exp_f32_e32 v188, v188\n\t"
  "v_add_f32_e32 v242, v242, v187\n\t"
  "v_cvt_pk_bf16_f32 v185, v186, v187\n\t"
  "v_exp_f32_e32 v189, v189\n\t"
  "v_add_f32_e32 v242, v242, v188\n\t"
  "v_exp_f32_e32 v190, v190\n\t"
  "v_add_f32_e32 v242, v242, v189\n\t"
  "v_cvt_pk_bf16_f32 v186, v188, v189\n\t"
  "v_exp_f32_e32 v191, v191\n\t"
  "v_add_f32_e32 v242, v242, v190\n\t"
  "v_add_f32_e32 v242, v242, v191\n\t"
  "v_cvt_pk_bf16_f32 v187, v190, v191\n\t"
  "s_waitcnt lgkmcnt(0)\n\t"
  "s_nop 7\n\t"
  "s_lshl_b32 s66, s62, 10\n\t"
  "s_add_u32 s66, s66, 110592\n\t"
  "v_lshlrev_b32_e32 v246, 4, v254\n\t"
  "v_add_u32_e32 v250, s66, v246\n\t"
  "v_xor_b32_e32 v251, 1024, v250\n\t"
  "s_lshl_b32 s66, s62, 14\n\t"
  "s_add_u32 s66, s66, 65536\n\t"
  "v_add_u32_e32 v230, s66, v246\n\t"
  "v_xor_b32_e32 v231, 16384, v230\n\t"
  "v_mov_b32_e32 v253, s66\n\t"
  "v_and_b32_e32 v246, 3, v254\n\t"
  "v_lshlrev_b32_e32 v246, 14, v246\n\t"
  "s_lshl_b32 s66, s62, 2\n\t"
  "s_add_u32 s66, s66, 65536\n\t"
  "v_add_u32_e32 v252, s66, v246\n\t"
  "v_and_b32_e32 v246, 15, v254\n\t"
  "v_lshrrev_b32_e32 v248, 4, v254\n\t"
  "v_lshlrev_b32_e32 v249, 2, v248\n\t"
  "v_sub_u32_e32 v246, v246, v249\n\t"
  "v_mov_b32_e32 v224, 0\n\t"
  "v_mov_b32_e32 v225, 0\n\t"
  "v_mov_b32_e32 v228, 0\n\t"
  "v_mov_b32_e32 v229, 0\n\t"
  "v_mov_b32_e32 v248, 0x3f80\n\t"
  "v_mov_b32_e32 v249, 0x3f800000\n\t"
  "v_cmp_eq_u32_e32 vcc, 0, v246\n\t"
  "s_nop 1\n\t"
  "v_cndmask_b32_e32 v226, 0, v248, vcc\n\t"
  "v_cmp_eq_u32_e32 vcc, 1, v246\n\t"
  "s_nop 1\n\t"
  "v_cndmask_b32_e32 v226, v226, v249, vcc\n\t"
  "v_cmp_eq_u32_e32 vcc, 2, v246\n\t"
  "s_nop 1\n\t"
  "v_cndmask_b32_e32 v227, 0, v248, vcc\n\t"
  "v_cmp_eq_u32_e32 vcc, 3, v246\n\t"
  "s_nop 1\n\t"
  "v_cndmask_b32_e32 v227, v227, v249, vcc\n\t"
  "s_and_b32 s66, s56, 2\n\t"
  "s_lshl_b32 s66, s66, 3\n\t"
  "v_and_b32_e32 v246, 31, v254\n\t"
  "v_xor_b32_e32 v246, s66, v246\n\t"
  "s_lshl_b32 s67, s62, 2\n\t"
  "s_xor_b32 s67, s67, s56\n\t"
  "v_lshrrev_b32_e32 v248, 3, v246\n\t"
  "v_xor_b32_e32 v248, s67, v248\n\t"
  "v_lshlrev_b32_e32 v248, 2, v248\n\t"
  "v_lshl_add_u32 v243, v246, 6, v248\n\t"
  "s_lshl_b32 s58, s61, 13\n\t"
  "s_lshl_b32 s66, s62, 11\n\t"
  "s_add_u32 s58, s58, s66\n\t"
  "s_add_u32 s20, s98, 16777216\n\t"
  "s_addc_u32 s21, s99, 0\n\t"
  "s_and_b32 s21, s21, 0xffff\n\t"
  "s_mov_b32 s22, 29360128\n\t"
  "s_lshl_b32 s66, s61, 4\n\t"
  "s_lshl_b32 s38, s62, 2\n\t"
  "s_add_u32 s66, s66, s38\n\t"
  "s_and_b32 s38, s56, 2\n\t"
  "s_add_u32 s37, s66, s38\n\t"
  "s_xor_b32 s38, s37, 2\n\t"
  "s_mul_i32 s37, s37, 7168\n\t"
  "s_mul_i32 s38, s38, 7168\n\t"
  "v_bfe_u32 v246, v254, 3, 1\n\t"
  "v_mul_u32_u24_e32 v249, 7168, v246\n\t"
  "v_lshrrev_b32_e32 v248, 4, v254\n\t"
  "v_lshl_add_u32 v249, v248, 7, v249\n\t"
  "v_and_b32_e32 v248, 7, v254\n\t"
  "v_lshl_add_u32 v249, v248, 4, v249\n\t"
  "s_andn2_b32 s67, s67, 2\n\t"
  "v_xor_b32_e32 v246, s67, v246\n\t"
  "v_mul_u32_u24_e32 v247, 1835008, v246\n\t"
  "v_add_u32_e32 v249, v249, v247\n\t"
  "v_cmp_eq_u32_e32 vcc, 0, v246\n\t"
  "v_mov_b32_e32 v246, 0x7f000000\n\t"
  "s_nop 0\n\t"
  "v_cndmask_b32_e32 v247, v249, v246, vcc\n\t"
  "v_add_u32_e32 v248, 3670016, v249\n\t"
  "s_branch .Ldrain\n\t"
  ".Lrare_ags:\n\t"
  "s_nop 7\n\t"
  "v_max3_f32 v247, v160, v161, v162\n\t"
  "v_max3_f32 v247, v247, v163, v164\n\t"
  "v_max3_f32 v247, v247, v165, v166\n\t"
  "v_max_f32_e32 v247, v247, v167\n\t"
  "v_mov_b32_e32 v246, v247\n\t"
  "s_nop 1\n\t"
  "v_permlane16_swap_b32 v246, v247\n\t"
  "s_nop 1\n\t"
  "v_max_f32_e32 v247, v247, v246\n\t"
  "v_mov_b32_e32 v246, v247\n\t"
  "s_nop 1\n\t"
  "v_permlane32_swap_b32 v246, v247\n\t"
  "s_nop 1\n\t"
  "v_max_f32_e32 v247, v247, v246\n\t"
  "v_max_f32_e32 v247, 0, v247\n\t"
  "v_sub_f32_e32 v250, 0, v247\n\t"
  "v_exp_f32_e32 v250, v250\n\t"
  "v_sub_f32_e32 v160, v160, v247\n\t"
  "v_sub_f32_e32 v161, v161, v247\n\t"
  "v_sub_f32_e32 v162, v162, v247\n\t"
  "v_sub_f32_e32 v163, v163, v247\n\t"
  "v_sub_f32_e32 v164, v164, v247\n\t"
  "v_sub_f32_e32 v165, v165, v247\n\t"
  "v_sub_f32_e32 v166, v166, v247\n\t"
  "v_sub_f32_e32 v167, v167, v247\n\t"
  "v_sub_f32_e32 v192, v192, v247\n\t"
  "v_sub_f32_e32 v193, v193, v247\n\t"
  "v_sub_f32_e32 v194, v194, v247\n\t"
  "v_sub_f32_e32 v195, v195, v247\n\t"
  "v_mul_f32_e32 v239, v239, v250\n\t"
  "v_max3_f32 v247, v168, v169, v170\n\t"
  "v_max3_f32 v247, v247, v171, v172\n\t"
  "v_max3_f32 v247, v247, v173, v174\n\t"
  "v_max_f32_e32 v247, v247, v175\n\t"
  "v_mov_b32_e32 v246, v247\n\t"
  "s_nop 1\n\t"
  "v_permlane16_swap_b32 v246, v247\n\t"
  "s_nop 1\n\t"
  "v_max_f32_e32 v247, v247, v246\n\t"
  "v_mov_b32_e32 v246, v247\n\t"
  "s_nop 1\n\t"
  "v_permlane32_swap_b32 v246, v247\n\t"
  "s_nop 1\n\t"
  "v_max_f32_e32 v247, v247, v246\n\t"
  "v_max_f32_e32 v247, 0, v247\n\t"
  "v_sub_f32_e32 v251, 0, v247\n\t"
  "v_exp_f32_e32 v251, v251\n\t"
  "v_sub_f32_e32 v168, v168, v247\n\t"
  "v_sub_f32_e32 v169, v169, v247\n\t"
  "v_sub_f32_e32 v170, v170, v247\n\t"
  "v_sub_f32_e32 v171, v171, v247\n\t"
  "v_sub_f32_e32 v172, v172, v247\n\t"
  "v_sub_f32_e32 v173, v173, v247\n\t"
  "v_sub_f32_e32 v174, v174, v247\n\t"
  "v_sub_f32_e32 v175, v175, v247\n\t"
  "v_sub_f32_e32 v196, v196, v247\n\t"
  "v_sub_f32_e32 v197, v197, v247\n\t"
  "v_sub_f32_e32 v198, v198, v247\n\t"
  "v_sub_f32_e32 v199, v199, v247\n\t"
  "v_mul_f32_e32 v240, v240, v251\n\t"
  "v_max3_f32 v247, v176, v177, v178\n\t"
  "v_max3_f32 v247, v247, v179, v180\n\t"
  "v_max3_f32 v247, v247, v181, v182\n\t"
  "v_max_f32_e32 v247, v247, v183\n\t"
  "v_mov_b32_e32 v246, v247\n\t"
  "s_nop 1\n\t"
  "v_permlane16_swap_b32 v246, v247\n\t"
  "s_nop 1\n\t"
  "v_max_f32_e32 v247, v247, v246\n\t"
  "v_mov_b32_e32 v246, v247\n\t"
  "s_nop 1\n\t"
  "v_permlane32_swap_b32 v246, v247\n\t"
  "s_nop 1\n\t"
  "v_max_f32_e32 v247, v247, v246\n\t"
  "v_max_f32_e32 v247, 0, v247\n\t"
  "v_sub_f32_e32 v252, 0, v247\n\t"
  "v_exp_f32_e32 v252, v252\n\t"
  "v_sub_f32_e32 v176, v176, v247\n\t"
  "v_sub_f32_e32 v177, v177, v247\n\t"
  "v_sub_f32_e32 v178, v178, v247\n\t"
  "v_sub_f32_e32 v179, v179, v247\n\t"
  "v_sub_f32_e32 v180, v180, v247\n\t"
  "v_sub_f32_e32 v181, v181, v247\n\t"
  "v_sub_f32_e32 v182, v182, v247\n\t"
  "v_sub_f32_e32 v183, v183, v247\n\t"
  "v_sub_f32_e32 v200, v200, v247\n\t"
  "v_sub_f32_e32 v201, v201, v247\n\t"
  "v_sub_f32_e32 v202, v202, v247\n\t"
  "v_sub_f32_e32 v203, v203, v247\n\t"
  "v_mul_f32_e32 v241, v241, v252\n\t"
  "v_max3_f32 v247, v184, v185, v186\n\t"
  "v_max3_f32 v247, v247, v187, v188\n\t"
  "v_max3_f32 v247, v247, v189, v190\n\t"
  "v_max_f32_e32 v247, v247, v191\n\t"
  "v_mov_b32_e32 v246, v247\n\t"
  "s_nop 1\n\t"
  "v_permlane16_swap_b32 v246, v247\n\t"
  "s_nop 1\n\t"
  "v_max_f32_e32 v247, v247, v246\n\t"
  "v_mov_b32_e32 v246, v247\n\t"
  "s_nop 1\n\t"
  "v_permlane32_swap_b32 v246, v247\n\t"
  "s_nop 1\n\t"
  "v_max_f32_e32 v247, v247, v246\n\t"
  "v_max_f32_e32 v247, 0, v247\n\t"
  "v_sub_f32_e32 v253, 0, v247\n\t"
  "v_exp_f32_e32 v253, v253\n\t"
  "v_sub_f32_e32 v184, v184, v247\n\t"
  "v_sub_f32_e32 v185, v185, v247\n\t"
  "v_sub_f32_e32 v186, v186, v247\n\t"
  "v_sub_f32_e32 v187, v187, v247\n\t"
  "v_sub_f32_e32 v188, v188, v247\n\t"
  "v_sub_f32_e32 v189, v189, v247\n\t"
  "v_sub_f32_e32 v190, v190, v247\n\t"
  "v_sub_f32_e32 v191, v191, v247\n\t"
  "v_sub_f32_e32 v204, v204, v247\n\t"
  "v_sub_f32_e32 v205, v205, v247\n\t"
  "v_sub_f32_e32 v206, v206, v247\n\t"
  "v_sub_f32_e32 v207, v207, v247\n\t"
  "v_mul_f32_e32 v242, v242, v253\n\t"
  "s_mov_b32 s34, 1\n\t"
  "s_nop 1\n\t"
  "s_branch .Lret_ags\n\t"
  ".Lrare_bgs:\n\t"
  "s_getpc_b64 s[66:67]\n\t"
  "s_branch .Lrescale\n\t"
  "s_branch .Lret_bgs\n\t"
  ".Lrare_aes:\n\t"
  "s_nop 7\n\t"
  "v_max3_f32 v247, v128, v129, v130\n\t"
  "v_max3_f32 v247, v247, v131, v132\n\t"
  "v_max3_f32 v247, v247, v133, v134\n\t"
  "v_max_f32_e32 v247, v247, v135\n\t"
  "v_mov_b32_e32 v246, v247\n\t"
  "s_nop 1\n\t"
  "v_permlane16_swap_b32 v246, v247\n\t"
  "s_nop 1\n\t"
  "v_max_f32_e32 v247, v247, v246\n\t"
  "v_mov_b32_e32 v246, v247\n\t"
  "s_nop 1\n\t"
  "v_permlane32_swap_b32 v246, v247\n\t"
  "s_nop 1\n\t"
  "v_max_f32_e32 v247, v247, v246\n\t"
  "v_max_f32_e32 v247, 0, v247\n\t"
  "v_sub_f32_e32 v250, 0, v247\n\t"
  "v_exp_f32_e32 v250, v250\n\t"
  "v_sub_f32_e32 v128, v128, v247\n\t"
  "v_sub_f32_e32 v129, v129, v247\n\t"
  "v_sub_f32_e32 v130, v130, v247\n\t"
  "v_sub_f32_e32 v131, v131, v247\n\t"
  "v_sub_f32_e32 v132, v132, v247\n\t"
  "v_sub_f32_e32 v133, v133, v247\n\t"
  "v_sub_f32_e32 v134, v134, v247\n\t"
  "v_sub_f32_e32 v135, v135, v247\n\t"
  "v_sub_f32_e32 v192, v192, v247\n\t"
  "v_sub_f32_e32 v193, v193, v247\n\t"
  "v_sub_f32_e32 v194, v194, v247\n\t"
  "v_sub_f32_e32 v195, v195, v247\n\t"
  "v_mul_f32_e32 v239, v239, v250\n\t"
  "v_max3_f32 v247, v136, v137, v138\n\t"
  "v_max3_f32 v247, v247, v139, v140\n\t"
  "v_max3_f32 v247, v247, v141, v142\n\t"
  "v_max_f32_e32 v247, v247, v143\n\t"
  "v_mov_b32_e32 v246, v247\n\t"
  "s_nop 1\n\t"
  "v_permlane16_swap_b32 v246, v247\n\t"
  "s_nop 1\n\t"
  "v_max_f32_e32 v247, v247, v246\n\t"
  "v_mov_b32_e32 v246, v247\n\t"
  "s_nop 1\n\t"
  "v_permlane32_swap_b32 v246, v247\n\t"
  "s_nop 1\n\t"
  "v_max_f32_e32 v247, v247, v246\n\t"
  "v_max_f32_e32 v247, 0, v247\n\t"
  "v_sub_f32_e32 v251, 0, v247\n\t"
  "v_exp_f32_e32 v251, v251\n\t"
  "v_sub_f32_e32 v136, v136, v247\n\t"
  "v_sub_f32_e32 v137, v137, v247\n\t"
  "v_sub_f32_e32 v138, v138, v247\n\t"
  "v_sub_f32_e32 v139, v139, v247\n\t"
  "v_sub_f32_e32 v140, v140, v247\n\t"
  "v_sub_f32_e32 v141, v141, v247\n\t"
  "v_sub_f32_e32 v142, v142, v247\n\t"
  "v_sub_f32_e32 v143, v143, v247\n\t"
  "v_sub_f32_e32 v196, v196, v247\n\t"
  "v_sub_f32_e32 v197, v197, v247\n\t"
  "v_sub_f32_e32 v198, v198, v247\n\t"
  "v_sub_f32_e32 v199, v199, v247\n\t"
  "v_mul_f32_e32 v240, v240, v251\n\t"
  "v_max3_f32 v247, v144, v145, v146\n\t"
  "v_max3_f32 v247, v247, v147, v148\n\t"
  "v_max3_f32 v247, v247, v149, v150\n\t"
  "v_max_f32_e32 v247, v247, v151\n\t"
  "v_mov_b32_e32 v246, v247\n\t"
  "s_nop 1\n\t"
  "v_permlane16_swap_b32 v246, v247\n\t"
  "s_nop 1\n\t"
  "v_max_f32_e32 v247, v247, v246\n\t"
  "v_mov_b32_e32 v246, v247\n\t"
  "s_nop 1\n\t"
  "v_permlane32_swap_b32 v246, v247\n\t"
  "s_nop 1\n\t"
  "v_max_f32_e32 v247, v247, v246\n\t"
  "v_max_f32_e32 v247, 0, v247\n\t"
  "v_sub_f32_e32 v252, 0, v247\n\t"
  "v_exp_f32_e32 v252, v252\n\t"
  "v_sub_f32_e32 v144, v144, v247\n\t"
  "v_sub_f32_e32 v145, v145, v247\n\t"
  "v_sub_f32_e32 v146, v146, v247\n\t"
  "v_sub_f32_e32 v147, v147, v247\n\t"
  "v_sub_f32_e32 v148, v148, v247\n\t"
  "v_sub_f32_e32 v149, v149, v247\n\t"
  "v_sub_f32_e32 v150, v150, v247\n\t"
  "v_sub_f32_e32 v151, v151, v247\n\t"
  "v_sub_f32_e32 v200, v200, v247\n\t"
  "v_sub_f32_e32 v201, v201, v247\n\t"
  "v_sub_f32_e32 v202, v202, v247\n\t"
  "v_sub_f32_e32 v203, v203, v247\n\t"
  "v_mul_f32_e32 v241, v241, v252\n\t"
  "v_max3_f32 v247, v152, v153, v154\n\t"
  "v_max3_f32 v247, v247, v155, v156\n\t"
  "v_max3_f32 v247, v247, v157, v158\n\t"
  "v_max_f32_e32 v247, v247, v159\n\t"
  "v_mov_b32_e32 v246, v247\n\t"
  "s_nop 1\n\t"
  "v_permlane16_swap_b32 v246, v247\n\t"
  "s_nop 1\n\t"
  "v_max_f32_e32 v247, v247, v246\n\t"
  "v_mov_b32_e32 v246, v247\n\t"
  "s_nop 1\n\t"
  "v_permlane32_swap_b32 v246, v247\n\t"
  "s_nop 1\n\t"
  "v_max_f32_e32 v247, v247, v246\n\t"
  "v_max_f32_e32 v247, 0, v247\n\t"
  "v_sub_f32_e32 v253, 0, v247\n\t"
  "v_exp_f32_e32 v253, v253\n\t"
  "v_sub_f32_e32 v152, v152, v247\n\t"
  "v_sub_f32_e32 v153, v153, v247\n\t"
  "v_sub_f32_e32 v154, v154, v247\n\t"
  "v_sub_f32_e32 v155, v155, v247\n\t"
  "v_sub_f32_e32 v156, v156, v247\n\t"
  "v_sub_f32_e32 v157, v157, v247\n\t"
  "v_sub_f32_e32 v158, v158, v247\n\t"
  "v_sub_f32_e32 v159, v159, v247\n\t"
  "v_sub_f32_e32 v204, v204, v247\n\t"
  "v_sub_f32_e32 v205, v205, v247\n\t"
  "v_sub_f32_e32 v206, v206, v247\n\t"
  "v_sub_f32_e32 v207, v207, v247\n\t"
  "v_mul_f32_e32 v242, v242, v253\n\t"
  "s_mov_b32 s34, 1\n\t"
  "s_nop 1\n\t"
  "s_branch .Lret_aes\n\t"
  ".Lrare_bes:\n\t"
  "s_getpc_b64 s[66:67]\n\t"
  "s_branch .Lrescale\n\t"
  "s_branch .Lret_bes\n\t"
  ".Lrare_aos:\n\t"
  "s_nop 7\n\t"
  "v_max3_f32 v247, v160, v161, v162\n\t"
  "v_max3_f32 v247, v247, v163, v164\n\t"
  "v_max3_f32 v247, v247, v165, v166\n\t"
  "v_max_f32_e32 v247, v247, v167\n\t"
  "v_mov_b32_e32 v246, v247\n\t"
  "s_nop 1\n\t"
  "v_permlane16_swap_b32 v246, v247\n\t"
  "s_nop 1\n\t"
  "v_max_f32_e32 v247, v247, v246\n\t"
  "v_mov_b32_e32 v246, v247\n\t"
  "s_nop 1\n\t"
  "v_permlane32_swap_b32 v246, v247\n\t"
  "s_nop 1\n\t"
  "v_max_f32_e32 v247, v247, v246\n\t"
  "v_max_f32_e32 v247, 0, v247\n\t"
  "v_sub_f32_e32 v250, 0, v247\n\t"
  "v_exp_f32_e32 v250, v250\n\t"
  "v_sub_f32_e32 v160, v160, v247\n\t"
  "v_sub_f32_e32 v161, v161, v247\n\t"
  "v_sub_f32_e32 v162, v162, v247\n\t"
  "v_sub_f32_e32 v163, v163, v247\n\t"
  "v_sub_f32_e32 v164, v164, v247\n\t"
  "v_sub_f32_e32 v165, v165, v247\n\t"
  "v_sub_f32_e32 v166, v166, v247\n\t"
  "v_sub_f32_e32 v167, v167, v247\n\t"
  "v_sub_f32_e32 v192, v192, v247\n\t"
  "v_sub_f32_e32 v193, v193, v247\n\t"
  "v_sub_f32_e32 v194, v194, v247\n\t"
  "v_sub_f32_e32 v195, v195, v247\n\t"
  "v_mul_f32_e32 v239, v239, v250\n\t"
  "v_max3_f32 v247, v168, v169, v170\n\t"
  "v_max3_f32 v247, v247, v171, v172\n\t"
  "v_max3_f32 v247, v247, v173, v174\n\t"
  "v_max_f32_e32 v247, v247, v175\n\t"
  "v_mov_b32_e32 v246, v247\n\t"
  "s_nop 1\n\t"
  "v_permlane16_swap_b32 v246, v247\n\t"
  "s_nop 1\n\t"
  "v_max_f32_e32 v247, v247, v246\n\t"
  "v_mov_b32_e32 v246, v247\n\t"
  "s_nop 1\n\t"
  "v_permlane32_swap_b32 v246, v247\n\t"
  "s_nop 1\n\t"
  "v_max_f32_e32 v247, v247, v246\n\t"
  "v_max_f32_e32 v247, 0, v247\n\t"
  "v_sub_f32_e32 v251, 0, v247\n\t"
  "v_exp_f32_e32 v251, v251\n\t"
  "v_sub_f32_e32 v168, v168, v247\n\t"
  "v_sub_f32_e32 v169, v169, v247\n\t"
  "v_sub_f32_e32 v170, v170, v247\n\t"
  "v_sub_f32_e32 v171, v171, v247\n\t"
  "v_sub_f32_e32 v172, v172, v247\n\t"
  "v_sub_f32_e32 v173, v173, v247\n\t"
  "v_sub_f32_e32 v174, v174, v247\n\t"
  "v_sub_f32_e32 v175, v175, v247\n\t"
  "v_sub_f32_e32 v196, v196, v247\n\t"
  "v_sub_f32_e32 v197, v197, v247\n\t"
  "v_sub_f32_e32 v198, v198, v247\n\t"
  "v_sub_f32_e32 v199, v199, v247\n\t"
  "v_mul_f32_e32 v240, v240, v251\n\t"
  "v_max3_f32 v247, v176, v177, v178\n\t"
  "v_max3_f32 v247, v247, v179, v180\n\t"
  "v_max3_f32 v247, v247, v181, v182\n\t"
  "v_max_f32_e32 v247, v247, v183\n\t"
  "v_mov_b32_e32 v246, v247\n\t"
  "s_nop 1\n\t"
  "v_permlane16_swap_b32 v246, v247\n\t"
  "s_nop 1\n\t"
  "v_max_f32_e32 v247, v247, v246\n\t"
  "v_mov_b32_e32 v246, v247\n\t"
  "s_nop 1\n\t"
  "v_permlane32_swap_b32 v246, v247\n\t"
  "s_nop 1\n\t"
  "v_max_f32_e32 v247, v247, v246\n\t"
  "v_max_f32_e32 v247, 0, v247\n\t"
  "v_sub_f32_e32 v252, 0, v247\n\t"
  "v_exp_f32_e32 v252, v252\n\t"
  "v_sub_f32_e32 v176, v176, v247\n\t"
  "v_sub_f32_e32 v177, v177, v247\n\t"
  "v_sub_f32_e32 v178, v178, v247\n\t"
  "v_sub_f32_e32 v179, v179, v247\n\t"
  "v_sub_f32_e32 v180, v180, v247\n\t"
  "v_sub_f32_e32 v181, v181, v247\n\t"
  "v_sub_f32_e32 v182, v182, v247\n\t"
  "v_sub_f32_e32 v183, v183, v247\n\t"
  "v_sub_f32_e32 v200, v200, v247\n\t"
  "v_sub_f32_e32 v201, v201, v247\n\t"
  "v_sub_f32_e32 v202, v202, v247\n\t"
  "v_sub_f32_e32 v203, v203, v247\n\t"
  "v_mul_f32_e32 v241, v241, v252\n\t"
  "v_max3_f32 v247, v184, v185, v186\n\t"
  "v_max3_f32 v247, v247, v187, v188\n\t"
  "v_max3_f32 v247, v247, v189, v190\n\t"
  "v_max_f32_e32 v247, v247, v191\n\t"
  "v_mov_b32_e32 v246, v247\n\t"
  "s_nop 1\n\t"
  "v_permlane16_swap_b32 v246, v247\n\t"
  "s_nop 1\n\t"
  "v_max_f32_e32 v247, v247, v246\n\t"
  "v_mov_b32_e32 v246, v247\n\t"
  "s_nop 1\n\t"
  "v_permlane32_swap_b32 v246, v247\n\t"
  "s_nop 1\n\t"
  "v_max_f32_e32 v247, v247, v246\n\t"
  "v_max_f32_e32 v247, 0, v247\n\t"
  "v_sub_f32_e32 v253, 0, v247\n\t"
  "v_exp_f32_e32 v253, v253\n\t"
  "v_sub_f32_e32 v184, v184, v247\n\t"
  "v_sub_f32_e32 v185, v185, v247\n\t"
  "v_sub_f32_e32 v186, v186, v247\n\t"
  "v_sub_f32_e32 v187, v187, v247\n\t"
  "v_sub_f32_e32 v188, v188, v247\n\t"
  "v_sub_f32_e32 v189, v189, v247\n\t"
  "v_sub_f32_e32 v190, v190, v247\n\t"
  "v_sub_f32_e32 v191, v191, v247\n\t"
  "v_sub_f32_e32 v204, v204, v247\n\t"
  "v_sub_f32_e32 v205, v205, v247\n\t"
  "v_sub_f32_e32 v206, v206, v247\n\t"
  "v_sub_f32_e32 v207, v207, v247\n\t"
  "v_mul_f32_e32 v242, v242, v253\n\t"
  "s_mov_b32 s34, 1\n\t"
  "s_nop 1\n\t"
  "s_branch .Lret_aos\n\t"
  ".Lrare_bos:\n\t"
  "s_getpc_b64 s[66:67]\n\t"
  "s_branch .Lrescale\n\t"
  "s_branch .Lret_bos\n\t"
  ".Lrare_ays:\n\t"
  "s_nop 7\n\t"
  "v_max3_f32 v247, v128, v129, v130\n\t"
  "v_max3_f32 v247, v247, v131, v132\n\t"
  "v_max3_f32 v247, v247, v133, v134\n\t"
  "v_max_f32_e32 v247, v247, v135\n\t"
  "v_mov_b32_e32 v246, v247\n\t"
  "s_nop 1\n\t"
  "v_permlane16_swap_b32 v246, v247\n\t"
  "s_nop 1\n\t"
  "v_max_f32_e32 v247, v247, v246\n\t"
  "v_mov_b32_e32 v246, v247\n\t"
  "s_nop 1\n\t"
  "v_permlane32_swap_b32 v246, v247\n\t"
  "s_nop 1\n\t"
  "v_max_f32_e32 v247, v247, v246\n\t"
  "v_max_f32_e32 v247, 0, v247\n\t"
  "v_sub_f32_e32 v250, 0, v247\n\t"
  "v_exp_f32_e32 v250, v250\n\t"
  "v_sub_f32_e32 v128, v128, v247\n\t"
  "v_sub_f32_e32 v129, v129, v247\n\t"
  "v_sub_f32_e32 v130, v130, v247\n\t"
  "v_sub_f32_e32 v131, v131, v247\n\t"
  "v_sub_f32_e32 v132, v132, v247\n\t"
  "v_sub_f32_e32 v133, v133, v247\n\t"
  "v_sub_f32_e32 v134, v134, v247\n\t"
  "v_sub_f32_e32 v135, v135, v247\n\t"
  "v_sub_f32_e32 v192, v192, v247\n\t"
  "v_sub_f32_e32 v193, v193, v247\n\t"
  "v_sub_f32_e32 v194, v194, v247\n\t"
  "v_sub_f32_e32 v195, v195, v247\n\t"
  "v_mul_f32_e32 v239, v239, v250\n\t"
  "v_max3_f32 v247, v136, v137, v138\n\t"
  "v_max3_f32 v247, v247, v139, v140\n\t"
  "v_max3_f32 v247, v247, v141, v142\n\t"
  "v_max_f32_e32 v247, v247, v143\n\t"
  "v_mov_b32_e32 v246, v247\n\t"
  "s_nop 1\n\t"
  "v_permlane16_swap_b32 v246, v247\n\t"
  "s_nop 1\n\t"
  "v_max_f32_e32 v247, v247, v246\n\t"
  "v_mov_b32_e32 v246, v247\n\t"
  "s_nop 1\n\t"
  "v_permlane32_swap_b32 v246, v247\n\t"
  "s_nop 1\n\t"
  "v_max_f32_e32 v247, v247, v246\n\t"
  "v_max_f32_e32 v247, 0, v247\n\t"
  "v_sub_f32_e32 v251, 0, v247\n\t"
  "v_exp_f32_e32 v251, v251\n\t"
  "v_sub_f32_e32 v136, v136, v247\n\t"
  "v_sub_f32_e32 v137, v137, v247\n\t"
  "v_sub_f32_e32 v138, v138, v247\n\t"
  "v_sub_f32_e32 v139, v139, v247\n\t"
  "v_sub_f32_e32 v140, v140, v247\n\t"
  "v_sub_f32_e32 v141, v141, v247\n\t"
  "v_sub_f32_e32 v142, v142, v247\n\t"
  "v_sub_f32_e32 v143, v143, v247\n\t"
  "v_sub_f32_e32 v196, v196, v247\n\t"
  "v_sub_f32_e32 v197, v197, v247\n\t"
  "v_sub_f32_e32 v198, v198, v247\n\t"
  "v_sub_f32_e32 v199, v199, v247\n\t"
  "v_mul_f32_e32 v240, v240, v251\n\t"
  "v_max3_f32 v247, v144, v145, v146\n\t"
  "v_max3_f32 v247, v247, v147, v148\n\t"
  "v_max3_f32 v247, v247, v149, v150\n\t"
  "v_max_f32_e32 v247, v247, v151\n\t"
  "v_mov_b32_e32 v246, v247\n\t"
  "s_nop 1\n\t"
  "v_permlane16_swap_b32 v246, v247\n\t"
  "s_nop 1\n\t"
  "v_max_f32_e32 v247, v247, v246\n\t"
  "v_mov_b32_e32 v246, v247\n\t"
  "s_nop 1\n\t"
  "v_permlane32_swap_b32 v246, v247\n\t"
  "s_nop 1\n\t"
  "v_max_f32_e32 v247, v247, v246\n\t"
  "v_max_f32_e32 v247, 0, v247\n\t"
  "v_sub_f32_e32 v252, 0, v247\n\t"
  "v_exp_f32_e32 v252, v252\n\t"
  "v_sub_f32_e32 v144, v144, v247\n\t"
  "v_sub_f32_e32 v145, v145, v247\n\t"
  "v_sub_f32_e32 v146, v146, v247\n\t"
  "v_sub_f32_e32 v147, v147, v247\n\t"
  "v_sub_f32_e32 v148, v148, v247\n\t"
  "v_sub_f32_e32 v149, v149, v247\n\t"
  "v_sub_f32_e32 v150, v150, v247\n\t"
  "v_sub_f32_e32 v151, v151, v247\n\t"
  "v_sub_f32_e32 v200, v200, v247\n\t"
  "v_sub_f32_e32 v201, v201, v247\n\t"
  "v_sub_f32_e32 v202, v202, v247\n\t"
  "v_sub_f32_e32 v203, v203, v247\n\t"
  "v_mul_f32_e32 v241, v241, v252\n\t"
  "v_max3_f32 v247, v152, v153, v154\n\t"
  "v_max3_f32 v247, v247, v155, v156\n\t"
  "v_max3_f32 v247, v247, v157, v158\n\t"
  "v_max_f32_e32 v247, v247, v159\n\t"
  "v_mov_b32_e32 v246, v247\n\t"
  "s_nop 1\n\t"
  "v_permlane16_swap_b32 v246, v247\n\t"
  "s_nop 1\n\t"
  "v_max_f32_e32 v247, v247, v246\n\t"
  "v_mov_b32_e32 v246, v247\n\t"
  "s_nop 1\n\t"
  "v_permlane32_swap_b32 v246, v247\n\t"
  "s_nop 1\n\t"
  "v_max_f32_e32 v247, v247, v246\n\t"
  "v_max_f32_e32 v247, 0, v247\n\t"
  "v_sub_f32_e32 v253, 0, v247\n\t"
  "v_exp_f32_e32 v253, v253\n\t"
  "v_sub_f32_e32 v152, v152, v247\n\t"
  "v_sub_f32_e32 v153, v153, v247\n\t"
  "v_sub_f32_e32 v154, v154, v247\n\t"
  "v_sub_f32_e32 v155, v155, v247\n\t"
  "v_sub_f32_e32 v156, v156, v247\n\t"
  "v_sub_f32_e32 v157, v157, v247\n\t"
  "v_sub_f32_e32 v158, v158, v247\n\t"
  "v_sub_f32_e32 v159, v159, v247\n\t"
  "v_sub_f32_e32 v204, v204, v247\n\t"
  "v_sub_f32_e32 v205, v205, v247\n\t"
  "v_sub_f32_e32 v206, v206, v247\n\t"
  "v_sub_f32_e32 v207, v207, v247\n\t"
  "v_mul_f32_e32 v242, v242, v253\n\t"
  "s_mov_b32 s34, 1\n\t"
  "s_nop 1\n\t"
  "s_branch .Lret_ays\n\t"
  ".Lrare_bys:\n\t"
  "s_getpc_b64 s[66:67]\n\t"
  "s_branch .Lrescale\n\t"
  "s_branch .Lret_bys\n\t"
  ".Lrare_azs:\n\t"
  "s_nop 7\n\t"
  "v_max3_f32 v247, v160, v161, v162\n\t"
  "v_max3_f32 v247, v247, v163, v164\n\t"
  "v_max3_f32 v247, v247, v165, v166\n\t"
  "v_max_f32_e32 v247, v247, v167\n\t"
  "v_mov_b32_e32 v246, v247\n\t"
  "s_nop 1\n\t"
  "v_permlane16_swap_b32 v246, v247\n\t"
  "s_nop 1\n\t"
  "v_max_f32_e32 v247, v247, v246\n\t"
  "v_mov_b32_e32 v246, v247\n\t"
  "s_nop 1\n\t"
  "v_permlane32_swap_b32 v246, v247\n\t"
  "s_nop 1\n\t"
  "v_max_f32_e32 v247, v247, v246\n\t"
  "v_max_f32_e32 v247, 0, v247\n\t"
  "v_sub_f32_e32 v250, 0, v247\n\t"
  "v_exp_f32_e32 v250, v250\n\t"
  "v_sub_f32_e32 v160, v160, v247\n\t"
  "v_sub_f32_e32 v161, v161, v247\n\t"
  "v_sub_f32_e32 v162, v162, v247\n\t"
  "v_sub_f32_e32 v163, v163, v247\n\t"
  "v_sub_f32_e32 v164, v164, v247\n\t"
  "v_sub_f32_e32 v165, v165, v247\n\t"
  "v_sub_f32_e32 v166, v166, v247\n\t"
  "v_sub_f32_e32 v167, v167, v247\n\t"
  "v_sub_f32_e32 v192, v192, v247\n\t"
  "v_sub_f32_e32 v193, v193, v247\n\t"
  "v_sub_f32_e32 v194, v194, v247\n\t"
  "v_sub_f32_e32 v195, v195, v247\n\t"
  "v_mul_f32_e32 v239, v239, v250\n\t"
  "v_max3_f32 v247, v168, v169, v170\n\t"
  "v_max3_f32 v247, v247, v171, v172\n\t"
  "v_max3_f32 v247, v247, v173, v174\n\t"
  "v_max_f32_e32 v247, v247, v175\n\t"
  "v_mov_b32_e32 v246, v247\n\t"
  "s_nop 1\n\t"
  "v_permlane16_swap_b32 v246, v247\n\t"
  "s_nop 1\n\t"
  "v_max_f32_e32 v247, v247, v246\n\t"
  "v_mov_b32_e32 v246, v247\n\t"
  "s_nop 1\n\t"
  "v_permlane32_swap_b32 v246, v247\n\t"
  "s_nop 1\n\t"
  "v_max_f32_e32 v247, v247, v246\n\t"
  "v_max_f32_e32 v247, 0, v247\n\t"
  "v_sub_f32_e32 v251, 0, v247\n\t"
  "v_exp_f32_e32 v251, v251\n\t"
  "v_sub_f32_e32 v168, v168, v247\n\t"
  "v_sub_f32_e32 v169, v169, v247\n\t"
  "v_sub_f32_e32 v170, v170, v247\n\t"
  "v_sub_f32_e32 v171, v171, v247\n\t"
  "v_sub_f32_e32 v172, v172, v247\n\t"
  "v_sub_f32_e32 v173, v173, v247\n\t"
  "v_sub_f32_e32 v174, v174, v247\n\t"
  "v_sub_f32_e32 v175, v175, v247\n\t"
  "v_sub_f32_e32 v196, v196, v247\n\t"
  "v_sub_f32_e32 v197, v197, v247\n\t"
  "v_sub_f32_e32 v198, v198, v247\n\t"
  "v_sub_f32_e32 v199, v199, v247\n\t"
  "v_mul_f32_e32 v240, v240, v251\n\t"
  "v_max3_f32 v247, v176, v177, v178\n\t"
  "v_max3_f32 v247, v247, v179, v180\n\t"
  "v_max3_f32 v247, v247, v181, v182\n\t"
  "v_max_f32_e32 v247, v247, v183\n\t"
  "v_mov_b32_e32 v246, v247\n\t"
  "s_nop 1\n\t"
  "v_permlane16_swap_b32 v246, v247\n\t"
  "s_nop 1\n\t"
  "v_max_f32_e32 v247, v247, v246\n\t"
  "v_mov_b32_e32 v246, v247\n\t"
  "s_nop 1\n\t"
  "v_permlane32_swap_b32 v246, v247\n\t"
  "s_nop 1\n\t"
  "v_max_f32_e32 v247, v247, v246\n\t"
  "v_max_f32_e32 v247, 0, v247\n\t"
  "v_sub_f32_e32 v252, 0, v247\n\t"
  "v_exp_f32_e32 v252, v252\n\t"
  "v_sub_f32_e32 v176, v176, v247\n\t"
  "v_sub_f32_e32 v177, v177, v247\n\t"
  "v_sub_f32_e32 v178, v178, v247\n\t"
  "v_sub_f32_e32 v179, v179, v247\n\t"
  "v_sub_f32_e32 v180, v180, v247\n\t"
  "v_sub_f32_e32 v181, v181, v247\n\t"
  "v_sub_f32_e32 v182, v182, v247\n\t"
  "v_sub_f32_e32 v183, v183, v247\n\t"
  "v_sub_f32_e32 v200, v200, v247\n\t"
  "v_sub_f32_e32 v201, v201, v247\n\t"
  "v_sub_f32_e32 v202, v202, v247\n\t"
  "v_sub_f32_e32 v203, v203, v247\n\t"
  "v_mul_f32_e32 v241, v241, v252\n\t"
  "v_max3_f32 v247, v184, v185, v186\n\t"
  "v_max3_f32 v247, v247, v187, v188\n\t"
  "v_max3_f32 v247, v247, v189, v190\n\t"
  "v_max_f32_e32 v247, v247, v191\n\t"
  "v_mov_b32_e32 v246, v247\n\t"
  "s_nop 1\n\t"
  "v_permlane16_swap_b32 v246, v247\n\t"
  "s_nop 1\n\t"
  "v_max_f32_e32 v247, v247, v246\n\t"
  "v_mov_b32_e32 v246, v247\n\t"
  "s_nop 1\n\t"
  "v_permlane32_swap_b32 v246, v247\n\t"
  "s_nop 1\n\t"
  "v_max_f32_e32 v247, v247, v246\n\t"
  "v_max_f32_e32 v247, 0, v247\n\t"
  "v_sub_f32_e32 v253, 0, v247\n\t"
  "v_exp_f32_e32 v253, v253\n\t"
  "v_sub_f32_e32 v184, v184, v247\n\t"
  "v_sub_f32_e32 v185, v185, v247\n\t"
  "v_sub_f32_e32 v186, v186, v247\n\t"
  "v_sub_f32_e32 v187, v187, v247\n\t"
  "v_sub_f32_e32 v188, v188, v247\n\t"
  "v_sub_f32_e32 v189, v189, v247\n\t"
  "v_sub_f32_e32 v190, v190, v247\n\t"
  "v_sub_f32_e32 v191, v191, v247\n\t"
  "v_sub_f32_e32 v204, v204, v247\n\t"
  "v_sub_f32_e32 v205, v205, v247\n\t"
  "v_sub_f32_e32 v206, v206, v247\n\t"
  "v_sub_f32_e32 v207, v207, v247\n\t"
  "v_mul_f32_e32 v242, v242, v253\n\t"
  "s_mov_b32 s34, 1\n\t"
  "s_nop 1\n\t"
  "s_branch .Lret_azs\n\t"
  ".Lrare_bzs:\n\t"
  "s_getpc_b64 s[66:67]\n\t"
  "s_branch .Lrescale\n\t"
  "s_branch .Lret_bzs\n\t"
  ".Lrescale:\n\t"
  "s_nop 15\n\t"
  "v_accvgpr_read_b32 v246, a0\n\t"
  "v_mul_f32_e32 v246, v250, v246\n\t"
  "v_accvgpr_write_b32 a0, v246\n\t"
  "v_accvgpr_read_b32 v247, a1\n\t"
  "v_mul_f32_e32 v247, v250, v247\n\t"
  "v_accvgpr_write_b32 a1, v247\n\t"
  "v_accvgpr_read_b32 v246, a2\n\t"
  "v_mul_f32_e32 v246, v250, v246\n\t"
  "v_accvgpr_write_b32 a2, v246\n\t"
  "v_accvgpr_read_b32 v247, a3\n\t"
  "v_mul_f32_e32 v247, v250, v247\n\t"
  "v_accvgpr_write_b32 a3, v247\n\t"
  "v_accvgpr_read_b32 v246, a4\n\t"
  "v_mul_f32_e32 v246, v251, v246\n\t"
  "v_accvgpr_write_b32 a4, v246\n\t"
  "v_accvgpr_read_b32 v247, a5\n\t"
  "v_mul_f32_e32 v247, v251, v247\n\t"
  "v_accvgpr_write_b32 a5, v247\n\t"
  "v_accvgpr_read_b32 v246, a6\n\t"
  "v_mul_f32_e32 v246, v251, v246\n\t"
  "v_accvgpr_write_b32 a6, v246\n\t"
  "v_accvgpr_read_b32 v247, a7\n\t"
  "v_mul_f32_e32 v247, v251, v247\n\t"
  "v_accvgpr_write_b32 a7, v247\n\t"
  "v_accvgpr_read_b32 v246, a8\n\t"
  "v_mul_f32_e32 v246, v250, v246\n\t"
  "v_accvgpr_write_b32 a8, v246\n\t"
  "v_accvgpr_read_b32 v247, a9\n\t"
  "v_mul_f32_e32 v247, v250, v247\n\t"
  "v_accvgpr_write_b32 a9, v247\n\t"
  "v_accvgpr_read_b32 v246, a10\n\t"
  "v_mul_f32_e32 v246, v250, v246\n\t"
  "v_accvgpr_write_b32 a10, v246\n\t"
  "v_accvgpr_read_b32 v247, a11\n\t"
  "v_mul_f32_e32 v247, v250, v247\n\t"
  "v_accvgpr_write_b32 a11, v247\n\t"
  "v_accvgpr_read_b32 v246, a12\n\t"
  "v_mul_f32_e32 v246, v251, v246\n\t"
  "v_accvgpr_write_b32 a12, v246\n\t"
  "v_accvgpr_read_b32 v247, a13\n\t"
  "v_mul_f32_e32 v247, v251, v247\n\t"
  "v_accvgpr_write_b32 a13, v247\n\t"
  "v_accvgpr_read_b32 v246, a14\n\t"
  "v_mul_f32_e32 v246, v251, v246\n\t"
  "v_accvgpr_write_b32 a14, v246\n\t"
  "v_accvgpr_read_b32 v247, a15\n\t"
  "v_mul_f32_e32 v247, v251, v247\n\t"
  "v_accvgpr_write_b32 a15, v247\n\t"
  "v_accvgpr_read_b32 v246, a16\n\t"
  "v_mul_f32_e32 v246, v250, v246\n\t"
  "v_accvgpr_write_b32 a16, v246\n\t"
  "v_accvgpr_read_b32 v247, a17\n\t"
  "v_mul_f32_e32 v247, v250, v247\n\t"
  "v_accvgpr_write_b32 a17, v247\n\t"
  "v_accvgpr_read_b32 v246, a18\n\t"
  "v_mul_f32_e32 v246, v250, v246\n\t"
  "v_accvgpr_write_b32 a18, v246\n\t"
  "v_accvgpr_read_b32 v247, a19\n\t"
  "v_mul_f32_e32 v247, v250, v247\n\t"
  "v_accvgpr_write_b32 a19, v247\n\t"
  "v_accvgpr_read_b32 v246, a20\n\t"
  "v_mul_f32_e32 v246, v251, v246\n\t"
  "v_accvgpr_write_b32 a20, v246\n\t"
  "v_accvgpr_read_b32 v247, a21\n\t"
  "v_mul_f32_e32 v247, v251, v247\n\t"
  "v_accvgpr_write_b32 a21, v247\n\t"
  "v_accvgpr_read_b32 v246, a22\n\t"
  "v_mul_f32_e32 v246, v251, v246\n\t"
  "v_accvgpr_write_b32 a22, v246\n\t"
  "v_accvgpr_read_b32 v247, a23\n\t"
  "v_mul_f32_e32 v247, v251, v247\n\t"
  "v_accvgpr_write_b32 a23, v247\n\t"
  "v_accvgpr_read_b32 v246, a24\n\t"
  "v_mul_f32_e32 v246, v250, v246\n\t"
  "v_accvgpr_write_b32 a24, v246\n\t"
  "v_accvgpr_read_b32 v247, a25\n\t"
  "v_mul_f32_e32 v247, v250, v247\n\t"
  "v_accvgpr_write_b32 a25, v247\n\t"
  "v_accvgpr_read_b32 v246, a26\n\t"
  "v_mul_f32_e32 v246, v250, v246\n\t"
  "v_accvgpr_write_b32 a26, v246\n\t"
  "v_accvgpr_read_b32 v247, a27\n\t"
  "v_mul_f32_e32 v247, v250, v247\n\t"
  "v_accvgpr_write_b32 a27, v247\n\t"
  "v_accvgpr_read_b32 v246, a28\n\t"
  "v_mul_f32_e32 v246, v251, v246\n\t"
  "v_accvgpr_write_b32 a28, v246\n\t"
  "v_accvgpr_read_b32 v247, a29\n\t"
  "v_mul_f32_e32 v247, v251, v247\n\t"
  "v_accvgpr_write_b32 a29, v247\n\t"
  "v_accvgpr_read_b32 v246, a30\n\t"
  "v_mul_f32_e32 v246, v251, v246\n\t"
  "v_accvgpr_write_b32 a30, v246\n\t"
  "v_accvgpr_read_b32 v247, a31\n\t"
  "v_mul_f32_e32 v247, v251, v247\n\t"
  "v_accvgpr_write_b32 a31, v247\n\t"
  "v_accvgpr_read_b32 v246, a32\n\t"
  "v_mul_f32_e32 v246, v250, v246\n\t"
  "v_accvgpr_write_b32 a32, v246\n\t"
  "v_accvgpr_read_b32 v247, a33\n\t"
  "v_mul_f32_e32 v247, v250, v247\n\t"
  "v_accvgpr_write_b32 a33, v247\n\t"
  "v_accvgpr_read_b32 v246, a34\n\t"
  "v_mul_f32_e32 v246, v250, v246\n\t"
  "v_accvgpr_write_b32 a34, v246\n\t"
  "v_accvgpr_read_b32 v247, a35\n\t"
  "v_mul_f32_e32 v247, v250, v247\n\t"
  "v_accvgpr_write_b32 a35, v247\n\t"
  "v_accvgpr_read_b32 v246, a36\n\t"
  "v_mul_f32_e32 v246, v251, v246\n\t"
  "v_accvgpr_write_b32 a36, v246\n\t"
  "v_accvgpr_read_b32 v247, a37\n\t"
  "v_mul_f32_e32 v247, v251, v247\n\t"
  "v_accvgpr_write_b32 a37, v247\n\t"
  "v_accvgpr_read_b32 v246, a38\n\t"
  "v_mul_f32_e32 v246, v251, v246\n\t"
  "v_accvgpr_write_b32 a38, v246\n\t"
  "v_accvgpr_read_b32 v247, a39\n\t"
  "v_mul_f32_e32 v247, v251, v247\n\t"
  "v_accvgpr_write_b32 a39, v247\n\t"
  "v_accvgpr_read_b32 v246, a40\n\t"
  "v_mul_f32_e32 v246, v250, v246\n\t"
  "v_accvgpr_write_b32 a40, v246\n\t"
  "v_accvgpr_read_b32 v247, a41\n\t"
  "v_mul_f32_e32 v247, v250, v247\n\t"
  "v_accvgpr_write_b32 a41, v247\n\t"
  "v_accvgpr_read_b32 v246, a42\n\t"
  "v_mul_f32_e32 v246, v250, v246\n\t"
  "v_accvgpr_write_b32 a42, v246\n\t"
  "v_accvgpr_read_b32 v247, a43\n\t"
  "v_mul_f32_e32 v247, v250, v247\n\t"
  "v_accvgpr_write_b32 a43, v247\n\t"
  "v_accvgpr_read_b32 v246, a44\n\t"
  "v_mul_f32_e32 v246, v251, v246\n\t"
  "v_accvgpr_write_b32 a44, v246\n\t"
  "v_accvgpr_read_b32 v247, a45\n\t"
  "v_mul_f32_e32 v247, v251, v247\n\t"
  "v_accvgpr_write_b32 a45, v247\n\t"
  "v_accvgpr_read_b32 v246, a46\n\t"
  "v_mul_f32_e32 v246, v251, v246\n\t"
  "v_accvgpr_write_b32 a46, v246\n\t"
  "v_accvgpr_read_b32 v247, a47\n\t"
  "v_mul_f32_e32 v247, v251, v247\n\t"
  "v_accvgpr_write_b32 a47, v247\n\t"
  "v_accvgpr_read_b32 v246, a48\n\t"
  "v_mul_f32_e32 v246, v250, v246\n\t"
  "v_accvgpr_write_b32 a48, v246\n\t"
  "v_accvgpr_read_b32 v247, a49\n\t"
  "v_mul_f32_e32 v247, v250, v247\n\t"
  "v_accvgpr_write_b32 a49, v247\n\t"
  "v_accvgpr_read_b32 v246, a50\n\t"
  "v_mul_f32_e32 v246, v250, v246\n\t"
  "v_accvgpr_write_b32 a50, v246\n\t"
  "v_accvgpr_read_b32 v247, a51\n\t"
  "v_mul_f32_e32 v247, v250, v247\n\t"
  "v_accvgpr_write_b32 a51, v247\n\t"
  "v_accvgpr_read_b32 v246, a52\n\t"
  "v_mul_f32_e32 v246, v251, v246\n\t"
  "v_accvgpr_write_b32 a52, v246\n\t"
  "v_accvgpr_read_b32 v247, a53\n\t"
  "v_mul_f32_e32 v247, v251, v247\n\t"
  "v_accvgpr_write_b32 a53, v247\n\t"
  "v_accvgpr_read_b32 v246, a54\n\t"
  "v_mul_f32_e32 v246, v251, v246\n\t"
  "v_accvgpr_write_b32 a54, v246\n\t"
  "v_accvgpr_read_b32 v247, a55\n\t"
  "v_mul_f32_e32 v247, v251, v247\n\t"
  "v_accvgpr_write_b32 a55, v247\n\t"
  "v_accvgpr_read_b32 v246, a56\n\t"
  "v_mul_f32_e32 v246, v250, v246\n\t"
  "v_accvgpr_write_b32 a56, v246\n\t"
  "v_accvgpr_read_b32 v247, a57\n\t"
  "v_mul_f32_e32 v247, v250, v247\n\t"
  "v_accvgpr_write_b32 a57, v247\n\t"
  "v_accvgpr_read_b32 v246, a58\n\t"
  "v_mul_f32_e32 v246, v250, v246\n\t"
  "v_accvgpr_write_b32 a58, v246\n\t"
  "v_accvgpr_read_b32 v247, a59\n\t"
  "v_mul_f32_e32 v247, v250, v247\n\t"
  "v_accvgpr_write_b32 a59, v247\n\t"
  "v_accvgpr_read_b32 v246, a60\n\t"
  "v_mul_f32_e32 v246, v251, v246\n\t"
  "v_accvgpr_write_b32 a60, v246\n\t"
  "v_accvgpr_read_b32 v247, a61\n\t"
  "v_mul_f32_e32 v247, v251, v247\n\t"
  "v_accvgpr_write_b32 a61, v247\n\t"
  "v_accvgpr_read_b32 v246, a62\n\t"
  "v_mul_f32_e32 v246, v251, v246\n\t"
  "v_accvgpr_write_b32 a62, v246\n\t"
  "v_accvgpr_read_b32 v247, a63\n\t"
  "v_mul_f32_e32 v247, v251, v247\n\t"
  "v_accvgpr_write_b32 a63, v247\n\t"
  "v_accvgpr_read_b32 v246, a64\n\t"
  "v_mul_f32_e32 v246, v250, v246\n\t"
  "v_accvgpr_write_b32 a64, v246\n\t"
  "v_accvgpr_read_b32 v247, a65\n\t"
  "v_mul_f32_e32 v247, v250, v247\n\t"
  "v_accvgpr_write_b32 a65, v247\n\t"
  "v_accvgpr_read_b32 v246, a66\n\t"
  "v_mul_f32_e32 v246, v250, v246\n\t"
  "v_accvgpr_write_b32 a66, v246\n\t"
  "v_accvgpr_read_b32 v247, a67\n\t"
  "v_mul_f32_e32 v247, v250, v247\n\t"
  "v_accvgpr_write_b32 a67, v247\n\t"
  "v_accvgpr_read_b32 v246, a68\n\t"
  "v_mul_f32_e32 v246, v251, v246\n\t"
  "v_accvgpr_write_b32 a68, v246\n\t"
  "v_accvgpr_read_b32 v247, a69\n\t"
  "v_mul_f32_e32 v247, v251, v247\n\t"
  "v_accvgpr_write_b32 a69, v247\n\t"
  "v_accvgpr_read_b32 v246, a70\n\t"
  "v_mul_f32_e32 v246, v251, v246\n\t"
  "v_accvgpr_write_b32 a70, v246\n\t"
  "v_accvgpr_read_b32 v247, a71\n\t"
  "v_mul_f32_e32 v247, v251, v247\n\t"
  "v_accvgpr_write_b32 a71, v247\n\t"
  "v_accvgpr_read_b32 v246, a72\n\t"
  "v_mul_f32_e32 v246, v250, v246\n\t"
  "v_accvgpr_write_b32 a72, v246\n\t"
  "v_accvgpr_read_b32 v247, a73\n\t"
  "v_mul_f32_e32 v247, v250, v247\n\t"
  "v_accvgpr_write_b32 a73, v247\n\t"
  "v_accvgpr_read_b32 v246, a74\n\t"
  "v_mul_f32_e32 v246, v250, v246\n\t"
  "v_accvgpr_write_b32 a74, v246\n\t"
  "v_accvgpr_read_b32 v247, a75\n\t"
  "v_mul_f32_e32 v247, v250, v247\n\t"
  "v_accvgpr_write_b32 a75, v247\n\t"
  "v_accvgpr_read_b32 v246, a76\n\t"
  "v_mul_f32_e32 v246, v251, v246\n\t"
  "v_accvgpr_write_b32 a76, v246\n\t"
  "v_accvgpr_read_b32 v247, a77\n\t"
  "v_mul_f32_e32 v247, v251, v247\n\t"
  "v_accvgpr_write_b32 a77, v247\n\t"
  "v_accvgpr_read_b32 v246, a78\n\t"
  "v_mul_f32_e32 v246, v251, v246\n\t"
  "v_accvgpr_write_b32 a78, v246\n\t"
  "v_accvgpr_read_b32 v247, a79\n\t"
  "v_mul_f32_e32 v247, v251, v247\n\t"
  "v_accvgpr_write_b32 a79, v247\n\t"
  "v_accvgpr_read_b32 v246, a80\n\t"
  "v_mul_f32_e32 v246, v250, v246\n\t"
  "v_accvgpr_write_b32 a80, v246\n\t"
  "v_accvgpr_read_b32 v247, a81\n\t"
  "v_mul_f32_e32 v247, v250, v247\n\t"
  "v_accvgpr_write_b32 a81, v247\n\t"
  "v_accvgpr_read_b32 v246, a82\n\t"
  "v_mul_f32_e32 v246, v250, v246\n\t"
  "v_accvgpr_write_b32 a82, v246\n\t"
  "v_accvgpr_read_b32 v247, a83\n\t"
  "v_mul_f32_e32 v247, v250, v247\n\t"
  "v_accvgpr_write_b32 a83, v247\n\t"
  "v_accvgpr_read_b32 v246, a84\n\t"
  "v_mul_f32_e32 v246, v251, v246\n\t"
  "v_accvgpr_write_b32 a84, v246\n\t"
  "v_accvgpr_read_b32 v247, a85\n\t"
  "v_mul_f32_e32 v247, v251, v247\n\t"
  "v_accvgpr_write_b32 a85, v247\n\t"
  "v_accvgpr_read_b32 v246, a86\n\t"
  "v_mul_f32_e32 v246, v251, v246\n\t"
  "v_accvgpr_write_b32 a86, v246\n\t"
  "v_accvgpr_read_b32 v247, a87\n\t"
  "v_mul_f32_e32 v247, v251, v247\n\t"
  "v_accvgpr_write_b32 a87, v247\n\t"
  "v_accvgpr_read_b32 v246, a88\n\t"
  "v_mul_f32_e32 v246, v250, v246\n\t"
  "v_accvgpr_write_b32 a88, v246\n\t"
  "v_accvgpr_read_b32 v247, a89\n\t"
  "v_mul_f32_e32 v247, v250, v247\n\t"
  "v_accvgpr_write_b32 a89, v247\n\t"
  "v_accvgpr_read_b32 v246, a90\n\t"
  "v_mul_f32_e32 v246, v250, v246\n\t"
  "v_accvgpr_write_b32 a90, v246\n\t"
  "v_accvgpr_read_b32 v247, a91\n\t"
  "v_mul_f32_e32 v247, v250, v247\n\t"
  "v_accvgpr_write_b32 a91, v247\n\t"
  "v_accvgpr_read_b32 v246, a92\n\t"
  "v_mul_f32_e32 v246, v251, v246\n\t"
  "v_accvgpr_write_b32 a92, v246\n\t"
  "v_accvgpr_read_b32 v247, a93\n\t"
  "v_mul_f32_e32 v247, v251, v247\n\t"
  "v_accvgpr_write_b32 a93, v247\n\t"
  "v_accvgpr_read_b32 v246, a94\n\t"
  "v_mul_f32_e32 v246, v251, v246\n\t"
  "v_accvgpr_write_b32 a94, v246\n\t"
  "v_accvgpr_read_b32 v247, a95\n\t"
  "v_mul_f32_e32 v247, v251, v247\n\t"
  "v_accvgpr_write_b32 a95, v247\n\t"
  "v_accvgpr_read_b32 v246, a96\n\t"
  "v_mul_f32_e32 v246, v250, v246\n\t"
  "v_accvgpr_write_b32 a96, v246\n\t"
  "v_accvgpr_read_b32 v247, a97\n\t"
  "v_mul_f32_e32 v247, v250, v247\n\t"
  "v_accvgpr_write_b32 a97, v247\n\t"
  "v_accvgpr_read_b32 v246, a98\n\t"
  "v_mul_f32_e32 v246, v250, v246\n\t"
  "v_accvgpr_write_b32 a98, v246\n\t"
  "v_accvgpr_read_b32 v247, a99\n\t"
  "v_mul_f32_e32 v247, v250, v247\n\t"
  "v_accvgpr_write_b32 a99, v247\n\t"
  "v_accvgpr_read_b32 v246, a100\n\t"
  "v_mul_f32_e32 v246, v251, v246\n\t"
  "v_accvgpr_write_b32 a100, v246\n\t"
  "v_accvgpr_read_b32 v247, a101\n\t"
  "v_mul_f32_e32 v247, v251, v247\n\t"
  "v_accvgpr_write_b32 a101, v247\n\t"
  "v_accvgpr_read_b32 v246, a102\n\t"
  "v_mul_f32_e32 v246, v251, v246\n\t"
  "v_accvgpr_write_b32 a102, v246\n\t"
  "v_accvgpr_read_b32 v247, a103\n\t"
  "v_mul_f32_e32 v247, v251, v247\n\t"
  "v_accvgpr_write_b32 a103, v247\n\t"
  "v_accvgpr_read_b32 v246, a104\n\t"
  "v_mul_f32_e32 v246, v250, v246\n\t"
  "v_accvgpr_write_b32 a104, v246\n\t"
  "v_accvgpr_read_b32 v247, a105\n\t"
  "v_mul_f32_e32 v247, v250, v247\n\t"
  "v_accvgpr_write_b32 a105, v247\n\t"
  "v_accvgpr_read_b32 v246, a106\n\t"
  "v_mul_f32_e32 v246, v250, v246\n\t"
  "v_accvgpr_write_b32 a106, v246\n\t"
  "v_accvgpr_read_b32 v247, a107\n\t"
  "v_mul_f32_e32 v247, v250, v247\n\t"
  "v_accvgpr_write_b32 a107, v247\n\t"
  "v_accvgpr_read_b32 v246, a108\n\t"
  "v_mul_f32_e32 v246, v251, v246\n\t"
  "v_accvgpr_write_b32 a108, v246\n\t"
  "v_accvgpr_read_b32 v247, a109\n\t"
  "v_mul_f32_e32 v247, v251, v247\n\t"
  "v_accvgpr_write_b32 a109, v247\n\t"
  "v_accvgpr_read_b32 v246, a110\n\t"
  "v_mul_f32_e32 v246, v251, v246\n\t"
  "v_accvgpr_write_b32 a110, v246\n\t"
  "v_accvgpr_read_b32 v247, a111\n\t"
  "v_mul_f32_e32 v247, v251, v247\n\t"
  "v_accvgpr_write_b32 a111, v247\n\t"
  "v_accvgpr_read_b32 v246, a112\n\t"
  "v_mul_f32_e32 v246, v250, v246\n\t"
  "v_accvgpr_write_b32 a112, v246\n\t"
  "v_accvgpr_read_b32 v247, a113\n\t"
  "v_mul_f32_e32 v247, v250, v247\n\t"
  "v_accvgpr_write_b32 a113, v247\n\t"
  "v_accvgpr_read_b32 v246, a114\n\t"
  "v_mul_f32_e32 v246, v250, v246\n\t"
  "v_accvgpr_write_b32 a114, v246\n\t"
  "v_accvgpr_read_b32 v247, a115\n\t"
  "v_mul_f32_e32 v247, v250, v247\n\t"
  "v_accvgpr_write_b32 a115, v247\n\t"
  "v_accvgpr_read_b32 v246, a116\n\t"
  "v_mul_f32_e32 v246, v251, v246\n\t"
  "v_accvgpr_write_b32 a116, v246\n\t"
  "v_accvgpr_read_b32 v247, a117\n\t"
  "v_mul_f32_e32 v247, v251, v247\n\t"
  "v_accvgpr_write_b32 a117, v247\n\t"
  "v_accvgpr_read_b32 v246, a118\n\t"
  "v_mul_f32_e32 v246, v251, v246\n\t"
  "v_accvgpr_write_b32 a118, v246\n\t"
  "v_accvgpr_read_b32 v247, a119\n\t"
  "v_mul_f32_e32 v247, v251, v247\n\t"
  "v_accvgpr_write_b32 a119, v247\n\t"
  "v_accvgpr_read_b32 v246, a120\n\t"
  "v_mul_f32_e32 v246, v250, v246\n\t"
  "v_accvgpr_write_b32 a120, v246\n\t"
  "v_accvgpr_read_b32 v247, a121\n\t"
  "v_mul_f32_e32 v247, v250, v247\n\t"
  "v_accvgpr_write_b32 a121, v247\n\t"
  "v_accvgpr_read_b32 v246, a122\n\t"
  "v_mul_f32_e32 v246, v250, v246\n\t"
  "v_accvgpr_write_b32 a122, v246\n\t"
  "v_accvgpr_read_b32 v247, a123\n\t"
  "v_mul_f32_e32 v247, v250, v247\n\t"
  "v_accvgpr_write_b32 a123, v247\n\t"
  "v_accvgpr_read_b32 v246, a124\n\t"
  "v_mul_f32_e32 v246, v251, v246\n\t"
  "v_accvgpr_write_b32 a124, v246\n\t"
  "v_accvgpr_read_b32 v247, a125\n\t"
  "v_mul_f32_e32 v247, v251, v247\n\t"
  "v_accvgpr_write_b32 a125, v247\n\t"
  "v_accvgpr_read_b32 v246, a126\n\t"
  "v_mul_f32_e32 v246, v251, v246\n\t"
  "v_accvgpr_write_b32 a126, v246\n\t"
  "v_accvgpr_read_b32 v247, a127\n\t"
  "v_mul_f32_e32 v247, v251, v247\n\t"
  "v_accvgpr_write_b32 a127, v247\n\t"
  "v_mul_f32_e32 v0, v252, v0\n\t"
  "v_mul_f32_e32 v1, v252, v1\n\t"
  "v_mul_f32_e32 v2, v252, v2\n\t"
  "v_mul_f32_e32 v3, v252, v3\n\t"
  "v_mul_f32_e32 v4, v252, v4\n\t"
  "v_mul_f32_e32 v5, v252, v5\n\t"
  "v_mul_f32_e32 v6, v252, v6\n\t"
  "v_mul_f32_e32 v7, v252, v7\n\t"
  "v_mul_f32_e32 v8, v253, v8\n\t"
  "v_mul_f32_e32 v9, v253, v9\n\t"
  "v_mul_f32_e32 v10, v253, v10\n\t"
  "v_mul_f32_e32 v11, v253, v11\n\t"
  "v_mul_f32_e32 v12, v253, v12\n\t"
  "v_mul_f32_e32 v13, v253, v13\n\t"
  "v_mul_f32_e32 v14, v253, v14\n\t"
  "v_mul_f32_e32 v15, v253, v15\n\t"
  "v_mul_f32_e32 v16, v252, v16\n\t"
  "v_mul_f32_e32 v17, v252, v17\n\t"
  "v_mul_f32_e32 v18, v252, v18\n\t"
  "v_mul_f32_e32 v19, v252, v19\n\t"
  "v_mul_f32_e32 v20, v252, v20\n\t"
  "v_mul_f32_e32 v21, v252, v21\n\t"
  "v_mul_f32_e32 v22, v252, v22\n\t"
  "v_mul_f32_e32 v23, v252, v23\n\t"
  "v_mul_f32_e32 v24, v253, v24\n\t"
  "v_mul_f32_e32 v25, v253, v25\n\t"
  "v_mul_f32_e32 v26, v253, v26\n\t"
  "v_mul_f32_e32 v27, v253, v27\n\t"
  "v_mul_f32_e32 v28, v253, v28\n\t"
  "v_mul_f32_e32 v29, v253, v29\n\t"
  "v_mul_f32_e32 v30, v253, v30\n\t"
  "v_mul_f32_e32 v31, v253, v31\n\t"
  "v_mul_f32_e32 v32, v252, v32\n\t"
  "v_mul_f32_e32 v33, v252, v33\n\t"
  "v_mul_f32_e32 v34, v252, v34\n\t"
  "v_mul_f32_e32 v35, v252, v35\n\t"
  "v_mul_f32_e32 v36, v252, v36\n\t"
  "v_mul_f32_e32 v37, v252, v37\n\t"
  "v_mul_f32_e32 v38, v252, v38\n\t"
  "v_mul_f32_e32 v39, v252, v39\n\t"
  "v_mul_f32_e32 v40, v253, v40\n\t"
  "v_mul_f32_e32 v41, v253, v41\n\t"
  "v_mul_f32_e32 v42, v253, v42\n\t"
  "v_mul_f32_e32 v43, v253, v43\n\t"
  "v_mul_f32_e32 v44, v253, v44\n\t"
  "v_mul_f32_e32 v45, v253, v45\n\t"
  "v_mul_f32_e32 v46, v253, v46\n\t"
  "v_mul_f32_e32 v47, v253, v47\n\t"
  "v_mul_f32_e32 v48, v252, v48\n\t"
  "v_mul_f32_e32 v49, v252, v49\n\t"
  "v_mul_f32_e32 v50, v252, v50\n\t"
  "v_mul_f32_e32 v51, v252, v51\n\t"
  "v_mul_f32_e32 v52, v252, v52\n\t"
  "v_mul_f32_e32 v53, v252, v53\n\t"
  "v_mul_f32_e32 v54, v252, v54\n\t"
  "v_mul_f32_e32 v55, v252, v55\n\t"
  "v_mul_f32_e32 v56, v253, v56\n\t"
  "v_mul_f32_e32 v57, v253, v57\n\t"
  "v_mul_f32_e32 v58, v253, v58\n\t"
  "v_mul_f32_e32 v59, v253, v59\n\t"
  "v_mul_f32_e32 v60, v253, v60\n\t"
  "v_mul_f32_e32 v61, v253, v61\n\t"
  "v_mul_f32_e32 v62, v253, v62\n\t"
  "v_mul_f32_e32 v63, v253, v63\n\t"
  "v_mul_f32_e32 v64, v252, v64\n\t"
  "v_mul_f32_e32 v65, v252, v65\n\t"
  "v_mul_f32_e32 v66, v252, v66\n\t"
  "v_mul_f32_e32 v67, v252, v67\n\t"
  "v_mul_f32_e32 v68, v252, v68\n\t"
  "v_mul_f32_e32 v69, v252, v69\n\t"
  "v_mul_f32_e32 v70, v252, v70\n\t"
  "v_mul_f32_e32 v71, v252, v71\n\t"
  "v_mul_f32_e32 v72, v253, v72\n\t"
  "v_mul_f32_e32 v73, v253, v73\n\t"
  "v_mul_f32_e32 v74, v253, v74\n\t"
  "v_mul_f32_e32 v75, v253, v75\n\t"
  "v_mul_f32_e32 v76, v253, v76\n\t"
  "v_mul_f32_e32 v77, v253, v77\n\t"
  "v_mul_f32_e32 v78, v253, v78\n\t"
  "v_mul_f32_e32 v79, v253, v79\n\t"
  "v_mul_f32_e32 v80, v252, v80\n\t"
  "v_mul_f32_e32 v81, v252, v81\n\t"
  "v_mul_f32_e32 v82, v252, v82\n\t"
  "v_mul_f32_e32 v83, v252, v83\n\t"
  "v_mul_f32_e32 v84, v252, v84\n\t"
  "v_mul_f32_e32 v85, v252, v85\n\t"
  "v_mul_f32_e32 v86, v252, v86\n\t"
  "v_mul_f32_e32 v87, v252, v87\n\t"
  "v_mul_f32_e32 v88, v253, v88\n\t"
  "v_mul_f32_e32 v89, v253, v89\n\t"
  "v_mul_f32_e32 v90, v253, v90\n\t"
  "v_mul_f32_e32 v91, v253, v91\n\t"
  "v_mul_f32_e32 v92, v253, v92\n\t"
  "v_mul_f32_e32 v93, v253, v93\n\t"
  "v_mul_f32_e32 v94, v253, v94\n\t"
  "v_mul_f32_e32 v95, v253, v95\n\t"
  "v_mul_f32_e32 v96, v252, v96\n\t"
  "v_mul_f32_e32 v97, v252, v97\n\t"
  "v_mul_f32_e32 v98, v252, v98\n\t"
  "v_mul_f32_e32 v99, v252, v99\n\t"
  "v_mul_f32_e32 v100, v252, v100\n\t"
  "v_mul_f32_e32 v101, v252, v101\n\t"
  "v_mul_f32_e32 v102, v252, v102\n\t"
  "v_mul_f32_e32 v103, v252, v103\n\t"
  "v_mul_f32_e32 v104, v253, v104\n\t"
  "v_mul_f32_e32 v105, v253, v105\n\t"
  "v_mul_f32_e32 v106, v253, v106\n\t"
  "v_mul_f32_e32 v107, v253, v107\n\t"
  "v_mul_f32_e32 v108, v253, v108\n\t"
  "v_mul_f32_e32 v109, v253, v109\n\t"
  "v_mul_f32_e32 v110, v253, v110\n\t"
  "v_mul_f32_e32 v111, v253, v111\n\t"
  "v_mul_f32_e32 v112, v252, v112\n\t"
  "v_mul_f32_e32 v113, v252, v113\n\t"
  "v_mul_f32_e32 v114, v252, v114\n\t"
  "v_mul_f32_e32 v115, v252, v115\n\t"
  "v_mul_f32_e32 v116, v252, v116\n\t"
  "v_mul_f32_e32 v117, v252, v117\n\t"
  "v_mul_f32_e32 v118, v252, v118\n\t"
  "v_mul_f32_e32 v119, v252, v119\n\t"
  "v_mul_f32_e32 v120, v253, v120\n\t"
  "v_mul_f32_e32 v121, v253, v121\n\t"
  "v_mul_f32_e32 v122, v253, v122\n\t"
  "v_mul_f32_e32 v123, v253, v123\n\t"
  "v_mul_f32_e32 v124, v253, v124\n\t"
  "v_mul_f32_e32 v125, v253, v125\n\t"
  "v_mul_f32_e32 v126, v253, v126\n\t"
  "v_mul_f32_e32 v127, v253, v127\n\t"
  "s_mov_b32 s34, 0\n\t"
  "s_add_u32 s66, s66, 4\n\t"
  "s_addc_u32 s67, s67, 0\n\t"
  "s_nop 1\n\t"
  "s_setpc_b64 s[66:67]\n\t"
  ".Lend:\n\t"
  :: "s"(__builtin_amdgcn_kernarg_segment_ptr()), "s"(blockIdx.x), "s"(wv) : "v0", "v1", "v2", "v3", "v4", "v5", "v6", "v7", "v8", "v9", "v10", "v11", "v12", "v13", "v14", "v15", "v16", "v17", "v18", "v19", "v20", "v21", "v22", "v23", "v24", "v25", "v26", "v27", "v28", "v29", "v30", "v31", "v32", "v33", "v34", "v35", "v36", "v37", "v38", "v39", "v40", "v41", "v42", "v43", "v44", "v45", "v46", "v47", "v48", "v49", "v50", "v51", "v52", "v53", "v54", "v55", "v56", "v57", "v58", "v59", "v60", "v61", "v62", "v63", "v64", "v65", "v66", "v67", "v68", "v69", "v70", "v71", "v72", "v73", "v74", "v75", "v76", "v77", "v78", "v79", "v80", "v81", "v82", "v83", "v84", "v85", "v86", "v87", "v88", "v89", "v90", "v91", "v92", "v93", "v94", "v95", "v96", "v97", "v98", "v99", "v100", "v101", "v102", "v103", "v104", "v105", "v106", "v107", "v108", "v109", "v110", "v111", "v112", "v113", "v114", "v115", "v116", "v117", "v118", "v119", "v120", "v121", "v122", "v123", "v124", "v125", "v126", "v127", "v128", "v129", "v130", "v131", "v132", "v133", "v134", "v135", "v136", "v137", "v138", "v139", "v140", "v141", "v142", "v143", "v144", "v145", "v146", "v147", "v148", "v149", "v150", "v151", "v152", "v153", "v154", "v155", "v156", "v157", "v158", "v159", "v160", "v161", "v162", "v163", "v164", "v165", "v166", "v167", "v168", "v169", "v170", "v171", "v172", "v173", "v174", "v175", "v176", "v177", "v178", "v179", "v180", "v181", "v182", "v183", "v184", "v185", "v186", "v187", "v188", "v189", "v190", "v191", "v192", "v193", "v194", "v195", "v196", "v197", "v198", "v199", "v200", "v201", "v202", "v203", "v204", "v205", "v206", "v207", "v208", "v209", "v210", "v211", "v212", "v213", "v214", "v215", "v216", "v217", "v218", "v219", "v220", "v221", "v222", "v223", "v224", "v225", "v226", "v227", "v228", "v229", "v230", "v231", "v232", "v233", "v234", "v235", "v236", "v237", "v238", "v239", "v240", "v241", "v242", "v243", "v244", "v245", "v246", "v247", "v248", "v249", "v250", "v251", "v252", "v253", "v254", "v255", "a0", "a1", "a2", "a3", "a4", "a5", "a6", "a7", "a8", "a9", "a10", "a11", "a12", "a13", "a14", "a15", "a16", "a17", "a18", "a19", "a20", "a21", "a22", "a23", "a24", "a25", "a26", "a27", "a28", "a29", "a30", "a31", "a32", "a33", "a34", "a35", "a36", "a37", "a38", "a39", "a40", "a41", "a42", "a43", "a44", "a45", "a46", "a47", "a48", "a49", "a50", "a51", "a52", "a53", "a54", "a55", "a56", "a57", "a58", "a59", "a60", "a61", "a62", "a63", "a64", "a65", "a66", "a67", "a68", "a69", "a70", "a71", "a72", "a73", "a74", "a75", "a76", "a77", "a78", "a79", "a80", "a81", "a82", "a83", "a84", "a85", "a86", "a87", "a88", "a89", "a90", "a91", "a92", "a93", "a94", "a95", "a96", "a97", "a98", "a99", "a100", "a101", "a102", "a103", "a104", "a105", "a106", "a107", "a108", "a109", "a110", "a111", "a112", "a113", "a114", "a115", "a116", "a117", "a118", "a119", "a120", "a121", "a122", "a123", "a124", "a125", "a126", "a127", "a128", "a129", "a130", "a131", "a132", "a133", "a134", "a135", "a136", "a137", "a138", "a139", "a140", "a141", "a142", "a143", "a144", "a145", "a146", "a147", "a148", "a149", "a150", "a151", "a152", "a153", "a154", "a155", "a156", "a157", "a158", "a159", "a160", "a161", "a162", "a163", "a164", "a165", "a166", "a167", "a168", "a169", "a170", "a171", "a172", "a173", "a174", "a175", "a176", "a177", "a178", "a179", "a180", "a181", "a182", "a183", "a184", "a185", "a186", "a187", "a188", "a189", "a190", "a191", "a192", "a193", "a194", "a195", "a196", "a197", "a198", "a199", "a200", "a201", "a202", "a203", "a204", "a205", "a206", "a207", "a208", "a209", "a210", "a211", "a212", "a213", "a214", "a215", "a216", "a217", "a218", "a219", "a220", "a221", "a222", "a223", "a224", "a225", "a226", "a227", "a228", "a229", "a230", "a231", "a232", "a233", "a234", "a235", "a236", "a237", "a238", "a239", "a240", "a241", "a242", "a243", "a244", "a245", "a246", "a247", "a248", "a249", "a250", "a251", "a252", "a253", "a254", "a255", "s8", "s9", "s10", "s11", "s12", "s13", "s14", "s15", "s16", "s17", "s18", "s19", "s20", "s21", "s22", "s23", "s24", "s25", "s26", "s27", "s28", "s29", "s30", "s31", "s34", "s35", "s36", "s37", "s38", "s39", "s40", "s41", "s42", "s43", "s44", "s45", "s46", "s47", "s48", "s49", "s50", "s51", "s52", "s53", "s54", "s55", "s56", "s57", "s58", "s59", "s60", "s61", "s62", "s63", "s64", "s65", "s66", "s67", "s68", "s69", "s70", "s71", "s72", "s73", "s74", "s75", "s76", "s77", "s78", "s79", "s80", "s81", "s82", "s83", "s84", "s85", "s86", "s87", "s88", "s89", "s90", "s91", "s92", "s93", "s94", "s95", "s96", "s97", "s98", "s99", "vcc", "memory");
}

extern "C" void kernel_launch(void* const* d_in, const int* in_sizes, int n_in, void* d_out, int out_size, void* d_ws, size_t ws_size, hipStream_t stream) {
  char* ws = (char*)d_ws;
  (void)hipFuncSetAttribute((const void*)k_mega, hipFuncAttributeMaxDynamicSharedMemorySize, 163840);
  hipLaunchKernelGGL(k_mega, dim3(256), dim3(256), 163840, stream, (const void*)d_in[0], (const void*)d_in[3], (const void*)d_in[1], (const void*)d_in[2],
                     (const void*)d_in[4], (const void*)d_in[6], (const void*)d_in[7], (const void*)d_out, (const void*)d_in[8], (const void*)d_in[9], (const void*)ws);
}
```
